```python
import torch

def _softmax(x, axis=-1):
    return torch.softmax(x, dim=axis)
def _gelu(x):
    return torch.nn.functional.gelu(x, approximate="tanh")
def _silu(x):
    return torch.nn.functional.silu(x)
def _sigmoid(x):
    return torch.sigmoid(x)
def _layernorm(x, axis=-1, eps=1e-5):
    m = x.mean(dim=axis, keepdim=True)
    v = x.var(dim=axis, keepdim=True, unbiased=False)
    return (x - m) / torch.sqrt(v + eps)
def _scatter_add(tgt, idx, src, axis=0):
    return tgt.clone().index_add_(axis, idx, src)
def _segment_sum(src, seg, n):
    out = torch.zeros((n,) + src.shape[1:], dtype=src.dtype)
    return out.index_add_(0, seg, src)
def _pad(x, pw):
    flat = tuple(v for pair in reversed(pw) for v in pair)
    return torch.nn.functional.pad(x, flat)

kernel_name = 'parallel_sum__gqa_block_cached__mixer_token_mlp__decode'

def setup_inputs():

    _w = (lambda a: (lambda t: t.to(torch.bfloat16) if t.is_floating_point() else t)(torch.as_tensor(a)))
    def _A_setup():
        B, Sq, D = 2, 1, 576
        Hq, Hkv, Sk, Dh = 32, 16, 32768, 128
        assert Hq % Hkv == 0, (Hq, Hkv)
        return {
            "x": _w((lambda *s: torch.randn(*s, dtype=torch.bfloat16) * 0.05)(B, Sq, D)),
            "k_cache": _w((lambda *s: torch.randn(*s, dtype=torch.bfloat16) * 3.2)(B, Hkv, Sk, Dh)),
            "v_cache": _w((lambda *s: torch.randn(*s, dtype=torch.bfloat16) * 0.8)(B, Hkv, Sk, Dh)),
            "wq": _w((lambda *s: torch.randn(*s, dtype=torch.bfloat16) * 3.2)(D, Hq * Dh) * (D ** -0.5)),
            "wo": _w((lambda *s: torch.randn(*s, dtype=torch.bfloat16) * 0.8)(Hq * Dh, D) * ((Hq * Dh) ** -0.5)),
        }
    _A_in = _A_setup()

    def _B_setup():
        B, S, D, F = 2, 1, 576, 4608
        return {
            "x": _w((lambda *s: torch.randn(*s, dtype=torch.bfloat16) * 0.05)(B, S, D)),
            "w1": _w((lambda *s: torch.randn(*s, dtype=torch.bfloat16) * 3.2)(S, F) * (S ** -0.5)),
            "b1": _w((lambda *s: torch.randn(*s, dtype=torch.bfloat16) * 3.2)(F) * 0.02),
            "w2": _w((lambda *s: torch.randn(*s, dtype=torch.bfloat16) * 0.8)(F, S) * (F ** -0.5)),
            "b2": _w((lambda *s: torch.randn(*s, dtype=torch.bfloat16) * 0.8)(S) * 0.02),
        }
    _B_in = _B_setup()

    return {
        "A_x": _A_in["x"],
        "A_k_cache": _A_in["k_cache"],
        "A_v_cache": _A_in["v_cache"],
        "A_wq": _A_in["wq"],
        "A_wo": _A_in["wo"],
        "B_x": _B_in["x"],
        "B_w1": _B_in["w1"],
        "B_b1": _B_in["b1"],
        "B_w2": _B_in["w2"],
        "B_b2": _B_in["b2"],
    }

def reference(A_x, A_k_cache, A_v_cache, A_wq, A_wo, B_x, B_w1, B_b1, B_w2, B_b2):
    def _A_ref(x, k_cache, v_cache, wq, wo):
        B, Sq, D = x.shape
        Hq, Dh = 32, 128
        Hkv = k_cache.shape[1]
        rep = Hq // Hkv
        q = (lambda x, *d: x.permute(*d))((x @ wq).reshape(B, Sq, Hq, Dh), 0, 2, 1, 3)
        k = (lambda x, n, axis: torch.repeat_interleave(x, n, dim=axis))(k_cache, rep, axis=1)
        v = (lambda x, n, axis: torch.repeat_interleave(x, n, dim=axis))(v_cache, rep, axis=1)
        scores = torch.einsum("bhqd,bhkd->bhqk", q, k) * (Dh ** -0.5)
        p = _softmax(scores, axis=-1)
        o = torch.einsum("bhqk,bhkd->bhqd", p, v)
        return (lambda x, *d: x.permute(*d))(o, 0, 2, 1, 3).reshape(B, Sq, Hq * Dh) @ wo

    def _B_ref(x, w1, b1, w2, b2):
        B, S, D = x.shape
        h = _layernorm(x, axis=-1)
        u = torch.einsum("bsd,sf->bdf", h, w1) + b1
        u = _gelu(u)
        y = torch.einsum("bdf,fs->bds", u, w2) + b2
        return (lambda x, *d: x.permute(*d))(y, 0, 2, 1)

    x = A_x
    xn = _layernorm(x, axis=-1)
    return x + _A_ref(xn, A_k_cache, A_v_cache, A_wq, A_wo) + _B_ref(xn, B_w1, B_b1, B_w2, B_b2)
```

```cpp
#include <hip/hip_runtime.h>
#include <hip/hip_bf16.h>
#include <stdint.h>

namespace {

typedef unsigned u32x2 __attribute__((ext_vector_type(2)));
typedef unsigned u32x4 __attribute__((ext_vector_type(4)));
typedef float f32x2 __attribute__((ext_vector_type(2)));
typedef float f32x4 __attribute__((ext_vector_type(4)));
typedef short s16x4 __attribute__((ext_vector_type(4)));
typedef short s16x8 __attribute__((ext_vector_type(8)));
typedef __bf16 bf16x2 __attribute__((ext_vector_type(2)));
typedef __amdgpu_buffer_rsrc_t rsrc_t;

constexpr int DM = 576, HKV = 16, SK = 32768, DH = 128, FF = 4608;
constexpr int NSPLIT = 8, NWG = 256, NWAVE = 8, NTHR = 512;
constexpr int KEYS_WG = SK / NSPLIT;
constexpr int TILE_BYTES = 32 * DH * 2;
constexpr int NT = KEYS_WG / 32;
constexpr int REGION = KEYS_WG * DH * 2;
#define NSTREAM 3
constexpr int S = NSTREAM;
constexpr int NPRE = 7;
constexpr int AUX_NT = 2, AUX_SC1 = 16;
constexpr unsigned RSRC3 = 0x00020000u;
constexpr unsigned SPIN_CAP = 1u << 19;

constexpr int WS_EPOCH = 0;
constexpr int WS_QX = 4096;
constexpr int PX_STRIDE = 2176;
constexpr int WS_PX = WS_QX + 32 * 1024;
constexpr int WS_OX = WS_PX + NWG * PX_STRIDE;
constexpr int WS_END = WS_OX + 2 * 8 * 9 * 1024;
constexpr int WS_STAMP = 1 << 20;

constexpr int L_QPART = 0;
constexpr int L_QS = L_QPART + 8 * 32 * 4;
constexpr int L_OS = L_QS + 512;
constexpr int L_ML = L_OS + 512;
constexpr int L_YP = L_ML + 8 * 4 * 4;
constexpr int L_CTR = L_YP + 4 * 8 * 4;
constexpr int L_PART = L_CTR + 16;
constexpr int L_END = L_PART + 8 * 256 * 4;

constexpr float LOG2E = 1.4426950408889634f;
constexpr float SCALE = 0.08838834764831845f;
constexpr float LN_EPS = 1e-5f;

__device__ __forceinline__ float asf(unsigned u) { return __builtin_bit_cast(float, u); }
__device__ __forceinline__ unsigned asu(float f) { return __builtin_bit_cast(unsigned, f); }
__device__ __forceinline__ float bflo(unsigned u) { return asf(u << 16); }
__device__ __forceinline__ float bfhi(unsigned u) { return asf(u & 0xffff0000u); }
__device__ __forceinline__ unsigned pack_bf16(float lo, float hi) {
  f32x2 v = {lo, hi};
  return __builtin_bit_cast(unsigned, __builtin_convertvector(v, bf16x2));
}
__device__ __forceinline__ float rbf(float f) { return (float)(__bf16)f; }
template <int CTRL> __device__ __forceinline__ unsigned dppu(unsigned x) {
  return (unsigned)__builtin_amdgcn_mov_dpp((int)x, CTRL, 0xf, 0xf, true);
}
template <int CTRL> __device__ __forceinline__ float dppf(float x) { return asf(dppu<CTRL>(asu(x))); }
constexpr int XOR1 = 0xB1, XOR2 = 0x4E, ROR4 = 0x124, ROR8 = 0x128;
constexpr int QB0 = 0x00, QB1 = 0x55, QB2 = 0xAA, QB3 = 0xFF;
__device__ __forceinline__ float rows_sum(float x) {
  auto s = __builtin_amdgcn_permlane16_swap(asu(x), asu(x), false, false);
  x = asf(s[0]) + asf(s[1]);
  auto t = __builtin_amdgcn_permlane32_swap(asu(x), asu(x), false, false);
  return asf(t[0]) + asf(t[1]);
}
__device__ __forceinline__ float rows_max(float x) {
  auto s = __builtin_amdgcn_permlane16_swap(asu(x), asu(x), false, false);
  x = fmaxf(asf(s[0]), asf(s[1]));
  auto t = __builtin_amdgcn_permlane32_swap(asu(x), asu(x), false, false);
  return fmaxf(asf(t[0]), asf(t[1]));
}
__device__ __forceinline__ float wave_sum(float x) {
  x += dppf<XOR1>(x); x += dppf<XOR2>(x); x += dppf<ROR4>(x); x += dppf<ROR8>(x);
  return rows_sum(x);
}
__device__ __forceinline__ rsrc_t mkrsrc(const void* p, int bytes) {
  return __builtin_amdgcn_make_buffer_rsrc(const_cast<void*>(p), 0, bytes, RSRC3);
}
__device__ __forceinline__ f32x4 mfma32(u32x4 a, u32x4 b, f32x4 c) {
  return __builtin_amdgcn_mfma_f32_16x16x32_bf16(__builtin_bit_cast(s16x8, a), __builtin_bit_cast(s16x8, b), c, 0, 0, 0);
}
__device__ __forceinline__ f32x4 mfma16(unsigned a0, unsigned a1, unsigned b0, unsigned b1, f32x4 c) {
  u32x2 a = {a0, a1}, b = {b0, b1};
  return __builtin_amdgcn_mfma_f32_16x16x16bf16_1k(__builtin_bit_cast(s16x4, a), __builtin_bit_cast(s16x4, b), c, 0, 0, 0);
}
__device__ __forceinline__ void wg_barrier() {
  asm volatile("s_waitcnt lgkmcnt(0)\n\ts_barrier" ::: "memory");
}
__device__ __forceinline__ void ln_stats(const float (&v)[10], bool last_valid, float& mean_b, float& den_b) {
  float s = 0.f;
#pragma unroll
  for (int i = 0; i < 8; ++i) s += v[i];
  s += last_valid ? (v[8] + v[9]) : 0.f;
  const float mean = wave_sum(s) * (1.0f / DM);
  float q = 0.f;
#pragma unroll
  for (int i = 0; i < 8; ++i) { const float d = v[i] - mean; q = fmaf(d, d, q); }
  { const float d8 = v[8] - mean, d9 = v[9] - mean; q += last_valid ? fmaf(d8, d8, d9 * d9) : 0.f; }
  const float var = wave_sum(q) * (1.0f / DM);
  mean_b = rbf(mean);
  den_b = rbf(sqrtf(rbf(rbf(var) + LN_EPS)));
}
__device__ __forceinline__ float ln_apply(float x, float mean_b, float den_b) { return rbf(rbf(x - mean_b) / den_b); }

#define STAMP(i) do { } while (0)

__global__ void __launch_bounds__(NTHR, 2)
fused_decode_kernel(const unsigned short* __restrict__ x, const unsigned char* __restrict__ kc, const unsigned char* __restrict__ vc,
                    const unsigned short* __restrict__ wq, const unsigned short* __restrict__ wo,
                    const unsigned short* __restrict__ w1, const unsigned short* __restrict__ b1,
                    const unsigned short* __restrict__ w2, const unsigned short* __restrict__ b2,
                    unsigned short* __restrict__ out, unsigned char* ws) {
  __shared__ __attribute__((aligned(16))) unsigned char lds[L_END];
  const int tid = threadIdx.x, lane = tid & 63, w = __builtin_amdgcn_readfirstlane(tid >> 6);
  const int id = blockIdx.x, g = id >> 3, s = id & 7, b = g >> 4, kvh = g & 15;
  STAMP(0);
  const rsrc_t rws = mkrsrc(ws, WS_END);
  const rsrc_t rx = mkrsrc(x + b * DM, DM * 2);
  const rsrc_t rk = mkrsrc(kc + (size_t)id * REGION, REGION);
  const rsrc_t rv = mkrsrc(vc + (size_t)id * REGION, REGION);

  const int rg = tid >> 2, c4 = tid & 3;
  unsigned xs[5], xo[5];
  u32x4 wqv[5];
#pragma unroll
  for (int i = 0; i < 5; ++i) xs[i] = __builtin_amdgcn_raw_buffer_load_b32(rx, (lane + 64 * i) * 4, 0, 0);
#pragma unroll
  for (int i = 0; i < 5; ++i) xo[i] = __builtin_amdgcn_raw_buffer_load_b32(rx, ((rg >> 1) + 64 * i) * 4, 0, 0);
  {
    const rsrc_t rq = mkrsrc(wq + kvh * 256 + s * 32, ((DM - 1) * 4096 + 32) * 2);
#pragma unroll
    for (int i = 0; i < 5; ++i) wqv[i] = __builtin_amdgcn_raw_buffer_load_b128(rq, (rg + 128 * i) * 8192 + c4 * 16, 0, 0);
  }
  const u32x2 ep = __builtin_amdgcn_raw_buffer_load_b64(rws, WS_EPOCH + b * 256, 0, AUX_SC1);
  __builtin_amdgcn_sched_barrier(0);
  const int voffK = ((lane & 15) >> 2) * 256 + (lane >> 4) * 64 + (lane & 3) * 16;
  const int voffV = lane * 16;
  u32x4 kb[8], vb[8];
  {
    const int so = w < NPRE ? w * TILE_BYTES : REGION;
#pragma unroll
    for (int u = 0; u < 8; ++u) kb[u] = __builtin_amdgcn_raw_buffer_load_b128(rk, voffK + u * 1024, so, AUX_NT);
#pragma unroll
    for (int u = 0; u < 8; ++u) vb[u] = __builtin_amdgcn_raw_buffer_load_b128(rv, voffV + u * 1024, so, AUX_NT);
  }
  __builtin_amdgcn_sched_barrier(0);
  if (tid == 0) *(unsigned*)(lds + L_CTR) = NPRE;

  float xv[10];
#pragma unroll
  for (int i = 0; i < 5; ++i) { xv[2 * i] = bflo(xs[i]); xv[2 * i + 1] = bfhi(xs[i]); }
  float mean1, den1;
  ln_stats(xv, lane < 32, mean1, den1);
  {
    float a[8];
#pragma unroll
    for (int j = 0; j < 8; ++j) a[j] = 0.f;
#pragma unroll
    for (int i = 0; i < 5; ++i) {
      const float xn = ln_apply((rg & 1) ? bfhi(xo[i]) : bflo(xo[i]), mean1, den1);
#pragma unroll
      for (int j = 0; j < 4; ++j) { a[2 * j] = fmaf(xn, bflo(wqv[i][j]), a[2 * j]); a[2 * j + 1] = fmaf(xn, bfhi(wqv[i][j]), a[2 * j + 1]); }
    }
#pragma unroll
    for (int j = 0; j < 4; ++j) { auto t = __builtin_amdgcn_permlane32_swap(asu(a[j]), asu(a[j + 4]), false, false); a[j] = asf(t[0]) + asf(t[1]); }
#pragma unroll
    for (int j = 0; j < 2; ++j) { auto t = __builtin_amdgcn_permlane16_swap(asu(a[j]), asu(a[j + 2]), false, false); a[j] = asf(t[0]) + asf(t[1]); }
#pragma unroll
    for (int j = 0; j < 2; ++j) { a[j] += dppf<ROR8>(a[j]); a[j] += dppf<ROR4>(a[j]); }
    if ((lane & 15) < 4) *(f32x2*)(lds + L_QPART + (w * 32 + (lane & 3) * 8 + (lane >> 4) * 2) * 4) = f32x2{a[0], a[1]};
  }
  const unsigned tagA = __builtin_amdgcn_readfirstlane(ep[0]) + 1u, tagB = __builtin_amdgcn_readfirstlane(ep[1]) + 1u;
  STAMP(1);
  wg_barrier();
  if (w == NWAVE - 1) {
    if (lane < 8) {
      f32x4 q4 = *(const f32x4*)(lds + L_QPART + lane * 16);
#pragma unroll
      for (int ww = 1; ww < NWAVE; ++ww) q4 += *(const f32x4*)(lds + L_QPART + ww * 128 + lane * 16);
      const u32x4 gr = {tagA, pack_bf16(q4[0], q4[1]), tagB, pack_bf16(q4[2], q4[3])};
      __builtin_amdgcn_raw_buffer_store_b128(gr, rws, WS_QX + g * 1024 + s * 128 + lane * 16, 0, AUX_SC1);
    }
    u32x4 gr;
    for (unsigned spin = 0; spin < SPIN_CAP; ++spin) {
      asm volatile("" ::: "memory");
      gr = __builtin_amdgcn_raw_buffer_load_b128(rws, WS_QX + g * 1024 + lane * 16, 0, AUX_SC1);
      if (__all(gr[0] == tagA && gr[2] == tagB)) break;
      __builtin_amdgcn_s_sleep(2);
    }
    *(u32x2*)(lds + L_QS + lane * 8) = u32x2{gr[1], gr[3]};
  }
  wg_barrier();
  STAMP(2);

  const int c = lane & 3, hb = (lane >> 2) & 1, tb = (lane >> 3) & 1, la = lane >> 4;
  float m = -3.0e38f, l = 0.f;
  f32x4 acc[2][2];
#pragma unroll
  for (int i = 0; i < 2; ++i)
#pragma unroll
    for (int j = 0; j < 2; ++j) acc[i][j] = f32x4{0.f, 0.f, 0.f, 0.f};
  if (w < NPRE) {
    const u32x4 qf = *(const u32x4*)(lds + L_QS + (hb * 128 + la * 32 + c * 8) * 2);
    const u32x4 zero4 = {0u, 0u, 0u, 0u};
    const u32x4 q0 = tb ? zero4 : qf, q1 = tb ? qf : zero4;
    const unsigned mask0 = c == 0 ? 0x0000ffffu : c == 1 ? 0xffff0000u : 0u;
    const unsigned mask1 = c == 2 ? 0x0000ffffu : c == 3 ? 0xffff0000u : 0u;
    if (w < S) __builtin_amdgcn_s_setprio(2);
    for (;;) {
      int nxt = NT;
      if (w < S) {
        unsigned n = 0;
        if (lane == 0) n = __hip_atomic_fetch_add((unsigned*)(lds + L_CTR), 1u, __ATOMIC_RELAXED, __HIP_MEMORY_SCOPE_WORKGROUP);
        nxt = __builtin_amdgcn_readfirstlane(n);
      }
      const int so = nxt < NT ? nxt * TILE_BYTES : REGION;
      f32x4 sc[4];
#pragma unroll
      for (int i = 0; i < 4; ++i) sc[i] = mfma32(kb[i], q0, f32x4{0.f, 0.f, 0.f, 0.f});
#pragma unroll
      for (int i = 0; i < 4; ++i) sc[i] = mfma32(kb[4 + i], q1, sc[i]);
      __builtin_amdgcn_sched_barrier(0);
#pragma unroll
      for (int u = 0; u < 8; ++u) kb[u] = __builtin_amdgcn_raw_buffer_load_b128(rk, voffK + u * 1024, so, AUX_NT);
      __builtin_amdgcn_sched_barrier(0);
      float t4[4];
#pragma unroll
      for (int i = 0; i < 4; ++i) t4[i] = (dppf<QB0>(sc[i][0]) + dppf<QB1>(sc[i][1])) + (dppf<QB2>(sc[i][2]) + dppf<QB3>(sc[i][3]));
      const float raw = (c & 2) ? ((c & 1) ? t4[3] : t4[2]) : ((c & 1) ? t4[1] : t4[0]);
      const float sv = rbf(rbf(raw) * SCALE);
      if (__any(sv > m)) {
        float mx = fmaxf(sv, dppf<XOR1>(sv));
        mx = fmaxf(mx, dppf<XOR2>(mx));
        mx = fmaxf(mx, dppf<ROR8>(mx));
        mx = fmaxf(rows_max(mx), m);
        const float alpha = __builtin_amdgcn_exp2f((m - mx) * LOG2E);
        m = mx;
        l *= alpha;
#pragma unroll
        for (int i = 0; i < 2; ++i)
#pragma unroll
          for (int j = 0; j < 2; ++j) acc[i][j] *= alpha;
      }
      const float p = __builtin_amdgcn_exp2f((sv - m) * LOG2E);
      l += p;
      const unsigned pk = pack_bf16(p, p);
      unsigned pb0[4], pb1[4];
      pb0[0] = dppu<QB0>(pk) & mask0; pb1[0] = dppu<QB0>(pk) & mask1;
      pb0[1] = dppu<QB1>(pk) & mask0; pb1[1] = dppu<QB1>(pk) & mask1;
      pb0[2] = dppu<QB2>(pk) & mask0; pb1[2] = dppu<QB2>(pk) & mask1;
      pb0[3] = dppu<QB3>(pk) & mask0; pb1[3] = dppu<QB3>(pk) & mask1;
#pragma unroll
      for (int u = 0; u < 8; ++u) {
        acc[u >> 2][0] = mfma16(vb[u][0], vb[u][1], pb0[u & 3], pb1[u & 3], acc[u >> 2][0]);
        acc[u >> 2][1] = mfma16(vb[u][2], vb[u][3], pb0[u & 3], pb1[u & 3], acc[u >> 2][1]);
      }
      __builtin_amdgcn_sched_barrier(0);
#pragma unroll
      for (int u = 0; u < 8; ++u) vb[u] = __builtin_amdgcn_raw_buffer_load_b128(rv, voffV + u * 1024, so, AUX_NT);
      __builtin_amdgcn_sched_barrier(0);
      if (nxt >= NT) break;
    }
  }
  STAMP(3);
  {
    float* part = (float*)(lds + L_PART) + w * 256 + hb * 128 + la * 32 + tb * 4 + c;
#pragma unroll
    for (int r = 0; r < 4; ++r) {
      const float own = tb ? acc[1][1][r] : acc[0][0][r], snd = tb ? acc[1][0][r] : acc[0][1][r];
      part[r * 8] = own + dppf<ROR8>(snd);
    }
    float ls = l + dppf<XOR1>(l);
    ls += dppf<XOR2>(ls);
    ls += dppf<ROR8>(ls);
    ls = rows_sum(ls);
    if ((lane & ~4) == 0) *(f32x2*)(lds + L_ML + (w * 2 + hb) * 8) = f32x2{m, ls};
  }

  const int col0 = 72 * s + 8 * kvh;
  u32x4 wof[8];
  if (w >= 3) {
    const int ct = w - 3;
    const int col = 16 * ct + (lane & 15);
    const rsrc_t ro = mkrsrc(wo + (size_t)kvh * 256 * DM + 72 * s, (255 * DM + 72) * 2 + 4);
    const int vo = col < 72 ? ((lane >> 4) * 8 * DM + col) * 2 : 0x7ffffff0 - 256 * DM * 2;
#pragma unroll
    for (int ks = 0; ks < 8; ++ks)
#pragma unroll
      for (int j = 0; j < 4; ++j) {
        const unsigned lo = __builtin_amdgcn_raw_buffer_load_b16(ro, vo + (ks * 32 + 2 * j) * DM * 2, 0, 0);
        const unsigned hi = __builtin_amdgcn_raw_buffer_load_b16(ro, vo + (ks * 32 + 2 * j + 1) * DM * 2, 0, 0);
        wof[ks][j] = lo | (hi << 16);
      }
  }
  if (w >= 4 && kvh < 9) {
    float xn[10];
#pragma unroll
    for (int i = 0; i < 10; ++i) xn[i] = ln_apply(xv[i], mean1, den1);
    float mean2, den2;
    ln_stats(xn, lane < 32, mean2, den2);
    const u32x4 xc = __builtin_amdgcn_raw_buffer_load_b128(rx, col0 * 2, 0, 0);
    float hv[8], y[8];
#pragma unroll
    for (int j = 0; j < 4; ++j) {
      hv[2 * j] = ln_apply(ln_apply(bflo(xc[j]), mean1, den1), mean2, den2);
      hv[2 * j + 1] = ln_apply(ln_apply(bfhi(xc[j]), mean1, den1), mean2, den2);
    }
#pragma unroll
    for (int j = 0; j < 8; ++j) y[j] = 0.f;
    const rsrc_t r1 = mkrsrc(w1, FF * 2), rb = mkrsrc(b1, FF * 2), r2 = mkrsrc(w2, FF * 2);
    const int ht = tid - 256;
    constexpr float GC0 = -2.0f * 0.7978845608028654f * LOG2E, GC1 = GC0 * 0.044715f;
#pragma unroll 1
    for (int i = 0; i < 9; ++i) {
      const unsigned a1 = __builtin_amdgcn_raw_buffer_load_b32(r1, (ht + 256 * i) * 4, 0, 0);
      const unsigned ab = __builtin_amdgcn_raw_buffer_load_b32(rb, (ht + 256 * i) * 4, 0, 0);
      const unsigned a2 = __builtin_amdgcn_raw_buffer_load_b32(r2, (ht + 256 * i) * 4, 0, 0);
#pragma unroll
      for (int j = 0; j < 8; ++j) {
        const unsigned pr = pack_bf16(hv[j] * bflo(a1), hv[j] * bfhi(a1));
        const unsigned uu = pack_bf16(bflo(pr) + bflo(ab), bfhi(pr) + bfhi(ab));
        const float u0 = bflo(uu), u1 = bfhi(uu);
        const float e0 = __builtin_amdgcn_exp2f(u0 * fmaf(u0 * u0, GC1, GC0));
        const float e1 = __builtin_amdgcn_exp2f(u1 * fmaf(u1 * u1, GC1, GC0));
        const unsigned gg = pack_bf16(u0 * __builtin_amdgcn_rcpf(1.0f + e0), u1 * __builtin_amdgcn_rcpf(1.0f + e1));
        y[j] = fmaf(bflo(gg), bflo(a2), y[j]);
        y[j] = fmaf(bfhi(gg), bfhi(a2), y[j]);
      }
    }
#pragma unroll
    for (int j = 0; j < 8; ++j) y[j] = wave_sum(y[j]);
    if (lane == 0) {
      *(f32x4*)(lds + L_YP + (w - 4) * 32) = f32x4{y[0], y[1], y[2], y[3]};
      *(f32x4*)(lds + L_YP + (w - 4) * 32 + 16) = f32x4{y[4], y[5], y[6], y[7]};
    }
  }
  unsigned xfin = 0, b2v = 0;
  if (w == 2) {
    xfin = __builtin_amdgcn_raw_buffer_load_b32(rx, (col0 + 2 * (lane & 3)) * 2, 0, 0);
    b2v = b2[0];
  }
  STAMP(4);
  wg_barrier();
  STAMP(5);

  if (w < 2) {
    float mw[NWAVE], lw[NWAVE];
#pragma unroll
    for (int ww = 0; ww < NWAVE; ++ww) { const f32x2 v = *(const f32x2*)(lds + L_ML + (ww * 2 + w) * 8); mw[ww] = v[0]; lw[ww] = v[1]; }
    float M = mw[0];
#pragma unroll
    for (int ww = 1; ww < NWAVE; ++ww) M = fmaxf(M, mw[ww]);
    float L = 0.f, a0 = 0.f, a1 = 0.f;
#pragma unroll
    for (int ww = 0; ww < NWAVE; ++ww) {
      const float wt = __builtin_amdgcn_exp2f((mw[ww] - M) * LOG2E);
      const f32x2 v = *(const f32x2*)(lds + L_PART + (ww * 256 + tid * 2) * 4);
      L = fmaf(wt, lw[ww], L); a0 = fmaf(wt, v[0], a0); a1 = fmaf(wt, v[1], a1);
    }
    __builtin_amdgcn_raw_buffer_store_b128(u32x4{tagA, asu(a0), tagB, asu(a1)}, rws, WS_PX + id * PX_STRIDE + tid * 16, 0, AUX_SC1);
    if (lane == 0) __builtin_amdgcn_raw_buffer_store_b128(u32x4{tagA, asu(M), tagB, asu(L)}, rws, WS_PX + id * PX_STRIDE + 2048 + w * 16, 0, AUX_SC1);
    float d0[NSPLIT], d1[NSPLIT], ms[NSPLIT], lsv[NSPLIT];
#pragma unroll
    for (int i = 0; i < NSPLIT; ++i) { d0[i] = 0.f; d1[i] = 0.f; ms[i] = 0.f; lsv[i] = 0.f; }
    unsigned have = 0;
    for (unsigned spin = 0; spin < SPIN_CAP; ++spin) {
      asm volatile("" ::: "memory");
      u32x4 gd[NSPLIT], gm[NSPLIT];
#pragma unroll
      for (int i = 0; i < NSPLIT; ++i) {
        const int so = ((have >> i) & 1u) ? WS_END : WS_PX + (g * 8 + i) * PX_STRIDE;
        gd[i] = __builtin_amdgcn_raw_buffer_load_b128(rws, tid * 16, so, AUX_SC1);
        gm[i] = __builtin_amdgcn_raw_buffer_load_b128(rws, 2048 + w * 16, so, AUX_SC1);
      }
#pragma unroll
      for (int i = 0; i < NSPLIT; ++i) {
        const bool fresh = !((have >> i) & 1u) && __all(gd[i][0] == tagA && gd[i][2] == tagB && gm[i][0] == tagA && gm[i][2] == tagB);
        d0[i] = fresh ? asf(gd[i][1]) : d0[i]; d1[i] = fresh ? asf(gd[i][3]) : d1[i];
        ms[i] = fresh ? asf(gm[i][1]) : ms[i]; lsv[i] = fresh ? asf(gm[i][3]) : lsv[i];
        have |= fresh ? (1u << i) : 0u;
      }
      if (have == 0xffu) break;
      __builtin_amdgcn_s_sleep(4);
    }
    float MM = ms[0];
#pragma unroll
    for (int i = 1; i < NSPLIT; ++i) MM = fmaxf(MM, ms[i]);
    float LL = 0.f, o0 = 0.f, o1 = 0.f;
#pragma unroll
    for (int i = 0; i < NSPLIT; ++i) {
      const float wt = __builtin_amdgcn_exp2f((ms[i] - MM) * LOG2E);
      LL = fmaf(wt, lsv[i], LL); o0 = fmaf(wt, d0[i], o0); o1 = fmaf(wt, d1[i], o1);
    }
    const float inv = 1.0f / LL;
    *(unsigned*)(lds + L_OS + tid * 4) = pack_bf16(o0 * inv, o1 * inv);
  }
  STAMP(6);
  wg_barrier();

  if (w >= 3) {
    f32x4 d = {0.f, 0.f, 0.f, 0.f};
#pragma unroll
    for (int ks = 0; ks < 8; ++ks) d = mfma32(*(const u32x4*)(lds + L_OS + (ks * 32 + (lane >> 4) * 8) * 2), wof[ks], d);
    const float v0 = d[0], v1 = dppf<XOR1>(v0);
    const int col = 16 * (w - 3) + lane;
    if (lane < 16 && !(lane & 1) && col < 72)
      __builtin_amdgcn_raw_buffer_store_b128(u32x4{tagA, asu(v0), tagB, asu(v1)}, rws,
                                             WS_OX + ((b * 8 + s) * 9 + (col >> 3)) * 1024 + kvh * 64 + ((col & 7) >> 1) * 16, 0, AUX_SC1);
  } else if (w == 2 && kvh < 9) {
    const int cp = 2 * (lane & 3);
    float y0 = 0.f, y1 = 0.f;
#pragma unroll
    for (int ww = 0; ww < 4; ++ww) { const f32x2 v = *(const f32x2*)(lds + L_YP + ww * 32 + cp * 4); y0 += v[0]; y1 += v[1]; }
    y0 = rbf(rbf(y0) + bflo(b2v)); y1 = rbf(rbf(y1) + bflo(b2v));
    u32x4 gr;
    for (unsigned spin = 0; spin < SPIN_CAP; ++spin) {
      asm volatile("" ::: "memory");
      gr = __builtin_amdgcn_raw_buffer_load_b128(rws, WS_OX + ((b * 8 + s) * 9 + kvh) * 1024 + lane * 16, 0, AUX_SC1);
      if (__all(gr[0] == tagA && gr[2] == tagB)) break;
      __builtin_amdgcn_s_sleep(2);
    }
    float v0 = asf(gr[1]), v1 = asf(gr[3]);
    v0 += dppf<ROR4>(v0); v0 += dppf<ROR8>(v0); v0 = rows_sum(v0);
    v1 += dppf<ROR4>(v1); v1 += dppf<ROR8>(v1); v1 = rows_sum(v1);
    const float r0 = rbf(bflo(xfin) + rbf(v0)) + y0, r1 = rbf(bfhi(xfin) + rbf(v1)) + y1;
    if (lane < 4) *(unsigned*)(out + b * DM + col0 + cp) = pack_bf16(r0, r1);
    if (s == 0 && kvh == 0 && lane == 0) __builtin_amdgcn_raw_buffer_store_b64(u32x2{tagA, tagB}, rws, WS_EPOCH + b * 256, 0, AUX_SC1);
  }
  STAMP(7);
}

}

extern "C" void kernel_launch(void* const* d_in, const int* in_sizes, int n_in, void* d_out, int out_size, void* d_ws,
                              size_t ws_size, hipStream_t stream) {
  (void)in_sizes; (void)n_in; (void)out_size; (void)ws_size;
  fused_decode_kernel<<<dim3(NWG), dim3(NTHR), 0, stream>>>(
      (const unsigned short*)d_in[0], (const unsigned char*)d_in[1], (const unsigned char*)d_in[2],
      (const unsigned short*)d_in[3], (const unsigned short*)d_in[4], (const unsigned short*)d_in[6],
      (const unsigned short*)d_in[7], (const unsigned short*)d_in[8], (const unsigned short*)d_in[9],
      (unsigned short*)d_out, (unsigned char*)d_ws);
}
```

```cpp
#include <hip/hip_runtime.h>
#include <hip/hip_bf16.h>
#include <stdint.h>
#include <type_traits>

namespace {

typedef unsigned u32x2 __attribute__((ext_vector_type(2)));
typedef unsigned u32x4 __attribute__((ext_vector_type(4)));
typedef float f32x2 __attribute__((ext_vector_type(2)));
typedef float f32x4 __attribute__((ext_vector_type(4)));
typedef short s16x4 __attribute__((ext_vector_type(4)));
typedef short s16x8 __attribute__((ext_vector_type(8)));
typedef __bf16 bf16x2 __attribute__((ext_vector_type(2)));
typedef __amdgpu_buffer_rsrc_t rsrc_t;

constexpr int DM = 576, HKV = 16, SK = 32768, DH = 128, FF = 4608;
#define NPOLL 1
constexpr int NSPLIT = 8, NWG = 256, NWAVE = 8, NTHR = (NWAVE + NPOLL) * 64;
constexpr int KEYS_WG = SK / NSPLIT;
constexpr int TILE_BYTES = 32 * DH * 2;
constexpr int NT = KEYS_WG / 32;
constexpr int REGION = KEYS_WG * DH * 2;
constexpr int S = 2;
#define CODE_KB 23
#define SCH0 20
#define SCHD 55
#define SCHHOLD 1
#define PSL 6
#define HUAT 2
#define DLYK 10
#define DLYV 10
static_assert((NT - NWAVE) % S == 0, "tiles split evenly over the streaming waves");
constexpr int AUX_NT = 2, AUX_SC1 = 16;
constexpr unsigned RSRC3 = 0x00020000u;
constexpr unsigned SPIN_CAP = 1u << 19;
constexpr int OOB = 0x7ff00000;

constexpr int WS_EPOCH = 0;
constexpr int WS_QX = 4096;
constexpr int WS_HU = WS_QX + 32 * 4096;
constexpr int WS_ML = WS_HU + 32 * 256;
constexpr int WS_PX = WS_ML + 32 * 512;
constexpr int WS_OX = WS_PX + NWG * 4096;
constexpr int WS_END = WS_OX + 2 * 72 * 8192;
constexpr int WS_STAMP = 4 << 20;

constexpr int L_FLAG = 0;
constexpr int L_TAG = 32;
constexpr int L_XN = 48;
constexpr int L_QPART = L_XN + 1280;
constexpr int L_QS = L_QPART + 8 * 64 * 4;
constexpr int L_OA = L_QS + 512;
constexpr int L_YP = L_OA + 64;
constexpr int L_ML = L_YP + 4 * 8 * 4;
constexpr int L_PART = L_ML + 6 * 16;
constexpr int L_WO = L_PART + 6 * 1024;
constexpr int L_END = L_WO + 6 * 6144;
constexpr int F_QP = 0, F_Q = 1, F_HP = 2, F_END = 3, F_GO = 4, F_O = 5, F_Y = 6, F_FIN = 7;

constexpr float LOG2E = 1.4426950408889634f;
constexpr float SCALE = 0.08838834764831845f;
constexpr float LN_EPS = 1e-5f;

__device__ __forceinline__ float asf(unsigned u) { return __builtin_bit_cast(float, u); }
__device__ __forceinline__ unsigned asu(float f) { return __builtin_bit_cast(unsigned, f); }
__device__ __forceinline__ float bflo(unsigned u) { return asf(u << 16); }
__device__ __forceinline__ float bfhi(unsigned u) { return asf(u & 0xffff0000u); }
__device__ __forceinline__ unsigned pack_bf16(float lo, float hi) {
  f32x2 v = {lo, hi};
  return __builtin_bit_cast(unsigned, __builtin_convertvector(v, bf16x2));
}
__device__ __forceinline__ float rbf(float f) { return (float)(__bf16)f; }
template <int CTRL> __device__ __forceinline__ unsigned dppu(unsigned x) {
  return (unsigned)__builtin_amdgcn_mov_dpp((int)x, CTRL, 0xf, 0xf, true);
}
template <int CTRL> __device__ __forceinline__ float dppf(float x) { return asf(dppu<CTRL>(asu(x))); }
constexpr int XOR1 = 0xB1, XOR2 = 0x4E, ROR4 = 0x124, ROR8 = 0x128;
__device__ __forceinline__ float row_sum(float x) { x += dppf<XOR1>(x); x += dppf<XOR2>(x); x += dppf<ROR4>(x); return x + dppf<ROR8>(x); }
constexpr int QB0 = 0x00, QB1 = 0x55, QB2 = 0xAA, QB3 = 0xFF;
__device__ __forceinline__ float rows_sum(float x) {
  auto s = __builtin_amdgcn_permlane16_swap(asu(x), asu(x), false, false);
  x = asf(s[0]) + asf(s[1]);
  auto t = __builtin_amdgcn_permlane32_swap(asu(x), asu(x), false, false);
  return asf(t[0]) + asf(t[1]);
}
__device__ __forceinline__ float rows_max(float x) {
  auto s = __builtin_amdgcn_permlane16_swap(asu(x), asu(x), false, false);
  x = fmaxf(asf(s[0]), asf(s[1]));
  auto t = __builtin_amdgcn_permlane32_swap(asu(x), asu(x), false, false);
  return fmaxf(asf(t[0]), asf(t[1]));
}
__device__ __forceinline__ float wave_sum(float x) { return rows_sum(row_sum(x)); }
__device__ __forceinline__ rsrc_t mkrsrc(const void* p, int bytes) {
  return __builtin_amdgcn_make_buffer_rsrc(const_cast<void*>(p), 0, bytes, RSRC3);
}
__device__ __forceinline__ f32x4 mfma32(u32x4 a, u32x4 b, f32x4 c) {
  return __builtin_amdgcn_mfma_f32_16x16x32_bf16(__builtin_bit_cast(s16x8, a), __builtin_bit_cast(s16x8, b), c, 0, 0, 0);
}
__device__ __forceinline__ f32x4 mfma16(unsigned a0, unsigned a1, unsigned b0, unsigned b1, f32x4 c) {
  u32x2 a = {a0, a1}, b = {b0, b1};
  return __builtin_amdgcn_mfma_f32_16x16x16bf16_1k(__builtin_bit_cast(s16x4, a), __builtin_bit_cast(s16x4, b), c, 0, 0, 0);
}
__device__ __forceinline__ void wg_barrier() {
  asm volatile("s_waitcnt lgkmcnt(0)\n\ts_barrier" ::: "memory");
}
__device__ __forceinline__ unsigned now32() { return (unsigned)__builtin_amdgcn_s_memrealtime(); }
__device__ __forceinline__ void wait_until(unsigned t) {
  while ((int)(now32() - t) < 0) __builtin_amdgcn_s_sleep(1);
}
__device__ __forceinline__ void flag_add(unsigned char* lds, int f, int lane) {
  asm volatile("s_waitcnt lgkmcnt(0)" ::: "memory");
  if (lane == 0) __hip_atomic_fetch_add((unsigned*)(lds + L_FLAG) + f, 1u, __ATOMIC_RELAXED, __HIP_MEMORY_SCOPE_WORKGROUP);
  asm volatile("" ::: "memory");
}
__device__ __forceinline__ bool flag_set(unsigned char* lds, int f) {
  return __builtin_amdgcn_readfirstlane(__hip_atomic_load((unsigned*)(lds + L_FLAG) + f, __ATOMIC_RELAXED, __HIP_MEMORY_SCOPE_WORKGROUP)) != 0;
}
template <int SLEEP> __device__ __forceinline__ void flag_wait(unsigned char* lds, int f, unsigned n) {
#pragma unroll 1
  for (unsigned spin = 0; spin < (SPIN_CAP << 4); ++spin) {
    const unsigned v = __hip_atomic_load((unsigned*)(lds + L_FLAG) + f, __ATOMIC_RELAXED, __HIP_MEMORY_SCOPE_WORKGROUP);
    if (__builtin_amdgcn_readfirstlane(v) >= n) break;
    if (SLEEP) __builtin_amdgcn_s_sleep(SLEEP);
  }
  asm volatile("" ::: "memory");
}
__device__ __forceinline__ void ln_stats(const float (&v)[10], bool last_valid, float& mean_b, float& den_b) {
  float s = 0.f;
#pragma unroll
  for (int i = 0; i < 8; ++i) s += v[i];
  s += last_valid ? (v[8] + v[9]) : 0.f;
  const float mean = wave_sum(s) * (1.0f / DM);
  float q = 0.f;
#pragma unroll
  for (int i = 0; i < 8; ++i) { const float d = v[i] - mean; q = fmaf(d, d, q); }
  { const float d8 = v[8] - mean, d9 = v[9] - mean; q += last_valid ? fmaf(d8, d8, d9 * d9) : 0.f; }
  const float var = wave_sum(q) * (1.0f / DM);
  mean_b = rbf(mean);
  den_b = rbf(__builtin_amdgcn_sqrtf(rbf(rbf(var) + LN_EPS)));
}
__device__ __forceinline__ float ln_apply(float x, float mean_b, float den_b) {
  const float d = rbf(x - mean_b), r = __builtin_amdgcn_rcpf(den_b), q = d * r;
  return rbf(fmaf(fmaf(-q, den_b, d), r, q));
}

#define STAMP(i) do { } while (0)

__global__ void __launch_bounds__(NTHR, 3)
fused_decode_kernel(const unsigned short* __restrict__ x, const unsigned char* __restrict__ kc, const unsigned char* __restrict__ vc,
                    const unsigned short* __restrict__ wq, const unsigned short* __restrict__ wo,
                    const unsigned short* __restrict__ w1, const unsigned short* __restrict__ b1,
                    const unsigned short* __restrict__ w2, const unsigned short* __restrict__ b2,
                    unsigned short* __restrict__ out, unsigned char* ws) {
  __shared__ __attribute__((aligned(16))) unsigned char lds[L_END];
  const int lane = threadIdx.x & 63, wv = __builtin_amdgcn_readfirstlane(threadIdx.x >> 6), w = wv < NWAVE ? (wv + 4) & 7 : wv;
  const int id = blockIdx.x, g = id >> 3, s = id & 7, b = g >> 4, kvh = g & 15;
  STAMP(0);
  const rsrc_t rws = mkrsrc(ws, WS_END);
  const rsrc_t rx = mkrsrc(x + b * DM, DM * 2);
  const rsrc_t rk = mkrsrc(kc + (size_t)id * REGION, REGION);
  const rsrc_t rv = mkrsrc(vc + (size_t)id * REGION, REGION);

  const int qh = s >> 2, cb = (s + (kvh >> 2)) & 3;
  const int la = lane >> 4;
  const int red = kvh * 8 + s;

  unsigned tbar = 0;
  u32x2 ep = {0u, 0u};
  if (w < 4 || w >= NWAVE) {
    if (w == NWAVE) ep = __builtin_amdgcn_raw_buffer_load_b64(rws, WS_EPOCH + b * 256, 0, AUX_SC1);
    if (CODE_KB && w == 2) {
      const rsrc_t rc = mkrsrc((const void*)(__builtin_amdgcn_s_getpc() & ~255ll), CODE_KB * 1024);
      const u32x4 cv = __builtin_amdgcn_raw_buffer_load_b128(rc, g * 1024 + lane * 16, 0, 0);
      asm volatile("s_barrier" :: "v"(lane));
      tbar = now32();
      asm volatile("" :: "v"(cv));
    } else {
    if (w == 3 && lane < 8) *((unsigned*)(lds + L_FLAG) + lane) = 0u;
    wg_barrier();
    tbar = now32();
    }
  }
  if (w >= NWAVE) {
    const int pw = w - NWAVE;
    const unsigned xfin = __builtin_amdgcn_raw_buffer_load_b32(rx, red * 16 + (lane & 3) * 4, 0, 0);
    const unsigned b2v = __builtin_amdgcn_raw_buffer_load_b16(mkrsrc(b2, 4), 0, 0, 0);
    __builtin_amdgcn_s_setprio(3);
    if (pw == 0) {
      if (lane == 0) *(u32x2*)(lds + L_TAG) = u32x2{ep[0] + 1u, ep[1] + 1u};
      flag_add(lds, F_QP, lane);
    }
    flag_wait<0>(lds, F_QP, 5);
    const u32x2 tagv = *(const u32x2*)(lds + L_TAG);
    const unsigned tagA = __builtin_amdgcn_readfirstlane(tagv[0]), tagB = __builtin_amdgcn_readfirstlane(tagv[1]);
    if (pw == 0) {
      f32x2 q2 = *(const f32x2*)(lds + L_QPART + (lane & 31) * 8);
#pragma unroll
      for (int ww = 1; ww < 8; ++ww) q2 += *(const f32x2*)(lds + L_QPART + ww * 256 + (lane & 31) * 8);
      __builtin_amdgcn_raw_buffer_store_b128(u32x4{tagA, asu(q2[0]), tagB, asu(q2[1])}, rws,
                                             lane < 32 ? WS_QX + g * 4096 + qh * 2048 + cb * 512 + lane * 16 : OOB, 0, AUX_SC1);
      STAMP(8);
    }
    for (int i = 0; i <= pw; ++i) __builtin_amdgcn_s_sleep(PSL);
#pragma unroll 1
    for (unsigned spin = 0; spin < SPIN_CAP; ++spin) {
      asm volatile("" ::: "memory");
      u32x4 gr[4];
#pragma unroll
      for (int i = 0; i < 4; ++i) gr[i] = __builtin_amdgcn_raw_buffer_load_b128(rws, WS_QX + g * 4096 + i * 1024 + lane * 16, 0, AUX_SC1);
      bool ok = true;
#pragma unroll
      for (int i = 0; i < 4; ++i) ok &= (gr[i][0] == tagA) & (gr[i][2] == tagB);
      if (__all(ok)) {
        *(unsigned*)(lds + L_QS + lane * 4) = pack_bf16(asf(gr[0][1]) + asf(gr[2][1]), asf(gr[0][3]) + asf(gr[2][3]));
        *(unsigned*)(lds + L_QS + 256 + lane * 4) = pack_bf16(asf(gr[1][1]) + asf(gr[3][1]), asf(gr[1][3]) + asf(gr[3][3]));
        flag_add(lds, F_Q, lane);
        break;
      }
      if (flag_set(lds, F_Q)) break;
    }
    const int cp = 2 * (lane & 3);
    float y0 = 0.f, y1 = 0.f;
    if (red < 72) {
      flag_wait<16>(lds, F_Y, 4);
#pragma unroll
      for (int ww = 0; ww < 4; ++ww) { const f32x2 v = *(const f32x2*)(lds + L_YP + ww * 32 + cp * 4); y0 += v[0]; y1 += v[1]; }
      y0 = rbf(rbf(y0) + bflo(b2v)); y1 = rbf(rbf(y1) + bflo(b2v));
    }
    if (pw == 0) {
      flag_wait<8>(lds, F_END, S);
      __builtin_amdgcn_raw_buffer_store_b64(u32x2{tagA, tagB}, rws, lane == 0 ? WS_HU + g * 256 + s * 8 : OOB, 0, AUX_SC1);
#pragma unroll 1
      for (unsigned spin = 0; spin < SPIN_CAP; ++spin) {
        asm volatile("" ::: "memory");
        const u32x2 hu = __builtin_amdgcn_raw_buffer_load_b64(rws, WS_HU + g * 256 + (lane & 7) * 8, 0, AUX_SC1);
        if (__all(((hu[0] == tagA) & (hu[1] == tagB)) | ((lane & 7) == s))) break;
        __builtin_amdgcn_s_sleep(2);
      }
      flag_add(lds, F_GO, lane);
      STAMP(5);
    } else {
      flag_wait<4>(lds, F_GO, 1);
      for (int i = 0; i < pw; ++i) __builtin_amdgcn_s_sleep(PSL);
    }
    {
      const int pi = lane & 15;
      int od = WS_PX + (id * 16 + pi) * 256 + la * 32, om = WS_ML + ((g * 2 + (s >> 2)) * 16 + pi) * 16;
      float v[8], pm = 0.f, pl = 0.f;
#pragma unroll
      for (int j = 0; j < 8; ++j) v[j] = 0.f;
#pragma unroll 1
      for (unsigned spin = 0; spin < SPIN_CAP; ++spin) {
        asm volatile("" ::: "memory");
        u32x4 g1[5];
        g1[0] = __builtin_amdgcn_raw_buffer_load_b128(rws, od, 0, AUX_SC1);
        g1[1] = __builtin_amdgcn_raw_buffer_load_b128(rws, od + 128, 0, AUX_SC1);
        g1[2] = __builtin_amdgcn_raw_buffer_load_b128(rws, od + 16, 0, AUX_SC1);
        g1[3] = __builtin_amdgcn_raw_buffer_load_b128(rws, od + 144, 0, AUX_SC1);
        g1[4] = __builtin_amdgcn_raw_buffer_load_b128(rws, om, 0, AUX_SC1);
        bool fresh = od != OOB;
#pragma unroll
        for (int i = 0; i < 5; ++i) fresh &= (g1[i][0] == tagA) & (g1[i][2] == tagB);
#pragma unroll
        for (int i = 0; i < 4; ++i) { v[2 * i] = fresh ? asf(g1[i][1]) : v[2 * i]; v[2 * i + 1] = fresh ? asf(g1[i][3]) : v[2 * i + 1]; }
        pm = fresh ? asf(g1[4][1]) : pm; pl = fresh ? asf(g1[4][3]) : pl;
        od = fresh ? OOB : od; om = fresh ? OOB : om;
        if (__all(od == OOB)) {
          float M = fmaxf(pm, dppf<XOR1>(pm));
          M = fmaxf(M, dppf<XOR2>(M)); M = fmaxf(M, dppf<ROR4>(M)); M = fmaxf(M, dppf<ROR8>(M));
          const float wgt = __builtin_amdgcn_exp2f((pm - M) * LOG2E);
          const float L = row_sum(wgt * pl);
          const float r0 = __builtin_amdgcn_rcpf(L), inv = fmaf(fmaf(-L, r0, 1.0f), r0, r0);
#pragma unroll
          for (int j = 0; j < 8; ++j) v[j] = row_sum(wgt * v[j]) * inv;
          if (pi == 0) *(u32x4*)(lds + L_OA + la * 16) = u32x4{pack_bf16(v[0], v[1]), pack_bf16(v[2], v[3]), pack_bf16(v[4], v[5]), pack_bf16(v[6], v[7])};
          flag_add(lds, F_O, lane);
          break;
        }
        if (flag_set(lds, F_O)) break;
      }
    }
    if (red < 72) {
      float v0[8], v1[8];
      int of[8];
#pragma unroll
      for (int i = 0; i < 8; ++i) { v0[i] = 0.f; v1[i] = 0.f; of[i] = WS_OX + (b * 72 + red) * 8192 + i * 1024 + lane * 16; }
#pragma unroll 1
      for (unsigned spin = 0; spin < SPIN_CAP; ++spin) {
        asm volatile("" ::: "memory");
        u32x4 g2[8];
#pragma unroll
        for (int i = 0; i < 8; ++i) g2[i] = __builtin_amdgcn_raw_buffer_load_b128(rws, of[i], 0, AUX_SC1);
        bool done = true;
#pragma unroll
        for (int i = 0; i < 8; ++i) {
          const bool fresh = (of[i] != OOB) & (g2[i][0] == tagA) & (g2[i][2] == tagB);
          v0[i] = fresh ? asf(g2[i][1]) : v0[i]; v1[i] = fresh ? asf(g2[i][3]) : v1[i];
          of[i] = fresh ? OOB : of[i];
          done &= of[i] == OOB;
        }
        if (flag_set(lds, F_FIN)) break;
        if (__all(done)) {
          float s0 = ((v0[0] + v0[1]) + (v0[2] + v0[3])) + ((v0[4] + v0[5]) + (v0[6] + v0[7]));
          float s1 = ((v1[0] + v1[1]) + (v1[2] + v1[3])) + ((v1[4] + v1[5]) + (v1[6] + v1[7]));
          s0 += dppf<ROR4>(s0); s0 += dppf<ROR8>(s0); s0 = rows_sum(s0);
          s1 += dppf<ROR4>(s1); s1 += dppf<ROR8>(s1); s1 = rows_sum(s1);
          const float r0 = rbf(bflo(xfin) + rbf(s0)) + y0, r1 = rbf(bfhi(xfin) + rbf(s1)) + y1;
          if (lane < 4) *(unsigned*)(out + b * DM + red * 8 + cp) = pack_bf16(r0, r1);
          if (red == 0 && lane == 0) __builtin_amdgcn_raw_buffer_store_b64(u32x2{tagA, tagB}, rws, WS_EPOCH + b * 256, 0, AUX_SC1);
          flag_add(lds, F_FIN, lane);
          STAMP(13);
          break;
        }
      }
    }
  } else {
  const int voffK = ((lane & 15) >> 2) * 256 + (lane >> 4) * 64 + (lane & 3) * 16;
  const int voffV = lane * 16;
  u32x4 kb[8], vb[8];
#define ISSUE_K(so) _Pragma("unroll") for (int u = 0; u < 8; ++u) kb[u] = __builtin_amdgcn_raw_buffer_load_b128(rk, voffK + u * 1024, so, AUX_NT)
#define ISSUE_V(so) _Pragma("unroll") for (int u = 0; u < 8; ++u) vb[u] = __builtin_amdgcn_raw_buffer_load_b128(rv, voffV + u * 1024, so, AUX_NT)
  if (w >= 4) {
    const int rg = (w - 4) * 8 + (lane >> 3), c8 = lane & 7;
    unsigned xs[5];
    u32x4 wqv[9];
#pragma unroll
    for (int i = 0; i < 5; ++i) xs[i] = __builtin_amdgcn_raw_buffer_load_b32(rx, (lane + 64 * i) * 4, 0, 0);
    __builtin_amdgcn_sched_barrier(0);
    const rsrc_t rq = mkrsrc(wq + qh * 288 * 4096 + kvh * 256 + cb * 64, (287 * 4096 + 64) * 2);
#pragma unroll
    for (int i = 0; i < 9; ++i) wqv[i] = __builtin_amdgcn_raw_buffer_load_b128(rq, (rg + 32 * i) * 8192 + c8 * 16, 0, AUX_NT);
    __builtin_amdgcn_sched_barrier(0);
    __builtin_amdgcn_s_barrier();
    tbar = now32();
    __builtin_amdgcn_sched_barrier(0);
    float xv[10], mean1, den1;
#pragma unroll
    for (int i = 0; i < 5; ++i) { xv[2 * i] = bflo(xs[i]); xv[2 * i + 1] = bfhi(xs[i]); }
    ln_stats(xv, lane < 32, mean1, den1);
#pragma unroll
    for (int i = 0; i < 5; ++i)
      *(unsigned*)(lds + L_XN + (lane + 64 * i) * 4) = pack_bf16(ln_apply(xv[2 * i], mean1, den1), ln_apply(xv[2 * i + 1], mean1, den1));
    STAMP(9);
    f32x4 qa = {0.f, 0.f, 0.f, 0.f};
#pragma unroll
    for (int i = 0; i < 9; ++i) {
      const unsigned t = (unsigned)*(const unsigned short*)(lds + L_XN + (qh * 288 + rg + 32 * i) * 2) << (16 * (lane & 1));
      const int jp = c8 >> 1;
      qa = mfma32(wqv[i], u32x4{jp == 0 ? t : 0u, jp == 1 ? t : 0u, jp == 2 ? t : 0u, jp == 3 ? t : 0u}, qa);
    }
    if ((lane >> 5) == ((lane >> 3) & 1)) {
      float* qp = (float*)(lds + L_QPART) + ((w - 4) * 2 + (lane >> 5)) * 64 + ((lane >> 4) & 1) * 32 + c8;
#pragma unroll
      for (int r = 0; r < 4; ++r) qp[r * 8] = qa[r];
    }
    flag_add(lds, F_QP, lane);
    STAMP(1);
  }
  wait_until(tbar + SCH0 + w * SCHD);
  __builtin_amdgcn_sched_barrier(0);
  ISSUE_K(w * TILE_BYTES);
  ISSUE_V(w * TILE_BYTES);
  __builtin_amdgcn_sched_barrier(0);
  flag_wait<1>(lds, F_Q, 1);
  if (w < S) STAMP(2);

  const int c = lane & 3, hb = (lane >> 2) & 1, tb = (lane >> 3) & 1;
  const u32x2 tagv = *(const u32x2*)(lds + L_TAG);
  float m = -3.0e38f, l = 0.f;
  f32x4 acc[2][2];
#pragma unroll
  for (int i = 0; i < 2; ++i)
#pragma unroll
    for (int j = 0; j < 2; ++j) acc[i][j] = f32x4{0.f, 0.f, 0.f, 0.f};
  {
    const u32x4 qf = *(const u32x4*)(lds + L_QS + (hb * 128 + la * 32 + c * 8) * 2);
    const u32x4 zero4 = {0u, 0u, 0u, 0u};
    const u32x4 q0 = tb ? zero4 : qf, q1 = tb ? qf : zero4;
    const unsigned mask0 = c == 0 ? 0x0000ffffu : c == 1 ? 0xffff0000u : 0u;
    const unsigned mask1 = c == 2 ? 0x0000ffffu : c == 3 ? 0xffff0000u : 0u;
    auto tile = [&](auto more, int so) {
      f32x4 sc[4];
#pragma unroll
      for (int i = 0; i < 4; ++i) sc[i] = mfma32(kb[i], q0, f32x4{0.f, 0.f, 0.f, 0.f});
#pragma unroll
      for (int i = 0; i < 4; ++i) sc[i] = mfma32(kb[4 + i], q1, sc[i]);
      if constexpr (more) {
        __builtin_amdgcn_sched_barrier(0);
        if (DLYK) __builtin_amdgcn_s_sleep(DLYK);
        ISSUE_K(so);
        __builtin_amdgcn_sched_barrier(0);
      }
      float t4[4];
#pragma unroll
      for (int i = 0; i < 4; ++i) t4[i] = (dppf<QB0>(sc[i][0]) + dppf<QB1>(sc[i][1])) + (dppf<QB2>(sc[i][2]) + dppf<QB3>(sc[i][3]));
      const float raw = (c & 2) ? ((c & 1) ? t4[3] : t4[2]) : ((c & 1) ? t4[1] : t4[0]);
      const float sv = rbf(rbf(raw) * SCALE);
      if (__any(sv > m)) {
        float mx = fmaxf(sv, dppf<XOR1>(sv));
        mx = fmaxf(mx, dppf<XOR2>(mx));
        mx = fmaxf(mx, dppf<ROR8>(mx));
        mx = fmaxf(rows_max(mx), m);
        const float alpha = __builtin_amdgcn_exp2f((m - mx) * LOG2E);
        m = mx;
        l *= alpha;
#pragma unroll
        for (int i = 0; i < 2; ++i)
#pragma unroll
          for (int j = 0; j < 2; ++j) acc[i][j] *= alpha;
      }
      const float p = __builtin_amdgcn_exp2f((sv - m) * LOG2E);
      l += p;
      const unsigned pk = pack_bf16(p, p);
      unsigned pb0[4], pb1[4];
      pb0[0] = dppu<QB0>(pk) & mask0; pb1[0] = dppu<QB0>(pk) & mask1;
      pb0[1] = dppu<QB1>(pk) & mask0; pb1[1] = dppu<QB1>(pk) & mask1;
      pb0[2] = dppu<QB2>(pk) & mask0; pb1[2] = dppu<QB2>(pk) & mask1;
      pb0[3] = dppu<QB3>(pk) & mask0; pb1[3] = dppu<QB3>(pk) & mask1;
#pragma unroll
      for (int u = 0; u < 8; ++u) {
        acc[u >> 2][0] = mfma16(vb[u][0], vb[u][1], pb0[u & 3], pb1[u & 3], acc[u >> 2][0]);
        acc[u >> 2][1] = mfma16(vb[u][2], vb[u][3], pb0[u & 3], pb1[u & 3], acc[u >> 2][1]);
      }
      if constexpr (more) {
        __builtin_amdgcn_sched_barrier(0);
        if (DLYV) __builtin_amdgcn_s_sleep(DLYV);
        ISSUE_V(so);
        __builtin_amdgcn_sched_barrier(0);
      }
    };
    if (w < S) {
      __builtin_amdgcn_s_setprio(2);
      if (SCHHOLD) wait_until(tbar + SCH0 + (NWAVE + w) * SCHD - 40);
      for (int nxt = NWAVE + w; nxt < NT; nxt += S) {
        if (nxt == NT - HUAT * S + w) flag_add(lds, F_END, lane);
        tile(std::true_type{}, nxt * TILE_BYTES);
      }
    }
    tile(std::false_type{}, 0);
  }
  if (w < S) STAMP(3); else STAMP(12);
  const unsigned tagA = __builtin_amdgcn_readfirstlane(tagv[0]), tagB = __builtin_amdgcn_readfirstlane(tagv[1]);
  f32x4 res;
#pragma unroll
  for (int r = 0; r < 4; ++r) {
    const float own = tb ? acc[1][1][r] : acc[0][0][r], snd = tb ? acc[1][0][r] : acc[0][1][r];
    res[r] = own + dppf<ROR8>(snd);
  }
  l += dppf<XOR1>(l);
  l += dppf<XOR2>(l);
  l += dppf<ROR8>(l);
  l = rows_sum(l);

  if (w < S) {
    flag_wait<1>(lds, F_HP, NWAVE - S);
    f32x2 hml[3];
    f32x4 hp[3];
#pragma unroll
    for (int k = 0; k < 3; ++k) {
      hml[k] = *(const f32x2*)(lds + L_ML + ((w * 3 + k) * 2 + hb) * 8);
      hp[k] = *(const f32x4*)(lds + L_PART + (w * 3 + k) * 1024 + lane * 16);
    }
    const float mx = fmaxf(fmaxf(m, hml[0][0]), fmaxf(hml[1][0], hml[2][0]));
    const float al = __builtin_amdgcn_exp2f((m - mx) * LOG2E);
    res *= al; l *= al;
#pragma unroll
    for (int k = 0; k < 3; ++k) {
      const float be = __builtin_amdgcn_exp2f((hml[k][0] - mx) * LOG2E);
      res += be * hp[k]; l = fmaf(be, hml[k][1], l);
    }
    const int po = WS_PX + ((g * 8 + hb * 4 + la) * 16 + 2 * s + w) * 256 + (tb * 4 + c) * 16;
    __builtin_amdgcn_raw_buffer_store_b128(u32x4{tagA, asu(res[0]), tagB, asu(res[1])}, rws, po, 0, AUX_SC1);
    __builtin_amdgcn_raw_buffer_store_b128(u32x4{tagA, asu(res[2]), tagB, asu(res[3])}, rws, po + 128, 0, AUX_SC1);
    __builtin_amdgcn_raw_buffer_store_b128(u32x4{tagA, asu(mx), tagB, asu(l)}, rws,
                                           (lane & ~4) == 0 ? WS_ML + ((g * 2 + hb) * 16 + 2 * s + w) * 16 : OOB, 0, AUX_SC1);
    STAMP(10);
  } else {
    const int hw = w - S;
    *(f32x4*)(lds + L_PART + hw * 1024 + lane * 16) = res;
    if ((lane & ~4) == 0) *(f32x2*)(lds + L_ML + (hw * 2 + hb) * 8) = f32x2{m, l};
    flag_add(lds, F_HP, lane);

    const bool mlp = w >= 4 && red < 72;
    u32x4 wt[6];
    {
      const rsrc_t ro = mkrsrc(wo + (size_t)(kvh * 256 + s * 32) * DM, 32 * DM * 2);
#pragma unroll
      for (int i = 0; i < 6; ++i) {
        const int p = lane + 64 * i, row = p / 12;
        wt[i] = __builtin_amdgcn_raw_buffer_load_b128(ro, row * (DM * 2) + hw * 192 + (p - row * 12) * 16, 0, AUX_NT);
      }
    }
    unsigned char* img = lds + L_WO + hw * 6144;
#pragma unroll
    for (int i = 0; i < 6; ++i) *(u32x4*)(img + (lane + 64 * i) * 16) = wt[i];
    asm volatile("s_waitcnt lgkmcnt(0)" ::: "memory");
    u32x4 wof[6];
    {
      typedef __attribute__((address_space(3))) s16x4* lds_s16x4;
      const int ra = (((lane & 15) >> 2) * 8 + la * 2) * 192 + (lane & 3) * 8;
#pragma unroll
      for (int t = 0; t < 6; ++t) {
        const u32x2 lo = __builtin_bit_cast(u32x2, __builtin_amdgcn_ds_read_tr16_b64_v4i16((lds_s16x4)(img + ra + t * 32)));
        const u32x2 hi = __builtin_bit_cast(u32x2, __builtin_amdgcn_ds_read_tr16_b64_v4i16((lds_s16x4)(img + ra + t * 32 + 192)));
        wof[t] = u32x4{lo[0], lo[1], hi[0], hi[1]};
      }
    }
    if (mlp) {
      const int ht = (w - 4) * 64 + lane;
      unsigned a1[9], ab[9], a2[9];
      const rsrc_t r1 = mkrsrc(w1, FF * 2), rb = mkrsrc(b1, FF * 2), r2 = mkrsrc(w2, FF * 2);
#pragma unroll
      for (int i = 0; i < 9; ++i) {
        a1[i] = __builtin_amdgcn_raw_buffer_load_b32(r1, (ht + 256 * i) * 4, 0, 0);
        ab[i] = __builtin_amdgcn_raw_buffer_load_b32(rb, (ht + 256 * i) * 4, 0, 0);
        a2[i] = __builtin_amdgcn_raw_buffer_load_b32(r2, (ht + 256 * i) * 4, 0, 0);
      }
      __builtin_amdgcn_sched_barrier(0);
      float xn[10];
#pragma unroll
      for (int i = 0; i < 5; ++i) { const unsigned v = *(const unsigned*)(lds + L_XN + (lane + 64 * i) * 4); xn[2 * i] = bflo(v); xn[2 * i + 1] = bfhi(v); }
      float mean2, den2;
      ln_stats(xn, lane < 32, mean2, den2);
      const u32x4 xc = *(const u32x4*)(lds + L_XN + red * 16);
      float hv[8], y[8];
#pragma unroll
      for (int j = 0; j < 4; ++j) { hv[2 * j] = ln_apply(bflo(xc[j]), mean2, den2); hv[2 * j + 1] = ln_apply(bfhi(xc[j]), mean2, den2); }
#pragma unroll
      for (int j = 0; j < 8; ++j) y[j] = 0.f;
      constexpr float GC0 = -2.0f * 0.7978845608028654f * LOG2E, GC1 = GC0 * 0.044715f;
#pragma unroll
      for (int i = 0; i < 9; ++i) {
#pragma unroll
        for (int j = 0; j < 8; ++j) {
          const unsigned pr = pack_bf16(hv[j] * bflo(a1[i]), hv[j] * bfhi(a1[i]));
          const unsigned uu = pack_bf16(bflo(pr) + bflo(ab[i]), bfhi(pr) + bfhi(ab[i]));
          const float u0 = bflo(uu), u1 = bfhi(uu);
          const float e0 = __builtin_amdgcn_exp2f(u0 * fmaf(u0 * u0, GC1, GC0));
          const float e1 = __builtin_amdgcn_exp2f(u1 * fmaf(u1 * u1, GC1, GC0));
          const unsigned gg = pack_bf16(u0 * __builtin_amdgcn_rcpf(1.0f + e0), u1 * __builtin_amdgcn_rcpf(1.0f + e1));
          y[j] = fmaf(bflo(gg), bflo(a2[i]), y[j]);
          y[j] = fmaf(bfhi(gg), bfhi(a2[i]), y[j]);
        }
        __builtin_amdgcn_sched_barrier(0);
      }
#pragma unroll
      for (int j = 0; j < 8; ++j) y[j] = wave_sum(y[j]);
      if (lane == 0) {
        *(f32x4*)(lds + L_YP + (w - 4) * 32) = f32x4{y[0], y[1], y[2], y[3]};
        *(f32x4*)(lds + L_YP + (w - 4) * 32 + 16) = f32x4{y[4], y[5], y[6], y[7]};
      }
      flag_add(lds, F_Y, lane);
    }
    STAMP(4);

    flag_wait<16>(lds, F_GO, 1);
    __builtin_amdgcn_s_setprio(3);
    flag_wait<0>(lds, F_O, 1);
    STAMP(6);
    {
      const u32x4 oa = *(const u32x4*)(lds + L_OA + la * 16);
      float d[6];
#pragma unroll
      for (int t = 0; t < 6; ++t) d[t] = mfma32(oa, wof[t], f32x4{0.f, 0.f, 0.f, 0.f})[0];
      const float e0 = (la & 2) ? ((la & 1) ? d[3] : d[2]) : ((la & 1) ? d[1] : d[0]), e1 = (la & 1) ? d[5] : d[4];
      const int cl = lane & 15;
      const int oo = WS_OX + (b * 72 + hw * 12 + la * 2 + (cl >> 3)) * 8192 + (id & 127) * 64 + ((cl & 7) >> 1) * 16;
      const float f0 = dppf<XOR1>(e0), f1 = dppf<XOR1>(e1);
      __builtin_amdgcn_raw_buffer_store_b128(u32x4{tagA, asu(e0), tagB, asu(f0)}, rws, (lane & 1) ? OOB : oo, 0, AUX_SC1);
      __builtin_amdgcn_raw_buffer_store_b128(u32x4{tagA, asu(e1), tagB, asu(f1)}, rws, ((lane & 1) | (la >> 1)) ? OOB : oo + 8 * 8192, 0, AUX_SC1);
    }
    STAMP(11);
  }
  }
  STAMP(7);
}

}

extern "C" void kernel_launch(void* const* d_in, const int* in_sizes, int n_in, void* d_out, int out_size, void* d_ws,
                              size_t ws_size, hipStream_t stream) {
  (void)in_sizes; (void)n_in; (void)out_size; (void)ws_size;
  fused_decode_kernel<<<dim3(NWG), dim3(NTHR), 0, stream>>>(
      (const unsigned short*)d_in[0], (const unsigned char*)d_in[1], (const unsigned char*)d_in[2],
      (const unsigned short*)d_in[3], (const unsigned short*)d_in[4], (const unsigned short*)d_in[6],
      (const unsigned short*)d_in[7], (const unsigned short*)d_in[8], (const unsigned short*)d_in[9],
      (unsigned short*)d_out, (unsigned char*)d_ws);
}
```

```cpp
#include <hip/hip_runtime.h>
#include <hip/hip_bf16.h>
#include <stdint.h>
#include <type_traits>

#define DIAG_M 0
#define DIAG_WG 0
namespace {

typedef unsigned u32x2 __attribute__((ext_vector_type(2)));
typedef unsigned u32x4 __attribute__((ext_vector_type(4)));
typedef float f32x2 __attribute__((ext_vector_type(2)));
typedef float f32x4 __attribute__((ext_vector_type(4)));
typedef short s16x4 __attribute__((ext_vector_type(4)));
typedef short s16x8 __attribute__((ext_vector_type(8)));
typedef __bf16 bf16x2 __attribute__((ext_vector_type(2)));
typedef __amdgpu_buffer_rsrc_t rsrc_t;

constexpr int DM = 576, HKV = 16, SK = 32768, DH = 128, FF = 4608;
#define NPOLL 1
constexpr int NSPLIT = 8, NWG = 256, NWAVE = 8, NTHR = (NWAVE + NPOLL) * 64;
constexpr int KEYS_WG = SK / NSPLIT;
constexpr int TILE_BYTES = 32 * DH * 2;
constexpr int NT = KEYS_WG / 32;
constexpr int REGION = KEYS_WG * DH * 2;
constexpr int S = 2;
#define CODE_KB 23
#define SCH0 20
#define SCHD 55
#define SCHHOLD 1
#define PSL 6
#define HUAT 2
#define DLYK 10
#define DLYV 10
static_assert((NT - NWAVE) % S == 0, "tiles split evenly over the streaming waves");
constexpr int AUX_NT = 2, AUX_SC1 = 16;
constexpr unsigned RSRC3 = 0x00020000u;
constexpr unsigned SPIN_CAP = 1u << 19;
constexpr int OOB = 0x7ff00000;

constexpr int WS_EPOCH = 0;
constexpr int WS_QX = 4096;
constexpr int WS_HU = WS_QX + 32 * 4096;
constexpr int WS_ML = WS_HU + 32 * 256;
constexpr int WS_PX = WS_ML + 32 * 512;
constexpr int WS_OX = WS_PX + NWG * 4096;
constexpr int WS_END = WS_OX + 2 * 72 * 8192;
constexpr int WS_STAMP = 4 << 20;

constexpr int L_FLAG = 0;
constexpr int L_TAG = 32;
constexpr int L_XN = 48;
constexpr int L_QPART = L_XN + 1280;
constexpr int L_QS = L_QPART + 8 * 64 * 4;
constexpr int L_OA = L_QS + 512;
constexpr int L_YP = L_OA + 64;
constexpr int L_ML = L_YP + 4 * 8 * 4;
constexpr int L_PART = L_ML + 6 * 16;
constexpr int L_WO = L_PART + 6 * 1024;
constexpr int L_DG = L_WO + 6 * 6144;
constexpr int L_END = L_DG + 64;
constexpr int F_QP = 0, F_Q = 1, F_HP = 2, F_END = 3, F_GO = 4, F_O = 5, F_Y = 6, F_FIN = 7;

constexpr float LOG2E = 1.4426950408889634f;
constexpr float SCALE = 0.08838834764831845f;
constexpr float LN_EPS = 1e-5f;

__device__ __forceinline__ float asf(unsigned u) { return __builtin_bit_cast(float, u); }
__device__ __forceinline__ unsigned asu(float f) { return __builtin_bit_cast(unsigned, f); }
__device__ __forceinline__ float bflo(unsigned u) { return asf(u << 16); }
__device__ __forceinline__ float bfhi(unsigned u) { return asf(u & 0xffff0000u); }
__device__ __forceinline__ unsigned pack_bf16(float lo, float hi) {
  f32x2 v = {lo, hi};
  return __builtin_bit_cast(unsigned, __builtin_convertvector(v, bf16x2));
}
__device__ __forceinline__ float rbf(float f) { return (float)(__bf16)f; }
template <int CTRL> __device__ __forceinline__ unsigned dppu(unsigned x) {
  return (unsigned)__builtin_amdgcn_mov_dpp((int)x, CTRL, 0xf, 0xf, true);
}
template <int CTRL> __device__ __forceinline__ float dppf(float x) { return asf(dppu<CTRL>(asu(x))); }
constexpr int XOR1 = 0xB1, XOR2 = 0x4E, ROR4 = 0x124, ROR8 = 0x128;
__device__ __forceinline__ float row_sum(float x) { x += dppf<XOR1>(x); x += dppf<XOR2>(x); x += dppf<ROR4>(x); return x + dppf<ROR8>(x); }
constexpr int QB0 = 0x00, QB1 = 0x55, QB2 = 0xAA, QB3 = 0xFF;
__device__ __forceinline__ float rows_sum(float x) {
  auto s = __builtin_amdgcn_permlane16_swap(asu(x), asu(x), false, false);
  x = asf(s[0]) + asf(s[1]);
  auto t = __builtin_amdgcn_permlane32_swap(asu(x), asu(x), false, false);
  return asf(t[0]) + asf(t[1]);
}
__device__ __forceinline__ float rows_max(float x) {
  auto s = __builtin_amdgcn_permlane16_swap(asu(x), asu(x), false, false);
  x = fmaxf(asf(s[0]), asf(s[1]));
  auto t = __builtin_amdgcn_permlane32_swap(asu(x), asu(x), false, false);
  return fmaxf(asf(t[0]), asf(t[1]));
}
__device__ __forceinline__ float wave_sum(float x) { return rows_sum(row_sum(x)); }
__device__ __forceinline__ rsrc_t mkrsrc(const void* p, int bytes) {
  return __builtin_amdgcn_make_buffer_rsrc(const_cast<void*>(p), 0, bytes, RSRC3);
}
__device__ __forceinline__ f32x4 mfma32(u32x4 a, u32x4 b, f32x4 c) {
  return __builtin_amdgcn_mfma_f32_16x16x32_bf16(__builtin_bit_cast(s16x8, a), __builtin_bit_cast(s16x8, b), c, 0, 0, 0);
}
__device__ __forceinline__ f32x4 mfma16(unsigned a0, unsigned a1, unsigned b0, unsigned b1, f32x4 c) {
  u32x2 a = {a0, a1}, b = {b0, b1};
  return __builtin_amdgcn_mfma_f32_16x16x16bf16_1k(__builtin_bit_cast(s16x4, a), __builtin_bit_cast(s16x4, b), c, 0, 0, 0);
}
__device__ __forceinline__ void wg_barrier() {
  asm volatile("s_waitcnt lgkmcnt(0)\n\ts_barrier" ::: "memory");
}
__device__ __forceinline__ unsigned now32() { return (unsigned)__builtin_amdgcn_s_memrealtime(); }
__device__ __forceinline__ void wait_until(unsigned t) {
  while ((int)(now32() - t) < 0) __builtin_amdgcn_s_sleep(1);
}
__device__ __forceinline__ void flag_add(unsigned char* lds, int f, int lane) {
  asm volatile("s_waitcnt lgkmcnt(0)" ::: "memory");
  if (lane == 0) __hip_atomic_fetch_add((unsigned*)(lds + L_FLAG) + f, 1u, __ATOMIC_RELAXED, __HIP_MEMORY_SCOPE_WORKGROUP);
  asm volatile("" ::: "memory");
}
__device__ __forceinline__ bool flag_set(unsigned char* lds, int f) {
  return __builtin_amdgcn_readfirstlane(__hip_atomic_load((unsigned*)(lds + L_FLAG) + f, __ATOMIC_RELAXED, __HIP_MEMORY_SCOPE_WORKGROUP)) != 0;
}
template <int SLEEP> __device__ __forceinline__ void flag_wait(unsigned char* lds, int f, unsigned n) {
#pragma unroll 1
  for (unsigned spin = 0; spin < (SPIN_CAP << 4); ++spin) {
    const unsigned v = __hip_atomic_load((unsigned*)(lds + L_FLAG) + f, __ATOMIC_RELAXED, __HIP_MEMORY_SCOPE_WORKGROUP);
    if (__builtin_amdgcn_readfirstlane(v) >= n) break;
    if (SLEEP) __builtin_amdgcn_s_sleep(SLEEP);
  }
  asm volatile("" ::: "memory");
}
__device__ __forceinline__ void ln_stats(const float (&v)[10], bool last_valid, float& mean_b, float& den_b) {
  float s = 0.f;
#pragma unroll
  for (int i = 0; i < 8; ++i) s += v[i];
  s += last_valid ? (v[8] + v[9]) : 0.f;
  const float mean = wave_sum(s) * (1.0f / DM);
  float q = 0.f;
#pragma unroll
  for (int i = 0; i < 8; ++i) { const float d = v[i] - mean; q = fmaf(d, d, q); }
  { const float d8 = v[8] - mean, d9 = v[9] - mean; q += last_valid ? fmaf(d8, d8, d9 * d9) : 0.f; }
  const float var = wave_sum(q) * (1.0f / DM);
  mean_b = rbf(mean);
  den_b = rbf(__builtin_amdgcn_sqrtf(rbf(rbf(var) + LN_EPS)));
}
__device__ __forceinline__ float ln_apply(float x, float mean_b, float den_b) {
  const float d = rbf(x - mean_b), r = __builtin_amdgcn_rcpf(den_b), q = d * r;
  return rbf(fmaf(fmaf(-q, den_b, d), r, q));
}

#define STAMP(i) do { } while (0)

__global__ void __launch_bounds__(NTHR, 3)
fused_decode_kernel(const unsigned short* __restrict__ x, const unsigned char* __restrict__ kc, const unsigned char* __restrict__ vc,
                    const unsigned short* __restrict__ wq, const unsigned short* __restrict__ wo,
                    const unsigned short* __restrict__ w1, const unsigned short* __restrict__ b1,
                    const unsigned short* __restrict__ w2, const unsigned short* __restrict__ b2,
                    unsigned short* __restrict__ out, unsigned char* ws) {
  __shared__ __attribute__((aligned(16))) unsigned char lds[L_END];
  const int lane = threadIdx.x & 63, wv = __builtin_amdgcn_readfirstlane(threadIdx.x >> 6), w = wv < NWAVE ? (wv + 4) & 7 : wv;
  const int id = blockIdx.x, g = id >> 3, s = id & 7, b = g >> 4, kvh = g & 15;
  STAMP(0);
  const unsigned t_start = now32();
  const unsigned long long c_start = __builtin_amdgcn_s_memtime();
  const rsrc_t rws = mkrsrc(ws, WS_END);
  const rsrc_t rx = mkrsrc(x + b * DM, DM * 2);
  const rsrc_t rk = mkrsrc(kc + (size_t)id * REGION, REGION);
  const rsrc_t rv = mkrsrc(vc + (size_t)id * REGION, REGION);

  const int qh = s >> 2, cb = (s + (kvh >> 2)) & 3;
  const int la = lane >> 4;
  const int red = kvh * 8 + s;

  unsigned tbar = 0;
  u32x2 ep = {0u, 0u};
  if (w < 4 || w >= NWAVE) {
    if (w == NWAVE) ep = __builtin_amdgcn_raw_buffer_load_b64(rws, WS_EPOCH + b * 256, 0, AUX_SC1);
    if (CODE_KB && w == 2) {
      const rsrc_t rc = mkrsrc((const void*)(__builtin_amdgcn_s_getpc() & ~255ll), CODE_KB * 1024);
      const u32x4 cv = __builtin_amdgcn_raw_buffer_load_b128(rc, g * 1024 + lane * 16, 0, 0);
      asm volatile("s_barrier" :: "v"(lane));
      tbar = now32();
      asm volatile("" :: "v"(cv));
    } else {
    if (w == 3 && lane < 8) *((unsigned*)(lds + L_FLAG) + lane) = 0u;
    wg_barrier();
    tbar = now32();
    }
  }
  if (w >= NWAVE) {
    const int pw = w - NWAVE;
    const unsigned xfin = __builtin_amdgcn_raw_buffer_load_b32(rx, red * 16 + (lane & 3) * 4, 0, 0);
    const unsigned b2v = __builtin_amdgcn_raw_buffer_load_b16(mkrsrc(b2, 4), 0, 0, 0);
    __builtin_amdgcn_s_setprio(3);
    if (pw == 0) {
      if (lane == 0) *(u32x2*)(lds + L_TAG) = u32x2{ep[0] + 1u, ep[1] + 1u};
      flag_add(lds, F_QP, lane);
    }
    flag_wait<0>(lds, F_QP, 5);
    const u32x2 tagv = *(const u32x2*)(lds + L_TAG);
    const unsigned tagA = __builtin_amdgcn_readfirstlane(tagv[0]), tagB = __builtin_amdgcn_readfirstlane(tagv[1]);
    if (pw == 0) {
      f32x2 q2 = *(const f32x2*)(lds + L_QPART + (lane & 31) * 8);
#pragma unroll
      for (int ww = 1; ww < 8; ++ww) q2 += *(const f32x2*)(lds + L_QPART + ww * 256 + (lane & 31) * 8);
      __builtin_amdgcn_raw_buffer_store_b128(u32x4{tagA, asu(q2[0]), tagB, asu(q2[1])}, rws,
                                             lane < 32 ? WS_QX + g * 4096 + qh * 2048 + cb * 512 + lane * 16 : OOB, 0, AUX_SC1);
      STAMP(8);
    }
    for (int i = 0; i <= pw; ++i) __builtin_amdgcn_s_sleep(PSL);
#pragma unroll 1
    for (unsigned spin = 0; spin < SPIN_CAP; ++spin) {
      asm volatile("" ::: "memory");
      u32x4 gr[4];
#pragma unroll
      for (int i = 0; i < 4; ++i) gr[i] = __builtin_amdgcn_raw_buffer_load_b128(rws, WS_QX + g * 4096 + i * 1024 + lane * 16, 0, AUX_SC1);
      bool ok = true;
#pragma unroll
      for (int i = 0; i < 4; ++i) ok &= (gr[i][0] == tagA) & (gr[i][2] == tagB);
      if (__all(ok)) {
        *(unsigned*)(lds + L_QS + lane * 4) = pack_bf16(asf(gr[0][1]) + asf(gr[2][1]), asf(gr[0][3]) + asf(gr[2][3]));
        *(unsigned*)(lds + L_QS + 256 + lane * 4) = pack_bf16(asf(gr[1][1]) + asf(gr[3][1]), asf(gr[1][3]) + asf(gr[3][3]));
        flag_add(lds, F_Q, lane);
        break;
      }
      if (flag_set(lds, F_Q)) break;
    }
    const int cp = 2 * (lane & 3);
    float y0 = 0.f, y1 = 0.f;
    if (red < 72) {
      flag_wait<16>(lds, F_Y, 4);
#pragma unroll
      for (int ww = 0; ww < 4; ++ww) { const f32x2 v = *(const f32x2*)(lds + L_YP + ww * 32 + cp * 4); y0 += v[0]; y1 += v[1]; }
      y0 = rbf(rbf(y0) + bflo(b2v)); y1 = rbf(rbf(y1) + bflo(b2v));
    }
    if (pw == 0) {
      flag_wait<8>(lds, F_END, S);
      __builtin_amdgcn_raw_buffer_store_b64(u32x2{tagA, tagB}, rws, lane == 0 ? WS_HU + g * 256 + s * 8 : OOB, 0, AUX_SC1);
#pragma unroll 1
      for (unsigned spin = 0; spin < SPIN_CAP; ++spin) {
        asm volatile("" ::: "memory");
        const u32x2 hu = __builtin_amdgcn_raw_buffer_load_b64(rws, WS_HU + g * 256 + (lane & 7) * 8, 0, AUX_SC1);
        if (__all(((hu[0] == tagA) & (hu[1] == tagB)) | ((lane & 7) == s))) break;
        __builtin_amdgcn_s_sleep(2);
      }
      flag_add(lds, F_GO, lane);
      STAMP(5);
    } else {
      flag_wait<4>(lds, F_GO, 1);
      for (int i = 0; i < pw; ++i) __builtin_amdgcn_s_sleep(PSL);
    }
    {
      const int pi = lane & 15;
      int od = WS_PX + (id * 16 + pi) * 256 + la * 32, om = WS_ML + ((g * 2 + (s >> 2)) * 16 + pi) * 16;
      float v[8], pm = 0.f, pl = 0.f;
#pragma unroll
      for (int j = 0; j < 8; ++j) v[j] = 0.f;
#pragma unroll 1
      for (unsigned spin = 0; spin < SPIN_CAP; ++spin) {
        asm volatile("" ::: "memory");
        u32x4 g1[5];
        g1[0] = __builtin_amdgcn_raw_buffer_load_b128(rws, od, 0, AUX_SC1);
        g1[1] = __builtin_amdgcn_raw_buffer_load_b128(rws, od + 128, 0, AUX_SC1);
        g1[2] = __builtin_amdgcn_raw_buffer_load_b128(rws, od + 16, 0, AUX_SC1);
        g1[3] = __builtin_amdgcn_raw_buffer_load_b128(rws, od + 144, 0, AUX_SC1);
        g1[4] = __builtin_amdgcn_raw_buffer_load_b128(rws, om, 0, AUX_SC1);
        bool fresh = od != OOB;
#pragma unroll
        for (int i = 0; i < 5; ++i) fresh &= (g1[i][0] == tagA) & (g1[i][2] == tagB);
#pragma unroll
        for (int i = 0; i < 4; ++i) { v[2 * i] = fresh ? asf(g1[i][1]) : v[2 * i]; v[2 * i + 1] = fresh ? asf(g1[i][3]) : v[2 * i + 1]; }
        pm = fresh ? asf(g1[4][1]) : pm; pl = fresh ? asf(g1[4][3]) : pl;
        od = fresh ? OOB : od; om = fresh ? OOB : om;
        if (__all(od == OOB)) {
          float M = fmaxf(pm, dppf<XOR1>(pm));
          M = fmaxf(M, dppf<XOR2>(M)); M = fmaxf(M, dppf<ROR4>(M)); M = fmaxf(M, dppf<ROR8>(M));
          const float wgt = __builtin_amdgcn_exp2f((pm - M) * LOG2E);
          const float L = row_sum(wgt * pl);
          const float r0 = __builtin_amdgcn_rcpf(L), inv = fmaf(fmaf(-L, r0, 1.0f), r0, r0);
#pragma unroll
          for (int j = 0; j < 8; ++j) v[j] = row_sum(wgt * v[j]) * inv;
          if (pi == 0) *(u32x4*)(lds + L_OA + la * 16) = u32x4{pack_bf16(v[0], v[1]), pack_bf16(v[2], v[3]), pack_bf16(v[4], v[5]), pack_bf16(v[6], v[7])};
          flag_add(lds, F_O, lane);
          break;
        }
        if (flag_set(lds, F_O)) break;
      }
    }
    if (red < 72) {
      float v0[8], v1[8];
      int of[8];
#pragma unroll
      for (int i = 0; i < 8; ++i) { v0[i] = 0.f; v1[i] = 0.f; of[i] = WS_OX + (b * 72 + red) * 8192 + i * 1024 + lane * 16; }
#pragma unroll 1
      for (unsigned spin = 0; spin < SPIN_CAP; ++spin) {
        asm volatile("" ::: "memory");
        u32x4 g2[8];
#pragma unroll
        for (int i = 0; i < 8; ++i) g2[i] = __builtin_amdgcn_raw_buffer_load_b128(rws, of[i], 0, AUX_SC1);
        bool done = true;
#pragma unroll
        for (int i = 0; i < 8; ++i) {
          const bool fresh = (of[i] != OOB) & (g2[i][0] == tagA) & (g2[i][2] == tagB);
          v0[i] = fresh ? asf(g2[i][1]) : v0[i]; v1[i] = fresh ? asf(g2[i][3]) : v1[i];
          of[i] = fresh ? OOB : of[i];
          done &= of[i] == OOB;
        }
        if (flag_set(lds, F_FIN)) break;
        if (__all(done)) {
          float s0 = ((v0[0] + v0[1]) + (v0[2] + v0[3])) + ((v0[4] + v0[5]) + (v0[6] + v0[7]));
          float s1 = ((v1[0] + v1[1]) + (v1[2] + v1[3])) + ((v1[4] + v1[5]) + (v1[6] + v1[7]));
          s0 += dppf<ROR4>(s0); s0 += dppf<ROR8>(s0); s0 = rows_sum(s0);
          s1 += dppf<ROR4>(s1); s1 += dppf<ROR8>(s1); s1 = rows_sum(s1);
          const float r0 = rbf(bflo(xfin) + rbf(s0)) + y0, r1 = rbf(bfhi(xfin) + rbf(s1)) + y1;
          if (lane < 4) *(unsigned*)(out + b * DM + red * 8 + cp) = pack_bf16(r0, r1);
          if (red == 0 && lane == 0) __builtin_amdgcn_raw_buffer_store_b64(u32x2{tagA, tagB}, rws, WS_EPOCH + b * 256, 0, AUX_SC1);
          flag_add(lds, F_FIN, lane);
          if (id == DIAG_WG) {
            const unsigned tn = now32();
            const unsigned long long cn = __builtin_amdgcn_s_memtime();
            const unsigned* dg = (const unsigned*)(lds + L_DG);
            unsigned met = 0;
            if (DIAG_M == 1) met = tn - t_start;
            if (DIAG_M == 2) met = dg[0] - t_start;
            if (DIAG_M == 3) met = (unsigned)((float)(cn - c_start) / (float)(tn - t_start) * 100.f);
            if (DIAG_M == 4) met = dg[3] - dg[2];
            if (DIAG_M == 5) met = dg[1] - t_start;
            if (DIAG_M == 6) met = tbar - t_start;
            if (DIAG_M == 7) met = dg[2] - t_start;
            met = __builtin_amdgcn_readfirstlane(met);
            wait_until(t_start + 15000u + 10u * (met < 20000u ? met : 20000u));
          }
          break;
        }
      }
    }
  } else {
  const int voffK = ((lane & 15) >> 2) * 256 + (lane >> 4) * 64 + (lane & 3) * 16;
  const int voffV = lane * 16;
  u32x4 kb[8], vb[8];
#define ISSUE_K(so) _Pragma("unroll") for (int u = 0; u < 8; ++u) kb[u] = __builtin_amdgcn_raw_buffer_load_b128(rk, voffK + u * 1024, so, AUX_NT)
#define ISSUE_V(so) _Pragma("unroll") for (int u = 0; u < 8; ++u) vb[u] = __builtin_amdgcn_raw_buffer_load_b128(rv, voffV + u * 1024, so, AUX_NT)
  if (w >= 4) {
    const int rg = (w - 4) * 8 + (lane >> 3), c8 = lane & 7;
    unsigned xs[5];
    u32x4 wqv[9];
#pragma unroll
    for (int i = 0; i < 5; ++i) xs[i] = __builtin_amdgcn_raw_buffer_load_b32(rx, (lane + 64 * i) * 4, 0, 0);
    __builtin_amdgcn_sched_barrier(0);
    const rsrc_t rq = mkrsrc(wq + qh * 288 * 4096 + kvh * 256 + cb * 64, (287 * 4096 + 64) * 2);
#pragma unroll
    for (int i = 0; i < 9; ++i) wqv[i] = __builtin_amdgcn_raw_buffer_load_b128(rq, (rg + 32 * i) * 8192 + c8 * 16, 0, AUX_NT);
    __builtin_amdgcn_sched_barrier(0);
    __builtin_amdgcn_s_barrier();
    tbar = now32();
    __builtin_amdgcn_sched_barrier(0);
    float xv[10], mean1, den1;
#pragma unroll
    for (int i = 0; i < 5; ++i) { xv[2 * i] = bflo(xs[i]); xv[2 * i + 1] = bfhi(xs[i]); }
    ln_stats(xv, lane < 32, mean1, den1);
#pragma unroll
    for (int i = 0; i < 5; ++i)
      *(unsigned*)(lds + L_XN + (lane + 64 * i) * 4) = pack_bf16(ln_apply(xv[2 * i], mean1, den1), ln_apply(xv[2 * i + 1], mean1, den1));
    STAMP(9);
    f32x4 qa = {0.f, 0.f, 0.f, 0.f};
#pragma unroll
    for (int i = 0; i < 9; ++i) {
      const unsigned t = (unsigned)*(const unsigned short*)(lds + L_XN + (qh * 288 + rg + 32 * i) * 2) << (16 * (lane & 1));
      const int jp = c8 >> 1;
      qa = mfma32(wqv[i], u32x4{jp == 0 ? t : 0u, jp == 1 ? t : 0u, jp == 2 ? t : 0u, jp == 3 ? t : 0u}, qa);
    }
    if ((lane >> 5) == ((lane >> 3) & 1)) {
      float* qp = (float*)(lds + L_QPART) + ((w - 4) * 2 + (lane >> 5)) * 64 + ((lane >> 4) & 1) * 32 + c8;
#pragma unroll
      for (int r = 0; r < 4; ++r) qp[r * 8] = qa[r];
    }
    flag_add(lds, F_QP, lane);
    STAMP(1);
  }
  wait_until(tbar + SCH0 + w * SCHD);
  __builtin_amdgcn_sched_barrier(0);
  ISSUE_K(w * TILE_BYTES);
  ISSUE_V(w * TILE_BYTES);
  __builtin_amdgcn_sched_barrier(0);
  flag_wait<1>(lds, F_Q, 1);
  if (w == 0) *(unsigned*)(lds + L_DG) = now32();

  const int c = lane & 3, hb = (lane >> 2) & 1, tb = (lane >> 3) & 1;
  const u32x2 tagv = *(const u32x2*)(lds + L_TAG);
  float m = -3.0e38f, l = 0.f;
  f32x4 acc[2][2];
#pragma unroll
  for (int i = 0; i < 2; ++i)
#pragma unroll
    for (int j = 0; j < 2; ++j) acc[i][j] = f32x4{0.f, 0.f, 0.f, 0.f};
  {
    const u32x4 qf = *(const u32x4*)(lds + L_QS + (hb * 128 + la * 32 + c * 8) * 2);
    const u32x4 zero4 = {0u, 0u, 0u, 0u};
    const u32x4 q0 = tb ? zero4 : qf, q1 = tb ? qf : zero4;
    const unsigned mask0 = c == 0 ? 0x0000ffffu : c == 1 ? 0xffff0000u : 0u;
    const unsigned mask1 = c == 2 ? 0x0000ffffu : c == 3 ? 0xffff0000u : 0u;
    auto tile = [&](auto more, int so) {
      f32x4 sc[4];
#pragma unroll
      for (int i = 0; i < 4; ++i) sc[i] = mfma32(kb[i], q0, f32x4{0.f, 0.f, 0.f, 0.f});
#pragma unroll
      for (int i = 0; i < 4; ++i) sc[i] = mfma32(kb[4 + i], q1, sc[i]);
      if constexpr (more) {
        __builtin_amdgcn_sched_barrier(0);
        if (DLYK) __builtin_amdgcn_s_sleep(DLYK);
        ISSUE_K(so);
        __builtin_amdgcn_sched_barrier(0);
      }
      float t4[4];
#pragma unroll
      for (int i = 0; i < 4; ++i) t4[i] = (dppf<QB0>(sc[i][0]) + dppf<QB1>(sc[i][1])) + (dppf<QB2>(sc[i][2]) + dppf<QB3>(sc[i][3]));
      const float raw = (c & 2) ? ((c & 1) ? t4[3] : t4[2]) : ((c & 1) ? t4[1] : t4[0]);
      const float sv = rbf(rbf(raw) * SCALE);
      if (__any(sv > m)) {
        float mx = fmaxf(sv, dppf<XOR1>(sv));
        mx = fmaxf(mx, dppf<XOR2>(mx));
        mx = fmaxf(mx, dppf<ROR8>(mx));
        mx = fmaxf(rows_max(mx), m);
        const float alpha = __builtin_amdgcn_exp2f((m - mx) * LOG2E);
        m = mx;
        l *= alpha;
#pragma unroll
        for (int i = 0; i < 2; ++i)
#pragma unroll
          for (int j = 0; j < 2; ++j) acc[i][j] *= alpha;
      }
      const float p = __builtin_amdgcn_exp2f((sv - m) * LOG2E);
      l += p;
      const unsigned pk = pack_bf16(p, p);
      unsigned pb0[4], pb1[4];
      pb0[0] = dppu<QB0>(pk) & mask0; pb1[0] = dppu<QB0>(pk) & mask1;
      pb0[1] = dppu<QB1>(pk) & mask0; pb1[1] = dppu<QB1>(pk) & mask1;
      pb0[2] = dppu<QB2>(pk) & mask0; pb1[2] = dppu<QB2>(pk) & mask1;
      pb0[3] = dppu<QB3>(pk) & mask0; pb1[3] = dppu<QB3>(pk) & mask1;
#pragma unroll
      for (int u = 0; u < 8; ++u) {
        acc[u >> 2][0] = mfma16(vb[u][0], vb[u][1], pb0[u & 3], pb1[u & 3], acc[u >> 2][0]);
        acc[u >> 2][1] = mfma16(vb[u][2], vb[u][3], pb0[u & 3], pb1[u & 3], acc[u >> 2][1]);
      }
      if constexpr (more) {
        __builtin_amdgcn_sched_barrier(0);
        if (DLYV) __builtin_amdgcn_s_sleep(DLYV);
        ISSUE_V(so);
        __builtin_amdgcn_sched_barrier(0);
      }
    };
    if (w < S) {
      __builtin_amdgcn_s_setprio(2);
      if (SCHHOLD) wait_until(tbar + SCH0 + (NWAVE + w) * SCHD - 40);
      for (int nxt = NWAVE + w; nxt < NT; nxt += S) {
        if (nxt == NT - HUAT * S + w) flag_add(lds, F_END, lane);
        tile(std::true_type{}, nxt * TILE_BYTES);
        if (nxt == 28) *(unsigned*)(lds + L_DG + 8) = now32();
        if (nxt == 108) *(unsigned*)(lds + L_DG + 12) = now32();
      }
    }
    tile(std::false_type{}, 0);
  }
  if (w == 0) *(unsigned*)(lds + L_DG + 4) = now32();
  const unsigned tagA = __builtin_amdgcn_readfirstlane(tagv[0]), tagB = __builtin_amdgcn_readfirstlane(tagv[1]);
  f32x4 res;
#pragma unroll
  for (int r = 0; r < 4; ++r) {
    const float own = tb ? acc[1][1][r] : acc[0][0][r], snd = tb ? acc[1][0][r] : acc[0][1][r];
    res[r] = own + dppf<ROR8>(snd);
  }
  l += dppf<XOR1>(l);
  l += dppf<XOR2>(l);
  l += dppf<ROR8>(l);
  l = rows_sum(l);

  if (w < S) {
    flag_wait<1>(lds, F_HP, NWAVE - S);
    f32x2 hml[3];
    f32x4 hp[3];
#pragma unroll
    for (int k = 0; k < 3; ++k) {
      hml[k] = *(const f32x2*)(lds + L_ML + ((w * 3 + k) * 2 + hb) * 8);
      hp[k] = *(const f32x4*)(lds + L_PART + (w * 3 + k) * 1024 + lane * 16);
    }
    const float mx = fmaxf(fmaxf(m, hml[0][0]), fmaxf(hml[1][0], hml[2][0]));
    const float al = __builtin_amdgcn_exp2f((m - mx) * LOG2E);
    res *= al; l *= al;
#pragma unroll
    for (int k = 0; k < 3; ++k) {
      const float be = __builtin_amdgcn_exp2f((hml[k][0] - mx) * LOG2E);
      res += be * hp[k]; l = fmaf(be, hml[k][1], l);
    }
    const int po = WS_PX + ((g * 8 + hb * 4 + la) * 16 + 2 * s + w) * 256 + (tb * 4 + c) * 16;
    __builtin_amdgcn_raw_buffer_store_b128(u32x4{tagA, asu(res[0]), tagB, asu(res[1])}, rws, po, 0, AUX_SC1);
    __builtin_amdgcn_raw_buffer_store_b128(u32x4{tagA, asu(res[2]), tagB, asu(res[3])}, rws, po + 128, 0, AUX_SC1);
    __builtin_amdgcn_raw_buffer_store_b128(u32x4{tagA, asu(mx), tagB, asu(l)}, rws,
                                           (lane & ~4) == 0 ? WS_ML + ((g * 2 + hb) * 16 + 2 * s + w) * 16 : OOB, 0, AUX_SC1);
    STAMP(10);
  } else {
    const int hw = w - S;
    *(f32x4*)(lds + L_PART + hw * 1024 + lane * 16) = res;
    if ((lane & ~4) == 0) *(f32x2*)(lds + L_ML + (hw * 2 + hb) * 8) = f32x2{m, l};
    flag_add(lds, F_HP, lane);

    const bool mlp = w >= 4 && red < 72;
    u32x4 wt[6];
    {
      const rsrc_t ro = mkrsrc(wo + (size_t)(kvh * 256 + s * 32) * DM, 32 * DM * 2);
#pragma unroll
      for (int i = 0; i < 6; ++i) {
        const int p = lane + 64 * i, row = p / 12;
        wt[i] = __builtin_amdgcn_raw_buffer_load_b128(ro, row * (DM * 2) + hw * 192 + (p - row * 12) * 16, 0, AUX_NT);
      }
    }
    unsigned char* img = lds + L_WO + hw * 6144;
#pragma unroll
    for (int i = 0; i < 6; ++i) *(u32x4*)(img + (lane + 64 * i) * 16) = wt[i];
    asm volatile("s_waitcnt lgkmcnt(0)" ::: "memory");
    u32x4 wof[6];
    {
      typedef __attribute__((address_space(3))) s16x4* lds_s16x4;
      const int ra = (((lane & 15) >> 2) * 8 + la * 2) * 192 + (lane & 3) * 8;
#pragma unroll
      for (int t = 0; t < 6; ++t) {
        const u32x2 lo = __builtin_bit_cast(u32x2, __builtin_amdgcn_ds_read_tr16_b64_v4i16((lds_s16x4)(img + ra + t * 32)));
        const u32x2 hi = __builtin_bit_cast(u32x2, __builtin_amdgcn_ds_read_tr16_b64_v4i16((lds_s16x4)(img + ra + t * 32 + 192)));
        wof[t] = u32x4{lo[0], lo[1], hi[0], hi[1]};
      }
    }
    if (mlp) {
      const int ht = (w - 4) * 64 + lane;
      unsigned a1[9], ab[9], a2[9];
      const rsrc_t r1 = mkrsrc(w1, FF * 2), rb = mkrsrc(b1, FF * 2), r2 = mkrsrc(w2, FF * 2);
#pragma unroll
      for (int i = 0; i < 9; ++i) {
        a1[i] = __builtin_amdgcn_raw_buffer_load_b32(r1, (ht + 256 * i) * 4, 0, 0);
        ab[i] = __builtin_amdgcn_raw_buffer_load_b32(rb, (ht + 256 * i) * 4, 0, 0);
        a2[i] = __builtin_amdgcn_raw_buffer_load_b32(r2, (ht + 256 * i) * 4, 0, 0);
      }
      __builtin_amdgcn_sched_barrier(0);
      float xn[10];
#pragma unroll
      for (int i = 0; i < 5; ++i) { const unsigned v = *(const unsigned*)(lds + L_XN + (lane + 64 * i) * 4); xn[2 * i] = bflo(v); xn[2 * i + 1] = bfhi(v); }
      float mean2, den2;
      ln_stats(xn, lane < 32, mean2, den2);
      const u32x4 xc = *(const u32x4*)(lds + L_XN + red * 16);
      float hv[8], y[8];
#pragma unroll
      for (int j = 0; j < 4; ++j) { hv[2 * j] = ln_apply(bflo(xc[j]), mean2, den2); hv[2 * j + 1] = ln_apply(bfhi(xc[j]), mean2, den2); }
#pragma unroll
      for (int j = 0; j < 8; ++j) y[j] = 0.f;
      constexpr float GC0 = -2.0f * 0.7978845608028654f * LOG2E, GC1 = GC0 * 0.044715f;
#pragma unroll
      for (int i = 0; i < 9; ++i) {
#pragma unroll
        for (int j = 0; j < 8; ++j) {
          const unsigned pr = pack_bf16(hv[j] * bflo(a1[i]), hv[j] * bfhi(a1[i]));
          const unsigned uu = pack_bf16(bflo(pr) + bflo(ab[i]), bfhi(pr) + bfhi(ab[i]));
          const float u0 = bflo(uu), u1 = bfhi(uu);
          const float e0 = __builtin_amdgcn_exp2f(u0 * fmaf(u0 * u0, GC1, GC0));
          const float e1 = __builtin_amdgcn_exp2f(u1 * fmaf(u1 * u1, GC1, GC0));
          const unsigned gg = pack_bf16(u0 * __builtin_amdgcn_rcpf(1.0f + e0), u1 * __builtin_amdgcn_rcpf(1.0f + e1));
          y[j] = fmaf(bflo(gg), bflo(a2[i]), y[j]);
          y[j] = fmaf(bfhi(gg), bfhi(a2[i]), y[j]);
        }
        __builtin_amdgcn_sched_barrier(0);
      }
#pragma unroll
      for (int j = 0; j < 8; ++j) y[j] = wave_sum(y[j]);
      if (lane == 0) {
        *(f32x4*)(lds + L_YP + (w - 4) * 32) = f32x4{y[0], y[1], y[2], y[3]};
        *(f32x4*)(lds + L_YP + (w - 4) * 32 + 16) = f32x4{y[4], y[5], y[6], y[7]};
      }
      flag_add(lds, F_Y, lane);
    }
    STAMP(4);

    flag_wait<16>(lds, F_GO, 1);
    __builtin_amdgcn_s_setprio(3);
    flag_wait<0>(lds, F_O, 1);
    STAMP(6);
    {
      const u32x4 oa = *(const u32x4*)(lds + L_OA + la * 16);
      float d[6];
#pragma unroll
      for (int t = 0; t < 6; ++t) d[t] = mfma32(oa, wof[t], f32x4{0.f, 0.f, 0.f, 0.f})[0];
      const float e0 = (la & 2) ? ((la & 1) ? d[3] : d[2]) : ((la & 1) ? d[1] : d[0]), e1 = (la & 1) ? d[5] : d[4];
      const int cl = lane & 15;
      const int oo = WS_OX + (b * 72 + hw * 12 + la * 2 + (cl >> 3)) * 8192 + (id & 127) * 64 + ((cl & 7) >> 1) * 16;
      const float f0 = dppf<XOR1>(e0), f1 = dppf<XOR1>(e1);
      __builtin_amdgcn_raw_buffer_store_b128(u32x4{tagA, asu(e0), tagB, asu(f0)}, rws, (lane & 1) ? OOB : oo, 0, AUX_SC1);
      __builtin_amdgcn_raw_buffer_store_b128(u32x4{tagA, asu(e1), tagB, asu(f1)}, rws, ((lane & 1) | (la >> 1)) ? OOB : oo + 8 * 8192, 0, AUX_SC1);
    }
    STAMP(11);
  }
  }
  STAMP(7);
}

}

extern "C" void kernel_launch(void* const* d_in, const int* in_sizes, int n_in, void* d_out, int out_size, void* d_ws,
                              size_t ws_size, hipStream_t stream) {
  (void)in_sizes; (void)n_in; (void)out_size; (void)ws_size;
  fused_decode_kernel<<<dim3(NWG), dim3(NTHR), 0, stream>>>(
      (const unsigned short*)d_in[0], (const unsigned char*)d_in[1], (const unsigned char*)d_in[2],
      (const unsigned short*)d_in[3], (const unsigned short*)d_in[4], (const unsigned short*)d_in[6],
      (const unsigned short*)d_in[7], (const unsigned short*)d_in[8], (const unsigned short*)d_in[9],
      (unsigned short*)d_out, (unsigned char*)d_ws);
}
```

```cpp
#include <hip/hip_runtime.h>
#include <hip/hip_bf16.h>
#include <stdint.h>
#include <type_traits>

namespace {

typedef unsigned u32x2 __attribute__((ext_vector_type(2)));
typedef unsigned u32x4 __attribute__((ext_vector_type(4)));
typedef float f32x2 __attribute__((ext_vector_type(2)));
typedef float f32x4 __attribute__((ext_vector_type(4)));
typedef short s16x4 __attribute__((ext_vector_type(4)));
typedef short s16x8 __attribute__((ext_vector_type(8)));
typedef __bf16 bf16x2 __attribute__((ext_vector_type(2)));
typedef __amdgpu_buffer_rsrc_t rsrc_t;

constexpr int DM = 576, HKV = 16, SK = 32768, DH = 128, FF = 4608;
constexpr int NSPLIT = 8, NWG = 256, NWAVE = 8, NTHR = (NWAVE + 1) * 64;
constexpr int KEYS_WG = SK / NSPLIT;
constexpr int TILE_BYTES = 32 * DH * 2;
constexpr int NT = KEYS_WG / 32;
constexpr int REGION = KEYS_WG * DH * 2;
constexpr int S = 2;
#define CODE_KB 23
#define SCH0 20
#define SCHD 55
#define SCHHOLD 1
#define PSL 6
#define T1D 8
#define T1S 1
#define T2D 14
#define T2S 1
#define DLY0 8
#define DLYMAX 20
#define DLYMIN 0
#define GAIN 4
#define TAU16 1762
#define REF0 520
constexpr int NITER = (NT - NWAVE) / S;
static_assert((NT - NWAVE) % S == 0, "tiles split evenly over the streaming waves");
constexpr int AUX_NT = 2, AUX_SC1 = 16;
constexpr unsigned RSRC3 = 0x00020000u;
constexpr unsigned SPIN_CAP = 1u << 19;
constexpr int OOB = 0x7ff00000;

constexpr int WS_EPOCH = 0;
constexpr int WS_QX = 4096;
constexpr int WS_PX = WS_QX + 32 * 4096;
constexpr int WS_OX = WS_PX + NWG * 2048;
constexpr int WS_END = WS_OX + 2 * 72 * 4096;
constexpr int WS_STAMP = 4 << 20;

constexpr int L_FLAG = 0;
constexpr int L_TAG = 32;
constexpr int L_XN = 48;
constexpr int L_QPART = L_XN + 1280;
constexpr int L_QS = L_QPART + 8 * 64 * 4;
constexpr int L_OA = L_QS + 512;
constexpr int L_YP = L_OA + 80;
constexpr int L_ML = L_YP + 4 * 8 * 4;
constexpr int L_PART = L_ML + 6 * 16;
constexpr int L_WO = L_PART + 6 * 1024;
constexpr int L_END = L_WO + 6 * 6144;
constexpr int F_QP = 0, F_Q = 1, F_HP = 2, F_GO = 3, F_END = 4, F_O = 5, F_Y = 6;

constexpr float LOG2E = 1.4426950408889634f;
constexpr float SCALE = 0.08838834764831845f;
constexpr float LN_EPS = 1e-5f;

__device__ __forceinline__ float asf(unsigned u) { return __builtin_bit_cast(float, u); }
__device__ __forceinline__ unsigned asu(float f) { return __builtin_bit_cast(unsigned, f); }
__device__ __forceinline__ float bflo(unsigned u) { return asf(u << 16); }
__device__ __forceinline__ float bfhi(unsigned u) { return asf(u & 0xffff0000u); }
__device__ __forceinline__ unsigned pack_bf16(float lo, float hi) {
  f32x2 v = {lo, hi};
  return __builtin_bit_cast(unsigned, __builtin_convertvector(v, bf16x2));
}
__device__ __forceinline__ float rbf(float f) { return (float)(__bf16)f; }
template <int CTRL> __device__ __forceinline__ unsigned dppu(unsigned x) {
  return (unsigned)__builtin_amdgcn_mov_dpp((int)x, CTRL, 0xf, 0xf, true);
}
template <int CTRL> __device__ __forceinline__ float dppf(float x) { return asf(dppu<CTRL>(asu(x))); }
constexpr int XOR1 = 0xB1, XOR2 = 0x4E, XOR7 = 0x141, ROR4 = 0x124, ROR8 = 0x128;
__device__ __forceinline__ float row_sum(float x) { x += dppf<XOR1>(x); x += dppf<XOR2>(x); x += dppf<ROR4>(x); return x + dppf<ROR8>(x); }
constexpr int QB0 = 0x00, QB1 = 0x55, QB2 = 0xAA, QB3 = 0xFF;
__device__ __forceinline__ float rows_sum(float x) {
  auto s = __builtin_amdgcn_permlane16_swap(asu(x), asu(x), false, false);
  x = asf(s[0]) + asf(s[1]);
  auto t = __builtin_amdgcn_permlane32_swap(asu(x), asu(x), false, false);
  return asf(t[0]) + asf(t[1]);
}
__device__ __forceinline__ float rows_max(float x) {
  auto s = __builtin_amdgcn_permlane16_swap(asu(x), asu(x), false, false);
  x = fmaxf(asf(s[0]), asf(s[1]));
  auto t = __builtin_amdgcn_permlane32_swap(asu(x), asu(x), false, false);
  return fmaxf(asf(t[0]), asf(t[1]));
}
__device__ __forceinline__ float wave_sum(float x) { return rows_sum(row_sum(x)); }
__device__ __forceinline__ rsrc_t mkrsrc(const void* p, int bytes) {
  return __builtin_amdgcn_make_buffer_rsrc(const_cast<void*>(p), 0, bytes, RSRC3);
}
__device__ __forceinline__ f32x4 mfma32(u32x4 a, u32x4 b, f32x4 c) {
  return __builtin_amdgcn_mfma_f32_16x16x32_bf16(__builtin_bit_cast(s16x8, a), __builtin_bit_cast(s16x8, b), c, 0, 0, 0);
}
__device__ __forceinline__ f32x4 mfma16(unsigned a0, unsigned a1, unsigned b0, unsigned b1, f32x4 c) {
  u32x2 a = {a0, a1}, b = {b0, b1};
  return __builtin_amdgcn_mfma_f32_16x16x16bf16_1k(__builtin_bit_cast(s16x4, a), __builtin_bit_cast(s16x4, b), c, 0, 0, 0);
}
__device__ __forceinline__ void wg_barrier() {
  asm volatile("s_waitcnt lgkmcnt(0)\n\ts_barrier" ::: "memory");
}
__device__ __forceinline__ unsigned put_lo(float v, unsigned byte) { return (asu(v) & 0xffffff00u) | (byte & 0xffu); }
__device__ __forceinline__ float cut_lo(unsigned u) { return asf(u & 0xffffff00u); }
__device__ __forceinline__ unsigned now32() { return (unsigned)__builtin_amdgcn_s_memrealtime(); }
__device__ __forceinline__ void wait_until(unsigned t) {
  while ((int)(now32() - t) < 0) __builtin_amdgcn_s_sleep(1);
}
__device__ __forceinline__ void flag_add(unsigned char* lds, int f, int lane) {
  asm volatile("s_waitcnt lgkmcnt(0)" ::: "memory");
  if (lane == 0) __hip_atomic_fetch_add((unsigned*)(lds + L_FLAG) + f, 1u, __ATOMIC_RELAXED, __HIP_MEMORY_SCOPE_WORKGROUP);
  asm volatile("" ::: "memory");
}
__device__ __forceinline__ bool flag_set(unsigned char* lds, int f) {
  return __builtin_amdgcn_readfirstlane(__hip_atomic_load((unsigned*)(lds + L_FLAG) + f, __ATOMIC_RELAXED, __HIP_MEMORY_SCOPE_WORKGROUP)) != 0;
}
template <int SLEEP> __device__ __forceinline__ void flag_wait(unsigned char* lds, int f, unsigned n) {
#pragma unroll 1
  for (unsigned spin = 0; spin < (SPIN_CAP << 4); ++spin) {
    const unsigned v = __hip_atomic_load((unsigned*)(lds + L_FLAG) + f, __ATOMIC_RELAXED, __HIP_MEMORY_SCOPE_WORKGROUP);
    if (__builtin_amdgcn_readfirstlane(v) >= n) break;
    if (SLEEP) __builtin_amdgcn_s_sleep(SLEEP);
  }
  asm volatile("" ::: "memory");
}
__device__ __forceinline__ void ln_stats(const float (&v)[10], bool last_valid, float& mean_b, float& den_b) {
  float s = 0.f;
#pragma unroll
  for (int i = 0; i < 8; ++i) s += v[i];
  s += last_valid ? (v[8] + v[9]) : 0.f;
  const float mean = wave_sum(s) * (1.0f / DM);
  float q = 0.f;
#pragma unroll
  for (int i = 0; i < 8; ++i) { const float d = v[i] - mean; q = fmaf(d, d, q); }
  { const float d8 = v[8] - mean, d9 = v[9] - mean; q += last_valid ? fmaf(d8, d8, d9 * d9) : 0.f; }
  const float var = wave_sum(q) * (1.0f / DM);
  mean_b = rbf(mean);
  den_b = rbf(__builtin_amdgcn_sqrtf(rbf(rbf(var) + LN_EPS)));
}
__device__ __forceinline__ float ln_apply(float x, float mean_b, float den_b) {
  const float d = rbf(x - mean_b), r = __builtin_amdgcn_rcpf(den_b), q = d * r;
  return rbf(fmaf(fmaf(-q, den_b, d), r, q));
}

#define STAMP(i) do { } while (0)

__global__ void __launch_bounds__(NTHR, 3)
fused_decode_kernel(const unsigned short* __restrict__ x, const unsigned char* __restrict__ kc, const unsigned char* __restrict__ vc,
                    const unsigned short* __restrict__ wq, const unsigned short* __restrict__ wo,
                    const unsigned short* __restrict__ w1, const unsigned short* __restrict__ b1,
                    const unsigned short* __restrict__ w2, const unsigned short* __restrict__ b2,
                    unsigned short* __restrict__ out, unsigned char* ws) {
  __shared__ __attribute__((aligned(16))) unsigned char lds[L_END];
  const int lane = threadIdx.x & 63, wv = __builtin_amdgcn_readfirstlane(threadIdx.x >> 6), w = wv < NWAVE ? (wv + 4) & 7 : wv;
  const int id = blockIdx.x, g = id >> 3, s = id & 7, b = g >> 4, kvh = g & 15;
  STAMP(0);
  const rsrc_t rws = mkrsrc(ws, WS_END);
  const rsrc_t rx = mkrsrc(x + b * DM, DM * 2);
  const rsrc_t rk = mkrsrc(kc + (size_t)id * REGION, REGION);
  const rsrc_t rv = mkrsrc(vc + (size_t)id * REGION, REGION);

  const int qh = s >> 2, cb = (s + (kvh >> 2)) & 3;
  const int la = lane >> 4;
  const int red = kvh * 8 + s;

  unsigned tbar = 0;
  u32x2 ep = {0u, 0u};
  if (w < 4 || w >= NWAVE) {
    if (w == NWAVE) ep = __builtin_amdgcn_raw_buffer_load_b64(rws, WS_EPOCH + b * 256, 0, AUX_SC1);
    if (CODE_KB && w == 2) {
      const rsrc_t rc = mkrsrc((const void*)(__builtin_amdgcn_s_getpc() & ~255ll), CODE_KB * 1024);
      const u32x4 cv = __builtin_amdgcn_raw_buffer_load_b128(rc, g * 1024 + lane * 16, 0, 0);
      asm volatile("s_barrier" :: "v"(lane));
      tbar = now32();
      asm volatile("" :: "v"(cv));
    } else {
    if (w == 3 && lane < 8) *((unsigned*)(lds + L_FLAG) + lane) = 0u;
    wg_barrier();
    tbar = now32();
    }
  }
  if (w >= NWAVE) {
    const u32x2 xfin = __builtin_amdgcn_raw_buffer_load_b64(rx, red * 16 + (lane & 1) * 8, 0, 0);
    const unsigned b2v = __builtin_amdgcn_raw_buffer_load_b16(mkrsrc(b2, 4), 0, 0, 0);
    __builtin_amdgcn_s_setprio(3);
    const unsigned e0 = __builtin_amdgcn_readfirstlane(ep[0]);
    const unsigned tagA = ((e0 & 0x7f7f7f7fu) + 0x01010101u) ^ (e0 & 0x80808080u);
    const unsigned tagB = __builtin_amdgcn_readfirstlane(ep[1]) + 1u;
    if (lane == 0) *(unsigned*)(lds + L_TAG) = tagA;
    if (lane < 4) *(unsigned*)(lds + L_OA + 64 + lane * 4) = 0u;
    flag_wait<0>(lds, F_QP, 4);
    {
      f32x2 q2 = *(const f32x2*)(lds + L_QPART + (lane & 31) * 8);
#pragma unroll
      for (int ww = 1; ww < 8; ++ww) q2 += *(const f32x2*)(lds + L_QPART + ww * 256 + (lane & 31) * 8);
      __builtin_amdgcn_raw_buffer_store_b128(u32x4{tagA, asu(q2[0]), tagB, asu(q2[1])}, rws,
                                             lane < 32 ? WS_QX + g * 4096 + qh * 2048 + cb * 512 + lane * 16 : OOB, 0, AUX_SC1);
      STAMP(8);
    }
    __builtin_amdgcn_s_sleep(PSL);
    {
      u32x4 gr[4];
#pragma unroll 1
      for (unsigned spin = 0; spin < SPIN_CAP; ++spin) {
        asm volatile("" ::: "memory");
#pragma unroll
        for (int i = 0; i < 4; ++i) gr[i] = __builtin_amdgcn_raw_buffer_load_b128(rws, WS_QX + g * 4096 + i * 1024 + lane * 16, 0, AUX_SC1);
        bool ok = true;
#pragma unroll
        for (int i = 0; i < 4; ++i) ok &= (gr[i][0] == tagA) & (gr[i][2] == tagB);
        if (__all(ok)) break;
      }
      *(unsigned*)(lds + L_QS + lane * 4) = pack_bf16(asf(gr[0][1]) + asf(gr[2][1]), asf(gr[0][3]) + asf(gr[2][3]));
      *(unsigned*)(lds + L_QS + 256 + lane * 4) = pack_bf16(asf(gr[1][1]) + asf(gr[3][1]), asf(gr[1][3]) + asf(gr[3][3]));
      flag_add(lds, F_Q, lane);
    }
    f32x4 yv = {0.f, 0.f, 0.f, 0.f};
    if (red < 72) {
      flag_wait<16>(lds, F_Y, 4);
#pragma unroll
      for (int ww = 0; ww < 4; ++ww) yv += *(const f32x4*)(lds + L_YP + ww * 32 + (lane & 1) * 16);
#pragma unroll
      for (int j = 0; j < 4; ++j) yv[j] = rbf(rbf(yv[j]) + bflo(b2v));
    }
    flag_wait<16>(lds, F_GO, 1);
    flag_wait<0>(lds, F_END, S);
    STAMP(5);
    __builtin_amdgcn_s_sleep(T1D);
    {
      u32x4 ga[2] = {u32x4{0u, 0u, 0u, 0u}, u32x4{0u, 0u, 0u, 0u}};
      int of[2] = {WS_PX + id * 2048 + lane * 16, WS_PX + id * 2048 + 1024 + lane * 16};
#pragma unroll 1
      for (unsigned spin = 0; spin < SPIN_CAP; ++spin) {
        asm volatile("" ::: "memory");
        u32x4 gn[2];
#pragma unroll
        for (int i = 0; i < 2; ++i) gn[i] = __builtin_amdgcn_raw_buffer_load_b128(rws, of[i], 0, AUX_SC1);
#pragma unroll
        for (int i = 0; i < 2; ++i) {
          unsigned bad = ((gn[i][0] ^ tagA) | (gn[i][2] ^ (tagA >> 8))) & 0xffu;
          bad |= dppu<XOR1>(bad); bad |= dppu<XOR2>(bad); bad |= dppu<XOR7>(bad);
          const bool fresh = (bad == 0u) & (of[i] != OOB);
          ga[i] = fresh ? gn[i] : ga[i];
          of[i] = fresh ? OOB : of[i];
        }
        if (__all((of[0] == OOB) & (of[1] == OOB))) break;
        __builtin_amdgcn_s_sleep(T1S);
      }
      float ls[2];
#pragma unroll
      for (int i = 0; i < 2; ++i) {
        const unsigned h = ((ga[i][1] & 0xffu) | ((ga[i][3] & 0xffu) << 8)) << (16 * (lane & 1));
        ls[i] = asf(h | dppu<XOR1>(h));
      }
      float M = fmaxf(ls[0], ls[1]);
      M = rows_max(fmaxf(M, dppf<ROR8>(M)));
      const float w0 = __builtin_amdgcn_exp2f(ls[0] - M), w1 = __builtin_amdgcn_exp2f(ls[1] - M);
      float L = w0 + w1, v[4];
      L = rows_sum(L + dppf<ROR8>(L));
#pragma unroll
      for (int r = 0; r < 4; ++r) { v[r] = fmaf(w1, cut_lo(ga[1][r]), w0 * cut_lo(ga[0][r])); v[r] = rows_sum(v[r] + dppf<ROR8>(v[r])); }
      const float r0 = __builtin_amdgcn_rcpf(L), inv = fmaf(fmaf(-L, r0, 1.0f), r0, r0);
      if (lane < 8) *(u32x2*)(lds + L_OA + lane * 8) = u32x2{pack_bf16(v[0] * inv, v[1] * inv), pack_bf16(v[2] * inv, v[3] * inv)};
      flag_add(lds, F_O, lane);
    }
    if (red < 72) {
      __builtin_amdgcn_s_sleep(T2D);
      u32x4 ga[4];
      int of[4];
#pragma unroll
      for (int i = 0; i < 4; ++i) { ga[i] = u32x4{0u, 0u, 0u, 0u}; of[i] = WS_OX + (b * 72 + red) * 4096 + i * 1024 + lane * 16; }
#pragma unroll 1
      for (unsigned spin = 0; spin < SPIN_CAP; ++spin) {
        asm volatile("" ::: "memory");
        u32x4 gn[4];
#pragma unroll
        for (int i = 0; i < 4; ++i) gn[i] = __builtin_amdgcn_raw_buffer_load_b128(rws, of[i], 0, AUX_SC1);
        bool done = true;
#pragma unroll
        for (int i = 0; i < 4; ++i) {
          unsigned bad = (__builtin_amdgcn_perm(gn[i][1], gn[i][0], 0x0c0c0400u) | __builtin_amdgcn_perm(gn[i][3], gn[i][2], 0x04000c0cu)) ^ tagA;
          bad |= dppu<XOR1>(bad);
          const bool fresh = (bad == 0u) & (of[i] != OOB);
          ga[i] = fresh ? gn[i] : ga[i];
          of[i] = fresh ? OOB : of[i];
          done &= of[i] == OOB;
        }
        if (__all(done)) break;
        if (T2S) __builtin_amdgcn_s_sleep(T2S);
      }
      STAMP(13);
      float sv[4];
#pragma unroll
      for (int j = 0; j < 4; ++j) {
        float t = (cut_lo(ga[0][j]) + cut_lo(ga[1][j])) + (cut_lo(ga[2][j]) + cut_lo(ga[3][j]));
        t += dppf<XOR2>(t); t += dppf<ROR4>(t); t += dppf<ROR8>(t);
        sv[j] = rbf(rows_sum(t));
      }
      const unsigned o0 = pack_bf16(rbf(bflo(xfin[0]) + sv[0]) + yv[0], rbf(bfhi(xfin[0]) + sv[1]) + yv[1]);
      const unsigned o1 = pack_bf16(rbf(bflo(xfin[1]) + sv[2]) + yv[2], rbf(bfhi(xfin[1]) + sv[3]) + yv[3]);
      if (lane < 2) *(u32x2*)(out + b * DM + red * 8 + lane * 4) = u32x2{o0, o1};
      if (red == 0 && lane == 0) __builtin_amdgcn_raw_buffer_store_b64(u32x2{tagA, tagB}, rws, WS_EPOCH + b * 256, 0, AUX_SC1);
    }
  } else {
  const int voffK = ((lane & 15) >> 2) * 256 + (lane >> 4) * 64 + (lane & 3) * 16;
  const int voffV = lane * 16;
  u32x4 kb[8], vb[8];
#define ISSUE_K(so) _Pragma("unroll") for (int u = 0; u < 8; ++u) kb[u] = __builtin_amdgcn_raw_buffer_load_b128(rk, voffK + u * 1024, so, AUX_NT)
#define ISSUE_V(so) _Pragma("unroll") for (int u = 0; u < 8; ++u) vb[u] = __builtin_amdgcn_raw_buffer_load_b128(rv, voffV + u * 1024, so, AUX_NT)
  if (w >= 4) {
    const int rg = (w - 4) * 8 + (lane >> 3), c8 = lane & 7;
    unsigned xs[5];
    u32x4 wqv[9];
#pragma unroll
    for (int i = 0; i < 5; ++i) xs[i] = __builtin_amdgcn_raw_buffer_load_b32(rx, (lane + 64 * i) * 4, 0, 0);
    __builtin_amdgcn_sched_barrier(0);
    const rsrc_t rq = mkrsrc(wq + qh * 288 * 4096 + kvh * 256 + cb * 64, (287 * 4096 + 64) * 2);
#pragma unroll
    for (int i = 0; i < 9; ++i) wqv[i] = __builtin_amdgcn_raw_buffer_load_b128(rq, (rg + 32 * i) * 8192 + c8 * 16, 0, AUX_NT);
    __builtin_amdgcn_sched_barrier(0);
    __builtin_amdgcn_s_barrier();
    tbar = now32();
    __builtin_amdgcn_sched_barrier(0);
    float xv[10], mean1, den1;
#pragma unroll
    for (int i = 0; i < 5; ++i) { xv[2 * i] = bflo(xs[i]); xv[2 * i + 1] = bfhi(xs[i]); }
    ln_stats(xv, lane < 32, mean1, den1);
#pragma unroll
    for (int i = 0; i < 5; ++i)
      *(unsigned*)(lds + L_XN + (lane + 64 * i) * 4) = pack_bf16(ln_apply(xv[2 * i], mean1, den1), ln_apply(xv[2 * i + 1], mean1, den1));
    STAMP(9);
    f32x4 qa = {0.f, 0.f, 0.f, 0.f};
#pragma unroll
    for (int i = 0; i < 9; ++i) {
      const unsigned t = (unsigned)*(const unsigned short*)(lds + L_XN + (qh * 288 + rg + 32 * i) * 2) << (16 * (lane & 1));
      const int jp = c8 >> 1;
      qa = mfma32(wqv[i], u32x4{jp == 0 ? t : 0u, jp == 1 ? t : 0u, jp == 2 ? t : 0u, jp == 3 ? t : 0u}, qa);
    }
    if ((lane >> 5) == ((lane >> 3) & 1)) {
      float* qp = (float*)(lds + L_QPART) + ((w - 4) * 2 + (lane >> 5)) * 64 + ((lane >> 4) & 1) * 32 + c8;
#pragma unroll
      for (int r = 0; r < 4; ++r) qp[r * 8] = qa[r];
    }
    flag_add(lds, F_QP, lane);
    STAMP(1);
  }
  wait_until(tbar + SCH0 + w * SCHD);
  __builtin_amdgcn_sched_barrier(0);
  ISSUE_K(w * TILE_BYTES);
  ISSUE_V(w * TILE_BYTES);
  __builtin_amdgcn_sched_barrier(0);
  flag_wait<1>(lds, F_Q, 1);
  if (w < S) STAMP(2);

  const int c = lane & 3, hb = (lane >> 2) & 1, tb = (lane >> 3) & 1;
  const unsigned tagA = __builtin_amdgcn_readfirstlane(*(const unsigned*)(lds + L_TAG));
  float m = -3.0e38f, l = 0.f;
  f32x4 acc[2][2];
#pragma unroll
  for (int i = 0; i < 2; ++i)
#pragma unroll
    for (int j = 0; j < 2; ++j) acc[i][j] = f32x4{0.f, 0.f, 0.f, 0.f};
  {
    const u32x4 qf = *(const u32x4*)(lds + L_QS + (hb * 128 + la * 32 + c * 8) * 2);
    const u32x4 zero4 = {0u, 0u, 0u, 0u};
    const u32x4 q0 = tb ? zero4 : qf, q1 = tb ? qf : zero4;
    const unsigned mask0 = c == 0 ? 0x0000ffffu : c == 1 ? 0xffff0000u : 0u;
    const unsigned mask1 = c == 2 ? 0x0000ffffu : c == 3 ? 0xffff0000u : 0u;
    auto tile = [&](auto more, int so, int dly) {
      f32x4 sc[4];
#pragma unroll
      for (int i = 0; i < 4; ++i) sc[i] = mfma32(kb[i], q0, f32x4{0.f, 0.f, 0.f, 0.f});
#pragma unroll
      for (int i = 0; i < 4; ++i) sc[i] = mfma32(kb[4 + i], q1, sc[i]);
      if constexpr (more) {
        __builtin_amdgcn_sched_barrier(0);
        for (int i = 0; i < dly; ++i) __builtin_amdgcn_s_sleep(1);
        __builtin_amdgcn_sched_barrier(0);
        ISSUE_K(so);
        __builtin_amdgcn_sched_barrier(0);
      }
      float t4[4];
#pragma unroll
      for (int i = 0; i < 4; ++i) t4[i] = (dppf<QB0>(sc[i][0]) + dppf<QB1>(sc[i][1])) + (dppf<QB2>(sc[i][2]) + dppf<QB3>(sc[i][3]));
      const float raw = (c & 2) ? ((c & 1) ? t4[3] : t4[2]) : ((c & 1) ? t4[1] : t4[0]);
      const float sv = rbf(rbf(raw) * SCALE);
      if (__any(sv > m)) {
        float mx = fmaxf(sv, dppf<XOR1>(sv));
        mx = fmaxf(mx, dppf<XOR2>(mx));
        mx = fmaxf(mx, dppf<ROR8>(mx));
        mx = fmaxf(rows_max(mx), m);
        const float alpha = __builtin_amdgcn_exp2f((m - mx) * LOG2E);
        m = mx;
        l *= alpha;
#pragma unroll
        for (int i = 0; i < 2; ++i)
#pragma unroll
          for (int j = 0; j < 2; ++j) acc[i][j] *= alpha;
      }
      const float p = __builtin_amdgcn_exp2f((sv - m) * LOG2E);
      l += p;
      const unsigned pk = pack_bf16(p, p);
      unsigned pb0[4], pb1[4];
      pb0[0] = dppu<QB0>(pk) & mask0; pb1[0] = dppu<QB0>(pk) & mask1;
      pb0[1] = dppu<QB1>(pk) & mask0; pb1[1] = dppu<QB1>(pk) & mask1;
      pb0[2] = dppu<QB2>(pk) & mask0; pb1[2] = dppu<QB2>(pk) & mask1;
      pb0[3] = dppu<QB3>(pk) & mask0; pb1[3] = dppu<QB3>(pk) & mask1;
#pragma unroll
      for (int u = 0; u < 8; ++u) {
        acc[u >> 2][0] = mfma16(vb[u][0], vb[u][1], pb0[u & 3], pb1[u & 3], acc[u >> 2][0]);
        acc[u >> 2][1] = mfma16(vb[u][2], vb[u][3], pb0[u & 3], pb1[u & 3], acc[u >> 2][1]);
      }
      if constexpr (more) {
        __builtin_amdgcn_sched_barrier(0);
        for (int i = 0; i < dly; ++i) __builtin_amdgcn_s_sleep(1);
        __builtin_amdgcn_sched_barrier(0);
        ISSUE_V(so);
        __builtin_amdgcn_sched_barrier(0);
      }
    };
    if (w < S) {
      __builtin_amdgcn_s_setprio(2);
      if (SCHHOLD) wait_until(tbar + SCH0 + (NWAVE + w) * SCHD - 40);
      for (int it = 0; it < NITER; ++it) {
        const int nxt = NWAVE + w + it * S;
        const int lead = (int)(tbar + REF0 + (unsigned)(it * TAU16 >> 4) - now32());
        if (it == NITER - 1) flag_add(lds, F_GO, lane);
        if (it == NITER / 2) {
          flag_wait<1>(lds, F_HP, NWAVE - S);
          f32x2 hml[3];
          f32x4 hp[3];
#pragma unroll
          for (int k = 0; k < 3; ++k) {
            hml[k] = *(const f32x2*)(lds + L_ML + ((w * 3 + k) * 2 + hb) * 8);
            hp[k] = *(const f32x4*)(lds + L_PART + (w * 3 + k) * 1024 + lane * 16);
          }
          const float mx = fmaxf(fmaxf(m, hml[0][0]), fmaxf(hml[1][0], hml[2][0]));
          const float al = __builtin_amdgcn_exp2f((m - mx) * LOG2E);
          f32x4 add = {0.f, 0.f, 0.f, 0.f};
          float ladd = 0.f;
#pragma unroll
          for (int k = 0; k < 3; ++k) {
            const float be = __builtin_amdgcn_exp2f((hml[k][0] - mx) * LOG2E);
            add += be * hp[k]; ladd = fmaf(be, hml[k][1], ladd);
          }
          m = mx;
          l = fmaf(l, al, ladd * (1.0f / 32));
          acc[0][0] = acc[0][0] * al + add; acc[1][1] = acc[1][1] * al + add;
          acc[0][1] *= al; acc[1][0] *= al;
        }
        tile(std::true_type{}, nxt * TILE_BYTES, min(max(DLY0 + ((lead * GAIN + 128) >> 8), DLYMIN), DLYMAX));
      }
    }
    tile(std::false_type{}, 0, 0);
  }
  if (w < S) STAMP(3); else STAMP(12);
  f32x4 res;
#pragma unroll
  for (int r = 0; r < 4; ++r) {
    const float own = tb ? acc[1][1][r] : acc[0][0][r], snd = tb ? acc[1][0][r] : acc[0][1][r];
    res[r] = own + dppf<ROR8>(snd);
  }
  l += dppf<XOR1>(l);
  l += dppf<XOR2>(l);
  l += dppf<ROR8>(l);
  l = rows_sum(l);

  if (w < S) {
    const float r0 = __builtin_amdgcn_rcpf(l), inv = fmaf(fmaf(-l, r0, 1.0f), r0, r0);
    const unsigned lse = asu(fmaf(m, LOG2E, __builtin_amdgcn_logf(l))) >> (16 * (c & 1));
    __builtin_amdgcn_raw_buffer_store_b128(
        u32x4{put_lo(res[0] * inv, tagA), put_lo(res[1] * inv, lse), put_lo(res[2] * inv, tagA >> 8), put_lo(res[3] * inv, lse >> 8)}, rws,
        WS_PX + ((g * 8 + hb * 4 + la) * 16 + 2 * s + w) * 128 + (tb * 4 + c) * 16, 0, AUX_SC1);
    flag_add(lds, F_END, lane);
    STAMP(10);
  } else {
    const int hw = w - S;
    *(f32x4*)(lds + L_PART + hw * 1024 + lane * 16) = res;
    if ((lane & ~4) == 0) *(f32x2*)(lds + L_ML + (hw * 2 + hb) * 8) = f32x2{m, l};
    flag_add(lds, F_HP, lane);

    const bool mlp = w >= 4 && red < 72;
    u32x4 wt[6];
    {
      const rsrc_t ro = mkrsrc(wo + (size_t)(kvh * 256 + s * 32) * DM, 32 * DM * 2);
#pragma unroll
      for (int i = 0; i < 6; ++i) {
        const int p = lane + 64 * i, row = p / 12;
        wt[i] = __builtin_amdgcn_raw_buffer_load_b128(ro, row * (DM * 2) + hw * 192 + (p - row * 12) * 16, 0, AUX_NT);
      }
    }
    unsigned char* img = lds + L_WO + hw * 6144;
#pragma unroll
    for (int i = 0; i < 6; ++i) *(u32x4*)(img + (lane + 64 * i) * 16) = wt[i];
    asm volatile("s_waitcnt lgkmcnt(0)" ::: "memory");
    u32x4 wof[6];
    {
      typedef __attribute__((address_space(3))) s16x4* lds_s16x4;
      const int ra = (((lane & 15) >> 2) * 8 + la * 2) * 192 + (lane & 3) * 8;
#pragma unroll
      for (int t = 0; t < 6; ++t) {
        const u32x2 lo = __builtin_bit_cast(u32x2, __builtin_amdgcn_ds_read_tr16_b64_v4i16((lds_s16x4)(img + ra + t * 32)));
        const u32x2 hi = __builtin_bit_cast(u32x2, __builtin_amdgcn_ds_read_tr16_b64_v4i16((lds_s16x4)(img + ra + t * 32 + 192)));
        wof[t] = u32x4{lo[0], lo[1], hi[0], hi[1]};
      }
    }
    if (mlp) {
      const int ht = (w - 4) * 64 + lane;
      unsigned a1[9], ab[9], a2[9];
      const rsrc_t r1 = mkrsrc(w1, FF * 2), rb = mkrsrc(b1, FF * 2), r2 = mkrsrc(w2, FF * 2);
#pragma unroll
      for (int i = 0; i < 9; ++i) {
        a1[i] = __builtin_amdgcn_raw_buffer_load_b32(r1, (ht + 256 * i) * 4, 0, 0);
        ab[i] = __builtin_amdgcn_raw_buffer_load_b32(rb, (ht + 256 * i) * 4, 0, 0);
        a2[i] = __builtin_amdgcn_raw_buffer_load_b32(r2, (ht + 256 * i) * 4, 0, 0);
      }
      __builtin_amdgcn_sched_barrier(0);
      float xn[10];
#pragma unroll
      for (int i = 0; i < 5; ++i) { const unsigned v = *(const unsigned*)(lds + L_XN + (lane + 64 * i) * 4); xn[2 * i] = bflo(v); xn[2 * i + 1] = bfhi(v); }
      float mean2, den2;
      ln_stats(xn, lane < 32, mean2, den2);
      const u32x4 xc = *(const u32x4*)(lds + L_XN + red * 16);
      float hv[8], y[8];
#pragma unroll
      for (int j = 0; j < 4; ++j) { hv[2 * j] = ln_apply(bflo(xc[j]), mean2, den2); hv[2 * j + 1] = ln_apply(bfhi(xc[j]), mean2, den2); }
#pragma unroll
      for (int j = 0; j < 8; ++j) y[j] = 0.f;
      constexpr float GC0 = -2.0f * 0.7978845608028654f * LOG2E, GC1 = GC0 * 0.044715f;
#pragma unroll
      for (int i = 0; i < 9; ++i) {
#pragma unroll
        for (int j = 0; j < 8; ++j) {
          const unsigned pr = pack_bf16(hv[j] * bflo(a1[i]), hv[j] * bfhi(a1[i]));
          const unsigned uu = pack_bf16(bflo(pr) + bflo(ab[i]), bfhi(pr) + bfhi(ab[i]));
          const float u0 = bflo(uu), u1 = bfhi(uu);
          const float e0 = __builtin_amdgcn_exp2f(u0 * fmaf(u0 * u0, GC1, GC0));
          const float e1 = __builtin_amdgcn_exp2f(u1 * fmaf(u1 * u1, GC1, GC0));
          const unsigned gg = pack_bf16(u0 * __builtin_amdgcn_rcpf(1.0f + e0), u1 * __builtin_amdgcn_rcpf(1.0f + e1));
          y[j] = fmaf(bflo(gg), bflo(a2[i]), y[j]);
          y[j] = fmaf(bfhi(gg), bfhi(a2[i]), y[j]);
        }
        __builtin_amdgcn_sched_barrier(0);
      }
#pragma unroll
      for (int j = 0; j < 8; ++j) y[j] = wave_sum(y[j]);
      if (lane == 0) {
        *(f32x4*)(lds + L_YP + (w - 4) * 32) = f32x4{y[0], y[1], y[2], y[3]};
        *(f32x4*)(lds + L_YP + (w - 4) * 32 + 16) = f32x4{y[4], y[5], y[6], y[7]};
      }
      flag_add(lds, F_Y, lane);
    }
    STAMP(4);

    flag_wait<16>(lds, F_GO, 1);
    __builtin_amdgcn_s_setprio(3);
    flag_wait<0>(lds, F_O, 1);
    STAMP(6);
    {
      const int n = lane & 15;
      f32x4 d = {0.f, 0.f, 0.f, 0.f};
#pragma unroll
      for (int t = 0; t < 6; ++t) d = mfma32(wof[t], *(const u32x4*)(lds + L_OA + (n == t ? la * 16 : 64)), d);
      __builtin_amdgcn_raw_buffer_store_b128(
          u32x4{put_lo(d[0], tagA), put_lo(d[1], tagA >> 8), put_lo(d[2], tagA >> 16), put_lo(d[3], tagA >> 24)}, rws,
          n < 6 ? WS_OX + (b * 72 + hw * 12 + 2 * n + (la >> 1)) * 4096 + (id & 127) * 32 + (la & 1) * 16 : OOB, 0, AUX_SC1);
    }
    STAMP(11);
  }
  }
  STAMP(7);
}

}

extern "C" void kernel_launch(void* const* d_in, const int* in_sizes, int n_in, void* d_out, int out_size, void* d_ws,
                              size_t ws_size, hipStream_t stream) {
  (void)in_sizes; (void)n_in; (void)out_size; (void)ws_size;
  fused_decode_kernel<<<dim3(NWG), dim3(NTHR), 0, stream>>>(
      (const unsigned short*)d_in[0], (const unsigned char*)d_in[1], (const unsigned char*)d_in[2],
      (const unsigned short*)d_in[3], (const unsigned short*)d_in[4], (const unsigned short*)d_in[6],
      (const unsigned short*)d_in[7], (const unsigned short*)d_in[8], (const unsigned short*)d_in[9],
      (unsigned short*)d_out, (unsigned char*)d_ws);
}
```

```cpp
#include <hip/hip_runtime.h>
#include <hip/hip_bf16.h>
#include <stdint.h>
#include <type_traits>

namespace {

typedef unsigned u32x2 __attribute__((ext_vector_type(2)));
typedef unsigned u32x4 __attribute__((ext_vector_type(4)));
typedef float f32x2 __attribute__((ext_vector_type(2)));
typedef float f32x4 __attribute__((ext_vector_type(4)));
typedef short s16x4 __attribute__((ext_vector_type(4)));
typedef short s16x8 __attribute__((ext_vector_type(8)));
typedef __bf16 bf16x2 __attribute__((ext_vector_type(2)));
typedef __amdgpu_buffer_rsrc_t rsrc_t;

constexpr int DM = 576, HKV = 16, SK = 32768, DH = 128, FF = 4608;
#define NTW 8
constexpr int NSPLIT = 8, NWG = 256, NWAVE = NTW, NTHR = (NWAVE + 1) * 64;
constexpr int KEYS_WG = SK / NSPLIT;
constexpr int TILE_BYTES = 32 * DH * 2;
constexpr int NT = KEYS_WG / 32;
constexpr int REGION = KEYS_WG * DH * 2;
constexpr int S = 2;
#define CODE_KB 13
#define PFD 3
#define PF1 0
#define PFS 0
#define PFW "0"
#define SCHHOLD 1
#define PSL 6
#define TOUCH 0
#define TAUX 16
#define GOAT 1
#define AQL 16
#define AQS 16
#define APL 16
#define APS 16
#define AOL 16
#define AOS 16
#define ASM_ 0
#define QPS 2
#define T1D 8
#define T1S 1
#define T2D 14
#define T2S 1
#define DLY0 8
#define DLYMAX 12
#define DLYMIN 0
#define GAIN 4
#define TAU16 1762
#define REF0 150
constexpr int NITER = (NT - NWAVE) / S, NH = NWAVE - S, NFOLD = NH / S;
static_assert((NT - NWAVE) % S == 0, "tiles split evenly over the streaming waves");
constexpr int AUX_NT = 2, AUX_SC1 = 16;
constexpr unsigned RSRC3 = 0x00020000u;
constexpr unsigned SPIN_CAP = 1u << 19;
constexpr int OOB = 0x7ff00000;

constexpr int WS_EPOCH = 0;
constexpr int WS_QX = 4096;
constexpr int WS_PX = WS_QX + 32 * 4096;
constexpr int WS_OX = WS_PX + NWG * 2048;
constexpr int WS_END = WS_OX + 2 * 72 * 4096;
constexpr int WS_STAMP = 4 << 20;

constexpr int L_FLAG = 0;
constexpr int L_TAG = 32;
constexpr int L_XN = 48;
constexpr int L_QPART = L_XN + 1280;
constexpr int L_QS = L_QPART + 8 * 64 * 4;
constexpr int L_OA = L_QS + 512;
constexpr int L_YP = L_OA + 80;
constexpr int L_ML = L_YP + 4 * 8 * 4;
constexpr int L_PART = L_ML + NH * 16;
constexpr int L_WO = L_PART + NH * 1024;
constexpr int L_END = L_WO + 6 * 6144;
constexpr int F_QP = 0, F_Q = 1, F_HP = 2, F_GO = 3, F_END = 4, F_O = 5, F_Y = 6, F_ARR = 7;

constexpr float LOG2E = 1.4426950408889634f;
constexpr float SCALE = 0.08838834764831845f;
constexpr float LN_EPS = 1e-5f;

__device__ __forceinline__ float asf(unsigned u) { return __builtin_bit_cast(float, u); }
__device__ __forceinline__ unsigned asu(float f) { return __builtin_bit_cast(unsigned, f); }
__device__ __forceinline__ float bflo(unsigned u) { return asf(u << 16); }
__device__ __forceinline__ float bfhi(unsigned u) { return asf(u & 0xffff0000u); }
__device__ __forceinline__ unsigned pack_bf16(float lo, float hi) {
  f32x2 v = {lo, hi};
  return __builtin_bit_cast(unsigned, __builtin_convertvector(v, bf16x2));
}
__device__ __forceinline__ float rbf(float f) { return (float)(__bf16)f; }
template <int CTRL> __device__ __forceinline__ unsigned dppu(unsigned x) {
  return (unsigned)__builtin_amdgcn_mov_dpp((int)x, CTRL, 0xf, 0xf, true);
}
template <int CTRL> __device__ __forceinline__ float dppf(float x) { return asf(dppu<CTRL>(asu(x))); }
constexpr int XOR1 = 0xB1, XOR2 = 0x4E, XOR7 = 0x141, ROR4 = 0x124, ROR8 = 0x128;
__device__ __forceinline__ float row_sum(float x) { x += dppf<XOR1>(x); x += dppf<XOR2>(x); x += dppf<ROR4>(x); return x + dppf<ROR8>(x); }
constexpr int QB0 = 0x00, QB1 = 0x55, QB2 = 0xAA, QB3 = 0xFF;
__device__ __forceinline__ float rows_sum(float x) {
  auto s = __builtin_amdgcn_permlane16_swap(asu(x), asu(x), false, false);
  x = asf(s[0]) + asf(s[1]);
  auto t = __builtin_amdgcn_permlane32_swap(asu(x), asu(x), false, false);
  return asf(t[0]) + asf(t[1]);
}
__device__ __forceinline__ float rows_max(float x) {
  auto s = __builtin_amdgcn_permlane16_swap(asu(x), asu(x), false, false);
  x = fmaxf(asf(s[0]), asf(s[1]));
  auto t = __builtin_amdgcn_permlane32_swap(asu(x), asu(x), false, false);
  return fmaxf(asf(t[0]), asf(t[1]));
}
__device__ __forceinline__ float wave_sum(float x) { return rows_sum(row_sum(x)); }
__device__ __forceinline__ rsrc_t mkrsrc(const void* p, int bytes) {
  return __builtin_amdgcn_make_buffer_rsrc(const_cast<void*>(p), 0, bytes, RSRC3);
}
__device__ __forceinline__ f32x4 mfma32(u32x4 a, u32x4 b, f32x4 c) {
  return __builtin_amdgcn_mfma_f32_16x16x32_bf16(__builtin_bit_cast(s16x8, a), __builtin_bit_cast(s16x8, b), c, 0, 0, 0);
}
__device__ __forceinline__ f32x4 mfma16(unsigned a0, unsigned a1, unsigned b0, unsigned b1, f32x4 c) {
  u32x2 a = {a0, a1}, b = {b0, b1};
  return __builtin_amdgcn_mfma_f32_16x16x16bf16_1k(__builtin_bit_cast(s16x4, a), __builtin_bit_cast(s16x4, b), c, 0, 0, 0);
}
__device__ __forceinline__ void wg_barrier() {
  asm volatile("s_waitcnt lgkmcnt(0)\n\ts_barrier" ::: "memory");
}
__device__ __forceinline__ unsigned put_lo(float v, unsigned byte) { return (asu(v) & 0xffffff00u) | (byte & 0xffu); }
__device__ __forceinline__ float cut_lo(unsigned u) { return asf(u & 0xffffff00u); }
__device__ __forceinline__ unsigned now32() { return (unsigned)__builtin_amdgcn_s_memrealtime(); }
__device__ __forceinline__ void wait_until(unsigned t) {
  while ((int)(now32() - t) < 0) __builtin_amdgcn_s_sleep(1);
}
__device__ __forceinline__ void flag_add(unsigned char* lds, int f, int lane) {
  asm volatile("" ::: "memory");
  if (lane == 0) __hip_atomic_fetch_add((unsigned*)(lds + L_FLAG) + f + lane, 1u, __ATOMIC_RELAXED, __HIP_MEMORY_SCOPE_WORKGROUP);
  asm volatile("" ::: "memory");
}
__device__ __forceinline__ bool flag_set(unsigned char* lds, int f) {
  return __builtin_amdgcn_readfirstlane(__hip_atomic_load((unsigned*)(lds + L_FLAG) + f, __ATOMIC_RELAXED, __HIP_MEMORY_SCOPE_WORKGROUP)) != 0;
}
template <int SLEEP> __device__ __forceinline__ void flag_wait(unsigned char* lds, int f, unsigned n) {
#pragma unroll 1
  for (unsigned spin = 0; spin < (SPIN_CAP << 4); ++spin) {
    const unsigned v = __hip_atomic_load((unsigned*)(lds + L_FLAG) + f, __ATOMIC_RELAXED, __HIP_MEMORY_SCOPE_WORKGROUP);
    if (__builtin_amdgcn_readfirstlane(v) >= n) break;
    if (SLEEP) __builtin_amdgcn_s_sleep(SLEEP);
  }
  asm volatile("" ::: "memory");
}
__device__ __forceinline__ void ln_stats(const float (&v)[10], bool last_valid, float& mean_b, float& den_b) {
  float s = 0.f;
#pragma unroll
  for (int i = 0; i < 8; ++i) s += v[i];
  s += last_valid ? (v[8] + v[9]) : 0.f;
  const float mean = wave_sum(s) * (1.0f / DM);
  float q = 0.f;
#pragma unroll
  for (int i = 0; i < 8; ++i) { const float d = v[i] - mean; q = fmaf(d, d, q); }
  { const float d8 = v[8] - mean, d9 = v[9] - mean; q += last_valid ? fmaf(d8, d8, d9 * d9) : 0.f; }
  const float var = wave_sum(q) * (1.0f / DM);
  mean_b = rbf(mean);
  den_b = rbf(__builtin_amdgcn_sqrtf(rbf(rbf(var) + LN_EPS)));
}
__device__ __forceinline__ float ln_apply(float x, float mean_b, float den_b) {
  const float d = rbf(x - mean_b), r = __builtin_amdgcn_rcpf(den_b), q = d * r;
  return rbf(fmaf(fmaf(-q, den_b, d), r, q));
}

#define STAMP(i) do { } while (0)

__global__ void __launch_bounds__(NTHR, 3)
fused_decode_kernel(const unsigned short* __restrict__ x, const unsigned char* __restrict__ kc, const unsigned char* __restrict__ vc,
                    const unsigned short* __restrict__ wq, const unsigned short* __restrict__ wo,
                    const unsigned short* __restrict__ w1, const unsigned short* __restrict__ b1,
                    const unsigned short* __restrict__ w2, const unsigned short* __restrict__ b2,
                    unsigned short* __restrict__ out, unsigned char* ws) {
  __shared__ __attribute__((aligned(16))) unsigned char lds[L_END];
  const int lane = threadIdx.x & 63, wv = __builtin_amdgcn_readfirstlane(threadIdx.x >> 6), w = wv < 8 ? wv ^ 4 : wv;
  const unsigned tst = now32();
  long long pc0;
  asm volatile("s_getpc_b64 %0" : "=s"(pc0));
  const int id = blockIdx.x, g = id >> 3, s = id & 7, b = g >> 4, kvh = g & 15;
  STAMP(0);
  const rsrc_t rws = mkrsrc(ws, WS_END);
  const rsrc_t rx = mkrsrc(x + b * DM, DM * 2);
  const rsrc_t rk = mkrsrc(kc + (size_t)id * REGION, REGION);
  const rsrc_t rv = mkrsrc(vc + (size_t)id * REGION, REGION);

  const int qh = s >> 2, cb = (s + (kvh >> 2)) & 3;
  const int la = lane >> 4;
  const int red = kvh * 8 + s;

  u32x2 ep = {0u, 0u};
  if (w == NWAVE) {
    ep = __builtin_amdgcn_raw_buffer_load_b64(rws, WS_EPOCH + b * 256, 0, 0);
    if (lane < 8) *((unsigned*)(lds + L_FLAG) + lane) = 0u;
    wg_barrier();
  }
  if (w >= NWAVE) {
    const u32x2 xfin = __builtin_amdgcn_raw_buffer_load_b64(rx, red * 16 + (lane & 1) * 8, 0, 0);
    const unsigned b2v = __builtin_amdgcn_raw_buffer_load_b16(mkrsrc(b2, 4), 0, 0, 0);
    __builtin_amdgcn_s_setprio(3);
    const unsigned e0 = __builtin_amdgcn_readfirstlane(ep[0]);
    const unsigned tagA = ((e0 & 0x7f7f7f7fu) + 0x01010101u) ^ (e0 & 0x80808080u);
    const unsigned tagB = __builtin_amdgcn_readfirstlane(ep[1]) + 1u;
    if (lane == 0) *(unsigned*)(lds + L_TAG) = tagA;
    if (lane < 4) *(unsigned*)(lds + L_OA + 64 + lane * 4) = 0u;
    flag_wait<0>(lds, F_QP, 4);
    {
      f32x2 q2 = *(const f32x2*)(lds + L_QPART + (lane & 31) * 8);
#pragma unroll
      for (int ww = 1; ww < 8; ++ww) q2 += *(const f32x2*)(lds + L_QPART + ww * 256 + (lane & 31) * 8);
      __builtin_amdgcn_raw_buffer_store_b128(u32x4{tagA, asu(q2[0]), tagB, asu(q2[1])}, rws,
                                             lane < 32 ? WS_QX + g * 4096 + qh * 2048 + cb * 512 + lane * 16 : OOB, 0, AQS);
      STAMP(8);
    }
    __builtin_amdgcn_s_sleep(PSL);
    {
      float qa[4], qb[4];
      int of[4];
#pragma unroll
      for (int i = 0; i < 4; ++i) { qa[i] = 0.f; qb[i] = 0.f; of[i] = WS_QX + g * 4096 + i * 1024 + lane * 16; }
#pragma unroll 1
      for (unsigned spin = 0; spin < SPIN_CAP; ++spin) {
        asm volatile("" ::: "memory");
        u32x4 gr[4];
#pragma unroll
        for (int i = 0; i < 4; ++i) gr[i] = __builtin_amdgcn_raw_buffer_load_b128(rws, of[i], 0, AQL);
        bool done = true;
#pragma unroll
        for (int i = 0; i < 4; ++i) {
          const bool fresh = (gr[i][0] == tagA) & (gr[i][2] == tagB) & (of[i] != OOB);
          qa[i] = fresh ? asf(gr[i][1]) : qa[i]; qb[i] = fresh ? asf(gr[i][3]) : qb[i];
          of[i] = fresh ? OOB : of[i];
          done &= of[i] == OOB;
        }
        if (__all(done)) break;
        if (QPS) __builtin_amdgcn_s_sleep(QPS);
      }
      *(unsigned*)(lds + L_QS + lane * 4) = pack_bf16(qa[0] + qa[2], qb[0] + qb[2]);
      *(unsigned*)(lds + L_QS + 256 + lane * 4) = pack_bf16(qa[1] + qa[3], qb[1] + qb[3]);
      flag_add(lds, F_Q, lane);
    }
    f32x4 yv = {0.f, 0.f, 0.f, 0.f};
    if (red < 72) {
      flag_wait<16>(lds, F_Y, 4);
#pragma unroll
      for (int ww = 0; ww < 4; ++ww) yv += *(const f32x4*)(lds + L_YP + ww * 32 + (lane & 1) * 16);
#pragma unroll
      for (int j = 0; j < 4; ++j) yv[j] = rbf(rbf(yv[j]) + bflo(b2v));
    }
    flag_wait<16>(lds, F_GO, 1);
    flag_wait<0>(lds, F_END, S);
    STAMP(5);
    __builtin_amdgcn_s_sleep(T1D);
    {
      u32x4 ga[2] = {u32x4{0u, 0u, 0u, 0u}, u32x4{0u, 0u, 0u, 0u}};
      int of[2] = {WS_PX + id * 2048 + lane * 16, WS_PX + id * 2048 + 1024 + lane * 16};
#pragma unroll 1
      for (unsigned spin = 0; spin < SPIN_CAP; ++spin) {
        asm volatile("" ::: "memory");
        u32x4 gn[2];
#pragma unroll
        for (int i = 0; i < 2; ++i) gn[i] = __builtin_amdgcn_raw_buffer_load_b128(rws, of[i], 0, APL);
#pragma unroll
        for (int i = 0; i < 2; ++i) {
          unsigned bad = ((gn[i][0] ^ tagA) | (gn[i][2] ^ (tagA >> 8))) & 0xffu;
          bad |= dppu<XOR1>(bad); bad |= dppu<XOR2>(bad); bad |= dppu<XOR7>(bad);
          const bool fresh = (bad == 0u) & (of[i] != OOB);
          ga[i] = fresh ? gn[i] : ga[i];
          of[i] = fresh ? OOB : of[i];
        }
        if (__all((of[0] == OOB) & (of[1] == OOB))) break;
        __builtin_amdgcn_s_sleep(T1S);
      }
      float ls[2];
#pragma unroll
      for (int i = 0; i < 2; ++i) {
        const unsigned h = ((ga[i][1] & 0xffu) | ((ga[i][3] & 0xffu) << 8)) << (16 * (lane & 1));
        ls[i] = asf(h | dppu<XOR1>(h));
      }
      float M = fmaxf(ls[0], ls[1]);
      M = rows_max(fmaxf(M, dppf<ROR8>(M)));
      const float w0 = __builtin_amdgcn_exp2f(ls[0] - M), w1 = __builtin_amdgcn_exp2f(ls[1] - M);
      float L = w0 + w1, v[4];
      L = rows_sum(L + dppf<ROR8>(L));
#pragma unroll
      for (int r = 0; r < 4; ++r) { v[r] = fmaf(w1, cut_lo(ga[1][r]), w0 * cut_lo(ga[0][r])); v[r] = rows_sum(v[r] + dppf<ROR8>(v[r])); }
      const float r0 = __builtin_amdgcn_rcpf(L), inv = fmaf(fmaf(-L, r0, 1.0f), r0, r0);
      if (lane < 8) *(u32x2*)(lds + L_OA + lane * 8) = u32x2{pack_bf16(v[0] * inv, v[1] * inv), pack_bf16(v[2] * inv, v[3] * inv)};
      flag_add(lds, F_O, lane);
    }
    if (red < 72) {
      __builtin_amdgcn_s_sleep(T2D);
      u32x4 ga[4];
      int of[4];
#pragma unroll
      for (int i = 0; i < 4; ++i) { ga[i] = u32x4{0u, 0u, 0u, 0u}; of[i] = WS_OX + (b * 72 + red) * 4096 + i * 1024 + lane * 16; }
#pragma unroll 1
      for (unsigned spin = 0; spin < SPIN_CAP; ++spin) {
        asm volatile("" ::: "memory");
        u32x4 gn[4];
#pragma unroll
        for (int i = 0; i < 4; ++i) gn[i] = __builtin_amdgcn_raw_buffer_load_b128(rws, of[i], 0, AOL);
        bool done = true;
#pragma unroll
        for (int i = 0; i < 4; ++i) {
          unsigned bad = (__builtin_amdgcn_perm(gn[i][1], gn[i][0], 0x0c0c0400u) | __builtin_amdgcn_perm(gn[i][3], gn[i][2], 0x04000c0cu)) ^ tagA;
          bad |= dppu<XOR1>(bad);
          const bool fresh = (bad == 0u) & (of[i] != OOB);
          ga[i] = fresh ? gn[i] : ga[i];
          of[i] = fresh ? OOB : of[i];
          done &= of[i] == OOB;
        }
        if (__all(done)) break;
        if (T2S) __builtin_amdgcn_s_sleep(T2S);
      }
      STAMP(13);
      float sv[4];
#pragma unroll
      for (int j = 0; j < 4; ++j) {
        float t = (cut_lo(ga[0][j]) + cut_lo(ga[1][j])) + (cut_lo(ga[2][j]) + cut_lo(ga[3][j]));
        t += dppf<XOR2>(t); t += dppf<ROR4>(t); t += dppf<ROR8>(t);
        sv[j] = rbf(rows_sum(t));
      }
      const unsigned o0 = pack_bf16(rbf(bflo(xfin[0]) + sv[0]) + yv[0], rbf(bfhi(xfin[0]) + sv[1]) + yv[1]);
      const unsigned o1 = pack_bf16(rbf(bflo(xfin[1]) + sv[2]) + yv[2], rbf(bfhi(xfin[1]) + sv[3]) + yv[3]);
      if (lane < 2) *(u32x2*)(out + b * DM + red * 8 + lane * 4) = u32x2{o0, o1};
      if (red == 0 && lane == 0) __builtin_amdgcn_raw_buffer_store_b64(u32x2{tagA, tagB}, rws, WS_EPOCH + b * 256, 0, AUX_SC1);
    }
  } else {
  const int voffK = ((lane & 15) >> 2) * 256 + (lane >> 4) * 64 + (lane & 3) * 16;
  const int voffV = lane * 16;
  u32x4 kb[8], vb[8];
#define ISSUE_K(so) _Pragma("unroll") for (int u = 0; u < 8; ++u) kb[u] = __builtin_amdgcn_raw_buffer_load_b128(rk, voffK + u * 1024, so, AUX_NT)
#define ISSUE_V(so) _Pragma("unroll") for (int u = 0; u < 8; ++u) vb[u] = __builtin_amdgcn_raw_buffer_load_b128(rv, voffV + u * 1024, so, AUX_NT)
  if ((w >> 2) == 1) {
    const int rg = (w - 4) * 8 + (lane >> 3), c8 = lane & 7;
    unsigned xs[5];
    u32x4 wqv[9];
#pragma unroll
    for (int i = 0; i < 5; ++i) xs[i] = __builtin_amdgcn_raw_buffer_load_b32(rx, (lane + 64 * i) * 4, 0, 0);
    __builtin_amdgcn_sched_barrier(0);
    const rsrc_t rq = mkrsrc(wq + qh * 288 * 4096 + kvh * 256 + cb * 64, (287 * 4096 + 64) * 2);
#pragma unroll
    for (int i = 0; i < 9; ++i) wqv[i] = __builtin_amdgcn_raw_buffer_load_b128(rq, (rg + 32 * i) * 8192 + c8 * 16, 0, AUX_NT);
    __builtin_amdgcn_sched_barrier(0);
    __builtin_amdgcn_s_barrier();
    __builtin_amdgcn_sched_barrier(0);
    float xv[10], mean1, den1;
#pragma unroll
    for (int i = 0; i < 5; ++i) { xv[2 * i] = bflo(xs[i]); xv[2 * i + 1] = bfhi(xs[i]); }
    ln_stats(xv, lane < 32, mean1, den1);
#pragma unroll
    for (int i = 0; i < 5; ++i)
      *(unsigned*)(lds + L_XN + (lane + 64 * i) * 4) = pack_bf16(ln_apply(xv[2 * i], mean1, den1), ln_apply(xv[2 * i + 1], mean1, den1));
    STAMP(9);
    f32x4 qa = {0.f, 0.f, 0.f, 0.f};
#pragma unroll
    for (int i = 0; i < 9; ++i) {
      const unsigned t = (unsigned)*(const unsigned short*)(lds + L_XN + (qh * 288 + rg + 32 * i) * 2) << (16 * (lane & 1));
      const int jp = c8 >> 1;
      qa = mfma32(wqv[i], u32x4{jp == 0 ? t : 0u, jp == 1 ? t : 0u, jp == 2 ? t : 0u, jp == 3 ? t : 0u}, qa);
    }
    if ((lane >> 5) == ((lane >> 3) & 1)) {
      float* qp = (float*)(lds + L_QPART) + ((w - 4) * 2 + (lane >> 5)) * 64 + ((lane >> 4) & 1) * 32 + c8;
#pragma unroll
      for (int r = 0; r < 4; ++r) qp[r * 8] = qa[r];
    }
    flag_add(lds, F_QP, lane);
    STAMP(1);
  }
  u32x4 cv = {0u, 0u, 0u, 0u};
  if (CODE_KB && w == 3)
    cv = __builtin_amdgcn_raw_buffer_load_b128(mkrsrc((const void*)(pc0 & ~255ll), CODE_KB * 1024), g * 1024 + lane * 16, 0, ASM_);
  if ((w >> 2) != 1) asm volatile("s_barrier" ::: "memory");
  asm volatile("" :: "v"(cv));
  if (w >= PFD) flag_wait<1>(lds, F_ARR, w - PFD + 1);
  if (w == 1) __builtin_amdgcn_s_sleep(PF1);
  if (PFS) __builtin_amdgcn_s_sleep(PFS);
  __builtin_amdgcn_sched_barrier(0);
  ISSUE_K(w * TILE_BYTES);
  ISSUE_V(w * TILE_BYTES);
  __builtin_amdgcn_sched_barrier(0);
  asm volatile("s_waitcnt vmcnt(" PFW ")" ::: "memory");
  flag_add(lds, F_ARR, lane);
  flag_wait<1>(lds, F_Q, 1);
  if (w < S) STAMP(2);

  const int c = lane & 3, hb = (lane >> 2) & 1, tb = (lane >> 3) & 1;
  const unsigned tagA = __builtin_amdgcn_readfirstlane(*(const unsigned*)(lds + L_TAG));
  float m = -3.0e38f, l = 0.f;
  f32x4 acc[2][2];
#pragma unroll
  for (int i = 0; i < 2; ++i)
#pragma unroll
    for (int j = 0; j < 2; ++j) acc[i][j] = f32x4{0.f, 0.f, 0.f, 0.f};
  {
    const u32x4 qf = *(const u32x4*)(lds + L_QS + (hb * 128 + la * 32 + c * 8) * 2);
    const u32x4 zero4 = {0u, 0u, 0u, 0u};
    const u32x4 q0 = tb ? zero4 : qf, q1 = tb ? qf : zero4;
    const unsigned mask0 = c == 0 ? 0x0000ffffu : c == 1 ? 0xffff0000u : 0u;
    const unsigned mask1 = c == 2 ? 0x0000ffffu : c == 3 ? 0xffff0000u : 0u;
    auto tile = [&](auto more, int so, int dly) {
      f32x4 sc[4];
#pragma unroll
      for (int i = 0; i < 4; ++i) sc[i] = mfma32(kb[i], q0, f32x4{0.f, 0.f, 0.f, 0.f});
#pragma unroll
      for (int i = 0; i < 4; ++i) sc[i] = mfma32(kb[4 + i], q1, sc[i]);
      if constexpr (more) {
        __builtin_amdgcn_sched_barrier(0);
        for (int i = 0; i < dly; ++i) __builtin_amdgcn_s_sleep(1);
        __builtin_amdgcn_sched_barrier(0);
        ISSUE_K(so);
        __builtin_amdgcn_sched_barrier(0);
      }
      float t4[4];
#pragma unroll
      for (int i = 0; i < 4; ++i) t4[i] = (dppf<QB0>(sc[i][0]) + dppf<QB1>(sc[i][1])) + (dppf<QB2>(sc[i][2]) + dppf<QB3>(sc[i][3]));
      const float raw = (c & 2) ? ((c & 1) ? t4[3] : t4[2]) : ((c & 1) ? t4[1] : t4[0]);
      const float sv = rbf(rbf(raw) * SCALE);
      if (__any(sv > m)) {
        float mx = fmaxf(sv, dppf<XOR1>(sv));
        mx = fmaxf(mx, dppf<XOR2>(mx));
        mx = fmaxf(mx, dppf<ROR8>(mx));
        mx = fmaxf(rows_max(mx), m);
        const float alpha = __builtin_amdgcn_exp2f((m - mx) * LOG2E);
        m = mx;
        l *= alpha;
#pragma unroll
        for (int i = 0; i < 2; ++i)
#pragma unroll
          for (int j = 0; j < 2; ++j) acc[i][j] *= alpha;
      }
      const float p = __builtin_amdgcn_exp2f((sv - m) * LOG2E);
      l += p;
      const unsigned pk = pack_bf16(p, p);
      unsigned pb0[4], pb1[4];
      pb0[0] = dppu<QB0>(pk) & mask0; pb1[0] = dppu<QB0>(pk) & mask1;
      pb0[1] = dppu<QB1>(pk) & mask0; pb1[1] = dppu<QB1>(pk) & mask1;
      pb0[2] = dppu<QB2>(pk) & mask0; pb1[2] = dppu<QB2>(pk) & mask1;
      pb0[3] = dppu<QB3>(pk) & mask0; pb1[3] = dppu<QB3>(pk) & mask1;
#pragma unroll
      for (int u = 0; u < 8; ++u) {
        acc[u >> 2][0] = mfma16(vb[u][0], vb[u][1], pb0[u & 3], pb1[u & 3], acc[u >> 2][0]);
        acc[u >> 2][1] = mfma16(vb[u][2], vb[u][3], pb0[u & 3], pb1[u & 3], acc[u >> 2][1]);
      }
      if constexpr (more) {
        __builtin_amdgcn_sched_barrier(0);
        for (int i = 0; i < dly; ++i) __builtin_amdgcn_s_sleep(1);
        __builtin_amdgcn_sched_barrier(0);
        ISSUE_V(so);
        __builtin_amdgcn_sched_barrier(0);
      }
    };
    if (w < S) {
      __builtin_amdgcn_s_setprio(2);
      if (SCHHOLD) flag_wait<1>(lds, F_ARR, NWAVE + w - PFD + 1);
      for (int it = 0; it < NITER; ++it) {
        const int nxt = NWAVE + w + it * S;
        const int lead = (int)(tst + REF0 + (unsigned)((NWAVE / 2 + it) * TAU16 >> 4) - now32());
        if (it == NITER - GOAT) flag_add(lds, F_GO, lane);
        if (it == NITER / 2) {
          flag_wait<1>(lds, F_HP, NH);
          f32x2 hml[NFOLD];
          f32x4 hp[NFOLD];
          float mx = m;
#pragma unroll
          for (int k = 0; k < NFOLD; ++k) {
            hml[k] = *(const f32x2*)(lds + L_ML + ((w * NFOLD + k) * 2 + hb) * 8);
            hp[k] = *(const f32x4*)(lds + L_PART + (w * NFOLD + k) * 1024 + lane * 16);
            mx = fmaxf(mx, hml[k][0]);
          }
          const float al = __builtin_amdgcn_exp2f((m - mx) * LOG2E);
          f32x4 add = {0.f, 0.f, 0.f, 0.f};
          float ladd = 0.f;
#pragma unroll
          for (int k = 0; k < NFOLD; ++k) {
            const float be = __builtin_amdgcn_exp2f((hml[k][0] - mx) * LOG2E);
            add += be * hp[k]; ladd = fmaf(be, hml[k][1], ladd);
          }
          m = mx;
          l = fmaf(l, al, ladd * (1.0f / 32));
          acc[0][0] = acc[0][0] * al + add; acc[1][1] = acc[1][1] * al + add;
          acc[0][1] *= al; acc[1][0] *= al;
        }
        tile(std::true_type{}, nxt * TILE_BYTES, min(max(DLY0 + ((lead * GAIN + 128) >> 8), DLYMIN), DLYMAX));
      }
    }
    tile(std::false_type{}, 0, 0);
  }
  if (w < S) STAMP(3); else STAMP(12);
  f32x4 res;
#pragma unroll
  for (int r = 0; r < 4; ++r) {
    const float own = tb ? acc[1][1][r] : acc[0][0][r], snd = tb ? acc[1][0][r] : acc[0][1][r];
    res[r] = own + dppf<ROR8>(snd);
  }
  l += dppf<XOR1>(l);
  l += dppf<XOR2>(l);
  l += dppf<ROR8>(l);
  l = rows_sum(l);

  if (w < S) {
    const float r0 = __builtin_amdgcn_rcpf(l), inv = fmaf(fmaf(-l, r0, 1.0f), r0, r0);
    const unsigned lse = asu(fmaf(m, LOG2E, __builtin_amdgcn_logf(l))) >> (16 * (c & 1));
    __builtin_amdgcn_raw_buffer_store_b128(
        u32x4{put_lo(res[0] * inv, tagA), put_lo(res[1] * inv, lse), put_lo(res[2] * inv, tagA >> 8), put_lo(res[3] * inv, lse >> 8)}, rws,
        WS_PX + ((g * 8 + hb * 4 + la) * 16 + 2 * s + w) * 128 + (tb * 4 + c) * 16, 0, APS);
    flag_add(lds, F_END, lane);
    STAMP(10);
  } else {
    const int hw = w - S;
    *(f32x4*)(lds + L_PART + hw * 1024 + lane * 16) = res;
    if ((lane & ~4) == 0) *(f32x2*)(lds + L_ML + (hw * 2 + hb) * 8) = f32x2{m, l};
    flag_add(lds, F_HP, lane);
    if (hw < 6) {
    const bool mlp = (w >> 2) == 1 && red < 72;
    u32x4 wt[6];
    {
      const rsrc_t ro = mkrsrc(wo + (size_t)(kvh * 256 + s * 32) * DM, 32 * DM * 2);
#pragma unroll
      for (int i = 0; i < 6; ++i) {
        const int p = lane + 64 * i, row = p / 12;
        wt[i] = __builtin_amdgcn_raw_buffer_load_b128(ro, row * (DM * 2) + hw * 192 + (p - row * 12) * 16, 0, AUX_NT);
      }
    }
    unsigned char* img = lds + L_WO + hw * 6144;
#pragma unroll
    for (int i = 0; i < 6; ++i) *(u32x4*)(img + (lane + 64 * i) * 16) = wt[i];
    asm volatile("s_waitcnt lgkmcnt(0)" ::: "memory");
    u32x4 wof[6];
    {
      typedef __attribute__((address_space(3))) s16x4* lds_s16x4;
      const int ra = (((lane & 15) >> 2) * 8 + la * 2) * 192 + (lane & 3) * 8;
#pragma unroll
      for (int t = 0; t < 6; ++t) {
        const u32x2 lo = __builtin_bit_cast(u32x2, __builtin_amdgcn_ds_read_tr16_b64_v4i16((lds_s16x4)(img + ra + t * 32)));
        const u32x2 hi = __builtin_bit_cast(u32x2, __builtin_amdgcn_ds_read_tr16_b64_v4i16((lds_s16x4)(img + ra + t * 32 + 192)));
        wof[t] = u32x4{lo[0], lo[1], hi[0], hi[1]};
      }
    }
    if (mlp) {
      const int ht = (w - 4) * 64 + lane;
      unsigned a1[9], ab[9], a2[9];
      const rsrc_t r1 = mkrsrc(w1, FF * 2), rb = mkrsrc(b1, FF * 2), r2 = mkrsrc(w2, FF * 2);
#pragma unroll
      for (int i = 0; i < 9; ++i) {
        a1[i] = __builtin_amdgcn_raw_buffer_load_b32(r1, (ht + 256 * i) * 4, 0, ASM_);
        ab[i] = __builtin_amdgcn_raw_buffer_load_b32(rb, (ht + 256 * i) * 4, 0, ASM_);
        a2[i] = __builtin_amdgcn_raw_buffer_load_b32(r2, (ht + 256 * i) * 4, 0, ASM_);
      }
      __builtin_amdgcn_sched_barrier(0);
      float xn[10];
#pragma unroll
      for (int i = 0; i < 5; ++i) { const unsigned v = *(const unsigned*)(lds + L_XN + (lane + 64 * i) * 4); xn[2 * i] = bflo(v); xn[2 * i + 1] = bfhi(v); }
      float mean2, den2;
      ln_stats(xn, lane < 32, mean2, den2);
      constexpr float GC0 = -2.0f * 0.7978845608028654f * LOG2E, GC1 = GC0 * 0.044715f;
#pragma unroll 1
      for (int j = 0; j < 8; ++j) {
        const float hv = ln_apply(bflo(*(const unsigned short*)(lds + L_XN + (red * 8 + j) * 2)), mean2, den2);
        float y = 0.f;
#pragma unroll
        for (int i = 0; i < 9; ++i) {
          const unsigned pr = pack_bf16(hv * bflo(a1[i]), hv * bfhi(a1[i]));
          const unsigned uu = pack_bf16(bflo(pr) + bflo(ab[i]), bfhi(pr) + bfhi(ab[i]));
          const float u0 = bflo(uu), u1 = bfhi(uu);
          const float e0 = __builtin_amdgcn_exp2f(u0 * fmaf(u0 * u0, GC1, GC0));
          const float e1 = __builtin_amdgcn_exp2f(u1 * fmaf(u1 * u1, GC1, GC0));
          const unsigned gg = pack_bf16(u0 * __builtin_amdgcn_rcpf(1.0f + e0), u1 * __builtin_amdgcn_rcpf(1.0f + e1));
          y = fmaf(bflo(gg), bflo(a2[i]), y);
          y = fmaf(bfhi(gg), bfhi(a2[i]), y);
        }
        y = wave_sum(y);
        if (lane == 0) *(float*)(lds + L_YP + (w - 4) * 32 + j * 4) = y;
      }
      flag_add(lds, F_Y, lane);
    }
    STAMP(4);

    {
      const int n = lane & 15;
      int oo = n < 6 ? WS_OX + (b * 72 + hw * 12 + 2 * n + (la >> 1)) * 4096 + (id & 127) * 32 + (la & 1) * 16 : OOB;
      int oa[6];
#pragma unroll
      for (int t = 0; t < 6; ++t) { oa[t] = L_OA + (n == t ? la * 16 : 64); asm volatile("" : "+v"(oa[t])); }
      asm volatile("" : "+v"(oo));
      flag_wait<16>(lds, F_GO, 1);
      const u32x4 tv = __builtin_amdgcn_raw_buffer_load_b128(
          rws, hw < 4 ? (((TOUCH & 2) && red < 72) ? WS_OX + (b * 72 + red) * 4096 + hw * 1024 + lane * 16 : OOB)
                      : ((TOUCH & 1) ? WS_PX + id * 2048 + (hw - 4) * 1024 + lane * 16 : OOB), 0, TAUX);
      __builtin_amdgcn_s_setprio(3);
      u32x4 ob[6];
#pragma unroll 1
      for (unsigned spin = 0; spin < (SPIN_CAP << 4); ++spin) {
        asm volatile("" ::: "memory");
        const unsigned f = __hip_atomic_load((unsigned*)(lds + L_FLAG) + F_O, __ATOMIC_RELAXED, __HIP_MEMORY_SCOPE_WORKGROUP);
        asm volatile("" ::: "memory");
#pragma unroll
        for (int t = 0; t < 6; ++t) ob[t] = *(const u32x4*)(lds + oa[t]);
        __builtin_amdgcn_sched_barrier(0);
        if (__builtin_amdgcn_readfirstlane(f) != 0u) break;
      }
      STAMP(6);
      f32x4 d = {0.f, 0.f, 0.f, 0.f};
#pragma unroll
      for (int t = 0; t < 6; ++t) d = mfma32(wof[t], ob[t], d);
      __builtin_amdgcn_raw_buffer_store_b128(
          u32x4{put_lo(d[0], tagA), put_lo(d[1], tagA >> 8), put_lo(d[2], tagA >> 16), put_lo(d[3], tagA >> 24)}, rws, oo, 0, AOS);
      asm volatile("" :: "v"(tv));
    }
    STAMP(11);
    }
  }
  }
  STAMP(7);
}

}

#define WSBACK 0
extern "C" size_t ws_base_off(size_t ws_size) { return WSBACK ? ((ws_size - ((size_t)WSBACK << 20)) & ~(size_t)0xfffff) : 0; }
extern "C" void kernel_launch(void* const* d_in, const int* in_sizes, int n_in, void* d_out, int out_size, void* d_ws,
                              size_t ws_size, hipStream_t stream) {
  (void)in_sizes; (void)n_in; (void)out_size;
  const size_t ws_off = ws_base_off(ws_size);
  fused_decode_kernel<<<dim3(NWG), dim3(NTHR), 0, stream>>>(
      (const unsigned short*)d_in[0], (const unsigned char*)d_in[1], (const unsigned char*)d_in[2],
      (const unsigned short*)d_in[3], (const unsigned short*)d_in[4], (const unsigned short*)d_in[6],
      (const unsigned short*)d_in[7], (const unsigned short*)d_in[8], (const unsigned short*)d_in[9],
      (unsigned short*)d_out, (unsigned char*)d_ws + ws_off);
}
```

```cpp
#include <hip/hip_runtime.h>
#include <hip/hip_bf16.h>
#include <stdint.h>
#include <type_traits>

namespace {

typedef unsigned u32x2 __attribute__((ext_vector_type(2)));
typedef unsigned u32x4 __attribute__((ext_vector_type(4)));
typedef float f32x2 __attribute__((ext_vector_type(2)));
typedef float f32x4 __attribute__((ext_vector_type(4)));
typedef short s16x4 __attribute__((ext_vector_type(4)));
typedef short s16x8 __attribute__((ext_vector_type(8)));
typedef __bf16 bf16x2 __attribute__((ext_vector_type(2)));
typedef __amdgpu_buffer_rsrc_t rsrc_t;

constexpr int DM = 576, HKV = 16, SK = 32768, DH = 128, FF = 4608;
constexpr int NSPLIT = 8, NWG = 256, NWAVE = 8, NTHR = (NWAVE + 1) * 64;
constexpr int KEYS_WG = SK / NSPLIT;
constexpr int TILE_BYTES = 32 * DH * 2;
constexpr int NT = KEYS_WG / 32;
constexpr int REGION = KEYS_WG * DH * 2;
constexpr int S = 2, NH = NWAVE - S;
constexpr int NITER = (NT - NWAVE - NH) / S;
static_assert(NWAVE + NITER * S + NH == NT, "every tile has one owner");
#define CODE_KB 15
#define PFD 3
#define PSL 6
#define QPS 2
#define T1D 8
#define T1S 1
#define T2D 20
#define T2S 1
#define DLY0 9
#define DLYMAX 12
#define GAIN 2
#define TAU16 1762
#define REF0 150
#define RSV0 80
#define RSVD 3
#define RSVS 0
#define ADAPT 1
#define CK0 4
#define CKI 16
#define CKTHR 56
#define DGAIN 4
#define CKRD 250
#define CKRT 100
#define CKBIAS 4
constexpr int AUX_NT = 2, AUX_SC1 = 16;
constexpr unsigned RSRC3 = 0x00020000u;
constexpr unsigned SPIN_CAP = 1u << 19;
constexpr int OOB = 0x7ff00000;

constexpr int WS_EPOCH = 0;
constexpr int WS_QX = 4096;
constexpr int WS_CK = WS_QX + 32 * 4096;
constexpr int WS_PX = WS_CK + 32 * 256;
constexpr int WS_OX = WS_PX + NWG * 1024;
constexpr int WS_END = WS_OX + 2 * 18 * 4096;
constexpr int WS_STAMP = 4 << 20;

constexpr int L_FLAG = 0;
constexpr int L_TAG = 48;
constexpr int L_REF = 56;
constexpr int L_CK = 64;
constexpr int L_XN = 96;
constexpr int L_QPART = L_XN + 1280;
constexpr int L_QS = L_QPART + 8 * 64 * 4;
constexpr int L_OA = L_QS + 512;
constexpr int L_YP = L_OA + 256;
constexpr int L_ML = L_YP + 4 * 8 * 4;
constexpr int L_PART = L_ML + NWAVE * 16;
constexpr int L_WO = L_PART + NWAVE * 1024;
constexpr int L_END = L_WO + 128 * 384;
constexpr int F_QP = 0, F_Q = 1, F_WP = 2, F_END = 3, F_O = 4, F_Y = 5, F_ARR = 6, F_AR2 = 7, F_CK = 8, F_LAST = 10;

constexpr float LOG2E = 1.4426950408889634f;
constexpr float SCALE = 0.08838834764831845f;
constexpr float LN_EPS = 1e-5f;

__device__ __forceinline__ float asf(unsigned u) { return __builtin_bit_cast(float, u); }
__device__ __forceinline__ unsigned asu(float f) { return __builtin_bit_cast(unsigned, f); }
__device__ __forceinline__ float bflo(unsigned u) { return asf(u << 16); }
__device__ __forceinline__ float bfhi(unsigned u) { return asf(u & 0xffff0000u); }
__device__ __forceinline__ unsigned pack_bf16(float lo, float hi) {
  f32x2 v = {lo, hi};
  return __builtin_bit_cast(unsigned, __builtin_convertvector(v, bf16x2));
}
__device__ __forceinline__ float rbf(float f) { return (float)(__bf16)f; }
template <int CTRL> __device__ __forceinline__ unsigned dppu(unsigned x) {
  return (unsigned)__builtin_amdgcn_mov_dpp((int)x, CTRL, 0xf, 0xf, true);
}
template <int CTRL> __device__ __forceinline__ float dppf(float x) { return asf(dppu<CTRL>(asu(x))); }
constexpr int XOR1 = 0xB1, XOR2 = 0x4E, XOR7 = 0x141, ROR4 = 0x124, ROR8 = 0x128;
__device__ __forceinline__ float row_sum(float x) { x += dppf<XOR1>(x); x += dppf<XOR2>(x); x += dppf<ROR4>(x); return x + dppf<ROR8>(x); }
constexpr int QB0 = 0x00, QB1 = 0x55, QB2 = 0xAA, QB3 = 0xFF;
__device__ __forceinline__ float rows_sum(float x) {
  auto s = __builtin_amdgcn_permlane16_swap(asu(x), asu(x), false, false);
  x = asf(s[0]) + asf(s[1]);
  auto t = __builtin_amdgcn_permlane32_swap(asu(x), asu(x), false, false);
  return asf(t[0]) + asf(t[1]);
}
__device__ __forceinline__ float rows_max(float x) {
  auto s = __builtin_amdgcn_permlane16_swap(asu(x), asu(x), false, false);
  x = fmaxf(asf(s[0]), asf(s[1]));
  auto t = __builtin_amdgcn_permlane32_swap(asu(x), asu(x), false, false);
  return fmaxf(asf(t[0]), asf(t[1]));
}
__device__ __forceinline__ float wave_sum(float x) { return rows_sum(row_sum(x)); }
__device__ __forceinline__ rsrc_t mkrsrc(const void* p, int bytes) {
  return __builtin_amdgcn_make_buffer_rsrc(const_cast<void*>(p), 0, bytes, RSRC3);
}
__device__ __forceinline__ f32x4 mfma32(u32x4 a, u32x4 b, f32x4 c) {
  return __builtin_amdgcn_mfma_f32_16x16x32_bf16(__builtin_bit_cast(s16x8, a), __builtin_bit_cast(s16x8, b), c, 0, 0, 0);
}
__device__ __forceinline__ f32x4 mfma16(unsigned a0, unsigned a1, unsigned b0, unsigned b1, f32x4 c) {
  u32x2 a = {a0, a1}, b = {b0, b1};
  return __builtin_amdgcn_mfma_f32_16x16x16bf16_1k(__builtin_bit_cast(s16x4, a), __builtin_bit_cast(s16x4, b), c, 0, 0, 0);
}
__device__ __forceinline__ void wg_barrier() {
  asm volatile("s_waitcnt lgkmcnt(0)\n\ts_barrier" ::: "memory");
}
__device__ __forceinline__ unsigned put_lo(float v, unsigned byte) { return (asu(v) & 0xffffff00u) | (byte & 0xffu); }
__device__ __forceinline__ float cut_lo(unsigned u) { return asf(u & 0xffffff00u); }
__device__ __forceinline__ unsigned now32() { return (unsigned)__builtin_amdgcn_s_memrealtime(); }
__device__ __forceinline__ void wait_until(unsigned t) {
  while ((int)(now32() - t) < 0) __builtin_amdgcn_s_sleep(1);
}
__device__ __forceinline__ void flag_add(unsigned char* lds, int f, int lane) {
  asm volatile("" ::: "memory");
  if (lane == 0) __hip_atomic_fetch_add((unsigned*)(lds + L_FLAG) + f + lane, 1u, __ATOMIC_RELAXED, __HIP_MEMORY_SCOPE_WORKGROUP);
  asm volatile("" ::: "memory");
}
__device__ __forceinline__ void flag_put(unsigned char* lds, int f, unsigned v) {
  __hip_atomic_store((unsigned*)(lds + L_FLAG) + f, v, __ATOMIC_RELAXED, __HIP_MEMORY_SCOPE_WORKGROUP);
}
__device__ __forceinline__ unsigned flag_get(unsigned char* lds, int f) {
  return __builtin_amdgcn_readfirstlane(__hip_atomic_load((unsigned*)(lds + L_FLAG) + f, __ATOMIC_RELAXED, __HIP_MEMORY_SCOPE_WORKGROUP));
}
template <int SLEEP> __device__ __forceinline__ void flag_wait(unsigned char* lds, int f, unsigned n) {
#pragma unroll 1
  for (unsigned spin = 0; spin < (SPIN_CAP << 4); ++spin) {
    const unsigned v = __hip_atomic_load((unsigned*)(lds + L_FLAG) + f, __ATOMIC_RELAXED, __HIP_MEMORY_SCOPE_WORKGROUP);
    if (__builtin_amdgcn_readfirstlane(v) >= n) break;
    if (SLEEP) __builtin_amdgcn_s_sleep(SLEEP);
  }
  asm volatile("" ::: "memory");
}
__device__ __forceinline__ void ln_stats(const float (&v)[10], bool last_valid, float& mean_b, float& den_b) {
  float s = 0.f;
#pragma unroll
  for (int i = 0; i < 8; ++i) s += v[i];
  s += last_valid ? (v[8] + v[9]) : 0.f;
  const float mean = wave_sum(s) * (1.0f / DM);
  float q = 0.f;
#pragma unroll
  for (int i = 0; i < 8; ++i) { const float d = v[i] - mean; q = fmaf(d, d, q); }
  { const float d8 = v[8] - mean, d9 = v[9] - mean; q += last_valid ? fmaf(d8, d8, d9 * d9) : 0.f; }
  const float var = wave_sum(q) * (1.0f / DM);
  mean_b = rbf(mean);
  den_b = rbf(__builtin_amdgcn_sqrtf(rbf(rbf(var) + LN_EPS)));
}
__device__ __forceinline__ float ln_apply(float x, float mean_b, float den_b) {
  const float d = rbf(x - mean_b), r = __builtin_amdgcn_rcpf(den_b), q = d * r;
  return rbf(fmaf(fmaf(-q, den_b, d), r, q));
}

#define STAMP(i) do { } while (0)

__global__ void __launch_bounds__(NTHR, 3)
fused_decode_kernel(const unsigned short* __restrict__ x, const unsigned char* __restrict__ kc, const unsigned char* __restrict__ vc,
                    const unsigned short* __restrict__ wq, const unsigned short* __restrict__ wo,
                    const unsigned short* __restrict__ w1, const unsigned short* __restrict__ b1,
                    const unsigned short* __restrict__ w2, const unsigned short* __restrict__ b2,
                    unsigned short* __restrict__ out, unsigned char* ws) {
  __shared__ __attribute__((aligned(16))) unsigned char lds[L_END];
  const int lane = threadIdx.x & 63, wv = __builtin_amdgcn_readfirstlane(threadIdx.x >> 6), w = wv < 8 ? wv ^ 4 : wv;
  const unsigned tst = now32();
  long long pc0;
  asm volatile("s_getpc_b64 %0" : "=s"(pc0));
  const int id = blockIdx.x, g = id >> 3, s = id & 7, b = g >> 4, kvh = g & 15;
  STAMP(0);
  const rsrc_t rws = mkrsrc(ws, WS_END);
  const rsrc_t rx = mkrsrc(x + b * DM, DM * 2);
  const rsrc_t rk = mkrsrc(kc + (size_t)id * REGION, REGION);
  const rsrc_t rv = mkrsrc(vc + (size_t)id * REGION, REGION);

  const int qh = s >> 2, cb = (s + (kvh >> 2)) & 3;
  const int la = lane >> 4;
  const int red = kvh * 8 + s;

  u32x2 ep = {0u, 0u};
  if (w == NWAVE) {
    ep = __builtin_amdgcn_raw_buffer_load_b64(rws, WS_EPOCH + b * 256, 0, 0);
    if (lane < 12) *((unsigned*)(lds + L_FLAG) + lane) = 0u;
    if (lane == 0) *(u32x2*)(lds + L_REF) = u32x2{tst + REF0 + (NWAVE / 2 * TAU16 >> 4), TAU16};
    wg_barrier();
  }
  if (w >= NWAVE) {
    const u32x2 xfin = __builtin_amdgcn_raw_buffer_load_b64(rx, red * 16 + (lane & 1) * 8, 0, 0);
    const unsigned b2v = __builtin_amdgcn_raw_buffer_load_b16(mkrsrc(b2, 4), 0, 0, 0);
    __builtin_amdgcn_s_setprio(3);
    const unsigned e0 = __builtin_amdgcn_readfirstlane(ep[0]);
    const unsigned tagA = ((e0 & 0x7f7f7f7fu) + 0x01010101u) ^ (e0 & 0x80808080u);
    const unsigned tagB = __builtin_amdgcn_readfirstlane(ep[1]) + 1u;
    if (lane == 0) *(unsigned*)(lds + L_TAG) = tagA;
    flag_wait<0>(lds, F_QP, 4);
    {
      f32x2 q2 = *(const f32x2*)(lds + L_QPART + (lane & 31) * 8);
#pragma unroll
      for (int ww = 1; ww < 8; ++ww) q2 += *(const f32x2*)(lds + L_QPART + ww * 256 + (lane & 31) * 8);
      __builtin_amdgcn_raw_buffer_store_b128(u32x4{tagA, asu(q2[0]), tagB, asu(q2[1])}, rws,
                                             lane < 32 ? WS_QX + g * 4096 + qh * 2048 + cb * 512 + lane * 16 : OOB, 0, AUX_SC1);
      STAMP(8);
    }
    __builtin_amdgcn_s_sleep(PSL);
    {
      float qa[4], qb[4];
      int of[4];
#pragma unroll
      for (int i = 0; i < 4; ++i) { qa[i] = 0.f; qb[i] = 0.f; of[i] = WS_QX + g * 4096 + i * 1024 + lane * 16; }
#pragma unroll 1
      for (unsigned spin = 0; spin < SPIN_CAP; ++spin) {
        asm volatile("" ::: "memory");
        u32x4 gr[4];
#pragma unroll
        for (int i = 0; i < 4; ++i) gr[i] = __builtin_amdgcn_raw_buffer_load_b128(rws, of[i], 0, AUX_SC1);
        bool done = true;
#pragma unroll
        for (int i = 0; i < 4; ++i) {
          const bool fresh = (gr[i][0] == tagA) & (gr[i][2] == tagB) & (of[i] != OOB);
          qa[i] = fresh ? asf(gr[i][1]) : qa[i]; qb[i] = fresh ? asf(gr[i][3]) : qb[i];
          of[i] = fresh ? OOB : of[i];
          done &= of[i] == OOB;
        }
        if (__all(done)) break;
        if (QPS) __builtin_amdgcn_s_sleep(QPS);
      }
      *(unsigned*)(lds + L_QS + lane * 4) = pack_bf16(qa[0] + qa[2], qb[0] + qb[2]);
      *(unsigned*)(lds + L_QS + 256 + lane * 4) = pack_bf16(qa[1] + qa[3], qb[1] + qb[3]);
      flag_add(lds, F_Q, lane);
    }
    if (ADAPT) {
      flag_wait<16>(lds, F_CK + 1, S);
      const u32x2 c0 = *(const u32x2*)(lds + L_CK), c1 = *(const u32x2*)(lds + L_CK + 16);
      const unsigned tm = c0[1] + (unsigned)((int)(c1[1] - c0[1]) >> 1), per = ((c0[1] - c0[0]) + (c1[1] - c1[0])) * 8u / CKI;
      if (__builtin_amdgcn_readfirstlane(per) > (unsigned)(TAU16 + CKTHR)) {
        const int co = WS_CK + g * 256;
        __builtin_amdgcn_raw_buffer_store_b128(u32x4{tagA, tm, tagB, per}, rws, lane == 0 ? co + s * 16 : OOB, 0, AUX_SC1);
        unsigned tr = now32() + CKRD;
#pragma unroll 1
        for (int spin = 0; spin < 4; ++spin) {
          wait_until(tr);
          asm volatile("" ::: "memory");
          const u32x4 v = __builtin_amdgcn_raw_buffer_load_b128(rws, co + (lane & 7) * 16, 0, AUX_SC1);
          const bool ok = (v[0] == tagA) & (v[2] == tagB);
          const int cnt = __builtin_popcountll(__ballot(ok)) >> 3;
          if (cnt == 8 || (spin == 3 && cnt >= 6)) {
            float d = ok ? (float)(int)(v[1] - tm) : 0.f, p = ok ? (float)v[3] : 0.f;
            d += dppf<XOR1>(d); d += dppf<XOR2>(d); d += dppf<XOR7>(d);
            p += dppf<XOR1>(p); p += dppf<XOR2>(p); p += dppf<XOR7>(p);
            const float rc = 1.0f / (float)cnt;
            const unsigned tau = min((unsigned)(p * rc) - CKBIAS, (unsigned)TAU16 + 400u);
            if (lane == 0) *(u32x2*)(lds + L_REF) = u32x2{tm + (unsigned)(int)(d * rc) - ((CK0 + CKI) * tau >> 4), tau};
            break;
          }
          tr = now32() + CKRT;
        }
      }
    }
    f32x4 yv = {0.f, 0.f, 0.f, 0.f};
    if (red < 72) {
      flag_wait<16>(lds, F_Y, 4);
#pragma unroll
      for (int ww = 0; ww < 4; ++ww) yv += *(const f32x4*)(lds + L_YP + ww * 32 + (lane & 1) * 16);
#pragma unroll
      for (int j = 0; j < 4; ++j) yv[j] = rbf(rbf(yv[j]) + bflo(b2v));
    }
    flag_wait<16>(lds, F_WP, 1);
    flag_wait<0>(lds, F_END, 1);
    STAMP(5);
    __builtin_amdgcn_s_sleep(T1D);
    {
      u32x4 ga[4];
#pragma unroll
      for (int i = 0; i < 4; ++i) ga[i] = u32x4{0u, 0u, 0u, 0u};
      int of = WS_PX + (g * 8 + (s & 4)) * 1024 + lane * 16;
#pragma unroll 1
      for (unsigned spin = 0; spin < SPIN_CAP; ++spin) {
        asm volatile("" ::: "memory");
        u32x4 gn[4];
#pragma unroll
        for (int i = 0; i < 4; ++i) gn[i] = __builtin_amdgcn_raw_buffer_load_b128(rws, of + i * 1024, 0, AUX_SC1);
        unsigned bad = 0u;
#pragma unroll
        for (int i = 0; i < 4; ++i) bad |= (gn[i][0] ^ tagA) | (gn[i][2] ^ (tagA >> 8));
        bad &= 0xffu;
        bad |= dppu<XOR1>(bad); bad |= dppu<XOR2>(bad); bad |= dppu<XOR7>(bad);
        const bool fresh = (bad == 0u) & (of != OOB);
#pragma unroll
        for (int i = 0; i < 4; ++i) ga[i] = fresh ? gn[i] : ga[i];
        of = fresh ? OOB : of;
        if (__all(of == OOB)) break;
        __builtin_amdgcn_s_sleep(T1S);
      }
      const unsigned h = ((ga[0][1] & 0xffu) | ((ga[0][3] & 0xffu) << 8)) << (16 * (lane & 1));
      const float ls = asf(h | dppu<XOR1>(h));
      const float M = rows_max(fmaxf(ls, dppf<ROR8>(ls)));
      const float w0 = __builtin_amdgcn_exp2f(ls - M);
      const float L = rows_sum(w0 + dppf<ROR8>(w0));
      const float r0 = __builtin_amdgcn_rcpf(L), inv = fmaf(fmaf(-L, r0, 1.0f), r0, r0);
      float v[4];
#pragma unroll
      for (int r = 0; r < 4; ++r) {
        const auto p0 = __builtin_amdgcn_permlane32_swap(asu(w0 * cut_lo(ga[0][r])), asu(w0 * cut_lo(ga[2][r])), false, false);
        const auto p1 = __builtin_amdgcn_permlane32_swap(asu(w0 * cut_lo(ga[1][r])), asu(w0 * cut_lo(ga[3][r])), false, false);
        const auto p2 = __builtin_amdgcn_permlane16_swap(asu(asf(p0[0]) + asf(p0[1])), asu(asf(p1[0]) + asf(p1[1])), false, false);
        const float t = asf(p2[0]) + asf(p2[1]);
        v[r] = (t + dppf<ROR8>(t)) * inv;
      }
      *(u32x2*)(lds + L_OA + la * 64 + (lane & 7) * 8) = u32x2{pack_bf16(v[0], v[1]), pack_bf16(v[2], v[3])};
      flag_add(lds, F_O, lane);
    }
    if (red < 72) {
      __builtin_amdgcn_s_sleep(T2D);
      u32x4 ga = {0u, 0u, 0u, 0u};
      int of = WS_OX + (b * 18 + (red >> 2)) * 4096 + (lane >> 1) * 128 + (red & 3) * 32 + (lane & 1) * 16;
#pragma unroll 1
      for (unsigned spin = 0; spin < SPIN_CAP; ++spin) {
        asm volatile("" ::: "memory");
        const u32x4 gn = __builtin_amdgcn_raw_buffer_load_b128(rws, of, 0, AUX_SC1);
        unsigned bad = (__builtin_amdgcn_perm(gn[1], gn[0], 0x0c0c0400u) | __builtin_amdgcn_perm(gn[3], gn[2], 0x04000c0cu)) ^ tagA;
        bad |= dppu<XOR1>(bad);
        const bool fresh = (bad == 0u) & (of != OOB);
        ga = fresh ? gn : ga;
        of = fresh ? OOB : of;
        if (__all(of == OOB)) break;
        if (T2S) __builtin_amdgcn_s_sleep(T2S);
      }
      STAMP(13);
      float sv[4];
#pragma unroll
      for (int j = 0; j < 4; ++j) {
        float t = cut_lo(ga[j]);
        t += dppf<XOR2>(t); t += dppf<ROR4>(t); t += dppf<ROR8>(t);
        sv[j] = rbf(rows_sum(t));
      }
      const unsigned o0 = pack_bf16(rbf(bflo(xfin[0]) + sv[0]) + yv[0], rbf(bfhi(xfin[0]) + sv[1]) + yv[1]);
      const unsigned o1 = pack_bf16(rbf(bflo(xfin[1]) + sv[2]) + yv[2], rbf(bfhi(xfin[1]) + sv[3]) + yv[3]);
      __builtin_amdgcn_raw_buffer_store_b64(u32x2{o0, o1}, mkrsrc(out + b * DM + red * 8, 16), lane * 8, 0, 0);
      if (red == 0 && lane == 0) __builtin_amdgcn_raw_buffer_store_b64(u32x2{tagA, tagB}, rws, WS_EPOCH + b * 256, 0, AUX_SC1);
    }
  } else {
  const int voffK = ((lane & 15) >> 2) * 256 + (lane >> 4) * 64 + (lane & 3) * 16;
  const int voffV = lane * 16;
  u32x4 kb[8], vb[8];
#define ISSUE_K(so) _Pragma("unroll") for (int u = 0; u < 8; ++u) kb[u] = __builtin_amdgcn_raw_buffer_load_b128(rk, voffK + u * 1024, so, AUX_NT)
#define ISSUE_V(so) _Pragma("unroll") for (int u = 0; u < 8; ++u) vb[u] = __builtin_amdgcn_raw_buffer_load_b128(rv, voffV + u * 1024, so, AUX_NT)
  if ((w >> 2) == 1) {
    const int rg = (w - 4) * 8 + (lane >> 3), c8 = lane & 7;
    unsigned xs[5];
    u32x4 wqv[9];
#pragma unroll
    for (int i = 0; i < 5; ++i) xs[i] = __builtin_amdgcn_raw_buffer_load_b32(rx, (lane + 64 * i) * 4, 0, 0);
    __builtin_amdgcn_sched_barrier(0);
    const rsrc_t rq = mkrsrc(wq + qh * 288 * 4096 + kvh * 256 + cb * 64, (287 * 4096 + 64) * 2);
#pragma unroll
    for (int i = 0; i < 9; ++i) wqv[i] = __builtin_amdgcn_raw_buffer_load_b128(rq, (rg + 32 * i) * 8192 + c8 * 16, 0, AUX_NT);
    __builtin_amdgcn_sched_barrier(0);
    __builtin_amdgcn_s_barrier();
    __builtin_amdgcn_sched_barrier(0);
    float xv[10], mean1, den1;
#pragma unroll
    for (int i = 0; i < 5; ++i) { xv[2 * i] = bflo(xs[i]); xv[2 * i + 1] = bfhi(xs[i]); }
    ln_stats(xv, lane < 32, mean1, den1);
#pragma unroll
    for (int i = 0; i < 5; ++i)
      *(unsigned*)(lds + L_XN + (lane + 64 * i) * 4) = pack_bf16(ln_apply(xv[2 * i], mean1, den1), ln_apply(xv[2 * i + 1], mean1, den1));
    STAMP(9);
    f32x4 qa = {0.f, 0.f, 0.f, 0.f};
#pragma unroll
    for (int i = 0; i < 9; ++i) {
      const unsigned t = (unsigned)*(const unsigned short*)(lds + L_XN + (qh * 288 + rg + 32 * i) * 2) << (16 * (lane & 1));
      const int jp = c8 >> 1;
      qa = mfma32(wqv[i], u32x4{jp == 0 ? t : 0u, jp == 1 ? t : 0u, jp == 2 ? t : 0u, jp == 3 ? t : 0u}, qa);
    }
    if ((lane >> 5) == ((lane >> 3) & 1)) {
      float* qp = (float*)(lds + L_QPART) + ((w - 4) * 2 + (lane >> 5)) * 64 + ((lane >> 4) & 1) * 32 + c8;
#pragma unroll
      for (int r = 0; r < 4; ++r) qp[r * 8] = qa[r];
    }
    flag_add(lds, F_QP, lane);
    STAMP(1);
  }
  u32x4 cv = {0u, 0u, 0u, 0u};
  if (CODE_KB && w == 3)
    cv = __builtin_amdgcn_raw_buffer_load_b128(mkrsrc((const void*)(pc0 & ~255ll), CODE_KB * 1024), g * 1024 + lane * 16, 0, 0);
  if ((w >> 2) != 1) asm volatile("s_barrier" ::: "memory");
  asm volatile("" :: "v"(cv));
  if (w >= PFD) flag_wait<1>(lds, F_ARR, w - PFD + 1);
  __builtin_amdgcn_sched_barrier(0);
  STAMP(14);
  ISSUE_K(w * TILE_BYTES);
  ISSUE_V(w * TILE_BYTES);
  __builtin_amdgcn_sched_barrier(0);
  asm volatile("s_waitcnt vmcnt(0)" ::: "memory");
  flag_add(lds, F_ARR, lane);
  STAMP(15);
  flag_wait<1>(lds, F_Q, 1);
  if (w < S) STAMP(2);

  const int c = lane & 3, hb = (lane >> 2) & 1, tb = (lane >> 3) & 1;
  const unsigned tagA = __builtin_amdgcn_readfirstlane(*(const unsigned*)(lds + L_TAG));
  float m = -3.0e38f, l = 0.f;
  f32x4 acc[2][2];
#pragma unroll
  for (int i = 0; i < 2; ++i)
#pragma unroll
    for (int j = 0; j < 2; ++j) acc[i][j] = f32x4{0.f, 0.f, 0.f, 0.f};
  {
    const u32x4 qf = *(const u32x4*)(lds + L_QS + (hb * 128 + la * 32 + c * 8) * 2);
    const u32x4 zero4 = {0u, 0u, 0u, 0u};
    const u32x4 q0 = tb ? zero4 : qf, q1 = tb ? qf : zero4;
    const unsigned mask0 = c == 0 ? 0x0000ffffu : c == 1 ? 0xffff0000u : 0u;
    const unsigned mask1 = c == 2 ? 0x0000ffffu : c == 3 ? 0xffff0000u : 0u;
    auto tile = [&](auto more, int so, int dly) {
      f32x4 sc[4];
#pragma unroll
      for (int i = 0; i < 4; ++i) sc[i] = mfma32(kb[i], q0, f32x4{0.f, 0.f, 0.f, 0.f});
#pragma unroll
      for (int i = 0; i < 4; ++i) sc[i] = mfma32(kb[4 + i], q1, sc[i]);
      if constexpr (more) {
        __builtin_amdgcn_sched_barrier(0);
        for (int i = 0; i < dly; ++i) __builtin_amdgcn_s_sleep(1);
        __builtin_amdgcn_sched_barrier(0);
        ISSUE_K(so);
        __builtin_amdgcn_sched_barrier(0);
      }
      float t4[4];
#pragma unroll
      for (int i = 0; i < 4; ++i) t4[i] = (dppf<QB0>(sc[i][0]) + dppf<QB1>(sc[i][1])) + (dppf<QB2>(sc[i][2]) + dppf<QB3>(sc[i][3]));
      const float raw = (c & 2) ? ((c & 1) ? t4[3] : t4[2]) : ((c & 1) ? t4[1] : t4[0]);
      const float sv = rbf(rbf(raw) * SCALE);
      if (__any(sv > m)) {
        float mx = fmaxf(sv, dppf<XOR1>(sv));
        mx = fmaxf(mx, dppf<XOR2>(mx));
        mx = fmaxf(mx, dppf<ROR8>(mx));
        mx = fmaxf(rows_max(mx), m);
        const float alpha = __builtin_amdgcn_exp2f((m - mx) * LOG2E);
        m = mx;
        l *= alpha;
#pragma unroll
        for (int i = 0; i < 2; ++i)
#pragma unroll
          for (int j = 0; j < 2; ++j) acc[i][j] *= alpha;
      }
      const float p = __builtin_amdgcn_exp2f((sv - m) * LOG2E);
      l += p;
      const unsigned pk = pack_bf16(p, p);
      unsigned pb0[4], pb1[4];
      pb0[0] = dppu<QB0>(pk) & mask0; pb1[0] = dppu<QB0>(pk) & mask1;
      pb0[1] = dppu<QB1>(pk) & mask0; pb1[1] = dppu<QB1>(pk) & mask1;
      pb0[2] = dppu<QB2>(pk) & mask0; pb1[2] = dppu<QB2>(pk) & mask1;
      pb0[3] = dppu<QB3>(pk) & mask0; pb1[3] = dppu<QB3>(pk) & mask1;
#pragma unroll
      for (int u = 0; u < 8; ++u) {
        acc[u >> 2][0] = mfma16(vb[u][0], vb[u][1], pb0[u & 3], pb1[u & 3], acc[u >> 2][0]);
        acc[u >> 2][1] = mfma16(vb[u][2], vb[u][3], pb0[u & 3], pb1[u & 3], acc[u >> 2][1]);
      }
      if constexpr (more) {
        __builtin_amdgcn_sched_barrier(0);
        for (int i = 0; i < dly; ++i) __builtin_amdgcn_s_sleep(1);
        __builtin_amdgcn_sched_barrier(0);
        ISSUE_V(so);
        __builtin_amdgcn_sched_barrier(0);
      }
    };
    const int hw = w - S, nwk = (s & 3) == 3 ? 6 : 4;
    if (w < S) {
      __builtin_amdgcn_s_setprio(2);
      flag_wait<1>(lds, F_ARR, NWAVE + w - PFD + 1);
      for (int it = 0; it < NITER; ++it) {
        const unsigned now = now32();
        const unsigned long long rf = __hip_atomic_load((unsigned long long*)(lds + L_REF), __ATOMIC_RELAXED, __HIP_MEMORY_SCOPE_WORKGROUP);
        const int tau = __builtin_amdgcn_readfirstlane((unsigned)(rf >> 32));
        const int lead = (int)(__builtin_amdgcn_readfirstlane((unsigned)rf) + (unsigned)(it * tau >> 4) - now);
        const int dadd = (tau - TAU16) * DGAIN >> 8;
        if (ADAPT && (it == CK0 || it == CK0 + CKI)) {
          *(unsigned*)(lds + L_CK + w * 16 + (it > CK0) * 4) = now;
          flag_add(lds, F_CK + (it > CK0), lane);
        }
        tile(std::true_type{}, (NWAVE + w + it * S) * TILE_BYTES, min(max(DLY0 + dadd + ((lead * GAIN + 128) >> 8), 0), DLYMAX + dadd));
      }
      flag_add(lds, F_LAST + w, lane);
    }
    asm volatile("" ::: "memory");
    if (w >= S) {
      tile(std::false_type{}, 0, 0);
      STAMP(12);
      {
        const rsrc_t ro = mkrsrc(wo + (size_t)(kvh * 256 + (s >> 2) * 128) * DM, 128 * DM * 2);
        u32x4 wt[8];
#pragma unroll
        for (int i = 0; i < 8; ++i) {
          const int p = hw * 64 + lane + 384 * i, row = p / 24, pc = p - row * 24;
          wt[i] = __builtin_amdgcn_raw_buffer_load_b128(ro, pc < 4 * nwk ? row * (DM * 2) + (s & 3) * 256 + pc * 16 : OOB, 0, AUX_NT);
        }
#pragma unroll
        for (int i = 0; i < 8; ++i) *(u32x4*)(lds + L_WO + (hw * 64 + lane + 384 * i) * 16) = wt[i];
      }
      if (w >= 4 && red < 72) {
        const int ht = (w - 4) * 64 + lane;
        unsigned a1[9], ab[9], a2[9];
        const rsrc_t r1 = mkrsrc(w1, FF * 2), rb = mkrsrc(b1, FF * 2), r2 = mkrsrc(w2, FF * 2);
#pragma unroll
        for (int i = 0; i < 9; ++i) {
          a1[i] = __builtin_amdgcn_raw_buffer_load_b32(r1, (ht + 256 * i) * 4, 0, 0);
          ab[i] = __builtin_amdgcn_raw_buffer_load_b32(rb, (ht + 256 * i) * 4, 0, 0);
          a2[i] = __builtin_amdgcn_raw_buffer_load_b32(r2, (ht + 256 * i) * 4, 0, 0);
        }
        __builtin_amdgcn_sched_barrier(0);
        float xn[10];
#pragma unroll
        for (int i = 0; i < 5; ++i) { const unsigned v = *(const unsigned*)(lds + L_XN + (lane + 64 * i) * 4); xn[2 * i] = bflo(v); xn[2 * i + 1] = bfhi(v); }
        float mean2, den2;
        ln_stats(xn, lane < 32, mean2, den2);
        constexpr float GC0 = -2.0f * 0.7978845608028654f * LOG2E, GC1 = GC0 * 0.044715f;
#pragma unroll 1
        for (int j = 0; j < 8; ++j) {
          const float hv = ln_apply(bflo(*(const unsigned short*)(lds + L_XN + (red * 8 + j) * 2)), mean2, den2);
          float y = 0.f;
#pragma unroll
          for (int i = 0; i < 9; ++i) {
            asm volatile("" : "+v"(a1[i]), "+v"(ab[i]), "+v"(a2[i]));
            const unsigned pr = pack_bf16(hv * bflo(a1[i]), hv * bfhi(a1[i]));
            const unsigned uu = pack_bf16(bflo(pr) + bflo(ab[i]), bfhi(pr) + bfhi(ab[i]));
            const float u0 = bflo(uu), u1 = bfhi(uu);
            const float e0 = __builtin_amdgcn_exp2f(u0 * fmaf(u0 * u0, GC1, GC0));
            const float e1 = __builtin_amdgcn_exp2f(u1 * fmaf(u1 * u1, GC1, GC0));
            const unsigned gg = pack_bf16(u0 * __builtin_amdgcn_rcpf(1.0f + e0), u1 * __builtin_amdgcn_rcpf(1.0f + e1));
            y = fmaf(bflo(gg), bflo(a2[i]), y);
            y = fmaf(bfhi(gg), bfhi(a2[i]), y);
          }
          y = wave_sum(y);
          if (lane == 0) *(float*)(lds + L_YP + (w - 4) * 32 + j * 4) = y;
        }
        flag_add(lds, F_Y, lane);
      }
      STAMP(4);
      if (hw < RSVD) {
#pragma unroll 1
        for (unsigned spin = 0; spin < (SPIN_CAP << 2); ++spin) {
          const unsigned long long rf = __hip_atomic_load((unsigned long long*)(lds + L_REF), __ATOMIC_RELAXED, __HIP_MEMORY_SCOPE_WORKGROUP);
          const unsigned due = __builtin_amdgcn_readfirstlane((unsigned)rf + ((2 * NITER + hw) * (unsigned)(rf >> 32) >> 5)) + RSV0;
          if (flag_get(lds, F_LAST + (hw & 1)) != 0u || (int)(now32() - due) >= 0) break;
          __builtin_amdgcn_s_sleep(2);
        }
      } else {
        flag_wait<2>(lds, F_AR2, hw - RSVD + 1);
      }
      __builtin_amdgcn_s_setprio(2);
      if (RSVS) __builtin_amdgcn_s_sleep(RSVS);
      __builtin_amdgcn_sched_barrier(0);
      ISSUE_K((NT - NH + hw) * TILE_BYTES);
      ISSUE_V((NT - NH + hw) * TILE_BYTES);
      __builtin_amdgcn_sched_barrier(0);
      if (hw < NH - RSVD) {
        asm volatile("s_waitcnt vmcnt(0)" ::: "memory");
        flag_add(lds, F_AR2, lane);
      }
    }
    tile(std::false_type{}, 0, 0);
    STAMP(3);
    {
      f32x4 res;
#pragma unroll
      for (int r = 0; r < 4; ++r) {
        const float own = tb ? acc[1][1][r] : acc[0][0][r], snd = tb ? acc[1][0][r] : acc[0][1][r];
        res[r] = own + dppf<ROR8>(snd);
      }
      l += dppf<XOR1>(l);
      l += dppf<XOR2>(l);
      l += dppf<ROR8>(l);
      l = rows_sum(l);
      *(f32x4*)(lds + L_PART + w * 1024 + lane * 16) = res;
      if ((lane & ~4) == 0) *(f32x2*)(lds + L_ML + (w * 2 + hb) * 8) = f32x2{m, l};
      asm volatile("" ::: "memory");
      unsigned old = 0;
      if (lane == 0) old = __hip_atomic_fetch_add((unsigned*)(lds + L_FLAG) + F_WP + lane, 1u, __ATOMIC_RELAXED, __HIP_MEMORY_SCOPE_WORKGROUP);
      asm volatile("" ::: "memory");
      f32x2 ml[NWAVE];
      f32x4 pp[NWAVE];
#pragma unroll
      for (int k = 0; k < NWAVE; ++k) {
        ml[k] = *(const f32x2*)(lds + L_ML + (k * 2 + hb) * 8);
        pp[k] = *(const f32x4*)(lds + L_PART + k * 1024 + lane * 16);
      }
      __builtin_amdgcn_sched_barrier(0);
      if (__builtin_amdgcn_readfirstlane(old) == NWAVE - 1) {
        float mx = ml[0][0];
#pragma unroll
        for (int k = 1; k < NWAVE; ++k) mx = fmaxf(mx, ml[k][0]);
        f32x4 sum = {0.f, 0.f, 0.f, 0.f};
        float L = 0.f;
#pragma unroll
        for (int k = 0; k < NWAVE; ++k) {
          const float be = __builtin_amdgcn_exp2f((ml[k][0] - mx) * LOG2E);
          sum += be * pp[k]; L = fmaf(be, ml[k][1], L);
        }
        const float r0 = __builtin_amdgcn_rcpf(L), inv = fmaf(fmaf(-L, r0, 1.0f), r0, r0);
        const unsigned lse = asu(fmaf(mx, LOG2E, __builtin_amdgcn_logf(L))) >> (16 * (c & 1));
        __builtin_amdgcn_raw_buffer_store_b128(
            u32x4{put_lo(sum[0] * inv, tagA), put_lo(sum[1] * inv, lse), put_lo(sum[2] * inv, tagA >> 8), put_lo(sum[3] * inv, lse >> 8)}, rws,
            WS_PX + ((g * 8 + hb * 4 + la) * 8 + s) * 128 + (tb * 4 + c) * 16, 0, AUX_SC1);
        flag_add(lds, F_END, lane);
        STAMP(10);
      }
    }
    if (w >= S && hw < nwk) {
      const int n = lane & 15;
      int oo = n < 2 ? WS_OX + (b * 18 + (s & 3) * 4 + hw) * 4096 + (kvh * 2 + (s >> 2)) * 128 + n * 64 + la * 16 : OOB;
      asm volatile("" : "+v"(oo));
      flag_wait<2>(lds, F_END, 1);
      u32x4 wof[2][4];
      {
        typedef __attribute__((address_space(3))) s16x4* lds_s16x4;
        unsigned char* img = lds + L_WO + ((n >> 2) * 8 + la * 2) * 384 + hw * 64 + (lane & 3) * 8;
#pragma unroll
        for (int t = 0; t < 2; ++t)
#pragma unroll
          for (int ks = 0; ks < 4; ++ks) {
            const u32x2 lo = __builtin_bit_cast(u32x2, __builtin_amdgcn_ds_read_tr16_b64_v4i16((lds_s16x4)(img + ks * 32 * 384 + t * 32)));
            const u32x2 hi = __builtin_bit_cast(u32x2, __builtin_amdgcn_ds_read_tr16_b64_v4i16((lds_s16x4)(img + ks * 32 * 384 + t * 32 + 384)));
            wof[t][ks] = u32x4{lo[0], lo[1], hi[0], hi[1]};
          }
      }
      u32x4 ob[4];
#pragma unroll 1
      for (unsigned spin = 0; spin < (SPIN_CAP << 4); ++spin) {
        asm volatile("" ::: "memory");
        const unsigned f = __hip_atomic_load((unsigned*)(lds + L_FLAG) + F_O, __ATOMIC_RELAXED, __HIP_MEMORY_SCOPE_WORKGROUP);
        asm volatile("" ::: "memory");
#pragma unroll
        for (int ks = 0; ks < 4; ++ks) ob[ks] = *(const u32x4*)(lds + L_OA + ks * 64 + la * 16);
        __builtin_amdgcn_sched_barrier(0);
        if (__builtin_amdgcn_readfirstlane(f) != 0u) break;
      }
      STAMP(6);
      f32x4 d0 = {0.f, 0.f, 0.f, 0.f}, d1 = {0.f, 0.f, 0.f, 0.f};
#pragma unroll
      for (int ks = 0; ks < 4; ++ks) { d0 = mfma32(wof[0][ks], ob[ks], d0); d1 = mfma32(wof[1][ks], ob[ks], d1); }
      const f32x4 d = n ? d1 : d0;
      __builtin_amdgcn_raw_buffer_store_b128(
          u32x4{put_lo(d[0], tagA), put_lo(d[1], tagA >> 8), put_lo(d[2], tagA >> 16), put_lo(d[3], tagA >> 24)}, rws, oo, 0, AUX_SC1);
      STAMP(11);
    }
  }
  }
  STAMP(7);
}

}

#define WSBACK 0
extern "C" size_t ws_base_off(size_t ws_size) { return WSBACK ? ((ws_size - ((size_t)WSBACK << 20)) & ~(size_t)0xfffff) : 0; }
extern "C" void kernel_launch(void* const* d_in, const int* in_sizes, int n_in, void* d_out, int out_size, void* d_ws,
                              size_t ws_size, hipStream_t stream) {
  (void)in_sizes; (void)n_in; (void)out_size;
  const size_t ws_off = ws_base_off(ws_size);
  fused_decode_kernel<<<dim3(NWG), dim3(NTHR), 0, stream>>>(
      (const unsigned short*)d_in[0], (const unsigned char*)d_in[1], (const unsigned char*)d_in[2],
      (const unsigned short*)d_in[3], (const unsigned short*)d_in[4], (const unsigned short*)d_in[6],
      (const unsigned short*)d_in[7], (const unsigned short*)d_in[8], (const unsigned short*)d_in[9],
      (unsigned short*)d_out, (unsigned char*)d_ws + ws_off);
}
```

```cpp
#include <hip/hip_runtime.h>
#include <hip/hip_bf16.h>
#include <stdint.h>
#include <type_traits>

namespace {

typedef unsigned u32x2 __attribute__((ext_vector_type(2)));
typedef unsigned u32x4 __attribute__((ext_vector_type(4)));
typedef float f32x2 __attribute__((ext_vector_type(2)));
typedef float f32x4 __attribute__((ext_vector_type(4)));
typedef short s16x4 __attribute__((ext_vector_type(4)));
typedef short s16x8 __attribute__((ext_vector_type(8)));
typedef __bf16 bf16x2 __attribute__((ext_vector_type(2)));
typedef __amdgpu_buffer_rsrc_t rsrc_t;

constexpr int DM = 576, HKV = 16, SK = 32768, DH = 128, FF = 4608;
constexpr int NSPLIT = 8, NWG = 256, NWAVE = 8, NTHR = (NWAVE + 1) * 64;
constexpr int KEYS_WG = SK / NSPLIT;
constexpr int TILE_BYTES = 32 * DH * 2;
constexpr int NT = KEYS_WG / 32;
constexpr int REGION = KEYS_WG * DH * 2;
constexpr int S = 2, NH = NWAVE - S;
constexpr int NITER = (NT - NWAVE - NH) / S;
static_assert(NWAVE + NITER * S + NH == NT, "every tile has one owner");
#define CODE_KB 15
#define PFD 3
#define MAPM 3
#define PERM 0, 1, 2, 3, 5, 4, 7, 8, 9, 10, 6, 11
#define QFLIP 0
constexpr int PM[12] = {PERM};
constexpr int uoff(int u) { return (((u & 1) << PM[0]) | (((u >> 1) & 1) << PM[1]) | (((u >> 2) & 1) << PM[2])) * 1024; }
#define DASY 2
#define PSL 6
#define QPS 2
#define T1D 8
#define T1S 1
#define T2D 20
#define T2S 1
#define DLY0 8
#define DLYMAX 12
#define GAIN 3
#define TAU16 1704
#define REF0 150
#define RSV0 80
#define RSVD 3
#define RSVS 0
#define ADAPT 1
#define CK0 4
#define CKI 16
#define CKTHR 56
#define DGAIN 4
#define CKRD 250
#define CKRT 100
#define CKBIAS 4
constexpr int AUX_NT = 2, AUX_SC1 = 16;
constexpr unsigned RSRC3 = 0x00020000u;
constexpr unsigned SPIN_CAP = 1u << 19;
constexpr int OOB = 0x7ff00000;

constexpr int WS_EPOCH = 0;
constexpr int WS_QX = 4096;
constexpr int WS_CK = WS_QX + 32 * 4096;
constexpr int WS_PX = WS_CK + 32 * 256;
constexpr int WS_OX = WS_PX + NWG * 1024;
constexpr int WS_END = WS_OX + 2 * 18 * 4096;
constexpr int WS_STAMP = 4 << 20;

constexpr int L_FLAG = 0;
constexpr int L_TAG = 48;
constexpr int L_REF = 56;
constexpr int L_CK = 64;
constexpr int L_XN = 96;
constexpr int L_QPART = L_XN + 1280;
constexpr int L_QS = L_QPART + 8 * 64 * 4;
constexpr int L_OA = L_QS + 512;
constexpr int L_YP = L_OA + 256;
constexpr int L_ML = L_YP + 4 * 8 * 4;
constexpr int L_PART = L_ML + NWAVE * 16;
constexpr int L_WO = L_PART + NWAVE * 1024;
constexpr int L_END = L_WO + 128 * 384;
constexpr int F_QP = 0, F_Q = 1, F_WP = 2, F_END = 3, F_O = 4, F_Y = 5, F_ARR = 6, F_AR2 = 7, F_CK = 8, F_LAST = 10;

constexpr float LOG2E = 1.4426950408889634f;
constexpr float SCALE = 0.08838834764831845f;
constexpr float LN_EPS = 1e-5f;

__device__ __forceinline__ float asf(unsigned u) { return __builtin_bit_cast(float, u); }
__device__ __forceinline__ unsigned asu(float f) { return __builtin_bit_cast(unsigned, f); }
__device__ __forceinline__ float bflo(unsigned u) { return asf(u << 16); }
__device__ __forceinline__ float bfhi(unsigned u) { return asf(u & 0xffff0000u); }
__device__ __forceinline__ unsigned pack_bf16(float lo, float hi) {
  f32x2 v = {lo, hi};
  return __builtin_bit_cast(unsigned, __builtin_convertvector(v, bf16x2));
}
__device__ __forceinline__ float rbf(float f) { return (float)(__bf16)f; }
template <int CTRL> __device__ __forceinline__ unsigned dppu(unsigned x) {
  return (unsigned)__builtin_amdgcn_mov_dpp((int)x, CTRL, 0xf, 0xf, true);
}
template <int CTRL> __device__ __forceinline__ float dppf(float x) { return asf(dppu<CTRL>(asu(x))); }
constexpr int XOR1 = 0xB1, XOR2 = 0x4E, XOR7 = 0x141, ROR4 = 0x124, ROR8 = 0x128;
__device__ __forceinline__ float row_sum(float x) { x += dppf<XOR1>(x); x += dppf<XOR2>(x); x += dppf<ROR4>(x); return x + dppf<ROR8>(x); }
constexpr int QB0 = 0x00, QB1 = 0x55, QB2 = 0xAA, QB3 = 0xFF;
__device__ __forceinline__ float rows_sum(float x) {
  auto s = __builtin_amdgcn_permlane16_swap(asu(x), asu(x), false, false);
  x = asf(s[0]) + asf(s[1]);
  auto t = __builtin_amdgcn_permlane32_swap(asu(x), asu(x), false, false);
  return asf(t[0]) + asf(t[1]);
}
__device__ __forceinline__ float rows_max(float x) {
  auto s = __builtin_amdgcn_permlane16_swap(asu(x), asu(x), false, false);
  x = fmaxf(asf(s[0]), asf(s[1]));
  auto t = __builtin_amdgcn_permlane32_swap(asu(x), asu(x), false, false);
  return fmaxf(asf(t[0]), asf(t[1]));
}
__device__ __forceinline__ float wave_sum(float x) { return rows_sum(row_sum(x)); }
__device__ __forceinline__ rsrc_t mkrsrc(const void* p, int bytes) {
  return __builtin_amdgcn_make_buffer_rsrc(const_cast<void*>(p), 0, bytes, RSRC3);
}
__device__ __forceinline__ f32x4 mfma32(u32x4 a, u32x4 b, f32x4 c) {
  return __builtin_amdgcn_mfma_f32_16x16x32_bf16(__builtin_bit_cast(s16x8, a), __builtin_bit_cast(s16x8, b), c, 0, 0, 0);
}
__device__ __forceinline__ f32x4 mfma16(unsigned a0, unsigned a1, unsigned b0, unsigned b1, f32x4 c) {
  u32x2 a = {a0, a1}, b = {b0, b1};
  return __builtin_amdgcn_mfma_f32_16x16x16bf16_1k(__builtin_bit_cast(s16x4, a), __builtin_bit_cast(s16x4, b), c, 0, 0, 0);
}
__device__ __forceinline__ void wg_barrier() {
  asm volatile("s_waitcnt lgkmcnt(0)\n\ts_barrier" ::: "memory");
}
__device__ __forceinline__ unsigned put_lo(float v, unsigned byte) { return (asu(v) & 0xffffff00u) | (byte & 0xffu); }
__device__ __forceinline__ float cut_lo(unsigned u) { return asf(u & 0xffffff00u); }
__device__ __forceinline__ unsigned now32() { return (unsigned)__builtin_amdgcn_s_memrealtime(); }
__device__ __forceinline__ void wait_until(unsigned t) {
  while ((int)(now32() - t) < 0) __builtin_amdgcn_s_sleep(1);
}
__device__ __forceinline__ void flag_add(unsigned char* lds, int f, int lane) {
  asm volatile("" ::: "memory");
  if (lane == 0) __hip_atomic_fetch_add((unsigned*)(lds + L_FLAG) + f + lane, 1u, __ATOMIC_RELAXED, __HIP_MEMORY_SCOPE_WORKGROUP);
  asm volatile("" ::: "memory");
}
__device__ __forceinline__ void flag_put(unsigned char* lds, int f, unsigned v) {
  __hip_atomic_store((unsigned*)(lds + L_FLAG) + f, v, __ATOMIC_RELAXED, __HIP_MEMORY_SCOPE_WORKGROUP);
}
__device__ __forceinline__ unsigned flag_get(unsigned char* lds, int f) {
  return __builtin_amdgcn_readfirstlane(__hip_atomic_load((unsigned*)(lds + L_FLAG) + f, __ATOMIC_RELAXED, __HIP_MEMORY_SCOPE_WORKGROUP));
}
template <int SLEEP> __device__ __forceinline__ void flag_wait(unsigned char* lds, int f, unsigned n) {
#pragma unroll 1
  for (unsigned spin = 0; spin < (SPIN_CAP << 4); ++spin) {
    const unsigned v = __hip_atomic_load((unsigned*)(lds + L_FLAG) + f, __ATOMIC_RELAXED, __HIP_MEMORY_SCOPE_WORKGROUP);
    if (__builtin_amdgcn_readfirstlane(v) >= n) break;
    if (SLEEP) __builtin_amdgcn_s_sleep(SLEEP);
  }
  asm volatile("" ::: "memory");
}
__device__ __forceinline__ void ln_stats(const float (&v)[10], bool last_valid, float& mean_b, float& den_b) {
  float s = 0.f;
#pragma unroll
  for (int i = 0; i < 8; ++i) s += v[i];
  s += last_valid ? (v[8] + v[9]) : 0.f;
  const float mean = wave_sum(s) * (1.0f / DM);
  float q = 0.f;
#pragma unroll
  for (int i = 0; i < 8; ++i) { const float d = v[i] - mean; q = fmaf(d, d, q); }
  { const float d8 = v[8] - mean, d9 = v[9] - mean; q += last_valid ? fmaf(d8, d8, d9 * d9) : 0.f; }
  const float var = wave_sum(q) * (1.0f / DM);
  mean_b = rbf(mean);
  den_b = rbf(__builtin_amdgcn_sqrtf(rbf(rbf(var) + LN_EPS)));
}
__device__ __forceinline__ float ln_apply(float x, float mean_b, float den_b) {
  const float d = rbf(x - mean_b), r = __builtin_amdgcn_rcpf(den_b), q = d * r;
  return rbf(fmaf(fmaf(-q, den_b, d), r, q));
}

#define STAMP(i) do { } while (0)

__global__ void __launch_bounds__(NTHR, 3)
fused_decode_kernel(const unsigned short* __restrict__ x, const unsigned char* __restrict__ kc, const unsigned char* __restrict__ vc,
                    const unsigned short* __restrict__ wq, const unsigned short* __restrict__ wo,
                    const unsigned short* __restrict__ w1, const unsigned short* __restrict__ b1,
                    const unsigned short* __restrict__ w2, const unsigned short* __restrict__ b2,
                    unsigned short* __restrict__ out, unsigned char* ws) {
  __shared__ __attribute__((aligned(16))) unsigned char lds[L_END];
  const int lane = threadIdx.x & 63, wv = __builtin_amdgcn_readfirstlane(threadIdx.x >> 6), w = wv < 8 ? wv ^ 4 : wv;
  const unsigned tst = now32();
  long long pc0;
  asm volatile("s_getpc_b64 %0" : "=s"(pc0));
  const int g = blockIdx.x >> 3, s = (blockIdx.x + MAPM * g) & 7, id = g * 8 + s, b = g >> 4, kvh = g & 15;
  STAMP(0);
  const rsrc_t rws = mkrsrc(ws, WS_END);
  const rsrc_t rx = mkrsrc(x + b * DM, DM * 2);
  const rsrc_t rk = mkrsrc(kc + (size_t)(id >> 2) * (4 * REGION), 4 * REGION);
  const rsrc_t rv = mkrsrc(vc + (size_t)(id >> 2) * (4 * REGION), 4 * REGION);
  auto toff = [&](int t) {
    int o = ((id & 1) << PM[10]) | (((id >> 1) & 1) << PM[11]);
#pragma unroll
    for (int i = 0; i < 7; ++i) o |= ((t >> i) & 1) << PM[3 + i];
    if (QFLIP) o ^= ((id >> 2) & 1) << 11;
    return o * 1024;
  };

  const int qh = s >> 2, cb = (s + (kvh >> 2)) & 3;
  const int la = lane >> 4;
  const int red = kvh * 8 + s;

  u32x2 ep = {0u, 0u};
  if (w == NWAVE) {
    ep = __builtin_amdgcn_raw_buffer_load_b64(rws, WS_EPOCH + b * 256, 0, 0);
    if (lane < 12) *((unsigned*)(lds + L_FLAG) + lane) = 0u;
    if (lane == 0) *(u32x2*)(lds + L_REF) = u32x2{tst + REF0 + (NWAVE / 2 * TAU16 >> 4), TAU16};
    wg_barrier();
  }
  if (w >= NWAVE) {
    const u32x2 xfin = __builtin_amdgcn_raw_buffer_load_b64(rx, red * 16 + (lane & 1) * 8, 0, 0);
    const unsigned b2v = __builtin_amdgcn_raw_buffer_load_b16(mkrsrc(b2, 4), 0, 0, 0);
    __builtin_amdgcn_s_setprio(3);
    const unsigned e0 = __builtin_amdgcn_readfirstlane(ep[0]);
    const unsigned tagA = ((e0 & 0x7f7f7f7fu) + 0x01010101u) ^ (e0 & 0x80808080u);
    const unsigned tagB = __builtin_amdgcn_readfirstlane(ep[1]) + 1u;
    if (lane == 0) *(unsigned*)(lds + L_TAG) = tagA;
    flag_wait<0>(lds, F_QP, 4);
    {
      f32x2 q2 = *(const f32x2*)(lds + L_QPART + (lane & 31) * 8);
#pragma unroll
      for (int ww = 1; ww < 8; ++ww) q2 += *(const f32x2*)(lds + L_QPART + ww * 256 + (lane & 31) * 8);
      __builtin_amdgcn_raw_buffer_store_b128(u32x4{tagA, asu(q2[0]), tagB, asu(q2[1])}, rws,
                                             lane < 32 ? WS_QX + g * 4096 + qh * 2048 + cb * 512 + lane * 16 : OOB, 0, AUX_SC1);
      STAMP(8);
    }
    __builtin_amdgcn_s_sleep(PSL);
    {
      float qa[4], qb[4];
      int of[4];
#pragma unroll
      for (int i = 0; i < 4; ++i) { qa[i] = 0.f; qb[i] = 0.f; of[i] = WS_QX + g * 4096 + i * 1024 + lane * 16; }
#pragma unroll 1
      for (unsigned spin = 0; spin < SPIN_CAP; ++spin) {
        asm volatile("" ::: "memory");
        u32x4 gr[4];
#pragma unroll
        for (int i = 0; i < 4; ++i) gr[i] = __builtin_amdgcn_raw_buffer_load_b128(rws, of[i], 0, AUX_SC1);
        bool done = true;
#pragma unroll
        for (int i = 0; i < 4; ++i) {
          const bool fresh = (gr[i][0] == tagA) & (gr[i][2] == tagB) & (of[i] != OOB);
          qa[i] = fresh ? asf(gr[i][1]) : qa[i]; qb[i] = fresh ? asf(gr[i][3]) : qb[i];
          of[i] = fresh ? OOB : of[i];
          done &= of[i] == OOB;
        }
        if (__all(done)) break;
        if (QPS) __builtin_amdgcn_s_sleep(QPS);
      }
      *(unsigned*)(lds + L_QS + lane * 4) = pack_bf16(qa[0] + qa[2], qb[0] + qb[2]);
      *(unsigned*)(lds + L_QS + 256 + lane * 4) = pack_bf16(qa[1] + qa[3], qb[1] + qb[3]);
      flag_add(lds, F_Q, lane);
    }
    if (ADAPT) {
      flag_wait<16>(lds, F_CK + 1, S);
      const u32x2 c0 = *(const u32x2*)(lds + L_CK), c1 = *(const u32x2*)(lds + L_CK + 16);
      const unsigned tm = c0[1] + (unsigned)((int)(c1[1] - c0[1]) >> 1), per = ((c0[1] - c0[0]) + (c1[1] - c1[0])) * 8u / CKI;
      if (__builtin_amdgcn_readfirstlane(per) > (unsigned)(TAU16 + CKTHR)) {
        const int co = WS_CK + g * 256;
        __builtin_amdgcn_raw_buffer_store_b128(u32x4{tagA, tm, tagB, per}, rws, lane == 0 ? co + s * 16 : OOB, 0, AUX_SC1);
        unsigned tr = now32() + CKRD;
#pragma unroll 1
        for (int spin = 0; spin < 4; ++spin) {
          wait_until(tr);
          asm volatile("" ::: "memory");
          const u32x4 v = __builtin_amdgcn_raw_buffer_load_b128(rws, co + (lane & 7) * 16, 0, AUX_SC1);
          const bool ok = (v[0] == tagA) & (v[2] == tagB);
          const int cnt = __builtin_popcountll(__ballot(ok)) >> 3;
          if (cnt == 8 || (spin == 3 && cnt >= 6)) {
            float d = ok ? (float)(int)(v[1] - tm) : 0.f, p = ok ? (float)v[3] : 0.f;
            d += dppf<XOR1>(d); d += dppf<XOR2>(d); d += dppf<XOR7>(d);
            p += dppf<XOR1>(p); p += dppf<XOR2>(p); p += dppf<XOR7>(p);
            const float rc = 1.0f / (float)cnt;
            const unsigned tau = min((unsigned)(p * rc) - CKBIAS, (unsigned)TAU16 + 400u);
            if (lane == 0) *(u32x2*)(lds + L_REF) = u32x2{tm + (unsigned)(int)(d * rc) - ((CK0 + CKI) * tau >> 4), tau};
            break;
          }
          tr = now32() + CKRT;
        }
      }
    }
    f32x4 yv = {0.f, 0.f, 0.f, 0.f};
    if (red < 72) {
      flag_wait<16>(lds, F_Y, 4);
#pragma unroll
      for (int ww = 0; ww < 4; ++ww) yv += *(const f32x4*)(lds + L_YP + ww * 32 + (lane & 1) * 16);
#pragma unroll
      for (int j = 0; j < 4; ++j) yv[j] = rbf(rbf(yv[j]) + bflo(b2v));
    }
    flag_wait<16>(lds, F_WP, 1);
    flag_wait<0>(lds, F_END, 1);
    STAMP(5);
    __builtin_amdgcn_s_sleep(T1D);
    {
      u32x4 ga[4];
#pragma unroll
      for (int i = 0; i < 4; ++i) ga[i] = u32x4{0u, 0u, 0u, 0u};
      int of = WS_PX + (g * 8 + (s & 4)) * 1024 + lane * 16;
#pragma unroll 1
      for (unsigned spin = 0; spin < SPIN_CAP; ++spin) {
        asm volatile("" ::: "memory");
        u32x4 gn[4];
#pragma unroll
        for (int i = 0; i < 4; ++i) gn[i] = __builtin_amdgcn_raw_buffer_load_b128(rws, of + i * 1024, 0, AUX_SC1);
        unsigned bad = 0u;
#pragma unroll
        for (int i = 0; i < 4; ++i) bad |= (gn[i][0] ^ tagA) | (gn[i][2] ^ (tagA >> 8));
        bad &= 0xffu;
        bad |= dppu<XOR1>(bad); bad |= dppu<XOR2>(bad); bad |= dppu<XOR7>(bad);
        const bool fresh = (bad == 0u) & (of != OOB);
#pragma unroll
        for (int i = 0; i < 4; ++i) ga[i] = fresh ? gn[i] : ga[i];
        of = fresh ? OOB : of;
        if (__all(of == OOB)) break;
        __builtin_amdgcn_s_sleep(T1S);
      }
      const unsigned h = ((ga[0][1] & 0xffu) | ((ga[0][3] & 0xffu) << 8)) << (16 * (lane & 1));
      const float ls = asf(h | dppu<XOR1>(h));
      const float M = rows_max(fmaxf(ls, dppf<ROR8>(ls)));
      const float w0 = __builtin_amdgcn_exp2f(ls - M);
      const float L = rows_sum(w0 + dppf<ROR8>(w0));
      const float r0 = __builtin_amdgcn_rcpf(L), inv = fmaf(fmaf(-L, r0, 1.0f), r0, r0);
      float v[4];
#pragma unroll
      for (int r = 0; r < 4; ++r) {
        const auto p0 = __builtin_amdgcn_permlane32_swap(asu(w0 * cut_lo(ga[0][r])), asu(w0 * cut_lo(ga[2][r])), false, false);
        const auto p1 = __builtin_amdgcn_permlane32_swap(asu(w0 * cut_lo(ga[1][r])), asu(w0 * cut_lo(ga[3][r])), false, false);
        const auto p2 = __builtin_amdgcn_permlane16_swap(asu(asf(p0[0]) + asf(p0[1])), asu(asf(p1[0]) + asf(p1[1])), false, false);
        const float t = asf(p2[0]) + asf(p2[1]);
        v[r] = (t + dppf<ROR8>(t)) * inv;
      }
      *(u32x2*)(lds + L_OA + la * 64 + (lane & 7) * 8) = u32x2{pack_bf16(v[0], v[1]), pack_bf16(v[2], v[3])};
      flag_add(lds, F_O, lane);
    }
    if (red < 72) {
      __builtin_amdgcn_s_sleep(T2D);
      u32x4 ga = {0u, 0u, 0u, 0u};
      int of = WS_OX + (b * 18 + (red >> 2)) * 4096 + (lane >> 1) * 128 + (red & 3) * 32 + (lane & 1) * 16;
#pragma unroll 1
      for (unsigned spin = 0; spin < SPIN_CAP; ++spin) {
        asm volatile("" ::: "memory");
        const u32x4 gn = __builtin_amdgcn_raw_buffer_load_b128(rws, of, 0, AUX_SC1);
        unsigned bad = (__builtin_amdgcn_perm(gn[1], gn[0], 0x0c0c0400u) | __builtin_amdgcn_perm(gn[3], gn[2], 0x04000c0cu)) ^ tagA;
        bad |= dppu<XOR1>(bad);
        const bool fresh = (bad == 0u) & (of != OOB);
        ga = fresh ? gn : ga;
        of = fresh ? OOB : of;
        if (__all(of == OOB)) break;
        if (T2S) __builtin_amdgcn_s_sleep(T2S);
      }
      STAMP(13);
      float sv[4];
#pragma unroll
      for (int j = 0; j < 4; ++j) {
        float t = cut_lo(ga[j]);
        t += dppf<XOR2>(t); t += dppf<ROR4>(t); t += dppf<ROR8>(t);
        sv[j] = rbf(rows_sum(t));
      }
      const unsigned o0 = pack_bf16(rbf(bflo(xfin[0]) + sv[0]) + yv[0], rbf(bfhi(xfin[0]) + sv[1]) + yv[1]);
      const unsigned o1 = pack_bf16(rbf(bflo(xfin[1]) + sv[2]) + yv[2], rbf(bfhi(xfin[1]) + sv[3]) + yv[3]);
      __builtin_amdgcn_raw_buffer_store_b64(u32x2{o0, o1}, mkrsrc(out + b * DM + red * 8, 16), lane * 8, 0, 0);
      if (red == 0 && lane == 0) __builtin_amdgcn_raw_buffer_store_b64(u32x2{tagA, tagB}, rws, WS_EPOCH + b * 256, 0, AUX_SC1);
    }
  } else {
  const int voffK = ((lane & 15) >> 2) * 256 + (lane >> 4) * 64 + (lane & 3) * 16;
  const int voffV = lane * 16;
  u32x4 kb[8], vb[8];
#define ISSUE_K(so) _Pragma("unroll") for (int u = 0; u < 8; ++u) kb[u] = __builtin_amdgcn_raw_buffer_load_b128(rk, voffK + uoff(u), so, AUX_NT)
#define ISSUE_V(so) _Pragma("unroll") for (int u = 0; u < 8; ++u) vb[u] = __builtin_amdgcn_raw_buffer_load_b128(rv, voffV + uoff(u), so, AUX_NT)
  if ((w >> 2) == 1) {
    const int rg = (w - 4) * 8 + (lane >> 3), c8 = lane & 7;
    unsigned xs[5];
    u32x4 wqv[9];
#pragma unroll
    for (int i = 0; i < 5; ++i) xs[i] = __builtin_amdgcn_raw_buffer_load_b32(rx, (lane + 64 * i) * 4, 0, 0);
    __builtin_amdgcn_sched_barrier(0);
    const rsrc_t rq = mkrsrc(wq + qh * 288 * 4096 + kvh * 256 + cb * 64, (287 * 4096 + 64) * 2);
#pragma unroll
    for (int i = 0; i < 9; ++i) wqv[i] = __builtin_amdgcn_raw_buffer_load_b128(rq, (rg + 32 * i) * 8192 + c8 * 16, 0, AUX_NT);
    __builtin_amdgcn_sched_barrier(0);
    __builtin_amdgcn_s_barrier();
    __builtin_amdgcn_sched_barrier(0);
    float xv[10], mean1, den1;
#pragma unroll
    for (int i = 0; i < 5; ++i) { xv[2 * i] = bflo(xs[i]); xv[2 * i + 1] = bfhi(xs[i]); }
    ln_stats(xv, lane < 32, mean1, den1);
#pragma unroll
    for (int i = 0; i < 5; ++i)
      *(unsigned*)(lds + L_XN + (lane + 64 * i) * 4) = pack_bf16(ln_apply(xv[2 * i], mean1, den1), ln_apply(xv[2 * i + 1], mean1, den1));
    STAMP(9);
    f32x4 qa = {0.f, 0.f, 0.f, 0.f};
#pragma unroll
    for (int i = 0; i < 9; ++i) {
      const unsigned t = (unsigned)*(const unsigned short*)(lds + L_XN + (qh * 288 + rg + 32 * i) * 2) << (16 * (lane & 1));
      const int jp = c8 >> 1;
      qa = mfma32(wqv[i], u32x4{jp == 0 ? t : 0u, jp == 1 ? t : 0u, jp == 2 ? t : 0u, jp == 3 ? t : 0u}, qa);
    }
    if ((lane >> 5) == ((lane >> 3) & 1)) {
      float* qp = (float*)(lds + L_QPART) + ((w - 4) * 2 + (lane >> 5)) * 64 + ((lane >> 4) & 1) * 32 + c8;
#pragma unroll
      for (int r = 0; r < 4; ++r) qp[r * 8] = qa[r];
    }
    flag_add(lds, F_QP, lane);
    STAMP(1);
  }
  u32x4 cv = {0u, 0u, 0u, 0u};
  if (CODE_KB && w == 3)
    cv = __builtin_amdgcn_raw_buffer_load_b128(mkrsrc((const void*)(pc0 & ~255ll), CODE_KB * 1024), (blockIdx.x >> 3) * 1024 + lane * 16, 0, 0);
  if ((w >> 2) != 1) asm volatile("s_barrier" ::: "memory");
  asm volatile("" :: "v"(cv));
  if (w >= PFD) flag_wait<1>(lds, F_ARR, w - PFD + 1);
  __builtin_amdgcn_sched_barrier(0);
  STAMP(14);
  ISSUE_K(toff(w));
  ISSUE_V(toff(w));
  __builtin_amdgcn_sched_barrier(0);
  asm volatile("s_waitcnt vmcnt(0)" ::: "memory");
  flag_add(lds, F_ARR, lane);
  STAMP(15);
  flag_wait<1>(lds, F_Q, 1);
  if (w < S) STAMP(2);

  const int c = lane & 3, hb = (lane >> 2) & 1, tb = (lane >> 3) & 1;
  const unsigned tagA = __builtin_amdgcn_readfirstlane(*(const unsigned*)(lds + L_TAG));
  float m = -3.0e38f, l = 0.f;
  f32x4 acc[2][2];
#pragma unroll
  for (int i = 0; i < 2; ++i)
#pragma unroll
    for (int j = 0; j < 2; ++j) acc[i][j] = f32x4{0.f, 0.f, 0.f, 0.f};
  {
    const u32x4 qf = *(const u32x4*)(lds + L_QS + (hb * 128 + la * 32 + c * 8) * 2);
    const u32x4 zero4 = {0u, 0u, 0u, 0u};
    const u32x4 q0 = tb ? zero4 : qf, q1 = tb ? qf : zero4;
    const unsigned mask0 = c == 0 ? 0x0000ffffu : c == 1 ? 0xffff0000u : 0u;
    const unsigned mask1 = c == 2 ? 0x0000ffffu : c == 3 ? 0xffff0000u : 0u;
    auto tile = [&](auto more, int so, int dly) {
      f32x4 sc[4];
#pragma unroll
      for (int i = 0; i < 4; ++i) sc[i] = mfma32(kb[i], q0, f32x4{0.f, 0.f, 0.f, 0.f});
#pragma unroll
      for (int i = 0; i < 4; ++i) sc[i] = mfma32(kb[4 + i], q1, sc[i]);
      if constexpr (more) {
        __builtin_amdgcn_sched_barrier(0);
        for (int i = -DASY; i < dly; ++i) __builtin_amdgcn_s_sleep(1);
        __builtin_amdgcn_sched_barrier(0);
        ISSUE_K(so);
        __builtin_amdgcn_sched_barrier(0);
      }
      float t4[4];
#pragma unroll
      for (int i = 0; i < 4; ++i) t4[i] = (dppf<QB0>(sc[i][0]) + dppf<QB1>(sc[i][1])) + (dppf<QB2>(sc[i][2]) + dppf<QB3>(sc[i][3]));
      const float raw = (c & 2) ? ((c & 1) ? t4[3] : t4[2]) : ((c & 1) ? t4[1] : t4[0]);
      const float sv = rbf(rbf(raw) * SCALE);
      if (__any(sv > m)) {
        float mx = fmaxf(sv, dppf<XOR1>(sv));
        mx = fmaxf(mx, dppf<XOR2>(mx));
        mx = fmaxf(mx, dppf<ROR8>(mx));
        mx = fmaxf(rows_max(mx), m);
        const float alpha = __builtin_amdgcn_exp2f((m - mx) * LOG2E);
        m = mx;
        l *= alpha;
#pragma unroll
        for (int i = 0; i < 2; ++i)
#pragma unroll
          for (int j = 0; j < 2; ++j) acc[i][j] *= alpha;
      }
      const float p = __builtin_amdgcn_exp2f((sv - m) * LOG2E);
      l += p;
      const unsigned pk = pack_bf16(p, p);
      unsigned pb0[4], pb1[4];
      pb0[0] = dppu<QB0>(pk) & mask0; pb1[0] = dppu<QB0>(pk) & mask1;
      pb0[1] = dppu<QB1>(pk) & mask0; pb1[1] = dppu<QB1>(pk) & mask1;
      pb0[2] = dppu<QB2>(pk) & mask0; pb1[2] = dppu<QB2>(pk) & mask1;
      pb0[3] = dppu<QB3>(pk) & mask0; pb1[3] = dppu<QB3>(pk) & mask1;
#pragma unroll
      for (int u = 0; u < 8; ++u) {
        acc[u >> 2][0] = mfma16(vb[u][0], vb[u][1], pb0[u & 3], pb1[u & 3], acc[u >> 2][0]);
        acc[u >> 2][1] = mfma16(vb[u][2], vb[u][3], pb0[u & 3], pb1[u & 3], acc[u >> 2][1]);
      }
      if constexpr (more) {
        __builtin_amdgcn_sched_barrier(0);
        for (int i = DASY; i < dly; ++i) __builtin_amdgcn_s_sleep(1);
        __builtin_amdgcn_sched_barrier(0);
        ISSUE_V(so);
        __builtin_amdgcn_sched_barrier(0);
      }
    };
    const int hw = w - S, nwk = (s & 3) == 3 ? 6 : 4;
    if (w < S) {
      __builtin_amdgcn_s_setprio(2);
      flag_wait<1>(lds, F_ARR, NWAVE + w - PFD + 1);
      for (int it = 0; it < NITER; ++it) {
        const unsigned now = now32();
        const unsigned long long rf = __hip_atomic_load((unsigned long long*)(lds + L_REF), __ATOMIC_RELAXED, __HIP_MEMORY_SCOPE_WORKGROUP);
        const int tau = __builtin_amdgcn_readfirstlane((unsigned)(rf >> 32));
        const int lead = (int)(__builtin_amdgcn_readfirstlane((unsigned)rf) + (unsigned)(it * tau >> 4) - now);
        const int dadd = (tau - TAU16) * DGAIN >> 8;
        if (ADAPT && (it == CK0 || it == CK0 + CKI)) {
          *(unsigned*)(lds + L_CK + w * 16 + (it > CK0) * 4) = now;
          flag_add(lds, F_CK + (it > CK0), lane);
        }
        tile(std::true_type{}, toff(NWAVE + w + it * S), min(max(DLY0 + dadd + ((lead * GAIN + 128) >> 8), 0), DLYMAX + dadd));
      }
      flag_add(lds, F_LAST + w, lane);
    }
    asm volatile("" ::: "memory");
    if (w >= S) {
      tile(std::false_type{}, 0, 0);
      STAMP(12);
      if (PM[11] != 11 && hw == 0) {
        const unsigned t0 = __builtin_amdgcn_raw_buffer_load_b32(rk, lane == 0 ? 0 : OOB, toff(NT - 1), AUX_NT);
        const unsigned t1 = __builtin_amdgcn_raw_buffer_load_b32(rv, lane == 0 ? 0 : OOB, toff(NT - 1), AUX_NT);
        asm volatile("" :: "v"(t0), "v"(t1));
      }
      {
        const rsrc_t ro = mkrsrc(wo + (size_t)(kvh * 256 + (s >> 2) * 128) * DM, 128 * DM * 2);
        u32x4 wt[8];
#pragma unroll
        for (int i = 0; i < 8; ++i) {
          const int p = hw * 64 + lane + 384 * i, row = p / 24, pc = p - row * 24;
          wt[i] = __builtin_amdgcn_raw_buffer_load_b128(ro, pc < 4 * nwk ? row * (DM * 2) + (s & 3) * 256 + pc * 16 : OOB, 0, AUX_NT);
        }
#pragma unroll
        for (int i = 0; i < 8; ++i) *(u32x4*)(lds + L_WO + (hw * 64 + lane + 384 * i) * 16) = wt[i];
      }
      if (w >= 4 && red < 72) {
        const int ht = (w - 4) * 64 + lane;
        unsigned a1[9], ab[9], a2[9];
        const rsrc_t r1 = mkrsrc(w1, FF * 2), rb = mkrsrc(b1, FF * 2), r2 = mkrsrc(w2, FF * 2);
#pragma unroll
        for (int i = 0; i < 9; ++i) {
          a1[i] = __builtin_amdgcn_raw_buffer_load_b32(r1, (ht + 256 * i) * 4, 0, 0);
          ab[i] = __builtin_amdgcn_raw_buffer_load_b32(rb, (ht + 256 * i) * 4, 0, 0);
          a2[i] = __builtin_amdgcn_raw_buffer_load_b32(r2, (ht + 256 * i) * 4, 0, 0);
        }
        __builtin_amdgcn_sched_barrier(0);
        float xn[10];
#pragma unroll
        for (int i = 0; i < 5; ++i) { const unsigned v = *(const unsigned*)(lds + L_XN + (lane + 64 * i) * 4); xn[2 * i] = bflo(v); xn[2 * i + 1] = bfhi(v); }
        float mean2, den2;
        ln_stats(xn, lane < 32, mean2, den2);
        constexpr float GC0 = -2.0f * 0.7978845608028654f * LOG2E, GC1 = GC0 * 0.044715f;
#pragma unroll 1
        for (int j = 0; j < 8; ++j) {
          const float hv = ln_apply(bflo(*(const unsigned short*)(lds + L_XN + (red * 8 + j) * 2)), mean2, den2);
          float y = 0.f;
#pragma unroll
          for (int i = 0; i < 9; ++i) {
            asm volatile("" : "+v"(a1[i]), "+v"(ab[i]), "+v"(a2[i]));
            const unsigned pr = pack_bf16(hv * bflo(a1[i]), hv * bfhi(a1[i]));
            const unsigned uu = pack_bf16(bflo(pr) + bflo(ab[i]), bfhi(pr) + bfhi(ab[i]));
            const float u0 = bflo(uu), u1 = bfhi(uu);
            const float e0 = __builtin_amdgcn_exp2f(u0 * fmaf(u0 * u0, GC1, GC0));
            const float e1 = __builtin_amdgcn_exp2f(u1 * fmaf(u1 * u1, GC1, GC0));
            const unsigned gg = pack_bf16(u0 * __builtin_amdgcn_rcpf(1.0f + e0), u1 * __builtin_amdgcn_rcpf(1.0f + e1));
            y = fmaf(bflo(gg), bflo(a2[i]), y);
            y = fmaf(bfhi(gg), bfhi(a2[i]), y);
          }
          y = wave_sum(y);
          if (lane == 0) *(float*)(lds + L_YP + (w - 4) * 32 + j * 4) = y;
        }
        flag_add(lds, F_Y, lane);
      }
      STAMP(4);
      if (hw < RSVD) {
#pragma unroll 1
        for (unsigned spin = 0; spin < (SPIN_CAP << 2); ++spin) {
          const unsigned long long rf = __hip_atomic_load((unsigned long long*)(lds + L_REF), __ATOMIC_RELAXED, __HIP_MEMORY_SCOPE_WORKGROUP);
          const unsigned due = __builtin_amdgcn_readfirstlane((unsigned)rf + ((2 * NITER + hw) * (unsigned)(rf >> 32) >> 5)) + RSV0;
          if (flag_get(lds, F_LAST + (hw & 1)) != 0u || (int)(now32() - due) >= 0) break;
          __builtin_amdgcn_s_sleep(2);
        }
      } else {
        flag_wait<2>(lds, F_AR2, hw - RSVD + 1);
      }
      __builtin_amdgcn_s_setprio(2);
      if (RSVS) __builtin_amdgcn_s_sleep(RSVS);
      __builtin_amdgcn_sched_barrier(0);
      ISSUE_K(toff(NT - NH + hw));
      ISSUE_V(toff(NT - NH + hw));
      __builtin_amdgcn_sched_barrier(0);
      if (hw < NH - RSVD) {
        asm volatile("s_waitcnt vmcnt(0)" ::: "memory");
        flag_add(lds, F_AR2, lane);
      }
    }
    tile(std::false_type{}, 0, 0);
    STAMP(3);
    {
      f32x4 res;
#pragma unroll
      for (int r = 0; r < 4; ++r) {
        const float own = tb ? acc[1][1][r] : acc[0][0][r], snd = tb ? acc[1][0][r] : acc[0][1][r];
        res[r] = own + dppf<ROR8>(snd);
      }
      l += dppf<XOR1>(l);
      l += dppf<XOR2>(l);
      l += dppf<ROR8>(l);
      l = rows_sum(l);
      *(f32x4*)(lds + L_PART + w * 1024 + lane * 16) = res;
      if ((lane & ~4) == 0) *(f32x2*)(lds + L_ML + (w * 2 + hb) * 8) = f32x2{m, l};
      asm volatile("" ::: "memory");
      unsigned old = 0;
      if (lane == 0) old = __hip_atomic_fetch_add((unsigned*)(lds + L_FLAG) + F_WP + lane, 1u, __ATOMIC_RELAXED, __HIP_MEMORY_SCOPE_WORKGROUP);
      asm volatile("" ::: "memory");
      f32x2 ml[NWAVE];
      f32x4 pp[NWAVE];
#pragma unroll
      for (int k = 0; k < NWAVE; ++k) {
        ml[k] = *(const f32x2*)(lds + L_ML + (k * 2 + hb) * 8);
        pp[k] = *(const f32x4*)(lds + L_PART + k * 1024 + lane * 16);
      }
      __builtin_amdgcn_sched_barrier(0);
      if (__builtin_amdgcn_readfirstlane(old) == NWAVE - 1) {
        float mx = ml[0][0];
#pragma unroll
        for (int k = 1; k < NWAVE; ++k) mx = fmaxf(mx, ml[k][0]);
        f32x4 sum = {0.f, 0.f, 0.f, 0.f};
        float L = 0.f;
#pragma unroll
        for (int k = 0; k < NWAVE; ++k) {
          const float be = __builtin_amdgcn_exp2f((ml[k][0] - mx) * LOG2E);
          sum += be * pp[k]; L = fmaf(be, ml[k][1], L);
        }
        const float r0 = __builtin_amdgcn_rcpf(L), inv = fmaf(fmaf(-L, r0, 1.0f), r0, r0);
        const unsigned lse = asu(fmaf(mx, LOG2E, __builtin_amdgcn_logf(L))) >> (16 * (c & 1));
        __builtin_amdgcn_raw_buffer_store_b128(
            u32x4{put_lo(sum[0] * inv, tagA), put_lo(sum[1] * inv, lse), put_lo(sum[2] * inv, tagA >> 8), put_lo(sum[3] * inv, lse >> 8)}, rws,
            WS_PX + ((g * 8 + hb * 4 + la) * 8 + s) * 128 + (tb * 4 + c) * 16, 0, AUX_SC1);
        flag_add(lds, F_END, lane);
        STAMP(10);
      }
    }
    if (w >= S && hw < nwk) {
      const int n = lane & 15;
      int oo = n < 2 ? WS_OX + (b * 18 + (s & 3) * 4 + hw) * 4096 + (kvh * 2 + (s >> 2)) * 128 + n * 64 + la * 16 : OOB;
      asm volatile("" : "+v"(oo));
      flag_wait<2>(lds, F_END, 1);
      u32x4 wof[2][4];
      {
        typedef __attribute__((address_space(3))) s16x4* lds_s16x4;
        unsigned char* img = lds + L_WO + ((n >> 2) * 8 + la * 2) * 384 + hw * 64 + (lane & 3) * 8;
#pragma unroll
        for (int t = 0; t < 2; ++t)
#pragma unroll
          for (int ks = 0; ks < 4; ++ks) {
            const u32x2 lo = __builtin_bit_cast(u32x2, __builtin_amdgcn_ds_read_tr16_b64_v4i16((lds_s16x4)(img + ks * 32 * 384 + t * 32)));
            const u32x2 hi = __builtin_bit_cast(u32x2, __builtin_amdgcn_ds_read_tr16_b64_v4i16((lds_s16x4)(img + ks * 32 * 384 + t * 32 + 384)));
            wof[t][ks] = u32x4{lo[0], lo[1], hi[0], hi[1]};
          }
      }
      u32x4 ob[4];
#pragma unroll 1
      for (unsigned spin = 0; spin < (SPIN_CAP << 4); ++spin) {
        asm volatile("" ::: "memory");
        const unsigned f = __hip_atomic_load((unsigned*)(lds + L_FLAG) + F_O, __ATOMIC_RELAXED, __HIP_MEMORY_SCOPE_WORKGROUP);
        asm volatile("" ::: "memory");
#pragma unroll
        for (int ks = 0; ks < 4; ++ks) ob[ks] = *(const u32x4*)(lds + L_OA + ks * 64 + la * 16);
        __builtin_amdgcn_sched_barrier(0);
        if (__builtin_amdgcn_readfirstlane(f) != 0u) break;
      }
      STAMP(6);
      f32x4 d0 = {0.f, 0.f, 0.f, 0.f}, d1 = {0.f, 0.f, 0.f, 0.f};
#pragma unroll
      for (int ks = 0; ks < 4; ++ks) { d0 = mfma32(wof[0][ks], ob[ks], d0); d1 = mfma32(wof[1][ks], ob[ks], d1); }
      const f32x4 d = n ? d1 : d0;
      __builtin_amdgcn_raw_buffer_store_b128(
          u32x4{put_lo(d[0], tagA), put_lo(d[1], tagA >> 8), put_lo(d[2], tagA >> 16), put_lo(d[3], tagA >> 24)}, rws, oo, 0, AUX_SC1);
      STAMP(11);
    }
  }
  }
  STAMP(7);
}

}

#define WSBACK 0
extern "C" size_t ws_base_off(size_t ws_size) { return WSBACK ? ((ws_size - ((size_t)WSBACK << 20)) & ~(size_t)0xfffff) : 0; }
extern "C" void kernel_launch(void* const* d_in, const int* in_sizes, int n_in, void* d_out, int out_size, void* d_ws,
                              size_t ws_size, hipStream_t stream) {
  (void)in_sizes; (void)n_in; (void)out_size;
  const size_t ws_off = ws_base_off(ws_size);
  fused_decode_kernel<<<dim3(NWG), dim3(NTHR), 0, stream>>>(
      (const unsigned short*)d_in[0], (const unsigned char*)d_in[1], (const unsigned char*)d_in[2],
      (const unsigned short*)d_in[3], (const unsigned short*)d_in[4], (const unsigned short*)d_in[6],
      (const unsigned short*)d_in[7], (const unsigned short*)d_in[8], (const unsigned short*)d_in[9],
      (unsigned short*)d_out, (unsigned char*)d_ws + ws_off);
}
```

```cpp
#include <hip/hip_runtime.h>
#include <hip/hip_bf16.h>
#include <stdint.h>
#include <type_traits>

namespace {

typedef unsigned u32x2 __attribute__((ext_vector_type(2)));
typedef unsigned u32x4 __attribute__((ext_vector_type(4)));
typedef float f32x2 __attribute__((ext_vector_type(2)));
typedef float f32x4 __attribute__((ext_vector_type(4)));
typedef short s16x4 __attribute__((ext_vector_type(4)));
typedef short s16x8 __attribute__((ext_vector_type(8)));
typedef __bf16 bf16x2 __attribute__((ext_vector_type(2)));
typedef __amdgpu_buffer_rsrc_t rsrc_t;

constexpr int DM = 576, SK = 32768, DH = 128, FF = 4608;
constexpr int NSPLIT = 8, NWG = 256, NWAVE = 8, NTHR = (NWAVE + 1) * 64;
constexpr int KEYS_WG = SK / NSPLIT;
constexpr int TILE_BYTES = 32 * DH * 2;
constexpr int NT = KEYS_WG / 32;
constexpr int SPAN = 2 * KEYS_WG * DH * 2;
constexpr int S = 2, NH = NWAVE - S;
constexpr int NPAIR = (NT / 2 - NH) / S;
constexpr int NITER = 2 * NPAIR - 1;
static_assert(2 * (NPAIR * S + NH) == NT, "every tile has one owner");

#define CODE_KB 16
#define PVAR 0
#define VREV 1
#define PFD 3
#define DLY0 7
#define DASY 1
#define DLYMAX 12
#define GAIN 3
#define TAU16 1704
#define REF0 90
#define RSV0 200
#define RSVH 25
#define RSVB 100
#define RSVD 3
#define WOL 6
#define CK0 4
#define CKI 16
#define CKTHR 90
#define DGAIN 4
#define PSL 6
#define QPS 2
#define T1D 7
#define T2D 18

constexpr int AUX_NT = 2, AUX_SC1 = 16;
constexpr unsigned RSRC3 = 0x00020000u;
constexpr unsigned SPIN_CAP = 1u << 19;
constexpr int OOB = 0x7ff00000;

constexpr int WS_EPOCH = 0;
constexpr int WS_QX = 4096;
constexpr int WS_CK = WS_QX + 32 * 4096;
constexpr int WS_PX = WS_CK + 32 * 256;
constexpr int WS_OX = WS_PX + NWG * 1024;
constexpr int WS_END = WS_OX + 2 * 18 * 4096;

constexpr int L_FLAG = 0;
constexpr int L_TAG = 52;
constexpr int L_REF = 56;
constexpr int L_CK = 64;
constexpr int L_XN = 96;
constexpr int L_QPART = L_XN + 1280;
constexpr int L_QS = L_QPART + 8 * 64 * 4;
constexpr int L_OA = L_QS + 512;
constexpr int L_YP = L_OA + 256;
constexpr int L_ML = L_YP + 4 * 8 * 4;
constexpr int L_PART = L_ML + NWAVE * 16;
constexpr int L_WO = L_PART + NWAVE * 1024;
constexpr int L_END = L_WO + 128 * 384;
constexpr int F_QP = 0, F_Q = 1, F_WP = 2, F_END = 3, F_O = 4, F_Y = 5, F_ARR = 6, F_AR2 = 7, F_CK = 8, F_LAST = 10, F_WO = 12;

constexpr float LOG2E = 1.4426950408889634f;
constexpr float SCALE = 0.08838834764831845f;
constexpr float LN_EPS = 1e-5f;

__device__ __forceinline__ float asf(unsigned u) { return __builtin_bit_cast(float, u); }
__device__ __forceinline__ unsigned asu(float f) { return __builtin_bit_cast(unsigned, f); }
__device__ __forceinline__ float bflo(unsigned u) { return asf(u << 16); }
__device__ __forceinline__ float bfhi(unsigned u) { return asf(u & 0xffff0000u); }
__device__ __forceinline__ unsigned pack_bf16(float lo, float hi) {
  f32x2 v = {lo, hi};
  return __builtin_bit_cast(unsigned, __builtin_convertvector(v, bf16x2));
}
__device__ __forceinline__ float rbf(float f) { return (float)(__bf16)f; }
template <int CTRL> __device__ __forceinline__ unsigned dppu(unsigned x) {
  return (unsigned)__builtin_amdgcn_mov_dpp((int)x, CTRL, 0xf, 0xf, true);
}
template <int CTRL> __device__ __forceinline__ float dppf(float x) { return asf(dppu<CTRL>(asu(x))); }
constexpr int XOR1 = 0xB1, XOR2 = 0x4E, XOR7 = 0x141, ROR4 = 0x124, ROR8 = 0x128;
constexpr int QB0 = 0x00, QB1 = 0x55, QB2 = 0xAA, QB3 = 0xFF;
__device__ __forceinline__ float rows_sum(float x) {
  auto s = __builtin_amdgcn_permlane16_swap(asu(x), asu(x), false, false);
  x = asf(s[0]) + asf(s[1]);
  auto t = __builtin_amdgcn_permlane32_swap(asu(x), asu(x), false, false);
  return asf(t[0]) + asf(t[1]);
}
__device__ __forceinline__ float rows_max(float x) {
  auto s = __builtin_amdgcn_permlane16_swap(asu(x), asu(x), false, false);
  x = fmaxf(asf(s[0]), asf(s[1]));
  auto t = __builtin_amdgcn_permlane32_swap(asu(x), asu(x), false, false);
  return fmaxf(asf(t[0]), asf(t[1]));
}
__device__ __forceinline__ float rows_sum_t(float v0, float v1, float v2, float v3) {
  const auto p0 = __builtin_amdgcn_permlane32_swap(asu(v0), asu(v2), false, false);
  const auto p1 = __builtin_amdgcn_permlane32_swap(asu(v1), asu(v3), false, false);
  const auto p2 = __builtin_amdgcn_permlane16_swap(asu(asf(p0[0]) + asf(p0[1])), asu(asf(p1[0]) + asf(p1[1])), false, false);
  return asf(p2[0]) + asf(p2[1]);
}
__device__ __forceinline__ float wave_sum(float x) {
  x += dppf<XOR1>(x); x += dppf<XOR2>(x); x += dppf<ROR4>(x); x += dppf<ROR8>(x);
  return rows_sum(x);
}
__device__ __forceinline__ float recip(float x) {
  const float r = __builtin_amdgcn_rcpf(x);
  return fmaf(fmaf(-x, r, 1.0f), r, r);
}
__device__ __forceinline__ rsrc_t mkrsrc(const void* p, int bytes) {
  return __builtin_amdgcn_make_buffer_rsrc(const_cast<void*>(p), 0, bytes, RSRC3);
}
__device__ __forceinline__ f32x4 mfma32(u32x4 a, u32x4 b, f32x4 c) {
  return __builtin_amdgcn_mfma_f32_16x16x32_bf16(__builtin_bit_cast(s16x8, a), __builtin_bit_cast(s16x8, b), c, 0, 0, 0);
}
__device__ __forceinline__ f32x4 mfma16(unsigned a0, unsigned a1, unsigned b0, unsigned b1, f32x4 c) {
  u32x2 a = {a0, a1}, b = {b0, b1};
  return __builtin_amdgcn_mfma_f32_16x16x16bf16_1k(__builtin_bit_cast(s16x4, a), __builtin_bit_cast(s16x4, b), c, 0, 0, 0);
}
__device__ __forceinline__ unsigned put_lo(float v, unsigned byte) { return (asu(v) & 0xffffff00u) | (byte & 0xffu); }
__device__ __forceinline__ float cut_lo(unsigned u) { return asf(u & 0xffffff00u); }
__device__ __forceinline__ unsigned now32() { return (unsigned)__builtin_amdgcn_s_memrealtime(); }
__device__ __forceinline__ void wait_until(unsigned t) {
  while ((int)(now32() - t) < 0) __builtin_amdgcn_s_sleep(1);
}
__device__ __forceinline__ int vzero() { int z = 0; asm("" : "+v"(z)); return z; }
__device__ __forceinline__ void flag_add(unsigned char* lds, int f, int lane) {
  asm volatile("" ::: "memory");
  if (lane == 0) __hip_atomic_fetch_add((unsigned*)(lds + L_FLAG) + f + vzero(), 1u, __ATOMIC_RELAXED, __HIP_MEMORY_SCOPE_WORKGROUP);
  asm volatile("" ::: "memory");
}
__device__ __forceinline__ unsigned flag_get(unsigned char* lds, int f) {
  return __builtin_amdgcn_readfirstlane(__hip_atomic_load((unsigned*)(lds + L_FLAG) + f, __ATOMIC_RELAXED, __HIP_MEMORY_SCOPE_WORKGROUP));
}
template <int SLEEP> __device__ __forceinline__ void flag_wait(unsigned char* lds, int f, unsigned n) {
#pragma unroll 1
  for (unsigned spin = 0; spin < (SPIN_CAP << 4); ++spin) {
    if (flag_get(lds, f) >= n) break;
    if (SLEEP) __builtin_amdgcn_s_sleep(SLEEP);
  }
  asm volatile("" ::: "memory");
}
__device__ __forceinline__ unsigned long long sched_get(unsigned char* lds) {
  return __hip_atomic_load((unsigned long long*)(lds + L_REF), __ATOMIC_RELAXED, __HIP_MEMORY_SCOPE_WORKGROUP);
}
__device__ __forceinline__ void ln_stats(const float (&v)[10], bool last_valid, float& mean_b, float& den_b) {
  float s = 0.f;
#pragma unroll
  for (int i = 0; i < 8; ++i) s += v[i];
  s += last_valid ? (v[8] + v[9]) : 0.f;
  const float mean = wave_sum(s) * (1.0f / DM);
  float q = 0.f;
#pragma unroll
  for (int i = 0; i < 8; ++i) { const float d = v[i] - mean; q = fmaf(d, d, q); }
  { const float d8 = v[8] - mean, d9 = v[9] - mean; q += last_valid ? fmaf(d8, d8, d9 * d9) : 0.f; }
  const float var = wave_sum(q) * (1.0f / DM);
  mean_b = rbf(mean);
  den_b = rbf(__builtin_amdgcn_sqrtf(rbf(rbf(var) + LN_EPS)));
}
__device__ __forceinline__ float ln_apply(float x, float mean_b, float den_b) {
  const float d = rbf(x - mean_b), r = __builtin_amdgcn_rcpf(den_b), q = d * r;
  return rbf(fmaf(fmaf(-q, den_b, d), r, q));
}

#define STAMP(i) do { } while (0)

__global__ void __launch_bounds__(NTHR, 3)
fused_decode_kernel(const unsigned short* __restrict__ x, const unsigned char* __restrict__ kc, const unsigned char* __restrict__ vc,
                    const unsigned short* __restrict__ wq, const unsigned short* __restrict__ wo,
                    const unsigned short* __restrict__ w1, const unsigned short* __restrict__ b1,
                    const unsigned short* __restrict__ w2, const unsigned short* __restrict__ b2,
                    unsigned short* __restrict__ out, unsigned char* ws) {
  __shared__ __attribute__((aligned(16))) unsigned char lds[L_END];
  const int lane = threadIdx.x & 63, wv = __builtin_amdgcn_readfirstlane(threadIdx.x >> 6), w = wv < 8 ? wv ^ 4 : wv;
  const unsigned tst = now32();
  long long pc0;
  asm volatile("s_getpc_b64 %0" : "=s"(pc0));
  const int g = blockIdx.x >> 3, s = (blockIdx.x + 3 * g) & 7, id = g * 8 + s, b = g >> 4, kvh = g & 15;
  STAMP(0);
  const rsrc_t rws = mkrsrc(ws, WS_END);
  const rsrc_t rx = mkrsrc(x + b * DM, DM * 2);
  const int qh = s >> 2, cb = (s + (kvh >> 2)) & 3;
  const int la = lane >> 4;
  const int red = kvh * 8 + s;

  u32x2 ep = {0u, 0u};
  if (w == NWAVE) {
    ep = __builtin_amdgcn_raw_buffer_load_b64(rws, WS_EPOCH + b * 256, 0, 0);
    if (lane < 13) *((unsigned*)(lds + L_FLAG) + lane) = 0u;
    if (lane == 0) *(u32x2*)(lds + L_REF) = u32x2{tst + REF0 + (NWAVE / 2 * TAU16 >> 4), TAU16};
    asm volatile("s_waitcnt lgkmcnt(0)\n\ts_barrier" ::: "memory");
  }
  if (w >= NWAVE) {
    __builtin_amdgcn_s_setprio(3);
    const unsigned e0 = __builtin_amdgcn_readfirstlane(ep[0]);
    const unsigned tagA = ((e0 & 0x7f7f7f7fu) + 0x01010101u) ^ (e0 & 0x80808080u);
    const unsigned tagB = __builtin_amdgcn_readfirstlane(ep[1]) + 1u;
    if (lane == 0) *(unsigned*)(lds + L_TAG) = tagA;
    flag_wait<0>(lds, F_QP, 4);
    {
      f32x2 q2 = *(const f32x2*)(lds + L_QPART + (lane & 31) * 8);
#pragma unroll
      for (int ww = 1; ww < 8; ++ww) q2 += *(const f32x2*)(lds + L_QPART + ww * 256 + (lane & 31) * 8);
      __builtin_amdgcn_raw_buffer_store_b128(u32x4{tagA, asu(q2[0]), tagB, asu(q2[1])}, rws,
                                             lane < 32 ? WS_QX + g * 4096 + qh * 2048 + cb * 512 + lane * 16 : OOB, 0, AUX_SC1);
      STAMP(8);
    }
    __builtin_amdgcn_s_sleep(PSL);
    {
      float qa[4], qb[4];
      int of[4];
#pragma unroll
      for (int i = 0; i < 4; ++i) { qa[i] = 0.f; qb[i] = 0.f; of[i] = WS_QX + g * 4096 + i * 1024 + lane * 16; }
#pragma unroll 1
      for (unsigned spin = 0; spin < SPIN_CAP; ++spin) {
        asm volatile("" ::: "memory");
        u32x4 gr[4];
#pragma unroll
        for (int i = 0; i < 4; ++i) gr[i] = __builtin_amdgcn_raw_buffer_load_b128(rws, of[i], 0, AUX_SC1);
        bool done = true;
#pragma unroll
        for (int i = 0; i < 4; ++i) {
          const bool fresh = (gr[i][0] == tagA) & (gr[i][2] == tagB) & (of[i] != OOB);
          qa[i] = fresh ? asf(gr[i][1]) : qa[i]; qb[i] = fresh ? asf(gr[i][3]) : qb[i];
          of[i] = fresh ? OOB : of[i];
          done &= of[i] == OOB;
        }
        if (__all(done)) break;
        __builtin_amdgcn_s_sleep(QPS);
      }
      *(unsigned*)(lds + L_QS + lane * 4) = pack_bf16(qa[0] + qa[2], qb[0] + qb[2]);
      *(unsigned*)(lds + L_QS + 256 + lane * 4) = pack_bf16(qa[1] + qa[3], qb[1] + qb[3]);
      flag_add(lds, F_Q, lane);
    }
    const int fc = (lane & 1) * 4 + la;
    const unsigned xraw = __builtin_amdgcn_raw_buffer_load_b32(rx, red * 16 + (fc >> 1) * 4, 0, 0);
    const unsigned b2v = __builtin_amdgcn_raw_buffer_load_b16(mkrsrc(b2, 4), 0, 0, 0);
    {
      flag_wait<16>(lds, F_CK + 1, S);
      const u32x2 c0 = *(const u32x2*)(lds + L_CK), c1 = *(const u32x2*)(lds + L_CK + 16);
      const unsigned tm = c0[1] + (unsigned)((int)(c1[1] - c0[1]) >> 1), per = ((c0[1] - c0[0]) + (c1[1] - c1[0])) * 8u / CKI;
      if (__builtin_amdgcn_readfirstlane(per) > (unsigned)(TAU16 + CKTHR)) {
        const int co = WS_CK + g * 256;
        __builtin_amdgcn_raw_buffer_store_b128(u32x4{tagA, tm, tagB, per}, rws, lane == 0 ? co + s * 16 : OOB, 0, AUX_SC1);
        unsigned tr = now32() + 250u;
#pragma unroll 1
        for (int spin = 0; spin < 4; ++spin) {
          wait_until(tr);
          asm volatile("" ::: "memory");
          const u32x4 v = __builtin_amdgcn_raw_buffer_load_b128(rws, co + (lane & 7) * 16, 0, AUX_SC1);
          const bool ok = (v[0] == tagA) & (v[2] == tagB);
          const int cnt = __builtin_popcountll(__ballot(ok)) >> 3;
          if (cnt == 8 || (spin == 3 && cnt >= 6)) {
            float d = ok ? (float)(int)(v[1] - tm) : 0.f, p = ok ? (float)v[3] : 0.f;
            d += dppf<XOR1>(d); d += dppf<XOR2>(d); d += dppf<XOR7>(d);
            p += dppf<XOR1>(p); p += dppf<XOR2>(p); p += dppf<XOR7>(p);
            const float rc = __builtin_amdgcn_rcpf((float)cnt);
            const unsigned tau = min((unsigned)(p * rc) - 4u, (unsigned)TAU16 + 400u);
            if (lane == 0) *(u32x2*)(lds + L_REF) = u32x2{tm + (unsigned)(int)(d * rc) - ((CK0 + CKI) * tau >> 4), tau};
            break;
          }
          tr = now32() + 100u;
        }
      }
    }
    float yv = 0.f, xfin = (fc & 1) ? bfhi(xraw) : bflo(xraw);
    const rsrc_t rout = mkrsrc(out + b * DM, DM * 2 + 2);
    int oout = (lane & 14) == 0 ? (red * 8 + fc) * 2 : OOB;
    if (red < 72) {
      flag_wait<16>(lds, F_Y, 4);
#pragma unroll
      for (int ww = 0; ww < 4; ++ww) yv += *(const float*)(lds + L_YP + ww * 32 + fc * 4);
      yv = rbf(rbf(yv) + bflo(b2v));
    }
    asm volatile("" : "+v"(yv), "+v"(xfin), "+v"(oout));
    flag_wait<16>(lds, F_WP, 1);
    flag_wait<0>(lds, F_END, 1);
    STAMP(5);
    __builtin_amdgcn_s_sleep(T1D);
    {
      u32x4 ga[4];
#pragma unroll
      for (int i = 0; i < 4; ++i) ga[i] = u32x4{0u, 0u, 0u, 0u};
      int of = WS_PX + (g * 8 + (s & 4)) * 1024 + lane * 16;
#pragma unroll 1
      for (unsigned spin = 0; spin < SPIN_CAP; ++spin) {
        asm volatile("" ::: "memory");
        u32x4 gn[4];
#pragma unroll
        for (int i = 0; i < 4; ++i) gn[i] = __builtin_amdgcn_raw_buffer_load_b128(rws, of + i * 1024, 0, AUX_SC1);
        unsigned bad = 0u;
#pragma unroll
        for (int i = 0; i < 4; ++i) bad |= (gn[i][0] ^ tagA) | (gn[i][2] ^ (tagA >> 8));
        bad &= 0xffu;
        bad |= dppu<XOR1>(bad); bad |= dppu<XOR2>(bad); bad |= dppu<XOR7>(bad);
        const bool fresh = (bad == 0u) & (of != OOB);
#pragma unroll
        for (int i = 0; i < 4; ++i) ga[i] = fresh ? gn[i] : ga[i];
        of = fresh ? OOB : of;
        if (__all(of == OOB)) break;
        __builtin_amdgcn_s_sleep(1);
      }
      const unsigned hm = __builtin_amdgcn_perm(ga[0][3], ga[0][1], 0x0c0c0400u) << (16 * (lane & 1));
      const unsigned hl = __builtin_amdgcn_perm(ga[1][3], ga[1][1], 0x0c0c0400u) << (16 * (lane & 1));
      const float pm = asf(hm | dppu<XOR1>(hm)), pl = asf(hl | dppu<XOR1>(hl));
      const float M = rows_max(fmaxf(pm, dppf<ROR8>(pm)));
      const float w0 = __builtin_amdgcn_exp2f((pm - M) * LOG2E) * pl;
      const float inv = recip(rows_sum(w0 + dppf<ROR8>(w0)));
      float v[4];
#pragma unroll
      for (int r = 0; r < 4; ++r) {
        const float t = rows_sum_t(w0 * cut_lo(ga[0][r]), w0 * cut_lo(ga[1][r]), w0 * cut_lo(ga[2][r]), w0 * cut_lo(ga[3][r]));
        v[r] = (t + dppf<ROR8>(t)) * inv;
      }
      *(u32x2*)(lds + L_OA + la * 64 + (lane & 7) * 8) = u32x2{pack_bf16(v[0], v[1]), pack_bf16(v[2], v[3])};
      flag_add(lds, F_O, lane);
    }
    if (red < 72) {
      __builtin_amdgcn_s_sleep(T2D);
      u32x4 ga = {0u, 0u, 0u, 0u};
      int of = WS_OX + (b * 18 + (red >> 2)) * 4096 + (lane >> 1) * 128 + (red & 3) * 32 + (lane & 1) * 16;
#pragma unroll 1
      for (unsigned spin = 0; spin < SPIN_CAP; ++spin) {
        asm volatile("" ::: "memory");
        const u32x4 gn = __builtin_amdgcn_raw_buffer_load_b128(rws, of, 0, AUX_SC1);
        unsigned bad = (__builtin_amdgcn_perm(gn[1], gn[0], 0x0c0c0400u) | __builtin_amdgcn_perm(gn[3], gn[2], 0x04000c0cu)) ^ tagA;
        bad |= dppu<XOR1>(bad);
        const bool fresh = (bad == 0u) & (of != OOB);
        ga = fresh ? gn : ga;
        of = fresh ? OOB : of;
        if (__all(of == OOB)) break;
        __builtin_amdgcn_s_sleep(1);
      }
      STAMP(13);
      float t = rows_sum_t(cut_lo(ga[0]), cut_lo(ga[1]), cut_lo(ga[2]), cut_lo(ga[3]));
      t += dppf<XOR2>(t); t += dppf<ROR4>(t); t += dppf<ROR8>(t);
      __builtin_amdgcn_raw_buffer_store_b16((unsigned short)pack_bf16(rbf(xfin + rbf(t)) + yv, 0.f), rout, oout, 0, 0);
      if (red == 0 && lane == 0) __builtin_amdgcn_raw_buffer_store_b64(u32x2{tagA, tagB}, rws, WS_EPOCH + b * 256, 0, AUX_SC1);
    }
  } else {
    const rsrc_t rk = mkrsrc(kc + (size_t)(id >> 1) * SPAN, SPAN);
    const rsrc_t rv = mkrsrc(vc + (size_t)(id >> 1) * SPAN, SPAN);
    auto poff = [&](int d, int h) {
      const int t = PVAR ? 2 * d + h : 4 * (d >> 1) + 2 * h + (d & 1);
      return ((t & 7) | ((id & 1) << 3) | ((t >> 3) << 4)) * TILE_BYTES;
    };
    const int voffK = ((lane & 15) >> 2) * 256 + la * 64 + (lane & 3) * 16;
    const int voffV = lane * 16;
    u32x4 bA[8], bB[8];
    auto request = [&](u32x4 (&x)[8], rsrc_t r, int voff, int so) {
      asm volatile("" : "+v"(voff));
#pragma unroll
      for (int u = 0; u < 8; ++u) x[u] = __builtin_amdgcn_raw_buffer_load_b128(r, voff + (u & 3) * 1024, so + (u >> 2) * 4096, AUX_NT);
      __builtin_amdgcn_sched_barrier(0);
    };
    if (w >= 4) {
      const int rg = (w - 4) * 8 + (lane >> 3), c8 = lane & 7;
      unsigned xs[5];
      u32x4 wqv[9];
#pragma unroll
      for (int i = 0; i < 5; ++i) xs[i] = __builtin_amdgcn_raw_buffer_load_b32(rx, (lane + 64 * i) * 4, 0, 0);
      __builtin_amdgcn_sched_barrier(0);
      const rsrc_t rq = mkrsrc(wq + qh * 288 * 4096 + kvh * 256 + cb * 64, (287 * 4096 + 64) * 2);
#pragma unroll
      for (int i = 0; i < 9; ++i) wqv[i] = __builtin_amdgcn_raw_buffer_load_b128(rq, (rg + 32 * i) * 8192 + c8 * 16, 0, AUX_NT);
      __builtin_amdgcn_sched_barrier(0);
      __builtin_amdgcn_s_barrier();
      __builtin_amdgcn_sched_barrier(0);
      float xv[10], mean1, den1;
#pragma unroll
      for (int i = 0; i < 5; ++i) { xv[2 * i] = bflo(xs[i]); xv[2 * i + 1] = bfhi(xs[i]); }
      ln_stats(xv, lane < 32, mean1, den1);
#pragma unroll
      for (int i = 0; i < 5; ++i)
        *(unsigned*)(lds + L_XN + (lane + 64 * i) * 4) = pack_bf16(ln_apply(xv[2 * i], mean1, den1), ln_apply(xv[2 * i + 1], mean1, den1));
      STAMP(9);
      f32x4 qa = {0.f, 0.f, 0.f, 0.f};
#pragma unroll
      for (int i = 0; i < 9; ++i) {
        const unsigned t = (unsigned)*(const unsigned short*)(lds + L_XN + (qh * 288 + rg + 32 * i) * 2) << (16 * (lane & 1));
        const int jp = c8 >> 1;
        qa = mfma32(wqv[i], u32x4{jp == 0 ? t : 0u, jp == 1 ? t : 0u, jp == 2 ? t : 0u, jp == 3 ? t : 0u}, qa);
      }
      if ((lane >> 5) == ((lane >> 3) & 1)) {
        float* qp = (float*)(lds + L_QPART) + ((w - 4) * 2 + (lane >> 5)) * 64 + (la & 1) * 32 + c8;
#pragma unroll
        for (int r = 0; r < 4; ++r) qp[r * 8] = qa[r];
      }
      flag_add(lds, F_QP, lane);
      STAMP(1);
    }
    u32x4 cv = {0u, 0u, 0u, 0u};
    if (CODE_KB && w == 3)
      cv = __builtin_amdgcn_raw_buffer_load_b128(mkrsrc((const void*)(pc0 & ~255ll), CODE_KB * 1024), g * 1024 + lane * 16, 0, 0);
    if (w < 4) asm volatile("s_barrier" ::: "memory");
    asm volatile("" :: "v"(cv));
    if (w >= PFD) flag_wait<1>(lds, F_ARR, w - PFD + 1);
    __builtin_amdgcn_sched_barrier(0);
    STAMP(14);
    request(bA, rk, voffK, poff(w, 0));
    request(bB, rk, voffK, poff(w, 1));
    asm volatile("s_waitcnt vmcnt(0)" ::: "memory");
    flag_add(lds, F_ARR, lane);
    STAMP(15);
    flag_wait<1>(lds, F_Q, 1);
    if (w < S) STAMP(2);

    const int c = lane & 3, hb = (lane >> 2) & 1, tb = (lane >> 3) & 1;
    const unsigned tagA = __builtin_amdgcn_readfirstlane(*(const unsigned*)(lds + L_TAG));
    float m, l = 0.f;
    f32x4 acc[2][2];
#pragma unroll
    for (int i = 0; i < 2; ++i)
#pragma unroll
      for (int j = 0; j < 2; ++j) acc[i][j] = f32x4{0.f, 0.f, 0.f, 0.f};
    const u32x4 qf = *(const u32x4*)(lds + L_QS + (hb * 128 + la * 32 + c * 8) * 2);
    const u32x4 zero4 = {0u, 0u, 0u, 0u};
    const u32x4 q0 = tb ? zero4 : qf, q1 = tb ? qf : zero4;
    const unsigned mask0 = c == 0 ? 0x0000ffffu : c == 1 ? 0xffff0000u : 0u;
    const unsigned mask1 = c == 2 ? 0x0000ffffu : c == 3 ? 0xffff0000u : 0u;
    auto qk = [&](const u32x4 (&x)[8], f32x4 (&sc)[4]) {
#pragma unroll
      for (int i = 0; i < 4; ++i) sc[i] = mfma32(x[i], q0, f32x4{0.f, 0.f, 0.f, 0.f});
#pragma unroll
      for (int i = 0; i < 4; ++i) sc[i] = mfma32(x[4 + i], q1, sc[i]);
    };
    auto score = [&](const f32x4 (&sc)[4]) {
      float t4[4];
#pragma unroll
      for (int i = 0; i < 4; ++i) t4[i] = (dppf<QB0>(sc[i][0]) + dppf<QB1>(sc[i][1])) + (dppf<QB2>(sc[i][2]) + dppf<QB3>(sc[i][3]));
      const float raw = (c & 2) ? ((c & 1) ? t4[3] : t4[2]) : ((c & 1) ? t4[1] : t4[0]);
      float sv = rbf(rbf(raw) * SCALE);
      asm volatile("" : "+v"(sv));
      return sv;
    };
    auto head_max = [&](float v) {
      v = fmaxf(v, dppf<XOR1>(v));
      v = fmaxf(v, dppf<XOR2>(v));
      v = fmaxf(v, dppf<ROR8>(v));
      return rows_max(v);
    };
    auto pv = [&](const u32x4 (&x)[8], float sv) {
      const float p = __builtin_amdgcn_exp2f((sv - m) * LOG2E);
      l += p;
      const unsigned pk = pack_bf16(p, p);
      unsigned pb0[4], pb1[4];
      pb0[0] = dppu<QB0>(pk) & mask0; pb1[0] = dppu<QB0>(pk) & mask1;
      pb0[1] = dppu<QB1>(pk) & mask0; pb1[1] = dppu<QB1>(pk) & mask1;
      pb0[2] = dppu<QB2>(pk) & mask0; pb1[2] = dppu<QB2>(pk) & mask1;
      pb0[3] = dppu<QB3>(pk) & mask0; pb1[3] = dppu<QB3>(pk) & mask1;
#pragma unroll
      for (int u = 0; u < 8; ++u) {
        acc[u >> 2][0] = mfma16(x[u][0], x[u][1], pb0[u & 3], pb1[u & 3], acc[u >> 2][0]);
        acc[u >> 2][1] = mfma16(x[u][2], x[u][3], pb0[u & 3], pb1[u & 3], acc[u >> 2][1]);
      }
    };
    auto hold = [&](int dly) {
      __builtin_amdgcn_sched_barrier(0);
      for (int i = 0; i < dly; ++i) __builtin_amdgcn_s_sleep(1);
      __builtin_amdgcn_sched_barrier(0);
    };
    const int hw = w - S, nwk = (s & 3) == 3 ? 6 : 4;
    f32x2 sl;
    if (w < S) {
      __builtin_amdgcn_s_setprio(2);
      auto pace = [&](int it) {
        const unsigned now = now32();
        const unsigned long long rf = sched_get(lds);
        const int tau = __builtin_amdgcn_readfirstlane((unsigned)(rf >> 32));
        const int lead = (int)(__builtin_amdgcn_readfirstlane((unsigned)rf) + (unsigned)(it * tau >> 4) - now);
        const int dadd = (tau - TAU16) * DGAIN >> 8;
        if (it == CK0 || it == CK0 + CKI) {
          *(unsigned*)(lds + L_CK + w * 16 + (it > CK0) * 4) = now;
          flag_add(lds, F_CK + (it > CK0), lane);
        }
        return min(max(DLY0 + dadd + ((lead * GAIN + 128) >> 8), 0), DLYMAX + dadd);
      };
      unsigned char* sco = lds + L_WO + w * (64 * 384) + lane * 8;
      flag_wait<1>(lds, F_ARR, NWAVE + w - PFD + 1);
      float mx = -INFINITY;
      for (int j = 0; j < NPAIR; ++j) {
        const int dly = pace(j) + DASY;
        const bool k = j + 1 < NPAIR;
        const rsrc_t r = k ? rk : rv;
        const int vo = k ? voffK : voffV, d = k ? NWAVE + S * j + w : VREV ? NWAVE + S * (j - 1) + w : w;
        f32x4 sc[4];
        qk(bA, sc);
        hold(dly);
        request(bA, r, vo, poff(d, 0));
        const float s0 = score(sc);
        qk(bB, sc);
        hold(dly);
        request(bB, r, vo, poff(d, 1));
        const float s1 = score(sc);
        mx = fmaxf(mx, fmaxf(s0, s1));
        *(f32x2*)(sco + j * 512) = f32x2{s0, s1};
      }
      m = head_max(mx);
      for (int j = 0; j + 1 < NPAIR; ++j) {
        const f32x2 sv = *(const f32x2*)(sco + (VREV ? NPAIR - 1 - j : j) * 512);
        const int dly = pace(NPAIR + j) - DASY;
        const int d = VREV ? (j + 2 < NPAIR ? NWAVE + S * (NPAIR - 3 - j) + w : w) : NWAVE + S * j + w;
        pv(bA, sv[0]);
        hold(dly);
        request(bA, rv, voffV, poff(d, 0));
        pv(bB, sv[1]);
        hold(dly);
        request(bB, rv, voffV, poff(d, 1));
      }
      flag_add(lds, F_LAST + w, lane);
      sl = *(const f32x2*)(sco + (VREV ? 0 : NPAIR - 1) * 512);
    }
    asm volatile("" ::: "memory");
    if (w >= S) {
      {
        f32x4 sc[4];
        qk(bA, sc);
        sl[0] = score(sc);
        qk(bB, sc);
        sl[1] = score(sc);
        m = head_max(fmaxf(sl[0], sl[1]));
      }
      STAMP(12);
      if (w >= 4 && red < 72) {
        const int ht = (w - 4) * 64 + lane;
        unsigned a1[9], ab[9], a2[9];
        const rsrc_t r1 = mkrsrc(w1, FF * 2), rb = mkrsrc(b1, FF * 2), r2 = mkrsrc(w2, FF * 2);
#pragma unroll
        for (int i = 0; i < 9; ++i) {
          a1[i] = __builtin_amdgcn_raw_buffer_load_b32(r1, (ht + 256 * i) * 4, 0, 0);
          ab[i] = __builtin_amdgcn_raw_buffer_load_b32(rb, (ht + 256 * i) * 4, 0, 0);
          a2[i] = __builtin_amdgcn_raw_buffer_load_b32(r2, (ht + 256 * i) * 4, 0, 0);
        }
        __builtin_amdgcn_sched_barrier(0);
        float xn[10];
#pragma unroll
        for (int i = 0; i < 5; ++i) { const unsigned v = *(const unsigned*)(lds + L_XN + (lane + 64 * i) * 4); xn[2 * i] = bflo(v); xn[2 * i + 1] = bfhi(v); }
        float mean2, den2;
        ln_stats(xn, lane < 32, mean2, den2);
        constexpr float GC0 = -2.0f * 0.7978845608028654f * LOG2E, GC1 = GC0 * 0.044715f;
#pragma unroll 1
        for (int j = 0; j < 8; ++j) {
          const float hv = ln_apply(bflo(*(const unsigned short*)(lds + L_XN + (red * 8 + j) * 2)), mean2, den2);
          float y = 0.f;
#pragma unroll
          for (int i = 0; i < 9; ++i) {
            asm volatile("" : "+v"(a1[i]), "+v"(ab[i]), "+v"(a2[i]));
            const unsigned pr = pack_bf16(hv * bflo(a1[i]), hv * bfhi(a1[i]));
            const unsigned uu = pack_bf16(bflo(pr) + bflo(ab[i]), bfhi(pr) + bfhi(ab[i]));
            const float u0 = bflo(uu), u1 = bfhi(uu);
            const float e0 = __builtin_amdgcn_exp2f(u0 * fmaf(u0 * u0, GC1, GC0));
            const float e1 = __builtin_amdgcn_exp2f(u1 * fmaf(u1 * u1, GC1, GC0));
            const unsigned gg = pack_bf16(u0 * __builtin_amdgcn_rcpf(1.0f + e0), u1 * __builtin_amdgcn_rcpf(1.0f + e1));
            y = fmaf(bflo(gg), bflo(a2[i]), y);
            y = fmaf(bfhi(gg), bfhi(a2[i]), y);
          }
          y = wave_sum(y);
          if (lane == 0) *(float*)(lds + L_YP + (w - 4) * 32 + j * 4) = y;
        }
        flag_add(lds, F_Y, lane);
      }
      if (hw < RSVD) {
        bool seen = false;
        unsigned tseen = 0;
#pragma unroll 1
        for (unsigned spin = 0; spin < (SPIN_CAP << 2); ++spin) {
          const unsigned long long rf = sched_get(lds);
          const unsigned due = __builtin_amdgcn_readfirstlane((unsigned)rf + ((2 * NITER + hw) * (unsigned)(rf >> 32) >> 5)) + RSV0;
          const unsigned now = now32();
          const int late = (int)(now - due);
          if (!seen && flag_get(lds, F_LAST + (hw & 1)) != 0u) { seen = true; tseen = now; }
          if (late >= 0 || (seen && (late >= -RSVH || (int)(now - tseen) >= RSVB))) break;
          __builtin_amdgcn_s_sleep(2);
        }
      } else {
        flag_wait<2>(lds, F_AR2, hw - RSVD + 1);
      }
      __builtin_amdgcn_s_setprio(2);
      __builtin_amdgcn_sched_barrier(0);
      request(bA, rv, voffV, poff(w, 0));
      request(bB, rv, voffV, poff(w, 1));
      if (hw < NH - RSVD) {
        asm volatile("s_waitcnt vmcnt(0)" ::: "memory");
        flag_add(lds, F_AR2, lane);
      }
    }
    int po = WS_PX + ((g * 8 + hb * 4 + la) * 8 + s) * 128 + (tb * 4 + c) * 16;
    asm volatile("" : "+v"(po));
    pv(bA, sl[0]);
    pv(bB, sl[1]);
    STAMP(3);
    {
      f32x4 res;
#pragma unroll
      for (int r = 0; r < 4; ++r) {
        const float own = tb ? acc[1][1][r] : acc[0][0][r], snd = tb ? acc[1][0][r] : acc[0][1][r];
        res[r] = own + dppf<ROR8>(snd);
      }
      l += dppf<XOR1>(l);
      l += dppf<XOR2>(l);
      l += dppf<ROR8>(l);
      l = rows_sum(l);
      *(f32x4*)(lds + L_PART + w * 1024 + lane * 16) = res;
      if ((lane & ~4) == 0) *(f32x2*)(lds + L_ML + (w * 2 + hb) * 8) = f32x2{m, l};
      asm volatile("" ::: "memory");
      unsigned old = 0;
      if (lane == 0) old = __hip_atomic_fetch_add((unsigned*)(lds + L_FLAG) + F_WP + vzero(), 1u, __ATOMIC_RELAXED, __HIP_MEMORY_SCOPE_WORKGROUP);
      asm volatile("" ::: "memory");
      f32x2 ml[NWAVE];
      f32x4 pp[NWAVE];
#pragma unroll
      for (int k = 0; k < NWAVE; ++k) {
        ml[k] = *(const f32x2*)(lds + L_ML + (k * 2 + hb) * 8);
        pp[k] = *(const f32x4*)(lds + L_PART + k * 1024 + lane * 16);
      }
      __builtin_amdgcn_sched_barrier(0);
      if (__builtin_amdgcn_readfirstlane(old) == NWAVE - 1) {
        float mx = ml[0][0];
#pragma unroll
        for (int k = 1; k < NWAVE; ++k) mx = fmaxf(mx, ml[k][0]);
        f32x4 sum = {0.f, 0.f, 0.f, 0.f};
        float L = 0.f;
#pragma unroll
        for (int k = 0; k < NWAVE; ++k) {
          const float be = __builtin_amdgcn_exp2f((ml[k][0] - mx) * LOG2E);
          sum += be * pp[k]; L = fmaf(be, ml[k][1], L);
        }
        const float inv = recip(L);
        const unsigned side = asu((la & 1) ? L : mx) >> (16 * (c & 1));
        __builtin_amdgcn_raw_buffer_store_b128(
            u32x4{put_lo(sum[0] * inv, tagA), put_lo(sum[1] * inv, side), put_lo(sum[2] * inv, tagA >> 8), put_lo(sum[3] * inv, side >> 8)},
            rws, po, 0, AUX_SC1);
        flag_add(lds, F_END, lane);
        STAMP(10);
      }
    }
    if (w < S) {
      flag_wait<1>(lds, F_WP, WOL);
      const rsrc_t ro = mkrsrc(wo + (size_t)(kvh * 256 + qh * 128 + w * 64) * DM, 64 * DM * 2);
      u32x4 wt[24];
#pragma unroll
      for (int i = 0; i < 24; ++i)
        wt[i] = __builtin_amdgcn_raw_buffer_load_b128(
            ro, 2 * (i % 3) < nwk ? ((i / 3) * 8 + (lane >> 3)) * (DM * 2) + (s & 3) * 256 + (i % 3) * 128 + (lane & 7) * 16 : OOB, 0, AUX_NT);
#pragma unroll
      for (int i = 0; i < 24; ++i) *(u32x4*)(lds + L_WO + (w * 64 + (i / 3) * 8 + (lane >> 3)) * 384 + (i % 3) * 128 + (lane & 7) * 16) = wt[i];
      flag_add(lds, F_WO, lane);
      STAMP(4);
    } else if (hw < nwk) {
      const int n = lane & 15;
      int oo = n < 2 ? WS_OX + (b * 18 + (s & 3) * 4 + hw) * 4096 + (kvh * 2 + qh) * 128 + n * 64 + la * 16 : OOB;
      asm volatile("" : "+v"(oo));
      flag_wait<1>(lds, F_WO, S);
      u32x4 wof[2][4];
      {
        typedef __attribute__((address_space(3))) s16x4* lds_s16x4;
        unsigned char* img = lds + L_WO + ((n >> 2) * 8 + la * 2) * 384 + hw * 64 + (lane & 3) * 8;
#pragma unroll
        for (int t = 0; t < 2; ++t)
#pragma unroll
          for (int ks = 0; ks < 4; ++ks) {
            const u32x2 lo = __builtin_bit_cast(u32x2, __builtin_amdgcn_ds_read_tr16_b64_v4i16((lds_s16x4)(img + ks * 32 * 384 + t * 32)));
            const u32x2 hi = __builtin_bit_cast(u32x2, __builtin_amdgcn_ds_read_tr16_b64_v4i16((lds_s16x4)(img + ks * 32 * 384 + t * 32 + 384)));
            wof[t][ks] = u32x4{lo[0], lo[1], hi[0], hi[1]};
          }
      }
      u32x4 ob[4];
#pragma unroll 1
      for (unsigned spin = 0; spin < (SPIN_CAP << 4); ++spin) {
        asm volatile("" ::: "memory");
        const unsigned f = __hip_atomic_load((unsigned*)(lds + L_FLAG) + F_O, __ATOMIC_RELAXED, __HIP_MEMORY_SCOPE_WORKGROUP);
        asm volatile("" ::: "memory");
#pragma unroll
        for (int ks = 0; ks < 4; ++ks) ob[ks] = *(const u32x4*)(lds + L_OA + ks * 64 + la * 16);
        __builtin_amdgcn_sched_barrier(0);
        if (__builtin_amdgcn_readfirstlane(f) != 0u) break;
      }
      STAMP(6);
      f32x4 d0 = {0.f, 0.f, 0.f, 0.f}, d1 = {0.f, 0.f, 0.f, 0.f};
#pragma unroll
      for (int ks = 0; ks < 4; ++ks) { d0 = mfma32(wof[0][ks], ob[ks], d0); d1 = mfma32(wof[1][ks], ob[ks], d1); }
      const f32x4 d = n ? d1 : d0;
      __builtin_amdgcn_raw_buffer_store_b128(
          u32x4{put_lo(d[0], tagA), put_lo(d[1], tagA >> 8), put_lo(d[2], tagA >> 16), put_lo(d[3], tagA >> 24)}, rws, oo, 0, AUX_SC1);
      STAMP(11);
    }
  }
}

}

extern "C" void kernel_launch(void* const* d_in, const int* in_sizes, int n_in, void* d_out, int out_size, void* d_ws,
                              size_t ws_size, hipStream_t stream) {
  (void)in_sizes; (void)n_in; (void)out_size; (void)ws_size;
  fused_decode_kernel<<<dim3(NWG), dim3(NTHR), 0, stream>>>(
      (const unsigned short*)d_in[0], (const unsigned char*)d_in[1], (const unsigned char*)d_in[2],
      (const unsigned short*)d_in[3], (const unsigned short*)d_in[4], (const unsigned short*)d_in[6],
      (const unsigned short*)d_in[7], (const unsigned short*)d_in[8], (const unsigned short*)d_in[9],
      (unsigned short*)d_out, (unsigned char*)d_ws);
}
```

```cpp
#include <hip/hip_runtime.h>
#include <hip/hip_bf16.h>
#include <stdint.h>

namespace {

typedef unsigned u32x2 __attribute__((ext_vector_type(2)));
typedef unsigned u32x4 __attribute__((ext_vector_type(4)));
typedef float f32x2 __attribute__((ext_vector_type(2)));
typedef float f32x4 __attribute__((ext_vector_type(4)));
typedef short s16x4 __attribute__((ext_vector_type(4)));
typedef short s16x8 __attribute__((ext_vector_type(8)));
typedef __bf16 bf16x2 __attribute__((ext_vector_type(2)));
typedef __amdgpu_buffer_rsrc_t rsrc_t;

constexpr int DM = 576, SK = 32768, DH = 128, FF = 4608;
constexpr int NSPLIT = 8, NWG = 256, NWAVE = 8, NTHR = (NWAVE + 1) * 64;
constexpr int KEYS_WG = SK / NSPLIT;
constexpr int TILE_BYTES = 32 * DH * 2;
constexpr int NT = KEYS_WG / 32;
constexpr int SPAN = 2 * KEYS_WG * DH * 2;
constexpr int S = 2, NH = NWAVE - S;
constexpr int NPAIR = (NT / 2 - NH) / S;
constexpr int NITER = 2 * NPAIR - 1;
static_assert(2 * (NPAIR * S + NH) == NT, "every tile has one owner");

#define CODE_KB 16
#define PFD 3
#define DLY0 7
#define DASY 1
#define DLYMAX 12
#define GAIN 3
#define TAU16 1704
#define REF0 90
#define RSV0 200
#define RSVH 25
#define RSVB 100
#define RSVD 3
#define WOL 6
#define CK0 4
#define CKI 16
#define CKTHR 64
#define DGAIN 4
#define PSL 6
#define QPS 2
#define T1D 7
#define T2D 18

constexpr int AUX_NT = 2, AUX_SC1 = 16;
constexpr unsigned RSRC3 = 0x00020000u;
constexpr unsigned SPIN_CAP = 1u << 19;
constexpr int OOB = 0x7ff00000;

constexpr int WS_EPOCH = 0;
constexpr int WS_QX = 4096;
constexpr int WS_CK = WS_QX + 32 * 4096;
constexpr int WS_PX = WS_CK + 32 * 256;
constexpr int WS_OX = WS_PX + NWG * 1024;
constexpr int WS_END = WS_OX + 2 * 18 * 4096;

constexpr int L_FLAG = 0;
constexpr int L_TAG = 52;
constexpr int L_REF = 56;
constexpr int L_CK = 64;
constexpr int L_XN = 96;
constexpr int L_QPART = L_XN + 1280;
constexpr int L_QS = L_QPART + 8 * 64 * 4;
constexpr int L_OA = L_QS + 512;
constexpr int L_YP = L_OA + 256;
constexpr int L_ML = L_YP + 4 * 8 * 4;
constexpr int L_PART = L_ML + NWAVE * 16;
constexpr int L_WO = L_PART + NWAVE * 1024;
constexpr int L_END = L_WO + 128 * 384;
static_assert(NPAIR * 512 <= 64 * 384, "a stream's scores fit its half of the wo image");
constexpr int F_QP = 0, F_Q = 1, F_WP = 2, F_END = 3, F_O = 4, F_Y = 5, F_ARR = 6, F_AR2 = 7, F_CK = 8, F_LAST = 10, F_WO = 12;

constexpr float LOG2E = 1.4426950408889634f;
constexpr float SCALE = 0.08838834764831845f;
constexpr float LN_EPS = 1e-5f;

__device__ __forceinline__ float asf(unsigned u) { return __builtin_bit_cast(float, u); }
__device__ __forceinline__ unsigned asu(float f) { return __builtin_bit_cast(unsigned, f); }
__device__ __forceinline__ float bflo(unsigned u) { return asf(u << 16); }
__device__ __forceinline__ float bfhi(unsigned u) { return asf(u & 0xffff0000u); }
__device__ __forceinline__ unsigned pack_bf16(float lo, float hi) {
  f32x2 v = {lo, hi};
  return __builtin_bit_cast(unsigned, __builtin_convertvector(v, bf16x2));
}
__device__ __forceinline__ float rbf(float f) { return (float)(__bf16)f; }
template <int CTRL> __device__ __forceinline__ unsigned dppu(unsigned x) {
  return (unsigned)__builtin_amdgcn_mov_dpp((int)x, CTRL, 0xf, 0xf, true);
}
template <int CTRL> __device__ __forceinline__ float dppf(float x) { return asf(dppu<CTRL>(asu(x))); }
constexpr int XOR1 = 0xB1, XOR2 = 0x4E, XOR7 = 0x141, ROR4 = 0x124, ROR8 = 0x128;
constexpr int QB0 = 0x00, QB1 = 0x55, QB2 = 0xAA, QB3 = 0xFF;
__device__ __forceinline__ float rows_sum(float x) {
  auto s = __builtin_amdgcn_permlane16_swap(asu(x), asu(x), false, false);
  x = asf(s[0]) + asf(s[1]);
  auto t = __builtin_amdgcn_permlane32_swap(asu(x), asu(x), false, false);
  return asf(t[0]) + asf(t[1]);
}
__device__ __forceinline__ float rows_max(float x) {
  auto s = __builtin_amdgcn_permlane16_swap(asu(x), asu(x), false, false);
  x = fmaxf(asf(s[0]), asf(s[1]));
  auto t = __builtin_amdgcn_permlane32_swap(asu(x), asu(x), false, false);
  return fmaxf(asf(t[0]), asf(t[1]));
}
__device__ __forceinline__ float rows_sum_t(float v0, float v1, float v2, float v3) {
  const auto p0 = __builtin_amdgcn_permlane32_swap(asu(v0), asu(v2), false, false);
  const auto p1 = __builtin_amdgcn_permlane32_swap(asu(v1), asu(v3), false, false);
  const auto p2 = __builtin_amdgcn_permlane16_swap(asu(asf(p0[0]) + asf(p0[1])), asu(asf(p1[0]) + asf(p1[1])), false, false);
  return asf(p2[0]) + asf(p2[1]);
}
__device__ __forceinline__ float wave_sum(float x) {
  x += dppf<XOR1>(x); x += dppf<XOR2>(x); x += dppf<ROR4>(x); x += dppf<ROR8>(x);
  return rows_sum(x);
}
__device__ __forceinline__ float recip(float x) {
  const float r = __builtin_amdgcn_rcpf(x);
  return fmaf(fmaf(-x, r, 1.0f), r, r);
}
__device__ __forceinline__ rsrc_t mkrsrc(const void* p, int bytes) {
  return __builtin_amdgcn_make_buffer_rsrc(const_cast<void*>(p), 0, bytes, RSRC3);
}
__device__ __forceinline__ f32x4 mfma32(u32x4 a, u32x4 b, f32x4 c) {
  return __builtin_amdgcn_mfma_f32_16x16x32_bf16(__builtin_bit_cast(s16x8, a), __builtin_bit_cast(s16x8, b), c, 0, 0, 0);
}
__device__ __forceinline__ f32x4 mfma16(unsigned a0, unsigned a1, unsigned b0, unsigned b1, f32x4 c) {
  u32x2 a = {a0, a1}, b = {b0, b1};
  return __builtin_amdgcn_mfma_f32_16x16x16bf16_1k(__builtin_bit_cast(s16x4, a), __builtin_bit_cast(s16x4, b), c, 0, 0, 0);
}
__device__ __forceinline__ unsigned put_lo(float v, unsigned byte) { return (asu(v) & 0xffffff00u) | (byte & 0xffu); }
__device__ __forceinline__ float cut_lo(unsigned u) { return asf(u & 0xffffff00u); }
__device__ __forceinline__ unsigned now32() { return (unsigned)__builtin_amdgcn_s_memrealtime(); }
__device__ __forceinline__ void wait_until(unsigned t) {
  while ((int)(now32() - t) < 0) __builtin_amdgcn_s_sleep(1);
}
__device__ __forceinline__ int vzero() { int z = 0; asm("" : "+v"(z)); return z; }
__device__ __forceinline__ void flag_add(unsigned char* lds, int f, int lane) {
  asm volatile("" ::: "memory");
  if (lane == 0) __hip_atomic_fetch_add((unsigned*)(lds + L_FLAG) + f + vzero(), 1u, __ATOMIC_RELAXED, __HIP_MEMORY_SCOPE_WORKGROUP);
  asm volatile("" ::: "memory");
}
__device__ __forceinline__ unsigned flag_get(unsigned char* lds, int f) {
  return __builtin_amdgcn_readfirstlane(__hip_atomic_load((unsigned*)(lds + L_FLAG) + f, __ATOMIC_RELAXED, __HIP_MEMORY_SCOPE_WORKGROUP));
}
template <int SLEEP> __device__ __forceinline__ void flag_wait(unsigned char* lds, int f, unsigned n) {
#pragma unroll 1
  for (unsigned spin = 0; spin < (SPIN_CAP << 4); ++spin) {
    if (flag_get(lds, f) >= n) break;
    if (SLEEP) __builtin_amdgcn_s_sleep(SLEEP);
  }
  asm volatile("" ::: "memory");
}
__device__ __forceinline__ unsigned long long sched_get(unsigned char* lds) {
  return __hip_atomic_load((unsigned long long*)(lds + L_REF), __ATOMIC_RELAXED, __HIP_MEMORY_SCOPE_WORKGROUP);
}
__device__ __forceinline__ void ln_stats(const float (&v)[10], bool last_valid, float& mean_b, float& den_b) {
  float s = 0.f;
#pragma unroll
  for (int i = 0; i < 8; ++i) s += v[i];
  s += last_valid ? (v[8] + v[9]) : 0.f;
  const float mean = wave_sum(s) * (1.0f / DM);
  float q = 0.f;
#pragma unroll
  for (int i = 0; i < 8; ++i) { const float d = v[i] - mean; q = fmaf(d, d, q); }
  { const float d8 = v[8] - mean, d9 = v[9] - mean; q += last_valid ? fmaf(d8, d8, d9 * d9) : 0.f; }
  const float var = wave_sum(q) * (1.0f / DM);
  mean_b = rbf(mean);
  den_b = rbf(__builtin_amdgcn_sqrtf(rbf(rbf(var) + LN_EPS)));
}
__device__ __forceinline__ float ln_apply(float x, float mean_b, float den_b) {
  const float d = rbf(x - mean_b), r = __builtin_amdgcn_rcpf(den_b), q = d * r;
  return rbf(fmaf(fmaf(-q, den_b, d), r, q));
}

#define STAMP(i) do { } while (0)

__global__ void __launch_bounds__(NTHR, 3)
fused_decode_kernel(const unsigned short* __restrict__ x, const unsigned char* __restrict__ kc, const unsigned char* __restrict__ vc,
                    const unsigned short* __restrict__ wq, const unsigned short* __restrict__ wo,
                    const unsigned short* __restrict__ w1, const unsigned short* __restrict__ b1,
                    const unsigned short* __restrict__ w2, const unsigned short* __restrict__ b2,
                    unsigned short* __restrict__ out, unsigned char* ws) {
  __shared__ __attribute__((aligned(16))) unsigned char lds[L_END];
  const int lane = threadIdx.x & 63, wv = __builtin_amdgcn_readfirstlane(threadIdx.x >> 6), w = wv < 8 ? wv ^ 4 : wv;
  const unsigned tst = now32();
  long long pc0;
  asm volatile("s_getpc_b64 %0" : "=s"(pc0));
  const int g = blockIdx.x >> 3, s = (blockIdx.x + 3 * g) & 7, id = g * 8 + s, b = g >> 4, kvh = g & 15;
  STAMP(0);
  const rsrc_t rws = mkrsrc(ws, WS_END);
  const rsrc_t rx = mkrsrc(x + b * DM, DM * 2);
  const int qh = s >> 2, cb = (s + (kvh >> 2)) & 3;
  const int la = lane >> 4;
  const int red = kvh * 8 + s;

  u32x2 ep = {0u, 0u};
  if (w == NWAVE) {
    ep = __builtin_amdgcn_raw_buffer_load_b64(rws, WS_EPOCH + b * 256, 0, 0);
    if (lane < 13) *((unsigned*)(lds + L_FLAG) + lane) = 0u;
    if (lane == 0) *(u32x2*)(lds + L_REF) = u32x2{tst + REF0 + (NWAVE / 2 * TAU16 >> 4), TAU16};
    asm volatile("s_waitcnt lgkmcnt(0)\n\ts_barrier" ::: "memory");
  }
  if (w >= NWAVE) {
    __builtin_amdgcn_s_setprio(3);
    const unsigned e0 = __builtin_amdgcn_readfirstlane(ep[0]);
    const unsigned tagA = ((e0 & 0x7f7f7f7fu) + 0x01010101u) ^ (e0 & 0x80808080u);
    const unsigned tagB = __builtin_amdgcn_readfirstlane(ep[1]) + 1u;
    if (lane == 0) *(unsigned*)(lds + L_TAG) = tagA;
    flag_wait<0>(lds, F_QP, 4);
    {
      f32x2 q2 = *(const f32x2*)(lds + L_QPART + (lane & 31) * 8);
#pragma unroll
      for (int ww = 1; ww < 8; ++ww) q2 += *(const f32x2*)(lds + L_QPART + ww * 256 + (lane & 31) * 8);
      __builtin_amdgcn_raw_buffer_store_b128(u32x4{tagA, asu(q2[0]), tagB, asu(q2[1])}, rws,
                                             lane < 32 ? WS_QX + g * 4096 + qh * 2048 + cb * 512 + lane * 16 : OOB, 0, AUX_SC1);
      STAMP(8);
    }
    __builtin_amdgcn_s_sleep(PSL);
    {
      float qa[4], qb[4];
      int of[4];
#pragma unroll
      for (int i = 0; i < 4; ++i) { qa[i] = 0.f; qb[i] = 0.f; of[i] = WS_QX + g * 4096 + i * 1024 + lane * 16; }
#pragma unroll 1
      for (unsigned spin = 0; spin < SPIN_CAP; ++spin) {
        asm volatile("" ::: "memory");
        u32x4 gr[4];
#pragma unroll
        for (int i = 0; i < 4; ++i) gr[i] = __builtin_amdgcn_raw_buffer_load_b128(rws, of[i], 0, AUX_SC1);
        bool done = true;
#pragma unroll
        for (int i = 0; i < 4; ++i) {
          const bool fresh = (gr[i][0] == tagA) & (gr[i][2] == tagB) & (of[i] != OOB);
          qa[i] = fresh ? asf(gr[i][1]) : qa[i]; qb[i] = fresh ? asf(gr[i][3]) : qb[i];
          of[i] = fresh ? OOB : of[i];
          done &= of[i] == OOB;
        }
        if (__all(done)) break;
        __builtin_amdgcn_s_sleep(QPS);
      }
      *(unsigned*)(lds + L_QS + lane * 4) = pack_bf16(qa[0] + qa[2], qb[0] + qb[2]);
      *(unsigned*)(lds + L_QS + 256 + lane * 4) = pack_bf16(qa[1] + qa[3], qb[1] + qb[3]);
      flag_add(lds, F_Q, lane);
    }
    const int fc = (lane & 1) * 4 + la;
    const unsigned xraw = __builtin_amdgcn_raw_buffer_load_b32(rx, red * 16 + (fc >> 1) * 4, 0, 0);
    const unsigned b2v = __builtin_amdgcn_raw_buffer_load_b16(mkrsrc(b2, 4), 0, 0, 0);
    {
      flag_wait<16>(lds, F_CK + 1, S);
      const u32x2 c0 = *(const u32x2*)(lds + L_CK), c1 = *(const u32x2*)(lds + L_CK + 16);
      const unsigned tm = c0[1] + (unsigned)((int)(c1[1] - c0[1]) >> 1), per = ((c0[1] - c0[0]) + (c1[1] - c1[0])) * 8u / CKI;
      if (__builtin_amdgcn_readfirstlane(per) > (unsigned)(TAU16 + CKTHR)) {
        const int co = WS_CK + g * 256;
        __builtin_amdgcn_raw_buffer_store_b128(u32x4{tagA, tm, tagB, per}, rws, lane == 0 ? co + s * 16 : OOB, 0, AUX_SC1);
        unsigned tr = now32() + 250u;
#pragma unroll 1
        for (int spin = 0; spin < 4; ++spin) {
          wait_until(tr);
          asm volatile("" ::: "memory");
          const u32x4 v = __builtin_amdgcn_raw_buffer_load_b128(rws, co + (lane & 7) * 16, 0, AUX_SC1);
          const bool ok = (v[0] == tagA) & (v[2] == tagB);
          const int cnt = __builtin_popcountll(__ballot(ok)) >> 3;
          if (cnt == 8 || (spin == 3 && cnt >= 6)) {
            float d = ok ? (float)(int)(v[1] - tm) : 0.f, p = ok ? (float)v[3] : 0.f;
            d += dppf<XOR1>(d); d += dppf<XOR2>(d); d += dppf<XOR7>(d);
            p += dppf<XOR1>(p); p += dppf<XOR2>(p); p += dppf<XOR7>(p);
            const float rc = __builtin_amdgcn_rcpf((float)cnt);
            const unsigned tau = min((unsigned)(p * rc) - 4u, (unsigned)TAU16 + 400u);
            if (lane == 0) *(u32x2*)(lds + L_REF) = u32x2{tm + (unsigned)(int)(d * rc) - ((CK0 + CKI) * tau >> 4) - 100u, tau};
            break;
          }
          tr = now32() + 100u;
        }
      }
    }
    float yv = 0.f, xfin = (fc & 1) ? bfhi(xraw) : bflo(xraw);
    const rsrc_t rout = mkrsrc(out + b * DM, DM * 2 + 2);
    int oout = (lane & 14) == 0 ? (red * 8 + fc) * 2 : OOB;
    if (red < 72) {
      flag_wait<16>(lds, F_Y, 4);
#pragma unroll
      for (int ww = 0; ww < 4; ++ww) yv += *(const float*)(lds + L_YP + ww * 32 + fc * 4);
      yv = rbf(rbf(yv) + bflo(b2v));
    }
    asm volatile("" : "+v"(yv), "+v"(xfin), "+v"(oout));
    flag_wait<16>(lds, F_WP, 1);
    flag_wait<0>(lds, F_END, 1);
    STAMP(5);
    __builtin_amdgcn_s_sleep(T1D);
    {
      u32x4 ga[4];
#pragma unroll
      for (int i = 0; i < 4; ++i) ga[i] = u32x4{0u, 0u, 0u, 0u};
      int of = WS_PX + (g * 8 + (s & 4)) * 1024 + lane * 16;
#pragma unroll 1
      for (unsigned spin = 0; spin < SPIN_CAP; ++spin) {
        asm volatile("" ::: "memory");
        u32x4 gn[4];
#pragma unroll
        for (int i = 0; i < 4; ++i) gn[i] = __builtin_amdgcn_raw_buffer_load_b128(rws, of + i * 1024, 0, AUX_SC1);
        unsigned bad = 0u;
#pragma unroll
        for (int i = 0; i < 4; ++i) bad |= (gn[i][0] ^ tagA) | (gn[i][2] ^ (tagA >> 8));
        bad &= 0xffu;
        bad |= dppu<XOR1>(bad); bad |= dppu<XOR2>(bad); bad |= dppu<XOR7>(bad);
        const bool fresh = (bad == 0u) & (of != OOB);
#pragma unroll
        for (int i = 0; i < 4; ++i) ga[i] = fresh ? gn[i] : ga[i];
        of = fresh ? OOB : of;
        if (__all(of == OOB)) break;
        __builtin_amdgcn_s_sleep(1);
      }
      const unsigned hm = __builtin_amdgcn_perm(ga[0][3], ga[0][1], 0x0c0c0400u) << (16 * (lane & 1));
      const unsigned hl = __builtin_amdgcn_perm(ga[1][3], ga[1][1], 0x0c0c0400u) << (16 * (lane & 1));
      const float pm = asf(hm | dppu<XOR1>(hm)), pl = asf(hl | dppu<XOR1>(hl));
      const float M = rows_max(fmaxf(pm, dppf<ROR8>(pm)));
      const float w0 = __builtin_amdgcn_exp2f((pm - M) * LOG2E) * pl;
      const float inv = recip(rows_sum(w0 + dppf<ROR8>(w0)));
      float v[4];
#pragma unroll
      for (int r = 0; r < 4; ++r) {
        const float t = rows_sum_t(w0 * cut_lo(ga[0][r]), w0 * cut_lo(ga[1][r]), w0 * cut_lo(ga[2][r]), w0 * cut_lo(ga[3][r]));
        v[r] = (t + dppf<ROR8>(t)) * inv;
      }
      *(u32x2*)(lds + L_OA + la * 64 + (lane & 7) * 8) = u32x2{pack_bf16(v[0], v[1]), pack_bf16(v[2], v[3])};
      flag_add(lds, F_O, lane);
    }
    if (red < 72) {
      __builtin_amdgcn_s_sleep(T2D);
      u32x4 ga = {0u, 0u, 0u, 0u};
      int of = WS_OX + (b * 18 + (red >> 2)) * 4096 + (lane >> 1) * 128 + (red & 3) * 32 + (lane & 1) * 16;
#pragma unroll 1
      for (unsigned spin = 0; spin < SPIN_CAP; ++spin) {
        asm volatile("" ::: "memory");
        const u32x4 gn = __builtin_amdgcn_raw_buffer_load_b128(rws, of, 0, AUX_SC1);
        unsigned bad = (__builtin_amdgcn_perm(gn[1], gn[0], 0x0c0c0400u) | __builtin_amdgcn_perm(gn[3], gn[2], 0x04000c0cu)) ^ tagA;
        bad |= dppu<XOR1>(bad);
        const bool fresh = (bad == 0u) & (of != OOB);
        ga = fresh ? gn : ga;
        of = fresh ? OOB : of;
        if (__all(of == OOB)) break;
        __builtin_amdgcn_s_sleep(1);
      }
      STAMP(13);
      float t = rows_sum_t(cut_lo(ga[0]), cut_lo(ga[1]), cut_lo(ga[2]), cut_lo(ga[3]));
      t += dppf<XOR2>(t); t += dppf<ROR4>(t); t += dppf<ROR8>(t);
      __builtin_amdgcn_raw_buffer_store_b16((unsigned short)pack_bf16(rbf(xfin + rbf(t)) + yv, 0.f), rout, oout, 0, 0);
      if (red == 0 && lane == 0) __builtin_amdgcn_raw_buffer_store_b64(u32x2{tagA, tagB}, rws, WS_EPOCH + b * 256, 0, AUX_SC1);
    }
  } else {
    const rsrc_t rk = mkrsrc(kc + (size_t)(id >> 1) * SPAN, SPAN);
    const rsrc_t rv = mkrsrc(vc + (size_t)(id >> 1) * SPAN, SPAN);
    auto poff = [&](int d, int h) {
      return ((d & 1) | (h << 1) | ((d & 2) << 1) | ((id & 1) << 3) | ((d >> 2) << 4)) * TILE_BYTES;
    };
    const int voffK = ((lane & 15) >> 2) * 256 + la * 64 + (lane & 3) * 16;
    const int voffV = lane * 16;
    u32x4 bA[8], bB[8];
    auto request = [&](u32x4 (&x)[8], rsrc_t r, int voff, int so) {
      asm volatile("" : "+v"(voff));
#pragma unroll
      for (int u = 0; u < 8; ++u) x[u] = __builtin_amdgcn_raw_buffer_load_b128(r, voff + (u & 3) * 1024, so + (u >> 2) * 4096, AUX_NT);
      __builtin_amdgcn_sched_barrier(0);
    };
    if (w >= 4) {
      const int rg = (w - 4) * 8 + (lane >> 3), c8 = lane & 7;
      unsigned xs[5];
      u32x4 wqv[9];
#pragma unroll
      for (int i = 0; i < 5; ++i) xs[i] = __builtin_amdgcn_raw_buffer_load_b32(rx, (lane + 64 * i) * 4, 0, 0);
      __builtin_amdgcn_sched_barrier(0);
      const rsrc_t rq = mkrsrc(wq + qh * 288 * 4096 + kvh * 256 + cb * 64, (287 * 4096 + 64) * 2);
#pragma unroll
      for (int i = 0; i < 9; ++i) wqv[i] = __builtin_amdgcn_raw_buffer_load_b128(rq, (rg + 32 * i) * 8192 + c8 * 16, 0, AUX_NT);
      __builtin_amdgcn_sched_barrier(0);
      __builtin_amdgcn_s_barrier();
      __builtin_amdgcn_sched_barrier(0);
      float xv[10], mean1, den1;
#pragma unroll
      for (int i = 0; i < 5; ++i) { xv[2 * i] = bflo(xs[i]); xv[2 * i + 1] = bfhi(xs[i]); }
      ln_stats(xv, lane < 32, mean1, den1);
#pragma unroll
      for (int i = 0; i < 5; ++i)
        *(unsigned*)(lds + L_XN + (lane + 64 * i) * 4) = pack_bf16(ln_apply(xv[2 * i], mean1, den1), ln_apply(xv[2 * i + 1], mean1, den1));
      asm volatile("" ::: "memory");
      STAMP(9);
      f32x4 qa = {0.f, 0.f, 0.f, 0.f};
#pragma unroll
      for (int i = 0; i < 9; ++i) {
        const unsigned t = (unsigned)*(const unsigned short*)(lds + L_XN + (qh * 288 + rg + 32 * i) * 2) << (16 * (lane & 1));
        const int jp = c8 >> 1;
        qa = mfma32(wqv[i], u32x4{jp == 0 ? t : 0u, jp == 1 ? t : 0u, jp == 2 ? t : 0u, jp == 3 ? t : 0u}, qa);
      }
      if ((lane >> 5) == ((lane >> 3) & 1)) {
        float* qp = (float*)(lds + L_QPART) + ((w - 4) * 2 + (lane >> 5)) * 64 + (la & 1) * 32 + c8;
#pragma unroll
        for (int r = 0; r < 4; ++r) qp[r * 8] = qa[r];
      }
      flag_add(lds, F_QP, lane);
      STAMP(1);
    }
    u32x4 cv = {0u, 0u, 0u, 0u};
    if (CODE_KB && w == 3)
      cv = __builtin_amdgcn_raw_buffer_load_b128(mkrsrc((const void*)(pc0 & ~255ll), CODE_KB * 1024), g * 1024 + lane * 16, 0, 0);
    if (w < 4) asm volatile("s_barrier" ::: "memory");
    asm volatile("" :: "v"(cv));
    if (w >= PFD) flag_wait<1>(lds, F_ARR, w - PFD + 1);
    __builtin_amdgcn_sched_barrier(0);
    STAMP(14);
    request(bA, rk, voffK, poff(w, 0));
    request(bB, rk, voffK, poff(w, 1));
    asm volatile("s_waitcnt vmcnt(0)" ::: "memory");
    flag_add(lds, F_ARR, lane);
    STAMP(15);
    flag_wait<1>(lds, F_Q, 1);
    if (w < S) STAMP(2);

    const int c = lane & 3, hb = (lane >> 2) & 1, tb = (lane >> 3) & 1;
    const unsigned tagA = __builtin_amdgcn_readfirstlane(*(const unsigned*)(lds + L_TAG));
    float m, l = 0.f;
    f32x4 acc[2][2];
#pragma unroll
    for (int i = 0; i < 2; ++i)
#pragma unroll
      for (int j = 0; j < 2; ++j) acc[i][j] = f32x4{0.f, 0.f, 0.f, 0.f};
    const u32x4 qf = *(const u32x4*)(lds + L_QS + (hb * 128 + la * 32 + c * 8) * 2);
    const u32x4 zero4 = {0u, 0u, 0u, 0u};
    const u32x4 q0 = tb ? zero4 : qf, q1 = tb ? qf : zero4;
    const unsigned mask0 = c == 0 ? 0x0000ffffu : c == 1 ? 0xffff0000u : 0u;
    const unsigned mask1 = c == 2 ? 0x0000ffffu : c == 3 ? 0xffff0000u : 0u;
    auto qk = [&](const u32x4 (&x)[8], f32x4 (&sc)[4]) {
#pragma unroll
      for (int i = 0; i < 4; ++i) sc[i] = mfma32(x[i], q0, f32x4{0.f, 0.f, 0.f, 0.f});
#pragma unroll
      for (int i = 0; i < 4; ++i) sc[i] = mfma32(x[4 + i], q1, sc[i]);
    };
    auto score = [&](const f32x4 (&sc)[4]) {
      float t4[4];
#pragma unroll
      for (int i = 0; i < 4; ++i) t4[i] = (dppf<QB0>(sc[i][0]) + dppf<QB1>(sc[i][1])) + (dppf<QB2>(sc[i][2]) + dppf<QB3>(sc[i][3]));
      const float raw = (c & 2) ? ((c & 1) ? t4[3] : t4[2]) : ((c & 1) ? t4[1] : t4[0]);
      float sv = rbf(rbf(raw) * SCALE);
      asm volatile("" : "+v"(sv));
      return sv;
    };
    auto head_max = [&](float v) {
      v = fmaxf(v, dppf<XOR1>(v));
      v = fmaxf(v, dppf<XOR2>(v));
      v = fmaxf(v, dppf<ROR8>(v));
      return rows_max(v);
    };
    auto pv = [&](const u32x4 (&x)[8], float sv) {
      const float p = __builtin_amdgcn_exp2f((sv - m) * LOG2E);
      l += p;
      const unsigned pk = pack_bf16(p, p);
      unsigned pb0[4], pb1[4];
      pb0[0] = dppu<QB0>(pk) & mask0; pb1[0] = dppu<QB0>(pk) & mask1;
      pb0[1] = dppu<QB1>(pk) & mask0; pb1[1] = dppu<QB1>(pk) & mask1;
      pb0[2] = dppu<QB2>(pk) & mask0; pb1[2] = dppu<QB2>(pk) & mask1;
      pb0[3] = dppu<QB3>(pk) & mask0; pb1[3] = dppu<QB3>(pk) & mask1;
#pragma unroll
      for (int u = 0; u < 8; ++u) {
        acc[u >> 2][0] = mfma16(x[u][0], x[u][1], pb0[u & 3], pb1[u & 3], acc[u >> 2][0]);
        acc[u >> 2][1] = mfma16(x[u][2], x[u][3], pb0[u & 3], pb1[u & 3], acc[u >> 2][1]);
      }
    };
    auto hold = [&](int dly) {
      __builtin_amdgcn_sched_barrier(0);
      for (int i = 0; i < dly; ++i) __builtin_amdgcn_s_sleep(1);
      __builtin_amdgcn_sched_barrier(0);
    };
    const int hw = w - S, nwk = (s & 3) == 3 ? 6 : 4;
    f32x2 sl;
    if (w < S) {
      __builtin_amdgcn_s_setprio(2);
      auto pace = [&](int it) {
        const unsigned now = now32();
        const unsigned long long rf = sched_get(lds);
        const int tau = __builtin_amdgcn_readfirstlane((unsigned)(rf >> 32));
        const int lead = (int)(__builtin_amdgcn_readfirstlane((unsigned)rf) + (unsigned)(it * tau >> 4) - now);
        const int dadd = (tau - TAU16) * DGAIN >> 8;
        if (it == CK0 || it == CK0 + CKI) {
          *(unsigned*)(lds + L_CK + w * 16 + (it > CK0) * 4) = now;
          flag_add(lds, F_CK + (it > CK0), lane);
        }
        return min(max(DLY0 + dadd + ((lead * GAIN + 128) >> 8), 0), DLYMAX + dadd);
      };
      unsigned char* sco = lds + L_WO + w * (64 * 384) + lane * 8;
      flag_wait<1>(lds, F_ARR, NWAVE + w - PFD + 1);
      float mx = -INFINITY;
      for (int j = 0; j < NPAIR; ++j) {
        const int dly = pace(j) + DASY;
        const bool k = j + 1 < NPAIR;
        const rsrc_t r = k ? rk : rv;
        const int vo = k ? voffK : voffV, d = NWAVE + S * (k ? j : j - 1) + w;
        f32x4 sc[4];
        qk(bA, sc);
        hold(dly);
        request(bA, r, vo, poff(d, 0));
        const float s0 = score(sc);
        qk(bB, sc);
        hold(dly);
        request(bB, r, vo, poff(d, 1));
        const float s1 = score(sc);
        mx = fmaxf(mx, fmaxf(s0, s1));
        *(f32x2*)(sco + j * 512) = f32x2{s0, s1};
      }
      m = head_max(mx);
      for (int j = 0; j + 1 < NPAIR; ++j) {
        const f32x2 sv = *(const f32x2*)(sco + (NPAIR - 1 - j) * 512);
        const int dly = pace(NPAIR + j) - DASY;
        const int d = j + 2 < NPAIR ? NWAVE + S * (NPAIR - 3 - j) + w : w;
        pv(bA, sv[0]);
        hold(dly);
        request(bA, rv, voffV, poff(d, 0));
        pv(bB, sv[1]);
        hold(dly);
        request(bB, rv, voffV, poff(d, 1));
      }
      flag_add(lds, F_LAST + w, lane);
      sl = *(const f32x2*)sco;
    }
    asm volatile("" ::: "memory");
    if (w >= S) {
      {
        f32x4 sc[4];
        qk(bA, sc);
        sl[0] = score(sc);
        qk(bB, sc);
        sl[1] = score(sc);
        m = head_max(fmaxf(sl[0], sl[1]));
      }
      STAMP(12);
      if (w >= 4 && red < 72) {
        const int ht = (w - 4) * 64 + lane;
        unsigned a1[9], ab[9], a2[9];
        const rsrc_t r1 = mkrsrc(w1, FF * 2), rb = mkrsrc(b1, FF * 2), r2 = mkrsrc(w2, FF * 2);
#pragma unroll
        for (int i = 0; i < 9; ++i) {
          a1[i] = __builtin_amdgcn_raw_buffer_load_b32(r1, (ht + 256 * i) * 4, 0, 0);
          ab[i] = __builtin_amdgcn_raw_buffer_load_b32(rb, (ht + 256 * i) * 4, 0, 0);
          a2[i] = __builtin_amdgcn_raw_buffer_load_b32(r2, (ht + 256 * i) * 4, 0, 0);
        }
        __builtin_amdgcn_sched_barrier(0);
        float xn[10];
#pragma unroll
        for (int i = 0; i < 5; ++i) { const unsigned v = *(const unsigned*)(lds + L_XN + (lane + 64 * i) * 4); xn[2 * i] = bflo(v); xn[2 * i + 1] = bfhi(v); }
        float mean2, den2;
        ln_stats(xn, lane < 32, mean2, den2);
        constexpr float GC0 = -2.0f * 0.7978845608028654f * LOG2E, GC1 = GC0 * 0.044715f;
#pragma unroll 1
        for (int j = 0; j < 8; ++j) {
          const float hv = ln_apply(bflo(*(const unsigned short*)(lds + L_XN + (red * 8 + j) * 2)), mean2, den2);
          float y = 0.f;
#pragma unroll
          for (int i = 0; i < 9; ++i) {
            asm volatile("" : "+v"(a1[i]), "+v"(ab[i]), "+v"(a2[i]));
            const unsigned pr = pack_bf16(hv * bflo(a1[i]), hv * bfhi(a1[i]));
            const unsigned uu = pack_bf16(bflo(pr) + bflo(ab[i]), bfhi(pr) + bfhi(ab[i]));
            const float u0 = bflo(uu), u1 = bfhi(uu);
            const float e0 = __builtin_amdgcn_exp2f(u0 * fmaf(u0 * u0, GC1, GC0));
            const float e1 = __builtin_amdgcn_exp2f(u1 * fmaf(u1 * u1, GC1, GC0));
            const unsigned gg = pack_bf16(u0 * __builtin_amdgcn_rcpf(1.0f + e0), u1 * __builtin_amdgcn_rcpf(1.0f + e1));
            y = fmaf(bflo(gg), bflo(a2[i]), y);
            y = fmaf(bfhi(gg), bfhi(a2[i]), y);
          }
          y = wave_sum(y);
          if (lane == 0) *(float*)(lds + L_YP + (w - 4) * 32 + j * 4) = y;
        }
        flag_add(lds, F_Y, lane);
      }
      if (hw < RSVD) {
        bool seen = false;
        unsigned tseen = 0;
#pragma unroll 1
        for (unsigned spin = 0; spin < (SPIN_CAP << 2); ++spin) {
          const unsigned long long rf = sched_get(lds);
          const unsigned due = __builtin_amdgcn_readfirstlane((unsigned)rf + ((2 * NITER + hw) * (unsigned)(rf >> 32) >> 5)) + RSV0;
          const unsigned now = now32();
          const int late = (int)(now - due);
          if (!seen && flag_get(lds, F_LAST + (hw & 1)) != 0u) { seen = true; tseen = now; }
          if (late >= 0 || (seen && (late >= -RSVH || (int)(now - tseen) >= RSVB))) break;
          __builtin_amdgcn_s_sleep(2);
        }
      } else {
        flag_wait<2>(lds, F_AR2, hw - RSVD + 1);
      }
      __builtin_amdgcn_s_setprio(2);
      __builtin_amdgcn_sched_barrier(0);
      request(bA, rv, voffV, poff(w, 0));
      request(bB, rv, voffV, poff(w, 1));
      if (hw < NH - RSVD) {
        asm volatile("s_waitcnt vmcnt(0)" ::: "memory");
        flag_add(lds, F_AR2, lane);
      }
    }
    int po = WS_PX + ((g * 8 + hb * 4 + la) * 8 + s) * 128 + (tb * 4 + c) * 16;
    asm volatile("" : "+v"(po));
    pv(bA, sl[0]);
    pv(bB, sl[1]);
    STAMP(3);
    {
      f32x4 res;
#pragma unroll
      for (int r = 0; r < 4; ++r) {
        const float own = tb ? acc[1][1][r] : acc[0][0][r], snd = tb ? acc[1][0][r] : acc[0][1][r];
        res[r] = own + dppf<ROR8>(snd);
      }
      l += dppf<XOR1>(l);
      l += dppf<XOR2>(l);
      l += dppf<ROR8>(l);
      l = rows_sum(l);
      *(f32x4*)(lds + L_PART + w * 1024 + lane * 16) = res;
      if ((lane & ~4) == 0) *(f32x2*)(lds + L_ML + (w * 2 + hb) * 8) = f32x2{m, l};
      asm volatile("" ::: "memory");
      unsigned old = 0;
      if (lane == 0) old = __hip_atomic_fetch_add((unsigned*)(lds + L_FLAG) + F_WP + vzero(), 1u, __ATOMIC_RELAXED, __HIP_MEMORY_SCOPE_WORKGROUP);
      asm volatile("" ::: "memory");
      f32x2 ml[NWAVE];
      f32x4 pp[NWAVE];
#pragma unroll
      for (int k = 0; k < NWAVE; ++k) {
        ml[k] = *(const f32x2*)(lds + L_ML + (k * 2 + hb) * 8);
        pp[k] = *(const f32x4*)(lds + L_PART + k * 1024 + lane * 16);
      }
      __builtin_amdgcn_sched_barrier(0);
      if (__builtin_amdgcn_readfirstlane(old) == NWAVE - 1) {
        float mx = ml[0][0];
#pragma unroll
        for (int k = 1; k < NWAVE; ++k) mx = fmaxf(mx, ml[k][0]);
        f32x4 sum = {0.f, 0.f, 0.f, 0.f};
        float L = 0.f;
#pragma unroll
        for (int k = 0; k < NWAVE; ++k) {
          const float be = __builtin_amdgcn_exp2f((ml[k][0] - mx) * LOG2E);
          sum += be * pp[k]; L = fmaf(be, ml[k][1], L);
        }
        const float inv = recip(L);
        const unsigned side = asu((la & 1) ? L : mx) >> (16 * (c & 1));
        __builtin_amdgcn_raw_buffer_store_b128(
            u32x4{put_lo(sum[0] * inv, tagA), put_lo(sum[1] * inv, side), put_lo(sum[2] * inv, tagA >> 8), put_lo(sum[3] * inv, side >> 8)},
            rws, po, 0, AUX_SC1);
        flag_add(lds, F_END, lane);
        STAMP(10);
      }
    }
    if (w < S) {
      flag_wait<1>(lds, F_WP, WOL);
      const rsrc_t ro = mkrsrc(wo + (size_t)(kvh * 256 + qh * 128 + w * 64) * DM, 64 * DM * 2);
      u32x4 wt[24];
#pragma unroll
      for (int i = 0; i < 24; ++i)
        wt[i] = __builtin_amdgcn_raw_buffer_load_b128(
            ro, 2 * (i % 3) < nwk ? ((i / 3) * 8 + (lane >> 3)) * (DM * 2) + (s & 3) * 256 + (i % 3) * 128 + (lane & 7) * 16 : OOB, 0, AUX_NT);
#pragma unroll
      for (int i = 0; i < 24; ++i) *(u32x4*)(lds + L_WO + (w * 64 + (i / 3) * 8 + (lane >> 3)) * 384 + (i % 3) * 128 + (lane & 7) * 16) = wt[i];
      flag_add(lds, F_WO, lane);
      STAMP(4);
    } else if (hw < nwk) {
      const int n = lane & 15;
      int oo = n < 2 ? WS_OX + (b * 18 + (s & 3) * 4 + hw) * 4096 + (kvh * 2 + qh) * 128 + n * 64 + la * 16 : OOB;
      asm volatile("" : "+v"(oo));
      flag_wait<1>(lds, F_WO, S);
      u32x4 wof[2][4];
      {
        typedef __attribute__((address_space(3))) s16x4* lds_s16x4;
        unsigned char* img = lds + L_WO + ((n >> 2) * 8 + la * 2) * 384 + hw * 64 + (lane & 3) * 8;
#pragma unroll
        for (int t = 0; t < 2; ++t)
#pragma unroll
          for (int ks = 0; ks < 4; ++ks) {
            const u32x2 lo = __builtin_bit_cast(u32x2, __builtin_amdgcn_ds_read_tr16_b64_v4i16((lds_s16x4)(img + ks * 32 * 384 + t * 32)));
            const u32x2 hi = __builtin_bit_cast(u32x2, __builtin_amdgcn_ds_read_tr16_b64_v4i16((lds_s16x4)(img + ks * 32 * 384 + t * 32 + 384)));
            wof[t][ks] = u32x4{lo[0], lo[1], hi[0], hi[1]};
          }
      }
      u32x4 ob[4];
#pragma unroll 1
      for (unsigned spin = 0; spin < (SPIN_CAP << 4); ++spin) {
        asm volatile("" ::: "memory");
        const unsigned f = __hip_atomic_load((unsigned*)(lds + L_FLAG) + F_O, __ATOMIC_RELAXED, __HIP_MEMORY_SCOPE_WORKGROUP);
        asm volatile("" ::: "memory");
#pragma unroll
        for (int ks = 0; ks < 4; ++ks) ob[ks] = *(const u32x4*)(lds + L_OA + ks * 64 + la * 16);
        __builtin_amdgcn_sched_barrier(0);
        if (__builtin_amdgcn_readfirstlane(f) != 0u) break;
      }
      STAMP(6);
      f32x4 d0 = {0.f, 0.f, 0.f, 0.f}, d1 = {0.f, 0.f, 0.f, 0.f};
#pragma unroll
      for (int ks = 0; ks < 4; ++ks) { d0 = mfma32(wof[0][ks], ob[ks], d0); d1 = mfma32(wof[1][ks], ob[ks], d1); }
      const f32x4 d = n ? d1 : d0;
      __builtin_amdgcn_raw_buffer_store_b128(
          u32x4{put_lo(d[0], tagA), put_lo(d[1], tagA >> 8), put_lo(d[2], tagA >> 16), put_lo(d[3], tagA >> 24)}, rws, oo, 0, AUX_SC1);
      STAMP(11);
    }
  }
}

}

extern "C" void kernel_launch(void* const* d_in, const int* in_sizes, int n_in, void* d_out, int out_size, void* d_ws,
                              size_t ws_size, hipStream_t stream) {
  (void)in_sizes; (void)n_in; (void)out_size; (void)ws_size;
  fused_decode_kernel<<<dim3(NWG), dim3(NTHR), 0, stream>>>(
      (const unsigned short*)d_in[0], (const unsigned char*)d_in[1], (const unsigned char*)d_in[2],
      (const unsigned short*)d_in[3], (const unsigned short*)d_in[4], (const unsigned short*)d_in[6],
      (const unsigned short*)d_in[7], (const unsigned short*)d_in[8], (const unsigned short*)d_in[9],
      (unsigned short*)d_out, (unsigned char*)d_ws);
}
```

```cpp
#include <hip/hip_runtime.h>
#include <hip/hip_bf16.h>
#include <stdint.h>

#define DIAG_M 0
namespace {

typedef unsigned u32x2 __attribute__((ext_vector_type(2)));
typedef unsigned u32x4 __attribute__((ext_vector_type(4)));
typedef float f32x2 __attribute__((ext_vector_type(2)));
typedef float f32x4 __attribute__((ext_vector_type(4)));
typedef short s16x4 __attribute__((ext_vector_type(4)));
typedef short s16x8 __attribute__((ext_vector_type(8)));
typedef __bf16 bf16x2 __attribute__((ext_vector_type(2)));
typedef __amdgpu_buffer_rsrc_t rsrc_t;

constexpr int DM = 576, SK = 32768, DH = 128, FF = 4608;
constexpr int NSPLIT = 8, NWG = 256, NWAVE = 8, NTHR = (NWAVE + 1) * 64;
constexpr int KEYS_WG = SK / NSPLIT;
constexpr int TILE_BYTES = 32 * DH * 2;
constexpr int NT = KEYS_WG / 32;
constexpr int SPAN = 2 * KEYS_WG * DH * 2;
constexpr int S = 2, NH = NWAVE - S;
constexpr int NPAIR = (NT / 2 - NH) / S;
constexpr int NITER = 2 * NPAIR - 1;
static_assert(2 * (NPAIR * S + NH) == NT, "every tile has one owner");

#define PFD 3
#define DLY0 7
#define DASY 1
#define DLYMAX 12
#define GAIN 3
#define TAU16 1704
#define REF0 90
#define RSV0 200
#define RSVH 25
#define RSVB 100
#define RSVD 3
#define WOL 6
#define CK0 4
#define CKI 16
#define CKTHR 64
#define DGAIN 4
#define PSL 6
#define QPS 2
#define T1D 7
#define T2D 18

constexpr int AUX_NT = 2, AUX_SC1 = 16;
constexpr unsigned RSRC3 = 0x00020000u;
constexpr unsigned SPIN_CAP = 1u << 19;
constexpr int OOB = 0x7ff00000;

constexpr int WS_EPOCH = 0;
constexpr int WS_QX = 4096;
constexpr int WS_CK = WS_QX + 32 * 4096;
constexpr int WS_PX = WS_CK + 32 * 256;
constexpr int WS_OX = WS_PX + NWG * 1024;
constexpr int WS_END = WS_OX + 2 * 18 * 4096;

constexpr int L_FLAG = 0;
constexpr int L_TAG = 52;
constexpr int L_REF = 56;
constexpr int L_CK = 64;
constexpr int L_XN = 96;
constexpr int L_QPART = L_XN + 1280;
constexpr int L_QS = L_QPART + 8 * 64 * 4;
constexpr int L_OA = L_QS + 512;
constexpr int L_YP = L_OA + 256;
constexpr int L_ML = L_YP + 4 * 8 * 4;
constexpr int L_PART = L_ML + NWAVE * 16;
constexpr int L_WO = L_PART + NWAVE * 1024;
constexpr int L_END = L_WO + 128 * 384;
static_assert(NPAIR * 512 <= 64 * 384, "a stream's scores fit its half of the wo image");
constexpr int F_QP = 0, F_Q = 1, F_WP = 2, F_END = 3, F_O = 4, F_Y = 5, F_ARR = 6, F_AR2 = 7, F_CK = 8, F_LAST = 10, F_WO = 12;

constexpr float LOG2E = 1.4426950408889634f;
constexpr float SCALE = 0.08838834764831845f;
constexpr float LN_EPS = 1e-5f;

__device__ __forceinline__ float asf(unsigned u) { return __builtin_bit_cast(float, u); }
__device__ __forceinline__ unsigned asu(float f) { return __builtin_bit_cast(unsigned, f); }
__device__ __forceinline__ float bflo(unsigned u) { return asf(u << 16); }
__device__ __forceinline__ float bfhi(unsigned u) { return asf(u & 0xffff0000u); }
__device__ __forceinline__ unsigned pack_bf16(float lo, float hi) {
  f32x2 v = {lo, hi};
  return __builtin_bit_cast(unsigned, __builtin_convertvector(v, bf16x2));
}
__device__ __forceinline__ float rbf(float f) { return (float)(__bf16)f; }
template <int CTRL> __device__ __forceinline__ unsigned dppu(unsigned x) {
  return (unsigned)__builtin_amdgcn_mov_dpp((int)x, CTRL, 0xf, 0xf, true);
}
template <int CTRL> __device__ __forceinline__ float dppf(float x) { return asf(dppu<CTRL>(asu(x))); }
constexpr int XOR1 = 0xB1, XOR2 = 0x4E, XOR7 = 0x141, ROR4 = 0x124, ROR8 = 0x128;
constexpr int QB0 = 0x00, QB1 = 0x55, QB2 = 0xAA, QB3 = 0xFF;
__device__ __forceinline__ float rows_sum(float x) {
  auto s = __builtin_amdgcn_permlane16_swap(asu(x), asu(x), false, false);
  x = asf(s[0]) + asf(s[1]);
  auto t = __builtin_amdgcn_permlane32_swap(asu(x), asu(x), false, false);
  return asf(t[0]) + asf(t[1]);
}
__device__ __forceinline__ float rows_max(float x) {
  auto s = __builtin_amdgcn_permlane16_swap(asu(x), asu(x), false, false);
  x = fmaxf(asf(s[0]), asf(s[1]));
  auto t = __builtin_amdgcn_permlane32_swap(asu(x), asu(x), false, false);
  return fmaxf(asf(t[0]), asf(t[1]));
}
__device__ __forceinline__ float rows_sum_t(float v0, float v1, float v2, float v3) {
  const auto p0 = __builtin_amdgcn_permlane32_swap(asu(v0), asu(v2), false, false);
  const auto p1 = __builtin_amdgcn_permlane32_swap(asu(v1), asu(v3), false, false);
  const auto p2 = __builtin_amdgcn_permlane16_swap(asu(asf(p0[0]) + asf(p0[1])), asu(asf(p1[0]) + asf(p1[1])), false, false);
  return asf(p2[0]) + asf(p2[1]);
}
__device__ __forceinline__ float wave_sum(float x) {
  x += dppf<XOR1>(x); x += dppf<XOR2>(x); x += dppf<ROR4>(x); x += dppf<ROR8>(x);
  return rows_sum(x);
}
__device__ __forceinline__ float recip(float x) {
  const float r = __builtin_amdgcn_rcpf(x);
  return fmaf(fmaf(-x, r, 1.0f), r, r);
}
__device__ __forceinline__ rsrc_t mkrsrc(const void* p, int bytes) {
  return __builtin_amdgcn_make_buffer_rsrc(const_cast<void*>(p), 0, bytes, RSRC3);
}
__device__ __forceinline__ f32x4 mfma32(u32x4 a, u32x4 b, f32x4 c) {
  return __builtin_amdgcn_mfma_f32_16x16x32_bf16(__builtin_bit_cast(s16x8, a), __builtin_bit_cast(s16x8, b), c, 0, 0, 0);
}
__device__ __forceinline__ f32x4 mfma16(unsigned a0, unsigned a1, unsigned b0, unsigned b1, f32x4 c) {
  u32x2 a = {a0, a1}, b = {b0, b1};
  return __builtin_amdgcn_mfma_f32_16x16x16bf16_1k(__builtin_bit_cast(s16x4, a), __builtin_bit_cast(s16x4, b), c, 0, 0, 0);
}
__device__ __forceinline__ unsigned put_lo(float v, unsigned byte) { return (asu(v) & 0xffffff00u) | (byte & 0xffu); }
__device__ __forceinline__ float cut_lo(unsigned u) { return asf(u & 0xffffff00u); }
__device__ __forceinline__ unsigned now32() { return (unsigned)__builtin_amdgcn_s_memrealtime(); }
__device__ __forceinline__ void wait_until(unsigned t) {
  while ((int)(now32() - t) < 0) __builtin_amdgcn_s_sleep(1);
}
__device__ __forceinline__ int vzero() { int z = 0; asm("" : "+v"(z)); return z; }
__device__ __forceinline__ void flag_add(unsigned char* lds, int f, int lane) {
  asm volatile("" ::: "memory");
  if (lane == 0) __hip_atomic_fetch_add((unsigned*)(lds + L_FLAG) + f + vzero(), 1u, __ATOMIC_RELAXED, __HIP_MEMORY_SCOPE_WORKGROUP);
  asm volatile("" ::: "memory");
}
__device__ __forceinline__ unsigned flag_get(unsigned char* lds, int f) {
  return __builtin_amdgcn_readfirstlane(__hip_atomic_load((unsigned*)(lds + L_FLAG) + f, __ATOMIC_RELAXED, __HIP_MEMORY_SCOPE_WORKGROUP));
}
template <int SLEEP> __device__ __forceinline__ void flag_wait(unsigned char* lds, int f, unsigned n) {
#pragma unroll 1
  for (unsigned spin = 0; spin < (SPIN_CAP << 4); ++spin) {
    if (flag_get(lds, f) >= n) break;
    if (SLEEP) __builtin_amdgcn_s_sleep(SLEEP);
  }
  asm volatile("" ::: "memory");
}
__device__ __forceinline__ unsigned long long sched_get(unsigned char* lds) {
  return __hip_atomic_load((unsigned long long*)(lds + L_REF), __ATOMIC_RELAXED, __HIP_MEMORY_SCOPE_WORKGROUP);
}
__device__ __forceinline__ void ln_stats(const float (&v)[10], bool last_valid, float& mean_b, float& den_b) {
  float s = 0.f;
#pragma unroll
  for (int i = 0; i < 8; ++i) s += v[i];
  s += last_valid ? (v[8] + v[9]) : 0.f;
  const float mean = wave_sum(s) * (1.0f / DM);
  float q = 0.f;
#pragma unroll
  for (int i = 0; i < 8; ++i) { const float d = v[i] - mean; q = fmaf(d, d, q); }
  { const float d8 = v[8] - mean, d9 = v[9] - mean; q += last_valid ? fmaf(d8, d8, d9 * d9) : 0.f; }
  const float var = wave_sum(q) * (1.0f / DM);
  mean_b = rbf(mean);
  den_b = rbf(__builtin_amdgcn_sqrtf(rbf(rbf(var) + LN_EPS)));
}
__device__ __forceinline__ float ln_apply(float x, float mean_b, float den_b) {
  const float d = rbf(x - mean_b), r = __builtin_amdgcn_rcpf(den_b), q = d * r;
  return rbf(fmaf(fmaf(-q, den_b, d), r, q));
}

#define STAMP(i) do { } while (0)

__global__ void __launch_bounds__(NTHR, 3)
fused_decode_kernel(const unsigned short* __restrict__ x, const unsigned char* __restrict__ kc, const unsigned char* __restrict__ vc,
                    const unsigned short* __restrict__ wq, const unsigned short* __restrict__ wo,
                    const unsigned short* __restrict__ w1, const unsigned short* __restrict__ b1,
                    const unsigned short* __restrict__ w2, const unsigned short* __restrict__ b2,
                    unsigned short* __restrict__ out, unsigned char* ws) {
  __shared__ __attribute__((aligned(16))) unsigned char lds[L_END];
  const int lane = threadIdx.x & 63, wv = __builtin_amdgcn_readfirstlane(threadIdx.x >> 6), w = wv < 8 ? wv ^ 4 : wv;
  const unsigned tst = now32();
  const int g = blockIdx.x >> 3, s = (blockIdx.x + 3 * g) & 7, id = g * 8 + s, b = g >> 4, kvh = g & 15;
  STAMP(0);
  const rsrc_t rws = mkrsrc(ws, WS_END);
  const rsrc_t rx = mkrsrc(x + b * DM, DM * 2);
  const int qh = s >> 2, cb = (s + (kvh >> 2)) & 3;
  const int la = lane >> 4;
  const int red = kvh * 8 + s;

  u32x2 ep = {0u, 0u};
  if (w == NWAVE) {
    ep = __builtin_amdgcn_raw_buffer_load_b64(rws, WS_EPOCH + b * 256, 0, 0);
    if (lane < 13) *((unsigned*)(lds + L_FLAG) + lane) = 0u;
    if (lane == 0) *(u32x2*)(lds + L_REF) = u32x2{tst + REF0 + (NWAVE / 2 * TAU16 >> 4), TAU16};
    asm volatile("s_waitcnt lgkmcnt(0)\n\ts_barrier" ::: "memory");
  }
  if (w >= NWAVE) {
    __builtin_amdgcn_s_setprio(3);
    const unsigned e0 = __builtin_amdgcn_readfirstlane(ep[0]);
    const unsigned tagA = ((e0 & 0x7f7f7f7fu) + 0x01010101u) ^ (e0 & 0x80808080u);
    const unsigned tagB = __builtin_amdgcn_readfirstlane(ep[1]) + 1u;
    if (lane == 0) *(unsigned*)(lds + L_TAG) = tagA;
    flag_wait<0>(lds, F_QP, 4);
    {
      f32x2 q2 = *(const f32x2*)(lds + L_QPART + (lane & 31) * 8);
#pragma unroll
      for (int ww = 1; ww < 8; ++ww) q2 += *(const f32x2*)(lds + L_QPART + ww * 256 + (lane & 31) * 8);
      __builtin_amdgcn_raw_buffer_store_b128(u32x4{tagA, asu(q2[0]), tagB, asu(q2[1])}, rws,
                                             lane < 32 ? WS_QX + g * 4096 + qh * 2048 + cb * 512 + lane * 16 : OOB, 0, AUX_SC1);
      STAMP(8);
    }
    __builtin_amdgcn_s_sleep(PSL);
    {
      float qa[4], qb[4];
      int of[4];
#pragma unroll
      for (int i = 0; i < 4; ++i) { qa[i] = 0.f; qb[i] = 0.f; of[i] = WS_QX + g * 4096 + i * 1024 + lane * 16; }
#pragma unroll 1
      for (unsigned spin = 0; spin < SPIN_CAP; ++spin) {
        asm volatile("" ::: "memory");
        u32x4 gr[4];
#pragma unroll
        for (int i = 0; i < 4; ++i) gr[i] = __builtin_amdgcn_raw_buffer_load_b128(rws, of[i], 0, AUX_SC1);
        bool done = true;
#pragma unroll
        for (int i = 0; i < 4; ++i) {
          const bool fresh = (gr[i][0] == tagA) & (gr[i][2] == tagB) & (of[i] != OOB);
          qa[i] = fresh ? asf(gr[i][1]) : qa[i]; qb[i] = fresh ? asf(gr[i][3]) : qb[i];
          of[i] = fresh ? OOB : of[i];
          done &= of[i] == OOB;
        }
        if (__all(done)) break;
        __builtin_amdgcn_s_sleep(QPS);
      }
      *(unsigned*)(lds + L_QS + lane * 4) = pack_bf16(qa[0] + qa[2], qb[0] + qb[2]);
      *(unsigned*)(lds + L_QS + 256 + lane * 4) = pack_bf16(qa[1] + qa[3], qb[1] + qb[3]);
      flag_add(lds, F_Q, lane);
    }
    const int fc = (lane & 1) * 4 + la;
    const unsigned xraw = __builtin_amdgcn_raw_buffer_load_b32(rx, red * 16 + (fc >> 1) * 4, 0, 0);
    const unsigned b2v = __builtin_amdgcn_raw_buffer_load_b16(mkrsrc(b2, 4), 0, 0, 0);
    {
      flag_wait<16>(lds, F_CK + 1, S);
      const u32x2 c0 = *(const u32x2*)(lds + L_CK), c1 = *(const u32x2*)(lds + L_CK + 16);
      const unsigned tm = c0[1] + (unsigned)((int)(c1[1] - c0[1]) >> 1), per = ((c0[1] - c0[0]) + (c1[1] - c1[0])) * 8u / CKI;
      if (__builtin_amdgcn_readfirstlane(per) > (unsigned)(TAU16 + CKTHR)) {
        const int co = WS_CK + g * 256;
        __builtin_amdgcn_raw_buffer_store_b128(u32x4{tagA, tm, tagB, per}, rws, lane == 0 ? co + s * 16 : OOB, 0, AUX_SC1);
        unsigned tr = now32() + 250u;
#pragma unroll 1
        for (int spin = 0; spin < 4; ++spin) {
          wait_until(tr);
          asm volatile("" ::: "memory");
          const u32x4 v = __builtin_amdgcn_raw_buffer_load_b128(rws, co + (lane & 7) * 16, 0, AUX_SC1);
          const bool ok = (v[0] == tagA) & (v[2] == tagB);
          const int cnt = __builtin_popcountll(__ballot(ok)) >> 3;
          if (cnt == 8 || (spin == 3 && cnt >= 6)) {
            float d = ok ? (float)(int)(v[1] - tm) : 0.f, p = ok ? (float)v[3] : 0.f;
            d += dppf<XOR1>(d); d += dppf<XOR2>(d); d += dppf<XOR7>(d);
            p += dppf<XOR1>(p); p += dppf<XOR2>(p); p += dppf<XOR7>(p);
            const float rc = __builtin_amdgcn_rcpf((float)cnt);
            const unsigned tau = min((unsigned)(p * rc) - 4u, (unsigned)TAU16 + 400u);
            if (lane == 0) *(u32x2*)(lds + L_REF) = u32x2{tm + (unsigned)(int)(d * rc) - ((CK0 + CKI) * tau >> 4) - 100u, tau};
            break;
          }
          tr = now32() + 100u;
        }
      }
    }
    float yv = 0.f, xfin = (fc & 1) ? bfhi(xraw) : bflo(xraw);
    const rsrc_t rout = mkrsrc(out + b * DM, DM * 2 + 2);
    int oout = (lane & 14) == 0 ? (red * 8 + fc) * 2 : OOB;
    if (red < 72) {
      flag_wait<16>(lds, F_Y, 4);
#pragma unroll
      for (int ww = 0; ww < 4; ++ww) yv += *(const float*)(lds + L_YP + ww * 32 + fc * 4);
      yv = rbf(rbf(yv) + bflo(b2v));
    }
    asm volatile("" : "+v"(yv), "+v"(xfin), "+v"(oout));
    flag_wait<16>(lds, F_WP, 1);
    flag_wait<0>(lds, F_END, 1);
    STAMP(5);
    __builtin_amdgcn_s_sleep(T1D);
    {
      u32x4 ga[4];
#pragma unroll
      for (int i = 0; i < 4; ++i) ga[i] = u32x4{0u, 0u, 0u, 0u};
      int of = WS_PX + (g * 8 + (s & 4)) * 1024 + lane * 16;
#pragma unroll 1
      for (unsigned spin = 0; spin < SPIN_CAP; ++spin) {
        asm volatile("" ::: "memory");
        u32x4 gn[4];
#pragma unroll
        for (int i = 0; i < 4; ++i) gn[i] = __builtin_amdgcn_raw_buffer_load_b128(rws, of + i * 1024, 0, AUX_SC1);
        unsigned bad = 0u;
#pragma unroll
        for (int i = 0; i < 4; ++i) bad |= (gn[i][0] ^ tagA) | (gn[i][2] ^ (tagA >> 8));
        bad &= 0xffu;
        bad |= dppu<XOR1>(bad); bad |= dppu<XOR2>(bad); bad |= dppu<XOR7>(bad);
        const bool fresh = (bad == 0u) & (of != OOB);
#pragma unroll
        for (int i = 0; i < 4; ++i) ga[i] = fresh ? gn[i] : ga[i];
        of = fresh ? OOB : of;
        if (__all(of == OOB)) break;
        __builtin_amdgcn_s_sleep(1);
      }
      const unsigned hm = __builtin_amdgcn_perm(ga[0][3], ga[0][1], 0x0c0c0400u) << (16 * (lane & 1));
      const unsigned hl = __builtin_amdgcn_perm(ga[1][3], ga[1][1], 0x0c0c0400u) << (16 * (lane & 1));
      const float pm = asf(hm | dppu<XOR1>(hm)), pl = asf(hl | dppu<XOR1>(hl));
      const float M = rows_max(fmaxf(pm, dppf<ROR8>(pm)));
      const float w0 = __builtin_amdgcn_exp2f((pm - M) * LOG2E) * pl;
      const float inv = recip(rows_sum(w0 + dppf<ROR8>(w0)));
      float v[4];
#pragma unroll
      for (int r = 0; r < 4; ++r) {
        const float t = rows_sum_t(w0 * cut_lo(ga[0][r]), w0 * cut_lo(ga[1][r]), w0 * cut_lo(ga[2][r]), w0 * cut_lo(ga[3][r]));
        v[r] = (t + dppf<ROR8>(t)) * inv;
      }
      *(u32x2*)(lds + L_OA + la * 64 + (lane & 7) * 8) = u32x2{pack_bf16(v[0], v[1]), pack_bf16(v[2], v[3])};
      flag_add(lds, F_O, lane);
    }
    if (red < 72) {
      __builtin_amdgcn_s_sleep(T2D);
      u32x4 ga = {0u, 0u, 0u, 0u};
      int of = WS_OX + (b * 18 + (red >> 2)) * 4096 + (lane >> 1) * 128 + (red & 3) * 32 + (lane & 1) * 16;
#pragma unroll 1
      for (unsigned spin = 0; spin < SPIN_CAP; ++spin) {
        asm volatile("" ::: "memory");
        const u32x4 gn = __builtin_amdgcn_raw_buffer_load_b128(rws, of, 0, AUX_SC1);
        unsigned bad = (__builtin_amdgcn_perm(gn[1], gn[0], 0x0c0c0400u) | __builtin_amdgcn_perm(gn[3], gn[2], 0x04000c0cu)) ^ tagA;
        bad |= dppu<XOR1>(bad);
        const bool fresh = (bad == 0u) & (of != OOB);
        ga = fresh ? gn : ga;
        of = fresh ? OOB : of;
        if (__all(of == OOB)) break;
        __builtin_amdgcn_s_sleep(1);
      }
      STAMP(13);
      float t = rows_sum_t(cut_lo(ga[0]), cut_lo(ga[1]), cut_lo(ga[2]), cut_lo(ga[3]));
      t += dppf<XOR2>(t); t += dppf<ROR4>(t); t += dppf<ROR8>(t);
      __builtin_amdgcn_raw_buffer_store_b16((unsigned short)pack_bf16(rbf(xfin + rbf(t)) + yv, 0.f), rout, oout, 0, 0);
      if (id == 0) {
        const unsigned met = DIAG_M ? now32() - tst : 0u;
        wait_until(tst + 15000u + 10u * (met < 20000u ? met : 20000u));
      }
      if (red == 0 && lane == 0) __builtin_amdgcn_raw_buffer_store_b64(u32x2{tagA, tagB}, rws, WS_EPOCH + b * 256, 0, AUX_SC1);
    }
  } else {
    const rsrc_t rk = mkrsrc(kc + (size_t)(id >> 1) * SPAN, SPAN);
    const rsrc_t rv = mkrsrc(vc + (size_t)(id >> 1) * SPAN, SPAN);
    auto poff = [&](int d, int h) {
      return ((d & 1) | (h << 1) | ((d & 2) << 1) | ((id & 1) << 3) | ((d >> 2) << 4)) * TILE_BYTES;
    };
    const int voffK = ((lane & 15) >> 2) * 256 + la * 64 + (lane & 3) * 16;
    const int voffV = lane * 16;
    u32x4 bA[8], bB[8];
    auto request = [&](u32x4 (&x)[8], rsrc_t r, int voff, int so) {
      asm volatile("" : "+v"(voff));
#pragma unroll
      for (int u = 0; u < 8; ++u) x[u] = __builtin_amdgcn_raw_buffer_load_b128(r, voff + (u & 3) * 1024, so + (u >> 2) * 4096, AUX_NT);
      __builtin_amdgcn_sched_barrier(0);
    };
    if (w >= 4) {
      const int rg = (w - 4) * 8 + (lane >> 3), c8 = lane & 7;
      unsigned xs[5];
      u32x4 wqv[9];
#pragma unroll
      for (int i = 0; i < 5; ++i) xs[i] = __builtin_amdgcn_raw_buffer_load_b32(rx, (lane + 64 * i) * 4, 0, 0);
      __builtin_amdgcn_sched_barrier(0);
      const rsrc_t rq = mkrsrc(wq + qh * 288 * 4096 + kvh * 256 + cb * 64, (287 * 4096 + 64) * 2);
#pragma unroll
      for (int i = 0; i < 9; ++i) wqv[i] = __builtin_amdgcn_raw_buffer_load_b128(rq, (rg + 32 * i) * 8192 + c8 * 16, 0, AUX_NT);
      __builtin_amdgcn_sched_barrier(0);
      __builtin_amdgcn_s_barrier();
      __builtin_amdgcn_sched_barrier(0);
      float xv[10], mean1, den1;
#pragma unroll
      for (int i = 0; i < 5; ++i) { xv[2 * i] = bflo(xs[i]); xv[2 * i + 1] = bfhi(xs[i]); }
      ln_stats(xv, lane < 32, mean1, den1);
#pragma unroll
      for (int i = 0; i < 5; ++i)
        *(unsigned*)(lds + L_XN + (lane + 64 * i) * 4) = pack_bf16(ln_apply(xv[2 * i], mean1, den1), ln_apply(xv[2 * i + 1], mean1, den1));
      asm volatile("" ::: "memory");
      STAMP(9);
      f32x4 qa = {0.f, 0.f, 0.f, 0.f};
#pragma unroll
      for (int i = 0; i < 9; ++i) {
        const unsigned t = (unsigned)*(const unsigned short*)(lds + L_XN + (qh * 288 + rg + 32 * i) * 2) << (16 * (lane & 1));
        const int jp = c8 >> 1;
        qa = mfma32(wqv[i], u32x4{jp == 0 ? t : 0u, jp == 1 ? t : 0u, jp == 2 ? t : 0u, jp == 3 ? t : 0u}, qa);
      }
      if ((lane >> 5) == ((lane >> 3) & 1)) {
        float* qp = (float*)(lds + L_QPART) + ((w - 4) * 2 + (lane >> 5)) * 64 + (la & 1) * 32 + c8;
#pragma unroll
        for (int r = 0; r < 4; ++r) qp[r * 8] = qa[r];
      }
      flag_add(lds, F_QP, lane);
      STAMP(1);
    }
    if (w < 4) asm volatile("s_barrier" ::: "memory");
    if (w >= PFD) flag_wait<1>(lds, F_ARR, w - PFD + 1);
    __builtin_amdgcn_sched_barrier(0);
    STAMP(14);
    request(bA, rk, voffK, poff(w, 0));
    request(bB, rk, voffK, poff(w, 1));
    asm volatile("s_waitcnt vmcnt(0)" ::: "memory");
    flag_add(lds, F_ARR, lane);
    STAMP(15);
    flag_wait<1>(lds, F_Q, 1);
    if (w < S) STAMP(2);

    const int c = lane & 3, hb = (lane >> 2) & 1, tb = (lane >> 3) & 1;
    const unsigned tagA = __builtin_amdgcn_readfirstlane(*(const unsigned*)(lds + L_TAG));
    float m, l = 0.f;
    f32x4 acc[2][2];
#pragma unroll
    for (int i = 0; i < 2; ++i)
#pragma unroll
      for (int j = 0; j < 2; ++j) acc[i][j] = f32x4{0.f, 0.f, 0.f, 0.f};
    const u32x4 qf = *(const u32x4*)(lds + L_QS + (hb * 128 + la * 32 + c * 8) * 2);
    const u32x4 zero4 = {0u, 0u, 0u, 0u};
    const u32x4 q0 = tb ? zero4 : qf, q1 = tb ? qf : zero4;
    const unsigned mask0 = c == 0 ? 0x0000ffffu : c == 1 ? 0xffff0000u : 0u;
    const unsigned mask1 = c == 2 ? 0x0000ffffu : c == 3 ? 0xffff0000u : 0u;
    auto qk = [&](const u32x4 (&x)[8], f32x4 (&sc)[4]) {
#pragma unroll
      for (int i = 0; i < 4; ++i) sc[i] = mfma32(x[i], q0, f32x4{0.f, 0.f, 0.f, 0.f});
#pragma unroll
      for (int i = 0; i < 4; ++i) sc[i] = mfma32(x[4 + i], q1, sc[i]);
    };
    auto score = [&](const f32x4 (&sc)[4]) {
      float t4[4];
#pragma unroll
      for (int i = 0; i < 4; ++i) t4[i] = (dppf<QB0>(sc[i][0]) + dppf<QB1>(sc[i][1])) + (dppf<QB2>(sc[i][2]) + dppf<QB3>(sc[i][3]));
      const float raw = (c & 2) ? ((c & 1) ? t4[3] : t4[2]) : ((c & 1) ? t4[1] : t4[0]);
      float sv = rbf(rbf(raw) * SCALE);
      asm volatile("" : "+v"(sv));
      return sv;
    };
    auto head_max = [&](float v) {
      v = fmaxf(v, dppf<XOR1>(v));
      v = fmaxf(v, dppf<XOR2>(v));
      v = fmaxf(v, dppf<ROR8>(v));
      return rows_max(v);
    };
    auto pv = [&](const u32x4 (&x)[8], float sv) {
      const float p = __builtin_amdgcn_exp2f((sv - m) * LOG2E);
      l += p;
      const unsigned pk = pack_bf16(p, p);
      unsigned pb0[4], pb1[4];
      pb0[0] = dppu<QB0>(pk) & mask0; pb1[0] = dppu<QB0>(pk) & mask1;
      pb0[1] = dppu<QB1>(pk) & mask0; pb1[1] = dppu<QB1>(pk) & mask1;
      pb0[2] = dppu<QB2>(pk) & mask0; pb1[2] = dppu<QB2>(pk) & mask1;
      pb0[3] = dppu<QB3>(pk) & mask0; pb1[3] = dppu<QB3>(pk) & mask1;
#pragma unroll
      for (int u = 0; u < 8; ++u) {
        acc[u >> 2][0] = mfma16(x[u][0], x[u][1], pb0[u & 3], pb1[u & 3], acc[u >> 2][0]);
        acc[u >> 2][1] = mfma16(x[u][2], x[u][3], pb0[u & 3], pb1[u & 3], acc[u >> 2][1]);
      }
    };
    auto hold = [&](int dly) {
      __builtin_amdgcn_sched_barrier(0);
      for (int i = 0; i < dly; ++i) __builtin_amdgcn_s_sleep(1);
      __builtin_amdgcn_sched_barrier(0);
    };
    const int hw = w - S, nwk = (s & 3) == 3 ? 6 : 4;
    f32x2 sl;
    if (w < S) {
      __builtin_amdgcn_s_setprio(2);
      auto pace = [&](int it) {
        const unsigned now = now32();
        const unsigned long long rf = sched_get(lds);
        const int tau = __builtin_amdgcn_readfirstlane((unsigned)(rf >> 32));
        const int lead = (int)(__builtin_amdgcn_readfirstlane((unsigned)rf) + (unsigned)(it * tau >> 4) - now);
        const int dadd = (tau - TAU16) * DGAIN >> 8;
        if (it == CK0 || it == CK0 + CKI) {
          *(unsigned*)(lds + L_CK + w * 16 + (it > CK0) * 4) = now;
          flag_add(lds, F_CK + (it > CK0), lane);
        }
        return min(max(DLY0 + dadd + ((lead * GAIN + 128) >> 8), 0), DLYMAX + dadd);
      };
      unsigned char* sco = lds + L_WO + w * (64 * 384) + lane * 8;
      flag_wait<1>(lds, F_ARR, NWAVE + w - PFD + 1);
      float mx = -INFINITY;
      for (int j = 0; j < NPAIR; ++j) {
        const int dly = pace(j) + DASY;
        const bool k = j + 1 < NPAIR;
        const rsrc_t r = k ? rk : rv;
        const int vo = k ? voffK : voffV, d = NWAVE + S * (k ? j : j - 1) + w;
        f32x4 sc[4];
        qk(bA, sc);
        hold(dly);
        request(bA, r, vo, poff(d, 0));
        const float s0 = score(sc);
        qk(bB, sc);
        hold(dly);
        request(bB, r, vo, poff(d, 1));
        const float s1 = score(sc);
        mx = fmaxf(mx, fmaxf(s0, s1));
        *(f32x2*)(sco + j * 512) = f32x2{s0, s1};
      }
      m = head_max(mx);
      for (int j = 0; j + 1 < NPAIR; ++j) {
        const f32x2 sv = *(const f32x2*)(sco + (NPAIR - 1 - j) * 512);
        const int dly = pace(NPAIR + j) - DASY;
        const int d = j + 2 < NPAIR ? NWAVE + S * (NPAIR - 3 - j) + w : w;
        pv(bA, sv[0]);
        hold(dly);
        request(bA, rv, voffV, poff(d, 0));
        pv(bB, sv[1]);
        hold(dly);
        request(bB, rv, voffV, poff(d, 1));
      }
      flag_add(lds, F_LAST + w, lane);
      sl = *(const f32x2*)sco;
    }
    asm volatile("" ::: "memory");
    if (w >= S) {
      {
        f32x4 sc[4];
        qk(bA, sc);
        sl[0] = score(sc);
        qk(bB, sc);
        sl[1] = score(sc);
        m = head_max(fmaxf(sl[0], sl[1]));
      }
      STAMP(12);
      if (w >= 4 && red < 72) {
        const int ht = (w - 4) * 64 + lane;
        unsigned a1[9], ab[9], a2[9];
        const rsrc_t r1 = mkrsrc(w1, FF * 2), rb = mkrsrc(b1, FF * 2), r2 = mkrsrc(w2, FF * 2);
#pragma unroll
        for (int i = 0; i < 9; ++i) {
          a1[i] = __builtin_amdgcn_raw_buffer_load_b32(r1, (ht + 256 * i) * 4, 0, 0);
          ab[i] = __builtin_amdgcn_raw_buffer_load_b32(rb, (ht + 256 * i) * 4, 0, 0);
          a2[i] = __builtin_amdgcn_raw_buffer_load_b32(r2, (ht + 256 * i) * 4, 0, 0);
        }
        __builtin_amdgcn_sched_barrier(0);
        float xn[10];
#pragma unroll
        for (int i = 0; i < 5; ++i) { const unsigned v = *(const unsigned*)(lds + L_XN + (lane + 64 * i) * 4); xn[2 * i] = bflo(v); xn[2 * i + 1] = bfhi(v); }
        float mean2, den2;
        ln_stats(xn, lane < 32, mean2, den2);
        constexpr float GC0 = -2.0f * 0.7978845608028654f * LOG2E, GC1 = GC0 * 0.044715f;
#pragma unroll 1
        for (int j = 0; j < 8; ++j) {
          const float hv = ln_apply(bflo(*(const unsigned short*)(lds + L_XN + (red * 8 + j) * 2)), mean2, den2);
          float y = 0.f;
#pragma unroll
          for (int i = 0; i < 9; ++i) {
            asm volatile("" : "+v"(a1[i]), "+v"(ab[i]), "+v"(a2[i]));
            const unsigned pr = pack_bf16(hv * bflo(a1[i]), hv * bfhi(a1[i]));
            const unsigned uu = pack_bf16(bflo(pr) + bflo(ab[i]), bfhi(pr) + bfhi(ab[i]));
            const float u0 = bflo(uu), u1 = bfhi(uu);
            const float e0 = __builtin_amdgcn_exp2f(u0 * fmaf(u0 * u0, GC1, GC0));
            const float e1 = __builtin_amdgcn_exp2f(u1 * fmaf(u1 * u1, GC1, GC0));
            const unsigned gg = pack_bf16(u0 * __builtin_amdgcn_rcpf(1.0f + e0), u1 * __builtin_amdgcn_rcpf(1.0f + e1));
            y = fmaf(bflo(gg), bflo(a2[i]), y);
            y = fmaf(bfhi(gg), bfhi(a2[i]), y);
          }
          y = wave_sum(y);
          if (lane == 0) *(float*)(lds + L_YP + (w - 4) * 32 + j * 4) = y;
        }
        flag_add(lds, F_Y, lane);
      }
      if (hw < RSVD) {
        bool seen = false;
        unsigned tseen = 0;
#pragma unroll 1
        for (unsigned spin = 0; spin < (SPIN_CAP << 2); ++spin) {
          const unsigned long long rf = sched_get(lds);
          const unsigned due = __builtin_amdgcn_readfirstlane((unsigned)rf + ((2 * NITER + hw) * (unsigned)(rf >> 32) >> 5)) + RSV0;
          const unsigned now = now32();
          const int late = (int)(now - due);
          if (!seen && flag_get(lds, F_LAST + (hw & 1)) != 0u) { seen = true; tseen = now; }
          if (late >= 0 || (seen && (late >= -RSVH || (int)(now - tseen) >= RSVB))) break;
          __builtin_amdgcn_s_sleep(2);
        }
      } else {
        flag_wait<2>(lds, F_AR2, hw - RSVD + 1);
      }
      __builtin_amdgcn_s_setprio(2);
      __builtin_amdgcn_sched_barrier(0);
      request(bA, rv, voffV, poff(w, 0));
      request(bB, rv, voffV, poff(w, 1));
      if (hw < NH - RSVD) {
        asm volatile("s_waitcnt vmcnt(0)" ::: "memory");
        flag_add(lds, F_AR2, lane);
      }
    }
    int po = WS_PX + ((g * 8 + hb * 4 + la) * 8 + s) * 128 + (tb * 4 + c) * 16;
    asm volatile("" : "+v"(po));
    pv(bA, sl[0]);
    pv(bB, sl[1]);
    STAMP(3);
    {
      f32x4 res;
#pragma unroll
      for (int r = 0; r < 4; ++r) {
        const float own = tb ? acc[1][1][r] : acc[0][0][r], snd = tb ? acc[1][0][r] : acc[0][1][r];
        res[r] = own + dppf<ROR8>(snd);
      }
      l += dppf<XOR1>(l);
      l += dppf<XOR2>(l);
      l += dppf<ROR8>(l);
      l = rows_sum(l);
      *(f32x4*)(lds + L_PART + w * 1024 + lane * 16) = res;
      if ((lane & ~4) == 0) *(f32x2*)(lds + L_ML + (w * 2 + hb) * 8) = f32x2{m, l};
      asm volatile("" ::: "memory");
      unsigned old = 0;
      if (lane == 0) old = __hip_atomic_fetch_add((unsigned*)(lds + L_FLAG) + F_WP + vzero(), 1u, __ATOMIC_RELAXED, __HIP_MEMORY_SCOPE_WORKGROUP);
      asm volatile("" ::: "memory");
      f32x2 ml[NWAVE];
      f32x4 pp[NWAVE];
#pragma unroll
      for (int k = 0; k < NWAVE; ++k) {
        ml[k] = *(const f32x2*)(lds + L_ML + (k * 2 + hb) * 8);
        pp[k] = *(const f32x4*)(lds + L_PART + k * 1024 + lane * 16);
      }
      __builtin_amdgcn_sched_barrier(0);
      if (__builtin_amdgcn_readfirstlane(old) == NWAVE - 1) {
        float mx = ml[0][0];
#pragma unroll
        for (int k = 1; k < NWAVE; ++k) mx = fmaxf(mx, ml[k][0]);
        f32x4 sum = {0.f, 0.f, 0.f, 0.f};
        float L = 0.f;
#pragma unroll
        for (int k = 0; k < NWAVE; ++k) {
          const float be = __builtin_amdgcn_exp2f((ml[k][0] - mx) * LOG2E);
          sum += be * pp[k]; L = fmaf(be, ml[k][1], L);
        }
        const float inv = recip(L);
        const unsigned side = asu((la & 1) ? L : mx) >> (16 * (c & 1));
        __builtin_amdgcn_raw_buffer_store_b128(
            u32x4{put_lo(sum[0] * inv, tagA), put_lo(sum[1] * inv, side), put_lo(sum[2] * inv, tagA >> 8), put_lo(sum[3] * inv, side >> 8)},
            rws, po, 0, AUX_SC1);
        flag_add(lds, F_END, lane);
        STAMP(10);
      }
    }
    if (w < S) {
      flag_wait<1>(lds, F_WP, WOL);
      const rsrc_t ro = mkrsrc(wo + (size_t)(kvh * 256 + qh * 128 + w * 64) * DM, 64 * DM * 2);
      u32x4 wt[24];
#pragma unroll
      for (int i = 0; i < 24; ++i)
        wt[i] = __builtin_amdgcn_raw_buffer_load_b128(
            ro, 2 * (i % 3) < nwk ? ((i / 3) * 8 + (lane >> 3)) * (DM * 2) + (s & 3) * 256 + (i % 3) * 128 + (lane & 7) * 16 : OOB, 0, AUX_NT);
#pragma unroll
      for (int i = 0; i < 24; ++i) *(u32x4*)(lds + L_WO + (w * 64 + (i / 3) * 8 + (lane >> 3)) * 384 + (i % 3) * 128 + (lane & 7) * 16) = wt[i];
      flag_add(lds, F_WO, lane);
      STAMP(4);
    } else if (hw < nwk) {
      const int n = lane & 15;
      int oo = n < 2 ? WS_OX + (b * 18 + (s & 3) * 4 + hw) * 4096 + (kvh * 2 + qh) * 128 + n * 64 + la * 16 : OOB;
      asm volatile("" : "+v"(oo));
      flag_wait<1>(lds, F_WO, S);
      u32x4 wof[2][4];
      {
        typedef __attribute__((address_space(3))) s16x4* lds_s16x4;
        unsigned char* img = lds + L_WO + ((n >> 2) * 8 + la * 2) * 384 + hw * 64 + (lane & 3) * 8;
#pragma unroll
        for (int t = 0; t < 2; ++t)
#pragma unroll
          for (int ks = 0; ks < 4; ++ks) {
            const u32x2 lo = __builtin_bit_cast(u32x2, __builtin_amdgcn_ds_read_tr16_b64_v4i16((lds_s16x4)(img + ks * 32 * 384 + t * 32)));
            const u32x2 hi = __builtin_bit_cast(u32x2, __builtin_amdgcn_ds_read_tr16_b64_v4i16((lds_s16x4)(img + ks * 32 * 384 + t * 32 + 384)));
            wof[t][ks] = u32x4{lo[0], lo[1], hi[0], hi[1]};
          }
      }
      u32x4 ob[4];
#pragma unroll 1
      for (unsigned spin = 0; spin < (SPIN_CAP << 4); ++spin) {
        asm volatile("" ::: "memory");
        const unsigned f = __hip_atomic_load((unsigned*)(lds + L_FLAG) + F_O, __ATOMIC_RELAXED, __HIP_MEMORY_SCOPE_WORKGROUP);
        asm volatile("" ::: "memory");
#pragma unroll
        for (int ks = 0; ks < 4; ++ks) ob[ks] = *(const u32x4*)(lds + L_OA + ks * 64 + la * 16);
        __builtin_amdgcn_sched_barrier(0);
        if (__builtin_amdgcn_readfirstlane(f) != 0u) break;
      }
      STAMP(6);
      f32x4 d0 = {0.f, 0.f, 0.f, 0.f}, d1 = {0.f, 0.f, 0.f, 0.f};
#pragma unroll
      for (int ks = 0; ks < 4; ++ks) { d0 = mfma32(wof[0][ks], ob[ks], d0); d1 = mfma32(wof[1][ks], ob[ks], d1); }
      const f32x4 d = n ? d1 : d0;
      __builtin_amdgcn_raw_buffer_store_b128(
          u32x4{put_lo(d[0], tagA), put_lo(d[1], tagA >> 8), put_lo(d[2], tagA >> 16), put_lo(d[3], tagA >> 24)}, rws, oo, 0, AUX_SC1);
      STAMP(11);
    }
  }
}

}

extern "C" void kernel_launch(void* const* d_in, const int* in_sizes, int n_in, void* d_out, int out_size, void* d_ws,
                              size_t ws_size, hipStream_t stream) {
  (void)in_sizes; (void)n_in; (void)out_size; (void)ws_size;
  fused_decode_kernel<<<dim3(NWG), dim3(NTHR), 0, stream>>>(
      (const unsigned short*)d_in[0], (const unsigned char*)d_in[1], (const unsigned char*)d_in[2],
      (const unsigned short*)d_in[3], (const unsigned short*)d_in[4], (const unsigned short*)d_in[6],
      (const unsigned short*)d_in[7], (const unsigned short*)d_in[8], (const unsigned short*)d_in[9],
      (unsigned short*)d_out, (unsigned char*)d_ws);
}
```

```cpp
#include <hip/hip_runtime.h>
#include <hip/hip_bf16.h>
#include <stdint.h>

namespace {

typedef unsigned u32x2 __attribute__((ext_vector_type(2)));
typedef unsigned u32x4 __attribute__((ext_vector_type(4)));
typedef float f32x2 __attribute__((ext_vector_type(2)));
typedef float f32x4 __attribute__((ext_vector_type(4)));
typedef short s16x4 __attribute__((ext_vector_type(4)));
typedef short s16x8 __attribute__((ext_vector_type(8)));
typedef __bf16 bf16x2 __attribute__((ext_vector_type(2)));
typedef __amdgpu_buffer_rsrc_t rsrc_t;

constexpr int DM = 576, SK = 32768, DH = 128, FF = 4608;
constexpr int NSPLIT = 8, NWG = 256, NWAVE = 8, NTHR = (NWAVE + 1) * 64;
constexpr int KEYS_WG = SK / NSPLIT;
constexpr int TILE_BYTES = 32 * DH * 2;
constexpr int NT = KEYS_WG / 32;
constexpr int SPAN = 2 * KEYS_WG * DH * 2;
constexpr int S = 2, NH = NWAVE - S;
constexpr int NPAIR = (NT / 2 - NH) / S;
constexpr int NITER = 2 * NPAIR - 1;
static_assert(2 * (NPAIR * S + NH) == NT, "every tile has one owner");

#define PRE 0
#define PFD 3
#define DLY0 7
#define DASY 1
#define DLYMAX 12
#define GAIN 3
#define TAU16 1704
#define REF0 90
#define RSV0 200
#define RSVH 25
#define RSVB 100
#define RSVD 3
#define WOL 6
#define CK0 4
#define CKI 16
#define CKTHR 64
#define DGAIN 4
#define PSL 6
#define QPS 2
#define T1D 7
#define T2D 18

constexpr int AUX_NT = 2, AUX_SC1 = 16;
constexpr unsigned RSRC3 = 0x00020000u;
constexpr unsigned SPIN_CAP = 1u << 19;
constexpr int OOB = 0x7ff00000;

constexpr int WS_EPOCH = 0;
constexpr int WS_QX = 4096;
constexpr int WS_CK = WS_QX + 32 * 4096;
constexpr int WS_PX = WS_CK + 32 * 256;
constexpr int WS_OX = WS_PX + NWG * 1024;
constexpr int WS_END = WS_OX + 2 * 18 * 4096;

constexpr int L_FLAG = 0;
constexpr int L_TAG = 52;
constexpr int L_REF = 56;
constexpr int L_CK = 64;
constexpr int L_XN = 96;
constexpr int L_QPART = L_XN + 1280;
constexpr int L_QS = L_QPART + 8 * 64 * 4;
constexpr int L_OA = L_QS + 512;
constexpr int L_YP = L_OA + 256;
constexpr int L_ML = L_YP + 4 * 8 * 4;
constexpr int L_PART = L_ML + NWAVE * 16;
constexpr int L_WO = L_PART + NWAVE * 1024;
constexpr int L_END = L_WO + 128 * 384;
static_assert(NPAIR * 512 <= 64 * 384, "a stream's scores fit its half of the wo image");
constexpr int F_QP = 0, F_Q = 1, F_WP = 2, F_END = 3, F_O = 4, F_Y = 5, F_ARR = 6, F_AR2 = 7, F_CK = 8, F_LAST = 10, F_WO = 12;

constexpr float LOG2E = 1.4426950408889634f;
constexpr float SCALE = 0.08838834764831845f;
constexpr float LN_EPS = 1e-5f;

__device__ __forceinline__ float asf(unsigned u) { return __builtin_bit_cast(float, u); }
__device__ __forceinline__ unsigned asu(float f) { return __builtin_bit_cast(unsigned, f); }
__device__ __forceinline__ float bflo(unsigned u) { return asf(u << 16); }
__device__ __forceinline__ float bfhi(unsigned u) { return asf(u & 0xffff0000u); }
__device__ __forceinline__ unsigned pack_bf16(float lo, float hi) {
  f32x2 v = {lo, hi};
  return __builtin_bit_cast(unsigned, __builtin_convertvector(v, bf16x2));
}
__device__ __forceinline__ float rbf(float f) { return (float)(__bf16)f; }
template <int CTRL> __device__ __forceinline__ unsigned dppu(unsigned x) {
  return (unsigned)__builtin_amdgcn_mov_dpp((int)x, CTRL, 0xf, 0xf, true);
}
template <int CTRL> __device__ __forceinline__ float dppf(float x) { return asf(dppu<CTRL>(asu(x))); }
constexpr int XOR1 = 0xB1, XOR2 = 0x4E, XOR7 = 0x141, ROR4 = 0x124, ROR8 = 0x128;
constexpr int QB0 = 0x00, QB1 = 0x55, QB2 = 0xAA, QB3 = 0xFF;
__device__ __forceinline__ float rows_sum(float x) {
  auto s = __builtin_amdgcn_permlane16_swap(asu(x), asu(x), false, false);
  x = asf(s[0]) + asf(s[1]);
  auto t = __builtin_amdgcn_permlane32_swap(asu(x), asu(x), false, false);
  return asf(t[0]) + asf(t[1]);
}
__device__ __forceinline__ float rows_max(float x) {
  auto s = __builtin_amdgcn_permlane16_swap(asu(x), asu(x), false, false);
  x = fmaxf(asf(s[0]), asf(s[1]));
  auto t = __builtin_amdgcn_permlane32_swap(asu(x), asu(x), false, false);
  return fmaxf(asf(t[0]), asf(t[1]));
}
__device__ __forceinline__ float rows_sum_t(float v0, float v1, float v2, float v3) {
  const auto p0 = __builtin_amdgcn_permlane32_swap(asu(v0), asu(v2), false, false);
  const auto p1 = __builtin_amdgcn_permlane32_swap(asu(v1), asu(v3), false, false);
  const auto p2 = __builtin_amdgcn_permlane16_swap(asu(asf(p0[0]) + asf(p0[1])), asu(asf(p1[0]) + asf(p1[1])), false, false);
  return asf(p2[0]) + asf(p2[1]);
}
__device__ __forceinline__ float wave_sum(float x) {
  x += dppf<XOR1>(x); x += dppf<XOR2>(x); x += dppf<ROR4>(x); x += dppf<ROR8>(x);
  return rows_sum(x);
}
__device__ __forceinline__ float recip(float x) {
  const float r = __builtin_amdgcn_rcpf(x);
  return fmaf(fmaf(-x, r, 1.0f), r, r);
}
__device__ __forceinline__ rsrc_t mkrsrc(const void* p, int bytes) {
  return __builtin_amdgcn_make_buffer_rsrc(const_cast<void*>(p), 0, bytes, RSRC3);
}
__device__ __forceinline__ f32x4 mfma32(u32x4 a, u32x4 b, f32x4 c) {
  return __builtin_amdgcn_mfma_f32_16x16x32_bf16(__builtin_bit_cast(s16x8, a), __builtin_bit_cast(s16x8, b), c, 0, 0, 0);
}
__device__ __forceinline__ f32x4 mfma16(unsigned a0, unsigned a1, unsigned b0, unsigned b1, f32x4 c) {
  u32x2 a = {a0, a1}, b = {b0, b1};
  return __builtin_amdgcn_mfma_f32_16x16x16bf16_1k(__builtin_bit_cast(s16x4, a), __builtin_bit_cast(s16x4, b), c, 0, 0, 0);
}
__device__ __forceinline__ unsigned put_lo(float v, unsigned byte) { return (asu(v) & 0xffffff00u) | (byte & 0xffu); }
__device__ __forceinline__ float cut_lo(unsigned u) { return asf(u & 0xffffff00u); }
__device__ __forceinline__ unsigned now32() { return (unsigned)__builtin_amdgcn_s_memrealtime(); }
__device__ __forceinline__ void wait_until(unsigned t) {
  while ((int)(now32() - t) < 0) __builtin_amdgcn_s_sleep(1);
}
__device__ __forceinline__ int vzero() { int z = 0; asm("" : "+v"(z)); return z; }
__device__ __forceinline__ void flag_add(unsigned char* lds, int f, int lane) {
  asm volatile("" ::: "memory");
  if (lane == 0) __hip_atomic_fetch_add((unsigned*)(lds + L_FLAG) + f + vzero(), 1u, __ATOMIC_RELAXED, __HIP_MEMORY_SCOPE_WORKGROUP);
  asm volatile("" ::: "memory");
}
__device__ __forceinline__ unsigned flag_get(unsigned char* lds, int f) {
  return __builtin_amdgcn_readfirstlane(__hip_atomic_load((unsigned*)(lds + L_FLAG) + f, __ATOMIC_RELAXED, __HIP_MEMORY_SCOPE_WORKGROUP));
}
template <int SLEEP> __device__ __forceinline__ void flag_wait(unsigned char* lds, int f, unsigned n) {
#pragma unroll 1
  for (unsigned spin = 0; spin < (SPIN_CAP << 4); ++spin) {
    if (flag_get(lds, f) >= n) break;
    if (SLEEP) __builtin_amdgcn_s_sleep(SLEEP);
  }
  asm volatile("" ::: "memory");
}
__device__ __forceinline__ unsigned long long sched_get(unsigned char* lds) {
  return __hip_atomic_load((unsigned long long*)(lds + L_REF), __ATOMIC_RELAXED, __HIP_MEMORY_SCOPE_WORKGROUP);
}
__device__ __forceinline__ void ln_stats(const float (&v)[10], bool last_valid, float& mean_b, float& den_b) {
  float s = 0.f;
#pragma unroll
  for (int i = 0; i < 8; ++i) s += v[i];
  s += last_valid ? (v[8] + v[9]) : 0.f;
  const float mean = wave_sum(s) * (1.0f / DM);
  float q = 0.f;
#pragma unroll
  for (int i = 0; i < 8; ++i) { const float d = v[i] - mean; q = fmaf(d, d, q); }
  { const float d8 = v[8] - mean, d9 = v[9] - mean; q += last_valid ? fmaf(d8, d8, d9 * d9) : 0.f; }
  const float var = wave_sum(q) * (1.0f / DM);
  mean_b = rbf(mean);
  den_b = rbf(__builtin_amdgcn_sqrtf(rbf(rbf(var) + LN_EPS)));
}
__device__ __forceinline__ float ln_apply(float x, float mean_b, float den_b) {
  const float d = rbf(x - mean_b), r = __builtin_amdgcn_rcpf(den_b), q = d * r;
  return rbf(fmaf(fmaf(-q, den_b, d), r, q));
}

#define STAMP(i) do { } while (0)

__global__ void __launch_bounds__(NTHR, 3)
fused_decode_kernel(const unsigned short* __restrict__ x, const unsigned char* __restrict__ kc, const unsigned char* __restrict__ vc,
                    const unsigned short* __restrict__ wq, const unsigned short* __restrict__ wo,
                    const unsigned short* __restrict__ w1, const unsigned short* __restrict__ b1,
                    const unsigned short* __restrict__ w2, const unsigned short* __restrict__ b2,
                    unsigned short* __restrict__ out, unsigned char* ws) {
  __shared__ __attribute__((aligned(16))) unsigned char lds[L_END];
  const int lane = threadIdx.x & 63, wv = __builtin_amdgcn_readfirstlane(threadIdx.x >> 6), w = wv < 8 ? wv ^ 4 : wv;
  const unsigned tst = now32();
  const int g = blockIdx.x >> 3, s = (blockIdx.x + 3 * g) & 7, id = g * 8 + s, b = g >> 4, kvh = g & 15;
  STAMP(0);
  const rsrc_t rws = mkrsrc(ws, WS_END);
  const rsrc_t rx = mkrsrc(x + b * DM, DM * 2);
  const int qh = s >> 2, cb = (s + (kvh >> 2)) & 3;
  const int la = lane >> 4;
  const int red = kvh * 8 + s;

  u32x2 ep = {0u, 0u};
  if (w == NWAVE) {
    ep = __builtin_amdgcn_raw_buffer_load_b64(rws, WS_EPOCH + b * 256, 0, 0);
    if (lane < 13) *((unsigned*)(lds + L_FLAG) + lane) = 0u;
    if (lane == 0) *(u32x2*)(lds + L_REF) = u32x2{tst + REF0 + (NWAVE / 2 * TAU16 >> 4), TAU16};
    asm volatile("s_waitcnt lgkmcnt(0)\n\ts_barrier" ::: "memory");
  }
  if (w >= NWAVE) {
    __builtin_amdgcn_s_setprio(3);
    const unsigned e0 = __builtin_amdgcn_readfirstlane(ep[0]);
    const unsigned tagA = ((e0 & 0x7f7f7f7fu) + 0x01010101u) ^ (e0 & 0x80808080u);
    const unsigned tagB = __builtin_amdgcn_readfirstlane(ep[1]) + 1u;
    if (lane == 0) *(unsigned*)(lds + L_TAG) = tagA;
    flag_wait<0>(lds, F_QP, 4);
    {
      f32x2 q2 = *(const f32x2*)(lds + L_QPART + (lane & 31) * 8);
#pragma unroll
      for (int ww = 1; ww < 8; ++ww) q2 += *(const f32x2*)(lds + L_QPART + ww * 256 + (lane & 31) * 8);
      __builtin_amdgcn_raw_buffer_store_b128(u32x4{tagA, asu(q2[0]), tagB, asu(q2[1])}, rws,
                                             lane < 32 ? WS_QX + g * 4096 + qh * 2048 + cb * 512 + lane * 16 : OOB, 0, AUX_SC1);
      STAMP(8);
    }
    __builtin_amdgcn_s_sleep(PSL);
    {
      float qa[4], qb[4];
      int of[4];
#pragma unroll
      for (int i = 0; i < 4; ++i) { qa[i] = 0.f; qb[i] = 0.f; of[i] = WS_QX + g * 4096 + i * 1024 + lane * 16; }
#pragma unroll 1
      for (unsigned spin = 0; spin < SPIN_CAP; ++spin) {
        asm volatile("" ::: "memory");
        u32x4 gr[4];
#pragma unroll
        for (int i = 0; i < 4; ++i) gr[i] = __builtin_amdgcn_raw_buffer_load_b128(rws, of[i], 0, AUX_SC1);
        bool done = true;
#pragma unroll
        for (int i = 0; i < 4; ++i) {
          const bool fresh = (gr[i][0] == tagA) & (gr[i][2] == tagB) & (of[i] != OOB);
          qa[i] = fresh ? asf(gr[i][1]) : qa[i]; qb[i] = fresh ? asf(gr[i][3]) : qb[i];
          of[i] = fresh ? OOB : of[i];
          done &= of[i] == OOB;
        }
        if (__all(done)) break;
        __builtin_amdgcn_s_sleep(QPS);
      }
      *(unsigned*)(lds + L_QS + lane * 4) = pack_bf16(qa[0] + qa[2], qb[0] + qb[2]);
      *(unsigned*)(lds + L_QS + 256 + lane * 4) = pack_bf16(qa[1] + qa[3], qb[1] + qb[3]);
      flag_add(lds, F_Q, lane);
    }
    const int fc = (lane & 1) * 4 + la;
    const unsigned xraw = __builtin_amdgcn_raw_buffer_load_b32(rx, red * 16 + (fc >> 1) * 4, 0, 0);
    const unsigned b2v = __builtin_amdgcn_raw_buffer_load_b16(mkrsrc(b2, 4), 0, 0, 0);
    {
      flag_wait<16>(lds, F_CK + 1, S);
      const u32x2 c0 = *(const u32x2*)(lds + L_CK), c1 = *(const u32x2*)(lds + L_CK + 16);
      const unsigned tm = c0[1] + (unsigned)((int)(c1[1] - c0[1]) >> 1), per = ((c0[1] - c0[0]) + (c1[1] - c1[0])) * 8u / CKI;
      if (__builtin_amdgcn_readfirstlane(per) > (unsigned)(TAU16 + CKTHR)) {
        const int co = WS_CK + g * 256;
        __builtin_amdgcn_raw_buffer_store_b128(u32x4{tagA, tm, tagB, per}, rws, lane == 0 ? co + s * 16 : OOB, 0, AUX_SC1);
        unsigned tr = now32() + 250u;
#pragma unroll 1
        for (int spin = 0; spin < 4; ++spin) {
          wait_until(tr);
          asm volatile("" ::: "memory");
          const u32x4 v = __builtin_amdgcn_raw_buffer_load_b128(rws, co + (lane & 7) * 16, 0, AUX_SC1);
          const bool ok = (v[0] == tagA) & (v[2] == tagB);
          const int cnt = __builtin_popcountll(__ballot(ok)) >> 3;
          if (cnt == 8 || (spin == 3 && cnt >= 6)) {
            float d = ok ? (float)(int)(v[1] - tm) : 0.f, p = ok ? (float)v[3] : 0.f;
            d += dppf<XOR1>(d); d += dppf<XOR2>(d); d += dppf<XOR7>(d);
            p += dppf<XOR1>(p); p += dppf<XOR2>(p); p += dppf<XOR7>(p);
            const float rc = __builtin_amdgcn_rcpf((float)cnt);
            const unsigned tau = min((unsigned)(p * rc) - 4u, (unsigned)TAU16 + 400u);
            if (lane == 0) *(u32x2*)(lds + L_REF) = u32x2{tm + (unsigned)(int)(d * rc) - ((CK0 + CKI) * tau >> 4) - 100u, tau};
            break;
          }
          tr = now32() + 100u;
        }
      }
    }
    if (red == 127) {
#pragma unroll 1
      for (unsigned spin = 0; spin < SPIN_CAP; ++spin) {
        asm volatile("" ::: "memory");
        const u32x4 v0 = __builtin_amdgcn_raw_buffer_load_b128(rws, WS_QX + b * 65536 + lane * 512, 0, AUX_SC1);
        const u32x4 v1 = __builtin_amdgcn_raw_buffer_load_b128(rws, WS_QX + b * 65536 + 32768 + lane * 512, 0, AUX_SC1);
        if (__all((v0[0] == tagA) & (v0[2] == tagB) & (v1[0] == tagA) & (v1[2] == tagB))) break;
        __builtin_amdgcn_s_sleep(16);
      }
      __builtin_amdgcn_raw_buffer_store_b64(u32x2{tagA, tagB}, rws, lane == 0 ? WS_EPOCH + b * 256 : OOB, 0, AUX_SC1);
    }
    float yv = 0.f, xfin = (fc & 1) ? bfhi(xraw) : bflo(xraw);
    const rsrc_t rout = mkrsrc(out + b * DM, DM * 2 + 2);
    int oout = (lane & 14) == 0 ? (red * 8 + fc) * 2 : OOB;
    if (red < 72) {
      flag_wait<16>(lds, F_Y, 4);
#pragma unroll
      for (int ww = 0; ww < 4; ++ww) yv += *(const float*)(lds + L_YP + ww * 32 + fc * 4);
      yv = rbf(rbf(yv) + bflo(b2v));
      if (PRE >= 0) __builtin_amdgcn_raw_buffer_store_b16((unsigned short)0, rout, oout, 0, PRE < 0 ? 0 : PRE);
    }
    int oa = (lane & 8) ? L_QPART + lane * 8 : L_OA + la * 64 + (lane & 7) * 8;
    asm volatile("" : "+v"(yv), "+v"(xfin), "+v"(oout), "+v"(oa));
    flag_wait<16>(lds, F_WP, 1);
    flag_wait<0>(lds, F_END, 1);
    STAMP(5);
    __builtin_amdgcn_s_sleep(T1D);
    {
      u32x4 ga[4];
#pragma unroll
      for (int i = 0; i < 4; ++i) ga[i] = u32x4{0u, 0u, 0u, 0u};
      int of = WS_PX + (g * 8 + (s & 4)) * 1024 + lane * 16;
#pragma unroll 1
      for (unsigned spin = 0; spin < SPIN_CAP; ++spin) {
        asm volatile("" ::: "memory");
        u32x4 gn[4];
#pragma unroll
        for (int i = 0; i < 4; ++i) gn[i] = __builtin_amdgcn_raw_buffer_load_b128(rws, of + i * 1024, 0, AUX_SC1);
        unsigned bad = 0u;
#pragma unroll
        for (int i = 0; i < 4; ++i) bad |= (gn[i][0] ^ tagA) | (gn[i][2] ^ (tagA >> 8));
        bad &= 0xffu;
        bad |= dppu<XOR1>(bad); bad |= dppu<XOR2>(bad); bad |= dppu<XOR7>(bad);
        const bool fresh = (bad == 0u) & (of != OOB);
#pragma unroll
        for (int i = 0; i < 4; ++i) ga[i] = fresh ? gn[i] : ga[i];
        of = fresh ? OOB : of;
        if (__all(of == OOB)) break;
        __builtin_amdgcn_s_sleep(1);
      }
      const unsigned hm = __builtin_amdgcn_perm(ga[0][3], ga[0][1], 0x0c0c0400u) << (16 * (lane & 1));
      const unsigned hl = __builtin_amdgcn_perm(ga[1][3], ga[1][1], 0x0c0c0400u) << (16 * (lane & 1));
      const float pm = asf(hm | dppu<XOR1>(hm)), pl = asf(hl | dppu<XOR1>(hl));
      const float M = rows_max(fmaxf(pm, dppf<ROR8>(pm)));
      const float w0 = __builtin_amdgcn_exp2f((pm - M) * LOG2E) * pl;
      const float inv = recip(rows_sum(w0 + dppf<ROR8>(w0)));
      float v[4];
#pragma unroll
      for (int r = 0; r < 4; ++r) {
        const float t = rows_sum_t(w0 * cut_lo(ga[0][r]), w0 * cut_lo(ga[1][r]), w0 * cut_lo(ga[2][r]), w0 * cut_lo(ga[3][r]));
        v[r] = (t + dppf<ROR8>(t)) * inv;
      }
      *(u32x2*)(lds + oa) = u32x2{pack_bf16(v[0], v[1]), pack_bf16(v[2], v[3])};
      flag_add(lds, F_O, lane);
    }
    if (red < 72) {
      __builtin_amdgcn_s_sleep(T2D);
      u32x4 ga = {0u, 0u, 0u, 0u};
      int of = WS_OX + (b * 18 + (red >> 2)) * 4096 + (lane >> 1) * 128 + (red & 3) * 32 + (lane & 1) * 16;
#pragma unroll 1
      for (unsigned spin = 0; spin < SPIN_CAP; ++spin) {
        asm volatile("" ::: "memory");
        const u32x4 gn = __builtin_amdgcn_raw_buffer_load_b128(rws, of, 0, AUX_SC1);
        unsigned bad = (__builtin_amdgcn_perm(gn[1], gn[0], 0x0c0c0400u) | __builtin_amdgcn_perm(gn[3], gn[2], 0x04000c0cu)) ^ tagA;
        bad |= dppu<XOR1>(bad);
        const bool fresh = (bad == 0u) & (of != OOB);
        ga = fresh ? gn : ga;
        of = fresh ? OOB : of;
        if (__all(of == OOB)) break;
        __builtin_amdgcn_s_sleep(1);
      }
      STAMP(13);
      float t = rows_sum_t(cut_lo(ga[0]), cut_lo(ga[1]), cut_lo(ga[2]), cut_lo(ga[3]));
      t += dppf<XOR2>(t); t += dppf<ROR4>(t); t += dppf<ROR8>(t);
      __builtin_amdgcn_raw_buffer_store_b16((unsigned short)pack_bf16(rbf(xfin + rbf(t)) + yv, 0.f), rout, oout, 0, 0);
    }
  } else {
    const rsrc_t rk = mkrsrc(kc + (size_t)(id >> 1) * SPAN, SPAN);
    const rsrc_t rv = mkrsrc(vc + (size_t)(id >> 1) * SPAN, SPAN);
    auto poff = [&](int d, int h) {
      return ((d & 1) | (h << 1) | ((d & 2) << 1) | ((id & 1) << 3) | ((d >> 2) << 4)) * TILE_BYTES;
    };
    const int voffK = ((lane & 15) >> 2) * 256 + la * 64 + (lane & 3) * 16;
    const int voffV = lane * 16;
    u32x4 bA[8], bB[8];
    auto request = [&](u32x4 (&x)[8], rsrc_t r, int voff, int so) {
      asm volatile("" : "+v"(voff));
#pragma unroll
      for (int u = 0; u < 8; ++u) x[u] = __builtin_amdgcn_raw_buffer_load_b128(r, voff + (u & 3) * 1024, so + (u >> 2) * 4096, AUX_NT);
      __builtin_amdgcn_sched_barrier(0);
    };
    if (w >= 4) {
      const int rg = (w - 4) * 8 + (lane >> 3), c8 = lane & 7;
      unsigned xs[5];
      u32x4 wqv[9];
#pragma unroll
      for (int i = 0; i < 5; ++i) xs[i] = __builtin_amdgcn_raw_buffer_load_b32(rx, (lane + 64 * i) * 4, 0, 0);
      __builtin_amdgcn_sched_barrier(0);
      const rsrc_t rq = mkrsrc(wq + qh * 288 * 4096 + kvh * 256 + cb * 64, (287 * 4096 + 64) * 2);
#pragma unroll
      for (int i = 0; i < 9; ++i) wqv[i] = __builtin_amdgcn_raw_buffer_load_b128(rq, (rg + 32 * i) * 8192 + c8 * 16, 0, AUX_NT);
      __builtin_amdgcn_sched_barrier(0);
      __builtin_amdgcn_s_barrier();
      __builtin_amdgcn_sched_barrier(0);
      float xv[10], mean1, den1;
#pragma unroll
      for (int i = 0; i < 5; ++i) { xv[2 * i] = bflo(xs[i]); xv[2 * i + 1] = bfhi(xs[i]); }
      ln_stats(xv, lane < 32, mean1, den1);
#pragma unroll
      for (int i = 0; i < 5; ++i)
        *(unsigned*)(lds + L_XN + (lane + 64 * i) * 4) = pack_bf16(ln_apply(xv[2 * i], mean1, den1), ln_apply(xv[2 * i + 1], mean1, den1));
      asm volatile("" ::: "memory");
      STAMP(9);
      f32x4 qa = {0.f, 0.f, 0.f, 0.f};
#pragma unroll
      for (int i = 0; i < 9; ++i) {
        const unsigned t = (unsigned)*(const unsigned short*)(lds + L_XN + (qh * 288 + rg + 32 * i) * 2) << (16 * (lane & 1));
        const int jp = c8 >> 1;
        qa = mfma32(wqv[i], u32x4{jp == 0 ? t : 0u, jp == 1 ? t : 0u, jp == 2 ? t : 0u, jp == 3 ? t : 0u}, qa);
      }
      if ((lane >> 5) == ((lane >> 3) & 1)) {
        float* qp = (float*)(lds + L_QPART) + ((w - 4) * 2 + (lane >> 5)) * 64 + (la & 1) * 32 + c8;
#pragma unroll
        for (int r = 0; r < 4; ++r) qp[r * 8] = qa[r];
      }
      flag_add(lds, F_QP, lane);
      STAMP(1);
    }
    if (w < 4) asm volatile("s_barrier" ::: "memory");
    if (w >= PFD) flag_wait<1>(lds, F_ARR, w - PFD + 1);
    __builtin_amdgcn_sched_barrier(0);
    STAMP(14);
    request(bA, rk, voffK, poff(w, 0));
    request(bB, rk, voffK, poff(w, 1));
    asm volatile("s_waitcnt vmcnt(0)" ::: "memory");
    flag_add(lds, F_ARR, lane);
    STAMP(15);
    flag_wait<1>(lds, F_Q, 1);
    if (w < S) STAMP(2);

    const int c = lane & 3, hb = (lane >> 2) & 1, tb = (lane >> 3) & 1;
    const unsigned tagA = __builtin_amdgcn_readfirstlane(*(const unsigned*)(lds + L_TAG));
    float m, l = 0.f;
    f32x4 acc[2][2];
#pragma unroll
    for (int i = 0; i < 2; ++i)
#pragma unroll
      for (int j = 0; j < 2; ++j) acc[i][j] = f32x4{0.f, 0.f, 0.f, 0.f};
    const u32x4 qf = *(const u32x4*)(lds + L_QS + (hb * 128 + la * 32 + c * 8) * 2);
    const u32x4 zero4 = {0u, 0u, 0u, 0u};
    const u32x4 q0 = tb ? zero4 : qf, q1 = tb ? qf : zero4;
    const unsigned mask0 = c == 0 ? 0x0000ffffu : c == 1 ? 0xffff0000u : 0u;
    const unsigned mask1 = c == 2 ? 0x0000ffffu : c == 3 ? 0xffff0000u : 0u;
    auto qk = [&](const u32x4 (&x)[8], f32x4 (&sc)[4]) {
#pragma unroll
      for (int i = 0; i < 4; ++i) sc[i] = mfma32(x[i], q0, f32x4{0.f, 0.f, 0.f, 0.f});
#pragma unroll
      for (int i = 0; i < 4; ++i) sc[i] = mfma32(x[4 + i], q1, sc[i]);
    };
    auto score = [&](const f32x4 (&sc)[4]) {
      float t4[4];
#pragma unroll
      for (int i = 0; i < 4; ++i) t4[i] = (dppf<QB0>(sc[i][0]) + dppf<QB1>(sc[i][1])) + (dppf<QB2>(sc[i][2]) + dppf<QB3>(sc[i][3]));
      const float raw = (c & 2) ? ((c & 1) ? t4[3] : t4[2]) : ((c & 1) ? t4[1] : t4[0]);
      float sv = rbf(rbf(raw) * SCALE);
      asm volatile("" : "+v"(sv));
      return sv;
    };
    auto head_max = [&](float v) {
      v = fmaxf(v, dppf<XOR1>(v));
      v = fmaxf(v, dppf<XOR2>(v));
      v = fmaxf(v, dppf<ROR8>(v));
      return rows_max(v);
    };
    auto pv = [&](const u32x4 (&x)[8], float sv) {
      const float p = __builtin_amdgcn_exp2f((sv - m) * LOG2E);
      l += p;
      const unsigned pk = pack_bf16(p, p);
      unsigned pb0[4], pb1[4];
      pb0[0] = dppu<QB0>(pk) & mask0; pb1[0] = dppu<QB0>(pk) & mask1;
      pb0[1] = dppu<QB1>(pk) & mask0; pb1[1] = dppu<QB1>(pk) & mask1;
      pb0[2] = dppu<QB2>(pk) & mask0; pb1[2] = dppu<QB2>(pk) & mask1;
      pb0[3] = dppu<QB3>(pk) & mask0; pb1[3] = dppu<QB3>(pk) & mask1;
#pragma unroll
      for (int u = 0; u < 8; ++u) {
        acc[u >> 2][0] = mfma16(x[u][0], x[u][1], pb0[u & 3], pb1[u & 3], acc[u >> 2][0]);
        acc[u >> 2][1] = mfma16(x[u][2], x[u][3], pb0[u & 3], pb1[u & 3], acc[u >> 2][1]);
      }
    };
    auto hold = [&](int dly) {
      __builtin_amdgcn_sched_barrier(0);
      for (int i = 0; i < dly; ++i) __builtin_amdgcn_s_sleep(1);
      __builtin_amdgcn_sched_barrier(0);
    };
    const int hw = w - S, nwk = (s & 3) == 3 ? 6 : 4;
    f32x2 sl;
    if (w < S) {
      __builtin_amdgcn_s_setprio(2);
      auto pace = [&](int it) {
        const unsigned now = now32();
        const unsigned long long rf = sched_get(lds);
        const int tau = __builtin_amdgcn_readfirstlane((unsigned)(rf >> 32));
        const int lead = (int)(__builtin_amdgcn_readfirstlane((unsigned)rf) + (unsigned)(it * tau >> 4) - now);
        const int dadd = (tau - TAU16) * DGAIN >> 8;
        if (it == CK0 || it == CK0 + CKI) {
          *(unsigned*)(lds + L_CK + w * 16 + (it > CK0) * 4) = now;
          flag_add(lds, F_CK + (it > CK0), lane);
        }
        return min(max(DLY0 + dadd + ((lead * GAIN + 128) >> 8), 0), DLYMAX + dadd);
      };
      unsigned char* sco = lds + L_WO + w * (64 * 384) + lane * 8;
      flag_wait<1>(lds, F_ARR, NWAVE + w - PFD + 1);
      float mx = -INFINITY;
      for (int j = 0; j < NPAIR; ++j) {
        const int dly = pace(j) + DASY;
        const bool k = j + 1 < NPAIR;
        const rsrc_t r = k ? rk : rv;
        const int vo = k ? voffK : voffV, d = NWAVE + S * (k ? j : j - 1) + w;
        f32x4 sc[4];
        qk(bA, sc);
        hold(dly);
        request(bA, r, vo, poff(d, 0));
        const float s0 = score(sc);
        qk(bB, sc);
        hold(dly);
        request(bB, r, vo, poff(d, 1));
        const float s1 = score(sc);
        mx = fmaxf(mx, fmaxf(s0, s1));
        *(f32x2*)(sco + j * 512) = f32x2{s0, s1};
      }
      m = head_max(mx);
      for (int j = 0; j + 1 < NPAIR; ++j) {
        const f32x2 sv = *(const f32x2*)(sco + (NPAIR - 1 - j) * 512);
        const int dly = pace(NPAIR + j) - DASY;
        const int d = j + 2 < NPAIR ? NWAVE + S * (NPAIR - 3 - j) + w : w;
        pv(bA, sv[0]);
        hold(dly);
        request(bA, rv, voffV, poff(d, 0));
        pv(bB, sv[1]);
        hold(dly);
        request(bB, rv, voffV, poff(d, 1));
      }
      flag_add(lds, F_LAST + w, lane);
      sl = *(const f32x2*)sco;
    }
    asm volatile("" ::: "memory");
    if (w >= S) {
      {
        f32x4 sc[4];
        qk(bA, sc);
        sl[0] = score(sc);
        qk(bB, sc);
        sl[1] = score(sc);
        m = head_max(fmaxf(sl[0], sl[1]));
      }
      STAMP(12);
      if (w >= 4 && red < 72) {
        const int ht = (w - 4) * 64 + lane;
        unsigned a1[9], ab[9], a2[9];
        const rsrc_t r1 = mkrsrc(w1, FF * 2), rb = mkrsrc(b1, FF * 2), r2 = mkrsrc(w2, FF * 2);
#pragma unroll
        for (int i = 0; i < 9; ++i) {
          a1[i] = __builtin_amdgcn_raw_buffer_load_b32(r1, (ht + 256 * i) * 4, 0, 0);
          ab[i] = __builtin_amdgcn_raw_buffer_load_b32(rb, (ht + 256 * i) * 4, 0, 0);
          a2[i] = __builtin_amdgcn_raw_buffer_load_b32(r2, (ht + 256 * i) * 4, 0, 0);
        }
        __builtin_amdgcn_sched_barrier(0);
        float xn[10];
#pragma unroll
        for (int i = 0; i < 5; ++i) { const unsigned v = *(const unsigned*)(lds + L_XN + (lane + 64 * i) * 4); xn[2 * i] = bflo(v); xn[2 * i + 1] = bfhi(v); }
        float mean2, den2;
        ln_stats(xn, lane < 32, mean2, den2);
        constexpr float GC0 = -2.0f * 0.7978845608028654f * LOG2E, GC1 = GC0 * 0.044715f;
#pragma unroll 1
        for (int j = 0; j < 8; ++j) {
          const float hv = ln_apply(bflo(*(const unsigned short*)(lds + L_XN + (red * 8 + j) * 2)), mean2, den2);
          float y = 0.f;
#pragma unroll
          for (int i = 0; i < 9; ++i) {
            asm volatile("" : "+v"(a1[i]), "+v"(ab[i]), "+v"(a2[i]));
            const unsigned pr = pack_bf16(hv * bflo(a1[i]), hv * bfhi(a1[i]));
            const unsigned uu = pack_bf16(bflo(pr) + bflo(ab[i]), bfhi(pr) + bfhi(ab[i]));
            const float u0 = bflo(uu), u1 = bfhi(uu);
            const float e0 = __builtin_amdgcn_exp2f(u0 * fmaf(u0 * u0, GC1, GC0));
            const float e1 = __builtin_amdgcn_exp2f(u1 * fmaf(u1 * u1, GC1, GC0));
            const unsigned gg = pack_bf16(u0 * __builtin_amdgcn_rcpf(1.0f + e0), u1 * __builtin_amdgcn_rcpf(1.0f + e1));
            y = fmaf(bflo(gg), bflo(a2[i]), y);
            y = fmaf(bfhi(gg), bfhi(a2[i]), y);
          }
          y = wave_sum(y);
          if (lane == 0) *(float*)(lds + L_YP + (w - 4) * 32 + j * 4) = y;
        }
        flag_add(lds, F_Y, lane);
      }
      if (hw < RSVD) {
        bool seen = false;
        unsigned tseen = 0;
#pragma unroll 1
        for (unsigned spin = 0; spin < (SPIN_CAP << 2); ++spin) {
          const unsigned long long rf = sched_get(lds);
          const unsigned due = __builtin_amdgcn_readfirstlane((unsigned)rf + ((2 * NITER + hw) * (unsigned)(rf >> 32) >> 5)) + RSV0;
          const unsigned now = now32();
          const int late = (int)(now - due);
          if (!seen && flag_get(lds, F_LAST + (hw & 1)) != 0u) { seen = true; tseen = now; }
          if (late >= 0 || (seen && (late >= -RSVH || (int)(now - tseen) >= RSVB))) break;
          __builtin_amdgcn_s_sleep(2);
        }
      } else {
        flag_wait<2>(lds, F_AR2, hw - RSVD + 1);
      }
      __builtin_amdgcn_s_setprio(2);
      __builtin_amdgcn_sched_barrier(0);
      request(bA, rv, voffV, poff(w, 0));
      request(bB, rv, voffV, poff(w, 1));
      if (hw < NH - RSVD) {
        asm volatile("s_waitcnt vmcnt(0)" ::: "memory");
        flag_add(lds, F_AR2, lane);
      }
    }
    int po = WS_PX + ((g * 8 + hb * 4 + la) * 8 + s) * 128 + (tb * 4 + c) * 16;
    asm volatile("" : "+v"(po));
    pv(bA, sl[0]);
    pv(bB, sl[1]);
    STAMP(3);
    {
      f32x4 res;
#pragma unroll
      for (int r = 0; r < 4; ++r) {
        const float own = tb ? acc[1][1][r] : acc[0][0][r], snd = tb ? acc[1][0][r] : acc[0][1][r];
        res[r] = own + dppf<ROR8>(snd);
      }
      l += dppf<XOR1>(l);
      l += dppf<XOR2>(l);
      l += dppf<ROR8>(l);
      l = rows_sum(l);
      *(f32x4*)(lds + L_PART + w * 1024 + lane * 16) = res;
      if ((lane & ~4) == 0) *(f32x2*)(lds + L_ML + (w * 2 + hb) * 8) = f32x2{m, l};
      asm volatile("" ::: "memory");
      unsigned old = 0;
      if (lane == 0) old = __hip_atomic_fetch_add((unsigned*)(lds + L_FLAG) + F_WP + vzero(), 1u, __ATOMIC_RELAXED, __HIP_MEMORY_SCOPE_WORKGROUP);
      asm volatile("" ::: "memory");
      f32x2 ml[NWAVE];
      f32x4 pp[NWAVE];
#pragma unroll
      for (int k = 0; k < NWAVE; ++k) {
        ml[k] = *(const f32x2*)(lds + L_ML + (k * 2 + hb) * 8);
        pp[k] = *(const f32x4*)(lds + L_PART + k * 1024 + lane * 16);
      }
      __builtin_amdgcn_sched_barrier(0);
      if (__builtin_amdgcn_readfirstlane(old) == NWAVE - 1) {
        float mx = ml[0][0];
#pragma unroll
        for (int k = 1; k < NWAVE; ++k) mx = fmaxf(mx, ml[k][0]);
        f32x4 sum = {0.f, 0.f, 0.f, 0.f};
        float L = 0.f;
#pragma unroll
        for (int k = 0; k < NWAVE; ++k) {
          const float be = __builtin_amdgcn_exp2f((ml[k][0] - mx) * LOG2E);
          sum += be * pp[k]; L = fmaf(be, ml[k][1], L);
        }
        const float inv = recip(L);
        const unsigned side = asu((la & 1) ? L : mx) >> (16 * (c & 1));
        __builtin_amdgcn_raw_buffer_store_b128(
            u32x4{put_lo(sum[0] * inv, tagA), put_lo(sum[1] * inv, side), put_lo(sum[2] * inv, tagA >> 8), put_lo(sum[3] * inv, side >> 8)},
            rws, po, 0, AUX_SC1);
        flag_add(lds, F_END, lane);
        STAMP(10);
      }
    }
    if (w < S) {
      flag_wait<1>(lds, F_WP, WOL);
      const rsrc_t ro = mkrsrc(wo + (size_t)(kvh * 256 + qh * 128 + w * 64) * DM, 64 * DM * 2);
      u32x4 wt[24];
#pragma unroll
      for (int i = 0; i < 24; ++i)
        wt[i] = __builtin_amdgcn_raw_buffer_load_b128(
            ro, 2 * (i % 3) < nwk ? ((i / 3) * 8 + (lane >> 3)) * (DM * 2) + (s & 3) * 256 + (i % 3) * 128 + (lane & 7) * 16 : OOB, 0, AUX_NT);
#pragma unroll
      for (int i = 0; i < 24; ++i) *(u32x4*)(lds + L_WO + (w * 64 + (i / 3) * 8 + (lane >> 3)) * 384 + (i % 3) * 128 + (lane & 7) * 16) = wt[i];
      flag_add(lds, F_WO, lane);
      STAMP(4);
    } else if (hw < nwk) {
      const int n = lane & 15;
      int oo = n < 2 ? WS_OX + (b * 18 + (s & 3) * 4 + hw) * 4096 + (kvh * 2 + qh) * 128 + n * 64 + la * 16 : OOB;
      asm volatile("" : "+v"(oo));
      flag_wait<1>(lds, F_WO, S);
      u32x4 wof[2][4];
      {
        typedef __attribute__((address_space(3))) s16x4* lds_s16x4;
        unsigned char* img = lds + L_WO + ((n >> 2) * 8 + la * 2) * 384 + hw * 64 + (lane & 3) * 8;
#pragma unroll
        for (int t = 0; t < 2; ++t)
#pragma unroll
          for (int ks = 0; ks < 4; ++ks) {
            const u32x2 lo = __builtin_bit_cast(u32x2, __builtin_amdgcn_ds_read_tr16_b64_v4i16((lds_s16x4)(img + ks * 32 * 384 + t * 32)));
            const u32x2 hi = __builtin_bit_cast(u32x2, __builtin_amdgcn_ds_read_tr16_b64_v4i16((lds_s16x4)(img + ks * 32 * 384 + t * 32 + 384)));
            wof[t][ks] = u32x4{lo[0], lo[1], hi[0], hi[1]};
          }
      }
      u32x4 ob[4];
#pragma unroll 1
      for (unsigned spin = 0; spin < (SPIN_CAP << 4); ++spin) {
        asm volatile("" ::: "memory");
        const unsigned f = __hip_atomic_load((unsigned*)(lds + L_FLAG) + F_O, __ATOMIC_RELAXED, __HIP_MEMORY_SCOPE_WORKGROUP);
        asm volatile("" ::: "memory");
#pragma unroll
        for (int ks = 0; ks < 4; ++ks) ob[ks] = *(const u32x4*)(lds + L_OA + ks * 64 + la * 16);
        __builtin_amdgcn_sched_barrier(0);
        if (__builtin_amdgcn_readfirstlane(f) != 0u) break;
      }
      STAMP(6);
      f32x4 d0 = {0.f, 0.f, 0.f, 0.f}, d1 = {0.f, 0.f, 0.f, 0.f};
#pragma unroll
      for (int ks = 0; ks < 4; ++ks) { d0 = mfma32(wof[0][ks], ob[ks], d0); d1 = mfma32(wof[1][ks], ob[ks], d1); }
      const f32x4 d = n ? d1 : d0;
      __builtin_amdgcn_raw_buffer_store_b128(
          u32x4{put_lo(d[0], tagA), put_lo(d[1], tagA >> 8), put_lo(d[2], tagA >> 16), put_lo(d[3], tagA >> 24)}, rws, oo, 0, AUX_SC1);
      STAMP(11);
    }
  }
}

}

extern "C" void kernel_launch(void* const* d_in, const int* in_sizes, int n_in, void* d_out, int out_size, void* d_ws,
                              size_t ws_size, hipStream_t stream) {
  (void)in_sizes; (void)n_in; (void)out_size; (void)ws_size;
  fused_decode_kernel<<<dim3(NWG), dim3(NTHR), 0, stream>>>(
      (const unsigned short*)d_in[0], (const unsigned char*)d_in[1], (const unsigned char*)d_in[2],
      (const unsigned short*)d_in[3], (const unsigned short*)d_in[4], (const unsigned short*)d_in[6],
      (const unsigned short*)d_in[7], (const unsigned short*)d_in[8], (const unsigned short*)d_in[9],
      (unsigned short*)d_out, (unsigned char*)d_ws);
}
```

```cpp
#include <hip/hip_runtime.h>
#include <hip/hip_bf16.h>
#include <stdint.h>

#define DIAG_M 2
#define DIAG_WG 0
namespace {

typedef unsigned u32x2 __attribute__((ext_vector_type(2)));
typedef unsigned u32x4 __attribute__((ext_vector_type(4)));
typedef float f32x2 __attribute__((ext_vector_type(2)));
typedef float f32x4 __attribute__((ext_vector_type(4)));
typedef short s16x4 __attribute__((ext_vector_type(4)));
typedef short s16x8 __attribute__((ext_vector_type(8)));
typedef __bf16 bf16x2 __attribute__((ext_vector_type(2)));
typedef __amdgpu_buffer_rsrc_t rsrc_t;

constexpr int DM = 576, SK = 32768, DH = 128, FF = 4608;
constexpr int NSPLIT = 8, NWG = 256, NWAVE = 8, NTHR = (NWAVE + 1) * 64;
constexpr int KEYS_WG = SK / NSPLIT;
constexpr int TILE_BYTES = 32 * DH * 2;
constexpr int NT = KEYS_WG / 32;
constexpr int SPAN = 2 * KEYS_WG * DH * 2;
constexpr int S = 2, NH = NWAVE - S;
constexpr int NPAIR = (NT / 2 - NH) / S;
constexpr int NITER = 2 * NPAIR - 1;
static_assert(2 * (NPAIR * S + NH) == NT, "every tile has one owner");

#define PRE 0
#define PFD 3
#define DLY0 7
#define DASY 1
#define DLYMAX 12
#define GAIN 3
#define TAU16 1704
#define REF0 90
#define RSV0 200
#define RSVH 25
#define RSVB 100
#define RSVD 3
#define WOL 6
#define CK0 4
#define CKI 16
#define CKTHR 64
#define DGAIN 4
#define PSL 6
#define QPS 2
#define T1D 7
#define T2D 18

constexpr int AUX_NT = 2, AUX_SC1 = 16;
constexpr unsigned RSRC3 = 0x00020000u;
constexpr unsigned SPIN_CAP = 1u << 19;
constexpr int OOB = 0x7ff00000;

constexpr int WS_EPOCH = 0;
constexpr int WS_QX = 4096;
constexpr int WS_CK = WS_QX + 32 * 4096;
constexpr int WS_PX = WS_CK + 32 * 256;
constexpr int WS_OX = WS_PX + NWG * 1024;
constexpr int WS_END = WS_OX + 2 * 18 * 4096;

constexpr int L_FLAG = 0;
constexpr int L_TAG = 52;
constexpr int L_REF = 56;
constexpr int L_CK = 64;
constexpr int L_XN = 96;
constexpr int L_QPART = L_XN + 1280;
constexpr int L_QS = L_QPART + 8 * 64 * 4;
constexpr int L_OA = L_QS + 512;
constexpr int L_YP = L_OA + 256;
constexpr int L_ML = L_YP + 4 * 8 * 4;
constexpr int L_PART = L_ML + NWAVE * 16;
constexpr int L_WO = L_PART + NWAVE * 1024;
constexpr int L_END = L_WO + 128 * 384;
static_assert(NPAIR * 512 <= 64 * 384, "a stream's scores fit its half of the wo image");
constexpr int F_QP = 0, F_Q = 1, F_WP = 2, F_END = 3, F_O = 4, F_Y = 5, F_ARR = 6, F_AR2 = 7, F_CK = 8, F_LAST = 10, F_WO = 12;

constexpr float LOG2E = 1.4426950408889634f;
constexpr float SCALE = 0.08838834764831845f;
constexpr float LN_EPS = 1e-5f;

__device__ __forceinline__ float asf(unsigned u) { return __builtin_bit_cast(float, u); }
__device__ __forceinline__ unsigned asu(float f) { return __builtin_bit_cast(unsigned, f); }
__device__ __forceinline__ float bflo(unsigned u) { return asf(u << 16); }
__device__ __forceinline__ float bfhi(unsigned u) { return asf(u & 0xffff0000u); }
__device__ __forceinline__ unsigned pack_bf16(float lo, float hi) {
  f32x2 v = {lo, hi};
  return __builtin_bit_cast(unsigned, __builtin_convertvector(v, bf16x2));
}
__device__ __forceinline__ float rbf(float f) { return (float)(__bf16)f; }
template <int CTRL> __device__ __forceinline__ unsigned dppu(unsigned x) {
  return (unsigned)__builtin_amdgcn_mov_dpp((int)x, CTRL, 0xf, 0xf, true);
}
template <int CTRL> __device__ __forceinline__ float dppf(float x) { return asf(dppu<CTRL>(asu(x))); }
constexpr int XOR1 = 0xB1, XOR2 = 0x4E, XOR7 = 0x141, ROR4 = 0x124, ROR8 = 0x128;
constexpr int QB0 = 0x00, QB1 = 0x55, QB2 = 0xAA, QB3 = 0xFF;
__device__ __forceinline__ float rows_sum(float x) {
  auto s = __builtin_amdgcn_permlane16_swap(asu(x), asu(x), false, false);
  x = asf(s[0]) + asf(s[1]);
  auto t = __builtin_amdgcn_permlane32_swap(asu(x), asu(x), false, false);
  return asf(t[0]) + asf(t[1]);
}
__device__ __forceinline__ float rows_max(float x) {
  auto s = __builtin_amdgcn_permlane16_swap(asu(x), asu(x), false, false);
  x = fmaxf(asf(s[0]), asf(s[1]));
  auto t = __builtin_amdgcn_permlane32_swap(asu(x), asu(x), false, false);
  return fmaxf(asf(t[0]), asf(t[1]));
}
__device__ __forceinline__ float rows_sum_t(float v0, float v1, float v2, float v3) {
  const auto p0 = __builtin_amdgcn_permlane32_swap(asu(v0), asu(v2), false, false);
  const auto p1 = __builtin_amdgcn_permlane32_swap(asu(v1), asu(v3), false, false);
  const auto p2 = __builtin_amdgcn_permlane16_swap(asu(asf(p0[0]) + asf(p0[1])), asu(asf(p1[0]) + asf(p1[1])), false, false);
  return asf(p2[0]) + asf(p2[1]);
}
__device__ __forceinline__ float wave_sum(float x) {
  x += dppf<XOR1>(x); x += dppf<XOR2>(x); x += dppf<ROR4>(x); x += dppf<ROR8>(x);
  return rows_sum(x);
}
__device__ __forceinline__ float recip(float x) {
  const float r = __builtin_amdgcn_rcpf(x);
  return fmaf(fmaf(-x, r, 1.0f), r, r);
}
__device__ __forceinline__ rsrc_t mkrsrc(const void* p, int bytes) {
  return __builtin_amdgcn_make_buffer_rsrc(const_cast<void*>(p), 0, bytes, RSRC3);
}
__device__ __forceinline__ f32x4 mfma32(u32x4 a, u32x4 b, f32x4 c) {
  return __builtin_amdgcn_mfma_f32_16x16x32_bf16(__builtin_bit_cast(s16x8, a), __builtin_bit_cast(s16x8, b), c, 0, 0, 0);
}
__device__ __forceinline__ f32x4 mfma16(unsigned a0, unsigned a1, unsigned b0, unsigned b1, f32x4 c) {
  u32x2 a = {a0, a1}, b = {b0, b1};
  return __builtin_amdgcn_mfma_f32_16x16x16bf16_1k(__builtin_bit_cast(s16x4, a), __builtin_bit_cast(s16x4, b), c, 0, 0, 0);
}
__device__ __forceinline__ unsigned put_lo(float v, unsigned byte) { return (asu(v) & 0xffffff00u) | (byte & 0xffu); }
__device__ __forceinline__ float cut_lo(unsigned u) { return asf(u & 0xffffff00u); }
__device__ __forceinline__ unsigned now32() { return (unsigned)__builtin_amdgcn_s_memrealtime(); }
__device__ __forceinline__ void wait_until(unsigned t) {
  while ((int)(now32() - t) < 0) __builtin_amdgcn_s_sleep(1);
}
__device__ __forceinline__ int vzero() { int z = 0; asm("" : "+v"(z)); return z; }
__device__ __forceinline__ void flag_add(unsigned char* lds, int f, int lane) {
  asm volatile("" ::: "memory");
  if (lane == 0) __hip_atomic_fetch_add((unsigned*)(lds + L_FLAG) + f + vzero(), 1u, __ATOMIC_RELAXED, __HIP_MEMORY_SCOPE_WORKGROUP);
  asm volatile("" ::: "memory");
}
__device__ __forceinline__ unsigned flag_get(unsigned char* lds, int f) {
  return __builtin_amdgcn_readfirstlane(__hip_atomic_load((unsigned*)(lds + L_FLAG) + f, __ATOMIC_RELAXED, __HIP_MEMORY_SCOPE_WORKGROUP));
}
template <int SLEEP> __device__ __forceinline__ void flag_wait(unsigned char* lds, int f, unsigned n) {
#pragma unroll 1
  for (unsigned spin = 0; spin < (SPIN_CAP << 4); ++spin) {
    if (flag_get(lds, f) >= n) break;
    if (SLEEP) __builtin_amdgcn_s_sleep(SLEEP);
  }
  asm volatile("" ::: "memory");
}
__device__ __forceinline__ unsigned long long sched_get(unsigned char* lds) {
  return __hip_atomic_load((unsigned long long*)(lds + L_REF), __ATOMIC_RELAXED, __HIP_MEMORY_SCOPE_WORKGROUP);
}
__device__ __forceinline__ void ln_stats(const float (&v)[10], bool last_valid, float& mean_b, float& den_b) {
  float s = 0.f;
#pragma unroll
  for (int i = 0; i < 8; ++i) s += v[i];
  s += last_valid ? (v[8] + v[9]) : 0.f;
  const float mean = wave_sum(s) * (1.0f / DM);
  float q = 0.f;
#pragma unroll
  for (int i = 0; i < 8; ++i) { const float d = v[i] - mean; q = fmaf(d, d, q); }
  { const float d8 = v[8] - mean, d9 = v[9] - mean; q += last_valid ? fmaf(d8, d8, d9 * d9) : 0.f; }
  const float var = wave_sum(q) * (1.0f / DM);
  mean_b = rbf(mean);
  den_b = rbf(__builtin_amdgcn_sqrtf(rbf(rbf(var) + LN_EPS)));
}
__device__ __forceinline__ float ln_apply(float x, float mean_b, float den_b) {
  const float d = rbf(x - mean_b), r = __builtin_amdgcn_rcpf(den_b), q = d * r;
  return rbf(fmaf(fmaf(-q, den_b, d), r, q));
}

#define STAMP(i) do { } while (0)

__global__ void __launch_bounds__(NTHR, 3)
fused_decode_kernel(const unsigned short* __restrict__ x, const unsigned char* __restrict__ kc, const unsigned char* __restrict__ vc,
                    const unsigned short* __restrict__ wq, const unsigned short* __restrict__ wo,
                    const unsigned short* __restrict__ w1, const unsigned short* __restrict__ b1,
                    const unsigned short* __restrict__ w2, const unsigned short* __restrict__ b2,
                    unsigned short* __restrict__ out, unsigned char* ws) {
  __shared__ __attribute__((aligned(16))) unsigned char lds[L_END];
  const int lane = threadIdx.x & 63, wv = __builtin_amdgcn_readfirstlane(threadIdx.x >> 6), w = wv < 8 ? wv ^ 4 : wv;
  const unsigned tst = now32();
  const int g = blockIdx.x >> 3, s = (blockIdx.x + 3 * g) & 7, id = g * 8 + s, b = g >> 4, kvh = g & 15;
  STAMP(0);
  const rsrc_t rws = mkrsrc(ws, WS_END);
  const rsrc_t rx = mkrsrc(x + b * DM, DM * 2);
  const int qh = s >> 2, cb = (s + (kvh >> 2)) & 3;
  const int la = lane >> 4;
  const int red = kvh * 8 + s;

  u32x2 ep = {0u, 0u};
  if (w == NWAVE) {
    ep = __builtin_amdgcn_raw_buffer_load_b64(rws, WS_EPOCH + b * 256, 0, 0);
    if (lane < 13) *((unsigned*)(lds + L_FLAG) + lane) = 0u;
    if (lane == 0) *(u32x2*)(lds + L_REF) = u32x2{tst + REF0 + (NWAVE / 2 * TAU16 >> 4), TAU16};
    asm volatile("s_waitcnt lgkmcnt(0)\n\ts_barrier" ::: "memory");
  }
  if (w >= NWAVE) {
    __builtin_amdgcn_s_setprio(3);
    const unsigned e0 = __builtin_amdgcn_readfirstlane(ep[0]);
    const unsigned tagA = ((e0 & 0x7f7f7f7fu) + 0x01010101u) ^ (e0 & 0x80808080u);
    const unsigned tagB = __builtin_amdgcn_readfirstlane(ep[1]) + 1u;
    if (lane == 0) *(unsigned*)(lds + L_TAG) = tagA;
    flag_wait<0>(lds, F_QP, 4);
    {
      f32x2 q2 = *(const f32x2*)(lds + L_QPART + (lane & 31) * 8);
#pragma unroll
      for (int ww = 1; ww < 8; ++ww) q2 += *(const f32x2*)(lds + L_QPART + ww * 256 + (lane & 31) * 8);
      __builtin_amdgcn_raw_buffer_store_b128(u32x4{tagA, asu(q2[0]), tagB, asu(q2[1])}, rws,
                                             lane < 32 ? WS_QX + g * 4096 + qh * 2048 + cb * 512 + lane * 16 : OOB, 0, AUX_SC1);
      STAMP(8);
    }
    __builtin_amdgcn_s_sleep(PSL);
    {
      float qa[4], qb[4];
      int of[4];
#pragma unroll
      for (int i = 0; i < 4; ++i) { qa[i] = 0.f; qb[i] = 0.f; of[i] = WS_QX + g * 4096 + i * 1024 + lane * 16; }
#pragma unroll 1
      for (unsigned spin = 0; spin < SPIN_CAP; ++spin) {
        asm volatile("" ::: "memory");
        u32x4 gr[4];
#pragma unroll
        for (int i = 0; i < 4; ++i) gr[i] = __builtin_amdgcn_raw_buffer_load_b128(rws, of[i], 0, AUX_SC1);
        bool done = true;
#pragma unroll
        for (int i = 0; i < 4; ++i) {
          const bool fresh = (gr[i][0] == tagA) & (gr[i][2] == tagB) & (of[i] != OOB);
          qa[i] = fresh ? asf(gr[i][1]) : qa[i]; qb[i] = fresh ? asf(gr[i][3]) : qb[i];
          of[i] = fresh ? OOB : of[i];
          done &= of[i] == OOB;
        }
        if (__all(done)) break;
        __builtin_amdgcn_s_sleep(QPS);
      }
      *(unsigned*)(lds + L_QS + lane * 4) = pack_bf16(qa[0] + qa[2], qb[0] + qb[2]);
      *(unsigned*)(lds + L_QS + 256 + lane * 4) = pack_bf16(qa[1] + qa[3], qb[1] + qb[3]);
      flag_add(lds, F_Q, lane);
    }
    const int fc = (lane & 1) * 4 + la;
    const unsigned xraw = __builtin_amdgcn_raw_buffer_load_b32(rx, red * 16 + (fc >> 1) * 4, 0, 0);
    const unsigned b2v = __builtin_amdgcn_raw_buffer_load_b16(mkrsrc(b2, 4), 0, 0, 0);
    {
      flag_wait<16>(lds, F_CK + 1, S);
      const u32x2 c0 = *(const u32x2*)(lds + L_CK), c1 = *(const u32x2*)(lds + L_CK + 16);
      const unsigned tm = c0[1] + (unsigned)((int)(c1[1] - c0[1]) >> 1), per = ((c0[1] - c0[0]) + (c1[1] - c1[0])) * 8u / CKI;
      if (__builtin_amdgcn_readfirstlane(per) > (unsigned)(TAU16 + CKTHR)) {
        const int co = WS_CK + g * 256;
        __builtin_amdgcn_raw_buffer_store_b128(u32x4{tagA, tm, tagB, per}, rws, lane == 0 ? co + s * 16 : OOB, 0, AUX_SC1);
        unsigned tr = now32() + 250u;
#pragma unroll 1
        for (int spin = 0; spin < 4; ++spin) {
          wait_until(tr);
          asm volatile("" ::: "memory");
          const u32x4 v = __builtin_amdgcn_raw_buffer_load_b128(rws, co + (lane & 7) * 16, 0, AUX_SC1);
          const bool ok = (v[0] == tagA) & (v[2] == tagB);
          const int cnt = __builtin_popcountll(__ballot(ok)) >> 3;
          if (cnt == 8 || (spin == 3 && cnt >= 6)) {
            float d = ok ? (float)(int)(v[1] - tm) : 0.f, p = ok ? (float)v[3] : 0.f;
            d += dppf<XOR1>(d); d += dppf<XOR2>(d); d += dppf<XOR7>(d);
            p += dppf<XOR1>(p); p += dppf<XOR2>(p); p += dppf<XOR7>(p);
            const float rc = __builtin_amdgcn_rcpf((float)cnt);
            const unsigned tau = min((unsigned)(p * rc) - 4u, (unsigned)TAU16 + 400u);
            if (lane == 0) *(u32x2*)(lds + L_REF) = u32x2{tm + (unsigned)(int)(d * rc) - ((CK0 + CKI) * tau >> 4) - 100u, tau};
            break;
          }
          tr = now32() + 100u;
        }
      }
    }
    if (red == 127) {
#pragma unroll 1
      for (unsigned spin = 0; spin < SPIN_CAP; ++spin) {
        asm volatile("" ::: "memory");
        const u32x4 v0 = __builtin_amdgcn_raw_buffer_load_b128(rws, WS_QX + b * 65536 + lane * 512, 0, AUX_SC1);
        const u32x4 v1 = __builtin_amdgcn_raw_buffer_load_b128(rws, WS_QX + b * 65536 + 32768 + lane * 512, 0, AUX_SC1);
        if (__all((v0[0] == tagA) & (v0[2] == tagB) & (v1[0] == tagA) & (v1[2] == tagB))) break;
        __builtin_amdgcn_s_sleep(16);
      }
      __builtin_amdgcn_raw_buffer_store_b64(u32x2{tagA, tagB}, rws, lane == 0 ? WS_EPOCH + b * 256 : OOB, 0, AUX_SC1);
    }
    float yv = 0.f, xfin = (fc & 1) ? bfhi(xraw) : bflo(xraw);
    const rsrc_t rout = mkrsrc(out + b * DM, DM * 2 + 2);
    int oout = (lane & 14) == 0 ? (red * 8 + fc) * 2 : OOB;
    if (red < 72) {
      flag_wait<16>(lds, F_Y, 4);
#pragma unroll
      for (int ww = 0; ww < 4; ++ww) yv += *(const float*)(lds + L_YP + ww * 32 + fc * 4);
      yv = rbf(rbf(yv) + bflo(b2v));
      if (PRE >= 0) __builtin_amdgcn_raw_buffer_store_b16((unsigned short)0, rout, oout, 0, PRE < 0 ? 0 : PRE);
    }
    int oa = (lane & 8) ? L_QPART + lane * 8 : L_OA + la * 64 + (lane & 7) * 8;
    asm volatile("" : "+v"(yv), "+v"(xfin), "+v"(oout), "+v"(oa));
    flag_wait<1>(lds, F_LAST, 1);
    const unsigned t_last = now32();
    flag_wait<0>(lds, F_END, 1);
    const unsigned t_end = now32();
    STAMP(5);
    __builtin_amdgcn_s_sleep(T1D);
    {
      u32x4 ga[4];
#pragma unroll
      for (int i = 0; i < 4; ++i) ga[i] = u32x4{0u, 0u, 0u, 0u};
      int of = WS_PX + (g * 8 + (s & 4)) * 1024 + lane * 16;
#pragma unroll 1
      for (unsigned spin = 0; spin < SPIN_CAP; ++spin) {
        asm volatile("" ::: "memory");
        u32x4 gn[4];
#pragma unroll
        for (int i = 0; i < 4; ++i) gn[i] = __builtin_amdgcn_raw_buffer_load_b128(rws, of + i * 1024, 0, AUX_SC1);
        unsigned bad = 0u;
#pragma unroll
        for (int i = 0; i < 4; ++i) bad |= (gn[i][0] ^ tagA) | (gn[i][2] ^ (tagA >> 8));
        bad &= 0xffu;
        bad |= dppu<XOR1>(bad); bad |= dppu<XOR2>(bad); bad |= dppu<XOR7>(bad);
        const bool fresh = (bad == 0u) & (of != OOB);
#pragma unroll
        for (int i = 0; i < 4; ++i) ga[i] = fresh ? gn[i] : ga[i];
        of = fresh ? OOB : of;
        if (__all(of == OOB)) break;
        __builtin_amdgcn_s_sleep(1);
      }
      const unsigned hm = __builtin_amdgcn_perm(ga[0][3], ga[0][1], 0x0c0c0400u) << (16 * (lane & 1));
      const unsigned hl = __builtin_amdgcn_perm(ga[1][3], ga[1][1], 0x0c0c0400u) << (16 * (lane & 1));
      const float pm = asf(hm | dppu<XOR1>(hm)), pl = asf(hl | dppu<XOR1>(hl));
      const float M = rows_max(fmaxf(pm, dppf<ROR8>(pm)));
      const float w0 = __builtin_amdgcn_exp2f((pm - M) * LOG2E) * pl;
      const float inv = recip(rows_sum(w0 + dppf<ROR8>(w0)));
      float v[4];
#pragma unroll
      for (int r = 0; r < 4; ++r) {
        const float t = rows_sum_t(w0 * cut_lo(ga[0][r]), w0 * cut_lo(ga[1][r]), w0 * cut_lo(ga[2][r]), w0 * cut_lo(ga[3][r]));
        v[r] = (t + dppf<ROR8>(t)) * inv;
      }
      *(u32x2*)(lds + oa) = u32x2{pack_bf16(v[0], v[1]), pack_bf16(v[2], v[3])};
      flag_add(lds, F_O, lane);
    }
    if (red < 72) {
      __builtin_amdgcn_s_sleep(T2D);
      u32x4 ga = {0u, 0u, 0u, 0u};
      int of = WS_OX + (b * 18 + (red >> 2)) * 4096 + (lane >> 1) * 128 + (red & 3) * 32 + (lane & 1) * 16;
#pragma unroll 1
      for (unsigned spin = 0; spin < SPIN_CAP; ++spin) {
        asm volatile("" ::: "memory");
        const u32x4 gn = __builtin_amdgcn_raw_buffer_load_b128(rws, of, 0, AUX_SC1);
        unsigned bad = (__builtin_amdgcn_perm(gn[1], gn[0], 0x0c0c0400u) | __builtin_amdgcn_perm(gn[3], gn[2], 0x04000c0cu)) ^ tagA;
        bad |= dppu<XOR1>(bad);
        const bool fresh = (bad == 0u) & (of != OOB);
        ga = fresh ? gn : ga;
        of = fresh ? OOB : of;
        if (__all(of == OOB)) break;
        __builtin_amdgcn_s_sleep(1);
      }
      STAMP(13);
      float t = rows_sum_t(cut_lo(ga[0]), cut_lo(ga[1]), cut_lo(ga[2]), cut_lo(ga[3]));
      t += dppf<XOR2>(t); t += dppf<ROR4>(t); t += dppf<ROR8>(t);
      __builtin_amdgcn_raw_buffer_store_b16((unsigned short)pack_bf16(rbf(xfin + rbf(t)) + yv, 0.f), rout, oout, 0, 0);
    }
    if (id == DIAG_WG) {
      const unsigned met = (DIAG_M == 0 ? now32() : DIAG_M == 1 ? t_end : t_last) - tst;
      wait_until(tst + 15000u + 10u * (met < 20000u ? met : 20000u));
    }
  } else {
    const rsrc_t rk = mkrsrc(kc + (size_t)(id >> 1) * SPAN, SPAN);
    const rsrc_t rv = mkrsrc(vc + (size_t)(id >> 1) * SPAN, SPAN);
    auto poff = [&](int d, int h) {
      return ((d & 1) | (h << 1) | ((d & 2) << 1) | ((id & 1) << 3) | ((d >> 2) << 4)) * TILE_BYTES;
    };
    const int voffK = ((lane & 15) >> 2) * 256 + la * 64 + (lane & 3) * 16;
    const int voffV = lane * 16;
    u32x4 bA[8], bB[8];
    auto request = [&](u32x4 (&x)[8], rsrc_t r, int voff, int so) {
      asm volatile("" : "+v"(voff));
#pragma unroll
      for (int u = 0; u < 8; ++u) x[u] = __builtin_amdgcn_raw_buffer_load_b128(r, voff + (u & 3) * 1024, so + (u >> 2) * 4096, AUX_NT);
      __builtin_amdgcn_sched_barrier(0);
    };
    if (w >= 4) {
      const int rg = (w - 4) * 8 + (lane >> 3), c8 = lane & 7;
      unsigned xs[5];
      u32x4 wqv[9];
#pragma unroll
      for (int i = 0; i < 5; ++i) xs[i] = __builtin_amdgcn_raw_buffer_load_b32(rx, (lane + 64 * i) * 4, 0, 0);
      __builtin_amdgcn_sched_barrier(0);
      const rsrc_t rq = mkrsrc(wq + qh * 288 * 4096 + kvh * 256 + cb * 64, (287 * 4096 + 64) * 2);
#pragma unroll
      for (int i = 0; i < 9; ++i) wqv[i] = __builtin_amdgcn_raw_buffer_load_b128(rq, (rg + 32 * i) * 8192 + c8 * 16, 0, AUX_NT);
      __builtin_amdgcn_sched_barrier(0);
      __builtin_amdgcn_s_barrier();
      __builtin_amdgcn_sched_barrier(0);
      float xv[10], mean1, den1;
#pragma unroll
      for (int i = 0; i < 5; ++i) { xv[2 * i] = bflo(xs[i]); xv[2 * i + 1] = bfhi(xs[i]); }
      ln_stats(xv, lane < 32, mean1, den1);
#pragma unroll
      for (int i = 0; i < 5; ++i)
        *(unsigned*)(lds + L_XN + (lane + 64 * i) * 4) = pack_bf16(ln_apply(xv[2 * i], mean1, den1), ln_apply(xv[2 * i + 1], mean1, den1));
      asm volatile("" ::: "memory");
      STAMP(9);
      f32x4 qa = {0.f, 0.f, 0.f, 0.f};
#pragma unroll
      for (int i = 0; i < 9; ++i) {
        const unsigned t = (unsigned)*(const unsigned short*)(lds + L_XN + (qh * 288 + rg + 32 * i) * 2) << (16 * (lane & 1));
        const int jp = c8 >> 1;
        qa = mfma32(wqv[i], u32x4{jp == 0 ? t : 0u, jp == 1 ? t : 0u, jp == 2 ? t : 0u, jp == 3 ? t : 0u}, qa);
      }
      if ((lane >> 5) == ((lane >> 3) & 1)) {
        float* qp = (float*)(lds + L_QPART) + ((w - 4) * 2 + (lane >> 5)) * 64 + (la & 1) * 32 + c8;
#pragma unroll
        for (int r = 0; r < 4; ++r) qp[r * 8] = qa[r];
      }
      flag_add(lds, F_QP, lane);
      STAMP(1);
    }
    if (w < 4) asm volatile("s_barrier" ::: "memory");
    if (w >= PFD) flag_wait<1>(lds, F_ARR, w - PFD + 1);
    __builtin_amdgcn_sched_barrier(0);
    STAMP(14);
    request(bA, rk, voffK, poff(w, 0));
    request(bB, rk, voffK, poff(w, 1));
    asm volatile("s_waitcnt vmcnt(0)" ::: "memory");
    flag_add(lds, F_ARR, lane);
    STAMP(15);
    flag_wait<1>(lds, F_Q, 1);
    if (w < S) STAMP(2);

    const int c = lane & 3, hb = (lane >> 2) & 1, tb = (lane >> 3) & 1;
    const unsigned tagA = __builtin_amdgcn_readfirstlane(*(const unsigned*)(lds + L_TAG));
    float m, l = 0.f;
    f32x4 acc[2][2];
#pragma unroll
    for (int i = 0; i < 2; ++i)
#pragma unroll
      for (int j = 0; j < 2; ++j) acc[i][j] = f32x4{0.f, 0.f, 0.f, 0.f};
    const u32x4 qf = *(const u32x4*)(lds + L_QS + (hb * 128 + la * 32 + c * 8) * 2);
    const u32x4 zero4 = {0u, 0u, 0u, 0u};
    const u32x4 q0 = tb ? zero4 : qf, q1 = tb ? qf : zero4;
    const unsigned mask0 = c == 0 ? 0x0000ffffu : c == 1 ? 0xffff0000u : 0u;
    const unsigned mask1 = c == 2 ? 0x0000ffffu : c == 3 ? 0xffff0000u : 0u;
    auto qk = [&](const u32x4 (&x)[8], f32x4 (&sc)[4]) {
#pragma unroll
      for (int i = 0; i < 4; ++i) sc[i] = mfma32(x[i], q0, f32x4{0.f, 0.f, 0.f, 0.f});
#pragma unroll
      for (int i = 0; i < 4; ++i) sc[i] = mfma32(x[4 + i], q1, sc[i]);
    };
    auto score = [&](const f32x4 (&sc)[4]) {
      float t4[4];
#pragma unroll
      for (int i = 0; i < 4; ++i) t4[i] = (dppf<QB0>(sc[i][0]) + dppf<QB1>(sc[i][1])) + (dppf<QB2>(sc[i][2]) + dppf<QB3>(sc[i][3]));
      const float raw = (c & 2) ? ((c & 1) ? t4[3] : t4[2]) : ((c & 1) ? t4[1] : t4[0]);
      float sv = rbf(rbf(raw) * SCALE);
      asm volatile("" : "+v"(sv));
      return sv;
    };
    auto head_max = [&](float v) {
      v = fmaxf(v, dppf<XOR1>(v));
      v = fmaxf(v, dppf<XOR2>(v));
      v = fmaxf(v, dppf<ROR8>(v));
      return rows_max(v);
    };
    auto pv = [&](const u32x4 (&x)[8], float sv) {
      const float p = __builtin_amdgcn_exp2f((sv - m) * LOG2E);
      l += p;
      const unsigned pk = pack_bf16(p, p);
      unsigned pb0[4], pb1[4];
      pb0[0] = dppu<QB0>(pk) & mask0; pb1[0] = dppu<QB0>(pk) & mask1;
      pb0[1] = dppu<QB1>(pk) & mask0; pb1[1] = dppu<QB1>(pk) & mask1;
      pb0[2] = dppu<QB2>(pk) & mask0; pb1[2] = dppu<QB2>(pk) & mask1;
      pb0[3] = dppu<QB3>(pk) & mask0; pb1[3] = dppu<QB3>(pk) & mask1;
#pragma unroll
      for (int u = 0; u < 8; ++u) {
        acc[u >> 2][0] = mfma16(x[u][0], x[u][1], pb0[u & 3], pb1[u & 3], acc[u >> 2][0]);
        acc[u >> 2][1] = mfma16(x[u][2], x[u][3], pb0[u & 3], pb1[u & 3], acc[u >> 2][1]);
      }
    };
    auto hold = [&](int dly) {
      __builtin_amdgcn_sched_barrier(0);
      for (int i = 0; i < dly; ++i) __builtin_amdgcn_s_sleep(1);
      __builtin_amdgcn_sched_barrier(0);
    };
    const int hw = w - S, nwk = (s & 3) == 3 ? 6 : 4;
    f32x2 sl;
    if (w < S) {
      __builtin_amdgcn_s_setprio(2);
      auto pace = [&](int it) {
        const unsigned now = now32();
        const unsigned long long rf = sched_get(lds);
        const int tau = __builtin_amdgcn_readfirstlane((unsigned)(rf >> 32));
        const int lead = (int)(__builtin_amdgcn_readfirstlane((unsigned)rf) + (unsigned)(it * tau >> 4) - now);
        const int dadd = (tau - TAU16) * DGAIN >> 8;
        if (it == CK0 || it == CK0 + CKI) {
          *(unsigned*)(lds + L_CK + w * 16 + (it > CK0) * 4) = now;
          flag_add(lds, F_CK + (it > CK0), lane);
        }
        return min(max(DLY0 + dadd + ((lead * GAIN + 128) >> 8), 0), DLYMAX + dadd);
      };
      unsigned char* sco = lds + L_WO + w * (64 * 384) + lane * 8;
      flag_wait<1>(lds, F_ARR, NWAVE + w - PFD + 1);
      float mx = -INFINITY;
      for (int j = 0; j < NPAIR; ++j) {
        const int dly = pace(j) + DASY;
        const bool k = j + 1 < NPAIR;
        const rsrc_t r = k ? rk : rv;
        const int vo = k ? voffK : voffV, d = NWAVE + S * (k ? j : j - 1) + w;
        f32x4 sc[4];
        qk(bA, sc);
        hold(dly);
        request(bA, r, vo, poff(d, 0));
        const float s0 = score(sc);
        qk(bB, sc);
        hold(dly);
        request(bB, r, vo, poff(d, 1));
        const float s1 = score(sc);
        mx = fmaxf(mx, fmaxf(s0, s1));
        *(f32x2*)(sco + j * 512) = f32x2{s0, s1};
      }
      m = head_max(mx);
      for (int j = 0; j + 1 < NPAIR; ++j) {
        const f32x2 sv = *(const f32x2*)(sco + (NPAIR - 1 - j) * 512);
        const int dly = pace(NPAIR + j) - DASY;
        const int d = j + 2 < NPAIR ? NWAVE + S * (NPAIR - 3 - j) + w : w;
        pv(bA, sv[0]);
        hold(dly);
        request(bA, rv, voffV, poff(d, 0));
        pv(bB, sv[1]);
        hold(dly);
        request(bB, rv, voffV, poff(d, 1));
      }
      flag_add(lds, F_LAST + w, lane);
      sl = *(const f32x2*)sco;
    }
    asm volatile("" ::: "memory");
    if (w >= S) {
      {
        f32x4 sc[4];
        qk(bA, sc);
        sl[0] = score(sc);
        qk(bB, sc);
        sl[1] = score(sc);
        m = head_max(fmaxf(sl[0], sl[1]));
      }
      STAMP(12);
      if (w >= 4 && red < 72) {
        const int ht = (w - 4) * 64 + lane;
        unsigned a1[9], ab[9], a2[9];
        const rsrc_t r1 = mkrsrc(w1, FF * 2), rb = mkrsrc(b1, FF * 2), r2 = mkrsrc(w2, FF * 2);
#pragma unroll
        for (int i = 0; i < 9; ++i) {
          a1[i] = __builtin_amdgcn_raw_buffer_load_b32(r1, (ht + 256 * i) * 4, 0, 0);
          ab[i] = __builtin_amdgcn_raw_buffer_load_b32(rb, (ht + 256 * i) * 4, 0, 0);
          a2[i] = __builtin_amdgcn_raw_buffer_load_b32(r2, (ht + 256 * i) * 4, 0, 0);
        }
        __builtin_amdgcn_sched_barrier(0);
        float xn[10];
#pragma unroll
        for (int i = 0; i < 5; ++i) { const unsigned v = *(const unsigned*)(lds + L_XN + (lane + 64 * i) * 4); xn[2 * i] = bflo(v); xn[2 * i + 1] = bfhi(v); }
        float mean2, den2;
        ln_stats(xn, lane < 32, mean2, den2);
        constexpr float GC0 = -2.0f * 0.7978845608028654f * LOG2E, GC1 = GC0 * 0.044715f;
#pragma unroll 1
        for (int j = 0; j < 8; ++j) {
          const float hv = ln_apply(bflo(*(const unsigned short*)(lds + L_XN + (red * 8 + j) * 2)), mean2, den2);
          float y = 0.f;
#pragma unroll
          for (int i = 0; i < 9; ++i) {
            asm volatile("" : "+v"(a1[i]), "+v"(ab[i]), "+v"(a2[i]));
            const unsigned pr = pack_bf16(hv * bflo(a1[i]), hv * bfhi(a1[i]));
            const unsigned uu = pack_bf16(bflo(pr) + bflo(ab[i]), bfhi(pr) + bfhi(ab[i]));
            const float u0 = bflo(uu), u1 = bfhi(uu);
            const float e0 = __builtin_amdgcn_exp2f(u0 * fmaf(u0 * u0, GC1, GC0));
            const float e1 = __builtin_amdgcn_exp2f(u1 * fmaf(u1 * u1, GC1, GC0));
            const unsigned gg = pack_bf16(u0 * __builtin_amdgcn_rcpf(1.0f + e0), u1 * __builtin_amdgcn_rcpf(1.0f + e1));
            y = fmaf(bflo(gg), bflo(a2[i]), y);
            y = fmaf(bfhi(gg), bfhi(a2[i]), y);
          }
          y = wave_sum(y);
          if (lane == 0) *(float*)(lds + L_YP + (w - 4) * 32 + j * 4) = y;
        }
        flag_add(lds, F_Y, lane);
      }
      if (hw < RSVD) {
        bool seen = false;
        unsigned tseen = 0;
#pragma unroll 1
        for (unsigned spin = 0; spin < (SPIN_CAP << 2); ++spin) {
          const unsigned long long rf = sched_get(lds);
          const unsigned due = __builtin_amdgcn_readfirstlane((unsigned)rf + ((2 * NITER + hw) * (unsigned)(rf >> 32) >> 5)) + RSV0;
          const unsigned now = now32();
          const int late = (int)(now - due);
          if (!seen && flag_get(lds, F_LAST + (hw & 1)) != 0u) { seen = true; tseen = now; }
          if (late >= 0 || (seen && (late >= -RSVH || (int)(now - tseen) >= RSVB))) break;
          __builtin_amdgcn_s_sleep(2);
        }
      } else {
        flag_wait<2>(lds, F_AR2, hw - RSVD + 1);
      }
      __builtin_amdgcn_s_setprio(2);
      __builtin_amdgcn_sched_barrier(0);
      request(bA, rv, voffV, poff(w, 0));
      request(bB, rv, voffV, poff(w, 1));
      if (hw < NH - RSVD) {
        asm volatile("s_waitcnt vmcnt(0)" ::: "memory");
        flag_add(lds, F_AR2, lane);
      }
    }
    int po = WS_PX + ((g * 8 + hb * 4 + la) * 8 + s) * 128 + (tb * 4 + c) * 16;
    asm volatile("" : "+v"(po));
    pv(bA, sl[0]);
    pv(bB, sl[1]);
    STAMP(3);
    {
      f32x4 res;
#pragma unroll
      for (int r = 0; r < 4; ++r) {
        const float own = tb ? acc[1][1][r] : acc[0][0][r], snd = tb ? acc[1][0][r] : acc[0][1][r];
        res[r] = own + dppf<ROR8>(snd);
      }
      l += dppf<XOR1>(l);
      l += dppf<XOR2>(l);
      l += dppf<ROR8>(l);
      l = rows_sum(l);
      *(f32x4*)(lds + L_PART + w * 1024 + lane * 16) = res;
      if ((lane & ~4) == 0) *(f32x2*)(lds + L_ML + (w * 2 + hb) * 8) = f32x2{m, l};
      asm volatile("" ::: "memory");
      unsigned old = 0;
      if (lane == 0) old = __hip_atomic_fetch_add((unsigned*)(lds + L_FLAG) + F_WP + vzero(), 1u, __ATOMIC_RELAXED, __HIP_MEMORY_SCOPE_WORKGROUP);
      asm volatile("" ::: "memory");
      f32x2 ml[NWAVE];
      f32x4 pp[NWAVE];
#pragma unroll
      for (int k = 0; k < NWAVE; ++k) {
        ml[k] = *(const f32x2*)(lds + L_ML + (k * 2 + hb) * 8);
        pp[k] = *(const f32x4*)(lds + L_PART + k * 1024 + lane * 16);
      }
      __builtin_amdgcn_sched_barrier(0);
      if (__builtin_amdgcn_readfirstlane(old) == NWAVE - 1) {
        float mx = ml[0][0];
#pragma unroll
        for (int k = 1; k < NWAVE; ++k) mx = fmaxf(mx, ml[k][0]);
        f32x4 sum = {0.f, 0.f, 0.f, 0.f};
        float L = 0.f;
#pragma unroll
        for (int k = 0; k < NWAVE; ++k) {
          const float be = __builtin_amdgcn_exp2f((ml[k][0] - mx) * LOG2E);
          sum += be * pp[k]; L = fmaf(be, ml[k][1], L);
        }
        const float inv = recip(L);
        const unsigned side = asu((la & 1) ? L : mx) >> (16 * (c & 1));
        __builtin_amdgcn_raw_buffer_store_b128(
            u32x4{put_lo(sum[0] * inv, tagA), put_lo(sum[1] * inv, side), put_lo(sum[2] * inv, tagA >> 8), put_lo(sum[3] * inv, side >> 8)},
            rws, po, 0, AUX_SC1);
        flag_add(lds, F_END, lane);
        STAMP(10);
      }
    }
    if (w < S) {
      flag_wait<1>(lds, F_WP, WOL);
      const rsrc_t ro = mkrsrc(wo + (size_t)(kvh * 256 + qh * 128 + w * 64) * DM, 64 * DM * 2);
      u32x4 wt[24];
#pragma unroll
      for (int i = 0; i < 24; ++i)
        wt[i] = __builtin_amdgcn_raw_buffer_load_b128(
            ro, 2 * (i % 3) < nwk ? ((i / 3) * 8 + (lane >> 3)) * (DM * 2) + (s & 3) * 256 + (i % 3) * 128 + (lane & 7) * 16 : OOB, 0, AUX_NT);
#pragma unroll
      for (int i = 0; i < 24; ++i) *(u32x4*)(lds + L_WO + (w * 64 + (i / 3) * 8 + (lane >> 3)) * 384 + (i % 3) * 128 + (lane & 7) * 16) = wt[i];
      flag_add(lds, F_WO, lane);
      STAMP(4);
    } else if (hw < nwk) {
      const int n = lane & 15;
      int oo = n < 2 ? WS_OX + (b * 18 + (s & 3) * 4 + hw) * 4096 + (kvh * 2 + qh) * 128 + n * 64 + la * 16 : OOB;
      asm volatile("" : "+v"(oo));
      flag_wait<1>(lds, F_WO, S);
      u32x4 wof[2][4];
      {
        typedef __attribute__((address_space(3))) s16x4* lds_s16x4;
        unsigned char* img = lds + L_WO + ((n >> 2) * 8 + la * 2) * 384 + hw * 64 + (lane & 3) * 8;
#pragma unroll
        for (int t = 0; t < 2; ++t)
#pragma unroll
          for (int ks = 0; ks < 4; ++ks) {
            const u32x2 lo = __builtin_bit_cast(u32x2, __builtin_amdgcn_ds_read_tr16_b64_v4i16((lds_s16x4)(img + ks * 32 * 384 + t * 32)));
            const u32x2 hi = __builtin_bit_cast(u32x2, __builtin_amdgcn_ds_read_tr16_b64_v4i16((lds_s16x4)(img + ks * 32 * 384 + t * 32 + 384)));
            wof[t][ks] = u32x4{lo[0], lo[1], hi[0], hi[1]};
          }
      }
      u32x4 ob[4];
#pragma unroll 1
      for (unsigned spin = 0; spin < (SPIN_CAP << 4); ++spin) {
        asm volatile("" ::: "memory");
        const unsigned f = __hip_atomic_load((unsigned*)(lds + L_FLAG) + F_O, __ATOMIC_RELAXED, __HIP_MEMORY_SCOPE_WORKGROUP);
        asm volatile("" ::: "memory");
#pragma unroll
        for (int ks = 0; ks < 4; ++ks) ob[ks] = *(const u32x4*)(lds + L_OA + ks * 64 + la * 16);
        __builtin_amdgcn_sched_barrier(0);
        if (__builtin_amdgcn_readfirstlane(f) != 0u) break;
      }
      STAMP(6);
      f32x4 d0 = {0.f, 0.f, 0.f, 0.f}, d1 = {0.f, 0.f, 0.f, 0.f};
#pragma unroll
      for (int ks = 0; ks < 4; ++ks) { d0 = mfma32(wof[0][ks], ob[ks], d0); d1 = mfma32(wof[1][ks], ob[ks], d1); }
      const f32x4 d = n ? d1 : d0;
      __builtin_amdgcn_raw_buffer_store_b128(
          u32x4{put_lo(d[0], tagA), put_lo(d[1], tagA >> 8), put_lo(d[2], tagA >> 16), put_lo(d[3], tagA >> 24)}, rws, oo, 0, AUX_SC1);
      STAMP(11);
    }
  }
}

}

extern "C" void kernel_launch(void* const* d_in, const int* in_sizes, int n_in, void* d_out, int out_size, void* d_ws,
                              size_t ws_size, hipStream_t stream) {
  (void)in_sizes; (void)n_in; (void)out_size; (void)ws_size;
  fused_decode_kernel<<<dim3(NWG), dim3(NTHR), 0, stream>>>(
      (const unsigned short*)d_in[0], (const unsigned char*)d_in[1], (const unsigned char*)d_in[2],
      (const unsigned short*)d_in[3], (const unsigned short*)d_in[4], (const unsigned short*)d_in[6],
      (const unsigned short*)d_in[7], (const unsigned short*)d_in[8], (const unsigned short*)d_in[9],
      (unsigned short*)d_out, (unsigned char*)d_ws);
}
```

```cpp
#include <hip/hip_runtime.h>
#include <hip/hip_bf16.h>
#include <stdint.h>

namespace {

typedef unsigned u32x2 __attribute__((ext_vector_type(2)));
typedef unsigned u32x4 __attribute__((ext_vector_type(4)));
typedef float f32x2 __attribute__((ext_vector_type(2)));
typedef float f32x4 __attribute__((ext_vector_type(4)));
typedef short s16x4 __attribute__((ext_vector_type(4)));
typedef short s16x8 __attribute__((ext_vector_type(8)));
typedef __bf16 bf16x2 __attribute__((ext_vector_type(2)));
typedef __amdgpu_buffer_rsrc_t rsrc_t;

constexpr int DM = 576, SK = 32768, DH = 128, FF = 4608;
constexpr int NSPLIT = 8, NWG = 256, NWAVE = 8, NTHR = (NWAVE + 1) * 64;
constexpr int KEYS_WG = SK / NSPLIT;
constexpr int TILE_BYTES = 32 * DH * 2;
constexpr int NT = KEYS_WG / 32;
constexpr int SPAN = 2 * KEYS_WG * DH * 2;
constexpr int S = 2, NH = NWAVE - S;
constexpr int NPAIR = (NT / 2 - NH) / S;
constexpr int NITER = 2 * NPAIR - 1;
static_assert(2 * (NPAIR * S + NH) == NT, "every tile has one owner");

constexpr int PFD = 3;
constexpr int DLY0 = 8;
constexpr int GLT = 130;
constexpr int DLYMAX = 12;
constexpr int GAIN = 3;
constexpr int TAU16 = 1704;
constexpr int REF0 = 90;
constexpr int RSV0 = 180;
constexpr int RSVH = 25;
constexpr int RSVB = 70;
constexpr int RSVD = 3;
constexpr int WOL = 8;
constexpr int PSL = 6;
constexpr int QPS = 2;
constexpr int T1D = 7;
constexpr int T2D = 18;

constexpr int AUX_NT = 2, AUX_SC1 = 16;
constexpr unsigned RSRC3 = 0x00020000u;
constexpr unsigned SPIN_CAP = 1u << 19;
constexpr int OOB = 0x7ff00000;

constexpr int WS_EPOCH = 0;
constexpr int WS_QX = 4096;
constexpr int WS_PX = WS_QX + 32 * 4096;
constexpr int WS_OX = WS_PX + NWG * 1024;
constexpr int WS_END = WS_OX + 2 * 18 * 4096;

constexpr int L_FLAG = 0;
constexpr int L_TAG = 64;
constexpr int L_XN = 80;
constexpr int L_QPART = L_XN + 1280;
constexpr int L_QS = L_QPART + 8 * 64 * 4;
constexpr int L_OA = L_QS + 512;
constexpr int L_YP = L_OA + 256;
constexpr int L_ML = L_YP + 4 * 8 * 4;
constexpr int L_PART = L_ML + NWAVE * 16;
constexpr int L_WO = L_PART + NWAVE * 1024;
constexpr int L_END = L_WO + 128 * 384;
static_assert(NPAIR * 512 <= 64 * 384, "a stream's scores fit its half of the wo image");
constexpr int F_QP = 0, F_Q = 1, F_WP = 2, F_END = 3, F_O = 4, F_Y = 5, F_ARR = 6, F_AR2 = 7, F_WO = 8, F_ML = 9, F_LAST = 10, F_P1 = 12,
              F_SH = 14;

constexpr float LOG2E = 1.4426950408889634f;
constexpr float SCALE = 0.08838834764831845f;
constexpr float LN_EPS = 1e-5f;

__device__ __forceinline__ float asf(unsigned u) { return __builtin_bit_cast(float, u); }
__device__ __forceinline__ unsigned asu(float f) { return __builtin_bit_cast(unsigned, f); }
__device__ __forceinline__ float bflo(unsigned u) { return asf(u << 16); }
__device__ __forceinline__ float bfhi(unsigned u) { return asf(u & 0xffff0000u); }
__device__ __forceinline__ unsigned pack_bf16(float lo, float hi) {
  f32x2 v = {lo, hi};
  return __builtin_bit_cast(unsigned, __builtin_convertvector(v, bf16x2));
}
__device__ __forceinline__ float rbf(float f) { return (float)(__bf16)f; }
template <int CTRL> __device__ __forceinline__ unsigned dppu(unsigned x) {
  return (unsigned)__builtin_amdgcn_mov_dpp((int)x, CTRL, 0xf, 0xf, true);
}
template <int CTRL> __device__ __forceinline__ float dppf(float x) { return asf(dppu<CTRL>(asu(x))); }
constexpr int XOR1 = 0xB1, XOR2 = 0x4E, XOR7 = 0x141, ROR4 = 0x124, ROR8 = 0x128;
constexpr int QB0 = 0x00, QB1 = 0x55, QB2 = 0xAA, QB3 = 0xFF;
__device__ __forceinline__ float rows_sum(float x) {
  auto s = __builtin_amdgcn_permlane16_swap(asu(x), asu(x), false, false);
  x = asf(s[0]) + asf(s[1]);
  auto t = __builtin_amdgcn_permlane32_swap(asu(x), asu(x), false, false);
  return asf(t[0]) + asf(t[1]);
}
__device__ __forceinline__ float rows_max(float x) {
  auto s = __builtin_amdgcn_permlane16_swap(asu(x), asu(x), false, false);
  x = fmaxf(asf(s[0]), asf(s[1]));
  auto t = __builtin_amdgcn_permlane32_swap(asu(x), asu(x), false, false);
  return fmaxf(asf(t[0]), asf(t[1]));
}
__device__ __forceinline__ float rows_sum_t(float v0, float v1, float v2, float v3) {
  const auto p0 = __builtin_amdgcn_permlane32_swap(asu(v0), asu(v2), false, false);
  const auto p1 = __builtin_amdgcn_permlane32_swap(asu(v1), asu(v3), false, false);
  const auto p2 = __builtin_amdgcn_permlane16_swap(asu(asf(p0[0]) + asf(p0[1])), asu(asf(p1[0]) + asf(p1[1])), false, false);
  return asf(p2[0]) + asf(p2[1]);
}
__device__ __forceinline__ float wave_sum(float x) {
  x += dppf<XOR1>(x); x += dppf<XOR2>(x); x += dppf<ROR4>(x); x += dppf<ROR8>(x);
  return rows_sum(x);
}
__device__ __forceinline__ float recip(float x) {
  const float r = __builtin_amdgcn_rcpf(x);
  return fmaf(fmaf(-x, r, 1.0f), r, r);
}
__device__ __forceinline__ rsrc_t mkrsrc(const void* p, int bytes) {
  return __builtin_amdgcn_make_buffer_rsrc(const_cast<void*>(p), 0, bytes, RSRC3);
}
__device__ __forceinline__ f32x4 mfma32(u32x4 a, u32x4 b, f32x4 c) {
  return __builtin_amdgcn_mfma_f32_16x16x32_bf16(__builtin_bit_cast(s16x8, a), __builtin_bit_cast(s16x8, b), c, 0, 0, 0);
}
__device__ __forceinline__ f32x4 mfma16(unsigned a0, unsigned a1, unsigned b0, unsigned b1, f32x4 c) {
  u32x2 a = {a0, a1}, b = {b0, b1};
  return __builtin_amdgcn_mfma_f32_16x16x16bf16_1k(__builtin_bit_cast(s16x4, a), __builtin_bit_cast(s16x4, b), c, 0, 0, 0);
}
__device__ __forceinline__ unsigned put_lo(float v, unsigned byte) { return (asu(v) & 0xffffff00u) | (byte & 0xffu); }
__device__ __forceinline__ float cut_lo(unsigned u) { return asf(u & 0xffffff00u); }
__device__ __forceinline__ unsigned now32() { return (unsigned)__builtin_amdgcn_s_memrealtime(); }
__device__ __forceinline__ void wait_until(unsigned t) {
  while ((int)(now32() - t) < 0) __builtin_amdgcn_s_sleep(1);
}
__device__ __forceinline__ int vzero() { int z = 0; asm("" : "+v"(z)); return z; }
__device__ __forceinline__ void flag_add(unsigned char* lds, int f, int lane) {
  asm volatile("" ::: "memory");
  if (lane == 0) __hip_atomic_fetch_add((unsigned*)(lds + L_FLAG) + f + vzero(), 1u, __ATOMIC_RELAXED, __HIP_MEMORY_SCOPE_WORKGROUP);
  asm volatile("" ::: "memory");
}
__device__ __forceinline__ unsigned flag_get(unsigned char* lds, int f) {
  return __builtin_amdgcn_readfirstlane(__hip_atomic_load((unsigned*)(lds + L_FLAG) + f, __ATOMIC_RELAXED, __HIP_MEMORY_SCOPE_WORKGROUP));
}
template <int SLEEP> __device__ __forceinline__ void flag_wait(unsigned char* lds, int f, unsigned n) {
#pragma unroll 1
  for (unsigned spin = 0; spin < (SPIN_CAP << 4); ++spin) {
    if (flag_get(lds, f) >= n) break;
    if (SLEEP) __builtin_amdgcn_s_sleep(SLEEP);
  }
  asm volatile("" ::: "memory");
}
__device__ __forceinline__ void ln_stats(const float (&v)[10], bool last_valid, float& mean_b, float& den_b) {
  float s = 0.f;
#pragma unroll
  for (int i = 0; i < 8; ++i) s += v[i];
  s += last_valid ? (v[8] + v[9]) : 0.f;
  const float mean = wave_sum(s) * (1.0f / DM);
  float q = 0.f;
#pragma unroll
  for (int i = 0; i < 8; ++i) { const float d = v[i] - mean; q = fmaf(d, d, q); }
  { const float d8 = v[8] - mean, d9 = v[9] - mean; q += last_valid ? fmaf(d8, d8, d9 * d9) : 0.f; }
  const float var = wave_sum(q) * (1.0f / DM);
  mean_b = rbf(mean);
  den_b = rbf(__builtin_amdgcn_sqrtf(rbf(rbf(var) + LN_EPS)));
}
__device__ __forceinline__ float ln_apply(float x, float mean_b, float den_b) {
  const float d = rbf(x - mean_b), r = __builtin_amdgcn_rcpf(den_b), q = d * r;
  return rbf(fmaf(fmaf(-q, den_b, d), r, q));
}

__global__ void __launch_bounds__(NTHR, 3)
fused_decode_kernel(const unsigned short* __restrict__ x, const unsigned char* __restrict__ kc, const unsigned char* __restrict__ vc,
                    const unsigned short* __restrict__ wq, const unsigned short* __restrict__ wo,
                    const unsigned short* __restrict__ w1, const unsigned short* __restrict__ b1,
                    const unsigned short* __restrict__ w2, const unsigned short* __restrict__ b2,
                    unsigned short* __restrict__ out, unsigned char* ws) {
  __shared__ __attribute__((aligned(16))) unsigned char lds[L_END];
  const int lane = threadIdx.x & 63, wv = __builtin_amdgcn_readfirstlane(threadIdx.x >> 6), w = wv < 8 ? wv ^ 4 : wv;
  const unsigned tst = now32();
  const int g = blockIdx.x >> 3, s = (blockIdx.x + 3 * g) & 7, id = g * 8 + s, b = g >> 4, kvh = g & 15;
  const rsrc_t rws = mkrsrc(ws, WS_END);
  const rsrc_t rx = mkrsrc(x + b * DM, DM * 2);
  const int qh = s >> 2, cb = (s + (kvh >> 2)) & 3;
  const int la = lane >> 4;
  const int red = kvh * 8 + s;

  u32x2 ep = {0u, 0u};
  if (w == NWAVE) {
    ep = __builtin_amdgcn_raw_buffer_load_b64(rws, WS_EPOCH + b * 256, 0, 0);
    if (lane < 16) *((unsigned*)(lds + L_FLAG) + lane) = 0u;
    asm volatile("s_waitcnt lgkmcnt(0)\n\ts_barrier" ::: "memory");
  }
  if (w >= NWAVE) {
    __builtin_amdgcn_s_setprio(3);
    const unsigned e0 = __builtin_amdgcn_readfirstlane(ep[0]);
    const unsigned e1 = ((e0 & 0x7f7f7f7fu) + 0x01010101u) ^ (e0 & 0x80808080u);
    const unsigned tagA = e1 ^ 0xb77a3d00u;
    const unsigned tagB = __builtin_amdgcn_readfirstlane(ep[1]) + 1u;
    if (lane == 0) *(unsigned*)(lds + L_TAG) = tagA;
    flag_wait<0>(lds, F_QP, 4);
    {
      f32x2 q2 = *(const f32x2*)(lds + L_QPART + (lane & 31) * 8);
#pragma unroll
      for (int ww = 1; ww < 8; ++ww) q2 += *(const f32x2*)(lds + L_QPART + ww * 256 + (lane & 31) * 8);
      __builtin_amdgcn_raw_buffer_store_b128(u32x4{tagA, asu(q2[0]), tagB, asu(q2[1])}, rws,
                                             lane < 32 ? WS_QX + g * 4096 + qh * 2048 + cb * 512 + lane * 16 : OOB, 0, AUX_SC1);
    }
    __builtin_amdgcn_s_sleep(PSL);
    {
      float qa[4], qb[4];
      int of[4];
#pragma unroll
      for (int i = 0; i < 4; ++i) { qa[i] = 0.f; qb[i] = 0.f; of[i] = WS_QX + g * 4096 + i * 1024 + lane * 16; }
#pragma unroll 1
      for (unsigned spin = 0; spin < SPIN_CAP; ++spin) {
        asm volatile("" ::: "memory");
        u32x4 gr[4];
#pragma unroll
        for (int i = 0; i < 4; ++i) gr[i] = __builtin_amdgcn_raw_buffer_load_b128(rws, of[i], 0, AUX_SC1);
        bool done = true;
#pragma unroll
        for (int i = 0; i < 4; ++i) {
          const bool fresh = (gr[i][0] == tagA) & (gr[i][2] == tagB) & (of[i] != OOB);
          qa[i] = fresh ? asf(gr[i][1]) : qa[i]; qb[i] = fresh ? asf(gr[i][3]) : qb[i];
          of[i] = fresh ? OOB : of[i];
          done &= of[i] == OOB;
        }
        if (__all(done)) break;
        __builtin_amdgcn_s_sleep(QPS);
      }
      *(unsigned*)(lds + L_QS + lane * 4) = pack_bf16(qa[0] + qa[2], qb[0] + qb[2]);
      *(unsigned*)(lds + L_QS + 256 + lane * 4) = pack_bf16(qa[1] + qa[3], qb[1] + qb[3]);
      flag_add(lds, F_Q, lane);
    }
    const int fc = (lane & 1) * 4 + la;
    const unsigned xraw = __builtin_amdgcn_raw_buffer_load_b32(rx, red * 16 + (fc >> 1) * 4, 0, 0);
    const unsigned b2v = __builtin_amdgcn_raw_buffer_load_b16(mkrsrc(b2, 4), 0, 0, 0);
    if (red == 127) {
      wait_until(tst + 2000u);
#pragma unroll 1
      for (unsigned spin = 0; spin < SPIN_CAP; ++spin) {
        asm volatile("" ::: "memory");
        const u32x4 v0 = __builtin_amdgcn_raw_buffer_load_b128(rws, WS_QX + b * 65536 + lane * 512, 0, AUX_SC1);
        const u32x4 v1 = __builtin_amdgcn_raw_buffer_load_b128(rws, WS_QX + b * 65536 + 32768 + lane * 512, 0, AUX_SC1);
        if (__all((v0[0] == tagA) & (v0[2] == tagB) & (v1[0] == tagA) & (v1[2] == tagB))) break;
        __builtin_amdgcn_s_sleep(16);
      }
      __builtin_amdgcn_raw_buffer_store_b64(u32x2{e1, tagB}, rws, lane == 0 ? WS_EPOCH + b * 256 : OOB, 0, AUX_SC1);
    }
    float yv = 0.f, xfin = (fc & 1) ? bfhi(xraw) : bflo(xraw);
    const rsrc_t rout = mkrsrc(out + b * DM, DM * 2 + 2);
    int oout = (lane & 14) == 0 ? (red * 8 + fc) * 2 : OOB;
    if (red < 72) {
      flag_wait<16>(lds, F_Y, 4);
#pragma unroll
      for (int ww = 0; ww < 4; ++ww) yv += *(const float*)(lds + L_YP + ww * 32 + fc * 4);
      yv = rbf(rbf(yv) + bflo(b2v));
      __builtin_amdgcn_raw_buffer_store_b16((unsigned short)0, rout, oout, 0, 0);
    }
    int oa = (lane & 8) ? L_QPART + lane * 8 : L_OA + la * 64 + (lane & 7) * 8;
    asm volatile("" : "+v"(yv), "+v"(xfin), "+v"(oout), "+v"(oa));
    flag_wait<16>(lds, F_WP, 1);
    u32x4 ga[4];
#pragma unroll
    for (int i = 0; i < 4; ++i) ga[i] = u32x4{0u, 0u, 0u, 0u};
    int of1 = WS_PX + (g * 8 + (s & 4)) * 1024 + lane * 16;
    asm volatile("" : "+v"(of1), "+v"(ga[0]), "+v"(ga[1]), "+v"(ga[2]), "+v"(ga[3]));
    flag_wait<0>(lds, F_END, 1);
    __builtin_amdgcn_s_sleep(T1D);
    {
      unsigned bad = 1u;
#pragma unroll 1
      for (unsigned spin = 0; spin < SPIN_CAP; ++spin) {
        asm volatile("" ::: "memory");
        if (bad != 0u) {
#pragma unroll
          for (int i = 0; i < 4; ++i) ga[i] = __builtin_amdgcn_raw_buffer_load_b128(rws, of1 + i * 1024, 0, AUX_SC1);
        }
        bad = 0u;
#pragma unroll
        for (int i = 0; i < 4; ++i) bad |= (ga[i][0] ^ tagA) | (ga[i][2] ^ (tagA >> 8));
        bad &= 0xffu;
        if (__all(bad == 0u)) break;
        bad |= dppu<XOR1>(bad); bad |= dppu<XOR2>(bad); bad |= dppu<XOR7>(bad);
        __builtin_amdgcn_s_sleep(1);
      }
      const unsigned hm = __builtin_amdgcn_perm(ga[0][3], ga[0][1], 0x0c0c0400u) << (16 * (lane & 1));
      const unsigned hl = __builtin_amdgcn_perm(ga[1][3], ga[1][1], 0x0c0c0400u) << (16 * (lane & 1));
      const float pm = asf(hm | dppu<XOR1>(hm)), pl = asf(hl | dppu<XOR1>(hl));
      const float M = rows_max(fmaxf(pm, dppf<ROR8>(pm)));
      const float w0 = __builtin_amdgcn_exp2f((pm - M) * LOG2E) * pl;
      const float inv = recip(rows_sum(w0 + dppf<ROR8>(w0)));
      float v[4];
#pragma unroll
      for (int r = 0; r < 4; ++r) {
        const float t = rows_sum_t(w0 * cut_lo(ga[0][r]), w0 * cut_lo(ga[1][r]), w0 * cut_lo(ga[2][r]), w0 * cut_lo(ga[3][r]));
        v[r] = (t + dppf<ROR8>(t)) * inv;
      }
      *(u32x2*)(lds + oa) = u32x2{pack_bf16(v[0], v[1]), pack_bf16(v[2], v[3])};
      flag_add(lds, F_O, lane);
    }
    if (red < 72) {
      __builtin_amdgcn_s_sleep(T2D);
      u32x4 gb = {0u, 0u, 0u, 0u};
      const int of2 = WS_OX + (b * 18 + (red >> 2)) * 4096 + (lane >> 1) * 128 + (red & 3) * 32 + (lane & 1) * 16;
      unsigned bad = 1u;
#pragma unroll 1
      for (unsigned spin = 0; spin < SPIN_CAP; ++spin) {
        asm volatile("" ::: "memory");
        if (bad != 0u) gb = __builtin_amdgcn_raw_buffer_load_b128(rws, of2, 0, AUX_SC1);
        bad = (__builtin_amdgcn_perm(gb[1], gb[0], 0x0c0c0400u) | __builtin_amdgcn_perm(gb[3], gb[2], 0x04000c0cu)) ^ tagA;
        if (__all(bad == 0u)) break;
        bad |= dppu<XOR1>(bad);
        __builtin_amdgcn_s_sleep(1);
      }
      float t = rows_sum_t(cut_lo(gb[0]), cut_lo(gb[1]), cut_lo(gb[2]), cut_lo(gb[3]));
      t += dppf<XOR2>(t); t += dppf<ROR4>(t); t += dppf<ROR8>(t);
      __builtin_amdgcn_raw_buffer_store_b16((unsigned short)pack_bf16(rbf(xfin + rbf(t)) + yv, 0.f), rout, oout, 0, 0);
    }
  } else {
    const rsrc_t rk = mkrsrc(kc + (size_t)(id >> 1) * SPAN, SPAN);
    const rsrc_t rv = mkrsrc(vc + (size_t)(id >> 1) * SPAN, SPAN);
    auto poff = [&](int d, int h) {
      return ((d & 1) | (h << 1) | ((d & 2) << 1) | ((id & 1) << 3) | ((d >> 2) << 4)) * TILE_BYTES;
    };
    const int voffK = ((lane & 15) >> 2) * 256 + la * 64 + (lane & 3) * 16;
    const int voffV = lane * 16;
    u32x4 bA[8], bB[8];
    auto request = [&](u32x4 (&x)[8], rsrc_t r, int voff, int so) {
      asm volatile("" : "+v"(voff));
#pragma unroll
      for (int u = 0; u < 8; ++u) x[u] = __builtin_amdgcn_raw_buffer_load_b128(r, voff + (u & 3) * 1024, so + (u >> 2) * 4096, AUX_NT);
      __builtin_amdgcn_sched_barrier(0);
    };
    if (w >= 4) {
      const int rg = (w - 4) * 8 + (lane >> 3), c8 = lane & 7;
      unsigned xs[5];
      u32x4 wqv[9];
#pragma unroll
      for (int i = 0; i < 5; ++i) xs[i] = __builtin_amdgcn_raw_buffer_load_b32(rx, (lane + 64 * i) * 4, 0, 0);
      __builtin_amdgcn_sched_barrier(0);
      const rsrc_t rq = mkrsrc(wq + qh * 288 * 4096 + kvh * 256 + cb * 64, (287 * 4096 + 64) * 2);
#pragma unroll
      for (int i = 0; i < 9; ++i) wqv[i] = __builtin_amdgcn_raw_buffer_load_b128(rq, (rg + 32 * i) * 8192 + c8 * 16, 0, AUX_NT);
      __builtin_amdgcn_sched_barrier(0);
      __builtin_amdgcn_s_barrier();
      __builtin_amdgcn_sched_barrier(0);
      float xv[10], mean1, den1;
#pragma unroll
      for (int i = 0; i < 5; ++i) { xv[2 * i] = bflo(xs[i]); xv[2 * i + 1] = bfhi(xs[i]); }
      ln_stats(xv, lane < 32, mean1, den1);
#pragma unroll
      for (int i = 0; i < 5; ++i)
        *(unsigned*)(lds + L_XN + (lane + 64 * i) * 4) = pack_bf16(ln_apply(xv[2 * i], mean1, den1), ln_apply(xv[2 * i + 1], mean1, den1));
      asm volatile("" ::: "memory");
      f32x4 qa = {0.f, 0.f, 0.f, 0.f};
#pragma unroll
      for (int i = 0; i < 9; ++i) {
        const unsigned t = (unsigned)*(const unsigned short*)(lds + L_XN + (qh * 288 + rg + 32 * i) * 2) << (16 * (lane & 1));
        const int jp = c8 >> 1;
        qa = mfma32(wqv[i], u32x4{jp == 0 ? t : 0u, jp == 1 ? t : 0u, jp == 2 ? t : 0u, jp == 3 ? t : 0u}, qa);
      }
      if ((lane >> 5) == ((lane >> 3) & 1)) {
        float* qp = (float*)(lds + L_QPART) + ((w - 4) * 2 + (lane >> 5)) * 64 + (la & 1) * 32 + c8;
#pragma unroll
        for (int r = 0; r < 4; ++r) qp[r * 8] = qa[r];
      }
      flag_add(lds, F_QP, lane);
    }
    if (w < 4) asm volatile("s_barrier" ::: "memory");
    if (w >= PFD) flag_wait<1>(lds, F_ARR, w - PFD + 1);
    __builtin_amdgcn_sched_barrier(0);
    request(bA, rk, voffK, poff(w, 0));
    request(bB, rk, voffK, poff(w, 1));
    asm volatile("s_waitcnt vmcnt(0)" ::: "memory");
    flag_add(lds, F_ARR, lane);
    flag_wait<1>(lds, F_Q, 1);

    const int c = lane & 3, hb = (lane >> 2) & 1, tb = (lane >> 3) & 1;
    const unsigned tagA = __builtin_amdgcn_readfirstlane(*(const unsigned*)(lds + L_TAG));
    float m;
    f32x4 acc[2][2];
#pragma unroll
    for (int i = 0; i < 2; ++i)
#pragma unroll
      for (int j = 0; j < 2; ++j) acc[i][j] = f32x4{0.f, 0.f, 0.f, 0.f};
    const u32x4 qf = *(const u32x4*)(lds + L_QS + (hb * 128 + la * 32 + c * 8) * 2);
    const u32x4 zero4 = {0u, 0u, 0u, 0u};
    const u32x4 q0 = tb ? zero4 : qf, q1 = tb ? qf : zero4;
    const unsigned mask0 = c == 0 ? 0x0000ffffu : c == 1 ? 0xffff0000u : 0u;
    const unsigned mask1 = c == 2 ? 0x0000ffffu : c == 3 ? 0xffff0000u : 0u;
    auto qk = [&](const u32x4 (&x)[8], f32x4 (&sc)[4]) {
#pragma unroll
      for (int i = 0; i < 4; ++i) sc[i] = mfma32(x[i], q0, f32x4{0.f, 0.f, 0.f, 0.f});
#pragma unroll
      for (int i = 0; i < 4; ++i) sc[i] = mfma32(x[4 + i], q1, sc[i]);
    };
    auto score = [&](const f32x4 (&sc)[4]) {
      float t4[4];
#pragma unroll
      for (int i = 0; i < 4; ++i) t4[i] = (dppf<QB0>(sc[i][0]) + dppf<QB1>(sc[i][1])) + (dppf<QB2>(sc[i][2]) + dppf<QB3>(sc[i][3]));
      const float raw = (c & 2) ? ((c & 1) ? t4[3] : t4[2]) : ((c & 1) ? t4[1] : t4[0]);
      float sv = rbf(rbf(raw) * SCALE);
      asm volatile("" : "+v"(sv));
      return sv;
    };
    auto head_max = [&](float v) {
      v = fmaxf(v, dppf<XOR1>(v));
      v = fmaxf(v, dppf<XOR2>(v));
      v = fmaxf(v, dppf<ROR8>(v));
      return rows_max(v);
    };
    auto pv = [&](const u32x4 (&x)[8], float sv) {
      const float p = __builtin_amdgcn_exp2f((sv - m) * LOG2E);
      const unsigned pk = pack_bf16(p, p);
      unsigned pb0[4], pb1[4];
      pb0[0] = dppu<QB0>(pk) & mask0; pb1[0] = dppu<QB0>(pk) & mask1;
      pb0[1] = dppu<QB1>(pk) & mask0; pb1[1] = dppu<QB1>(pk) & mask1;
      pb0[2] = dppu<QB2>(pk) & mask0; pb1[2] = dppu<QB2>(pk) & mask1;
      pb0[3] = dppu<QB3>(pk) & mask0; pb1[3] = dppu<QB3>(pk) & mask1;
#pragma unroll
      for (int u = 0; u < 8; ++u) {
        acc[u >> 2][0] = mfma16(x[u][0], x[u][1], pb0[u & 3], pb1[u & 3], acc[u >> 2][0]);
        acc[u >> 2][1] = mfma16(x[u][2], x[u][3], pb0[u & 3], pb1[u & 3], acc[u >> 2][1]);
      }
    };
    auto put_ml = [&](int k, float mk, float lk) {
      lk += dppf<XOR1>(lk);
      lk += dppf<XOR2>(lk);
      lk += dppf<ROR8>(lk);
      lk = rows_sum(lk);
      if ((lane & ~4) == 0) *(f32x2*)(lds + L_ML + (k * 2 + hb) * 8) = f32x2{mk, lk};
      flag_add(lds, F_ML, lane);
    };
    auto hold = [&](int dly) {
      __builtin_amdgcn_sched_barrier(0);
      for (int i = 0; i < dly; ++i) __builtin_amdgcn_s_sleep(1);
      __builtin_amdgcn_sched_barrier(0);
    };
    const int hw = w - S, nwk = (s & 3) == 3 ? 6 : 4;
    f32x2 sl;
    if (w < S) {
      __builtin_amdgcn_s_setprio(2);
      int shift = 0, lprev = -0x40000000;
      auto pace = [&](int it) {
        int lead = (int)(tst + REF0 + (unsigned)((NWAVE / 2 + it) * TAU16 >> 4) - now32()) + shift;
        if (lprev - lead > GLT) {
          shift += lprev - lead;
          lead = lprev;
          __hip_atomic_store((unsigned*)(lds + L_FLAG) + F_SH + w, (unsigned)shift, __ATOMIC_RELAXED, __HIP_MEMORY_SCOPE_WORKGROUP);
        }
        lprev = it ? lead : -0x40000000;
        return min(max(DLY0 + ((lead * GAIN + 128) >> 8), 0), DLYMAX);
      };
      unsigned char* sco = lds + L_WO + w * (64 * 384) + lane * 8;
      flag_wait<1>(lds, F_ARR, NWAVE + w - PFD + 1);
      float mx = -INFINITY;
      for (int j = 0; j < NPAIR; ++j) {
        const int dly = pace(j);
        const bool k = j + 1 < NPAIR;
        const rsrc_t r = k ? rk : rv;
        const int vo = k ? voffK : voffV, d = NWAVE + S * (k ? j : j - 1) + w;
        f32x4 sc[4];
        qk(bA, sc);
        hold(dly);
        request(bA, r, vo, poff(d, 0));
        const float s0 = score(sc);
        qk(bB, sc);
        hold(dly);
        request(bB, r, vo, poff(d, 1));
        const float s1 = score(sc);
        mx = fmaxf(mx, fmaxf(s0, s1));
        *(f32x2*)(sco + j * 512) = f32x2{s0, s1};
      }
      m = head_max(mx);
      if ((lane & ~4) == 0) *(float*)(lds + L_ML + (w * 2 + hb) * 8) = m;
      flag_add(lds, F_P1 + w, lane);
      for (int j = 0; j + 1 < NPAIR; ++j) {
        const f32x2 sv = *(const f32x2*)(sco + (NPAIR - 1 - j) * 512);
        const int dly = pace(NPAIR + j);
        const int d = j + 2 < NPAIR ? NWAVE + S * (NPAIR - 3 - j) + w : w;
        pv(bA, sv[0]);
        hold(dly);
        request(bA, rv, voffV, poff(d, 0));
        pv(bB, sv[1]);
        hold(dly);
        request(bB, rv, voffV, poff(d, 1));
      }
      flag_add(lds, F_LAST + w, lane);
      sl = *(const f32x2*)sco;
    }
    asm volatile("" ::: "memory");
    if (w >= S) {
      {
        f32x4 sc[4];
        qk(bA, sc);
        sl[0] = score(sc);
        qk(bB, sc);
        sl[1] = score(sc);
        m = head_max(fmaxf(sl[0], sl[1]));
        put_ml(w, m, __builtin_amdgcn_exp2f((sl[0] - m) * LOG2E) + __builtin_amdgcn_exp2f((sl[1] - m) * LOG2E));
      }
      if (hw < S) {
        flag_wait<16>(lds, F_P1 + hw, 1);
        const float ms = *(const float*)(lds + L_ML + (hw * 2 + hb) * 8);
        float ls = 0.f;
#pragma unroll 1
        for (int j = 0; j < NPAIR; ++j) {
          const f32x2 sv = *(const f32x2*)(lds + L_WO + hw * (64 * 384) + lane * 8 + j * 512);
          ls += __builtin_amdgcn_exp2f((sv[0] - ms) * LOG2E) + __builtin_amdgcn_exp2f((sv[1] - ms) * LOG2E);
        }
        put_ml(hw, ms, ls);
      }
      if (w >= 4 && red < 72) {
        const int ht = (w - 4) * 64 + lane;
        unsigned a1[9], ab[9], a2[9];
        const rsrc_t r1 = mkrsrc(w1, FF * 2), rb = mkrsrc(b1, FF * 2), r2 = mkrsrc(w2, FF * 2);
#pragma unroll
        for (int i = 0; i < 9; ++i) {
          a1[i] = __builtin_amdgcn_raw_buffer_load_b32(r1, (ht + 256 * i) * 4, 0, 0);
          ab[i] = __builtin_amdgcn_raw_buffer_load_b32(rb, (ht + 256 * i) * 4, 0, 0);
          a2[i] = __builtin_amdgcn_raw_buffer_load_b32(r2, (ht + 256 * i) * 4, 0, 0);
        }
        __builtin_amdgcn_sched_barrier(0);
        float xn[10];
#pragma unroll
        for (int i = 0; i < 5; ++i) { const unsigned v = *(const unsigned*)(lds + L_XN + (lane + 64 * i) * 4); xn[2 * i] = bflo(v); xn[2 * i + 1] = bfhi(v); }
        float mean2, den2;
        ln_stats(xn, lane < 32, mean2, den2);
        constexpr float GC0 = -2.0f * 0.7978845608028654f * LOG2E, GC1 = GC0 * 0.044715f;
#pragma unroll 1
        for (int j = 0; j < 8; ++j) {
          const float hv = ln_apply(bflo(*(const unsigned short*)(lds + L_XN + (red * 8 + j) * 2)), mean2, den2);
          float y = 0.f;
#pragma unroll
          for (int i = 0; i < 9; ++i) {
            asm volatile("" : "+v"(a1[i]), "+v"(ab[i]), "+v"(a2[i]));
            const unsigned pr = pack_bf16(hv * bflo(a1[i]), hv * bfhi(a1[i]));
            const unsigned uu = pack_bf16(bflo(pr) + bflo(ab[i]), bfhi(pr) + bfhi(ab[i]));
            const float u0 = bflo(uu), u1 = bfhi(uu);
            const float e0 = __builtin_amdgcn_exp2f(u0 * fmaf(u0 * u0, GC1, GC0));
            const float e1 = __builtin_amdgcn_exp2f(u1 * fmaf(u1 * u1, GC1, GC0));
            const unsigned gg = pack_bf16(u0 * __builtin_amdgcn_rcpf(1.0f + e0), u1 * __builtin_amdgcn_rcpf(1.0f + e1));
            y = fmaf(bflo(gg), bflo(a2[i]), y);
            y = fmaf(bfhi(gg), bfhi(a2[i]), y);
          }
          y = wave_sum(y);
          if (lane == 0) *(float*)(lds + L_YP + (w - 4) * 32 + j * 4) = y;
        }
        flag_add(lds, F_Y, lane);
      }
      if (hw < RSVD) {
        const unsigned due = tst + REF0 + RSV0 + (unsigned)((NWAVE + 2 * NITER + hw) * TAU16 >> 5);
        bool seen = false;
        unsigned tseen = 0;
#pragma unroll 1
        for (unsigned spin = 0; spin < (SPIN_CAP << 2); ++spin) {
          const unsigned now = now32();
          const int late = (int)(now - due) - (int)flag_get(lds, F_SH + (hw & 1));
          if (!seen && flag_get(lds, F_LAST + (hw & 1)) != 0u) { seen = true; tseen = now; }
          if (late >= 0 || (seen && (late >= -RSVH || (int)(now - tseen) >= RSVB))) break;
          __builtin_amdgcn_s_sleep(2);
        }
      } else {
        flag_wait<2>(lds, F_AR2, hw - RSVD + 1);
      }
      __builtin_amdgcn_s_setprio(2);
      __builtin_amdgcn_sched_barrier(0);
      request(bA, rv, voffV, poff(w, 0));
      request(bB, rv, voffV, poff(w, 1));
    }
    int po = WS_PX + ((g * 8 + hb * 4 + la) * 8 + s) * 128 + (tb * 4 + c) * 16;
    float fw;
    unsigned side;
    {
      flag_wait<1>(lds, F_ML, NWAVE);
      f32x2 ml[NWAVE];
#pragma unroll
      for (int k = 0; k < NWAVE; ++k) ml[k] = *(const f32x2*)(lds + L_ML + (k * 2 + hb) * 8);
      float mx = ml[0][0], L = 0.f;
#pragma unroll
      for (int k = 1; k < NWAVE; ++k) mx = fmaxf(mx, ml[k][0]);
#pragma unroll
      for (int k = 0; k < NWAVE; ++k) L = fmaf(__builtin_amdgcn_exp2f((ml[k][0] - mx) * LOG2E), ml[k][1], L);
      fw = __builtin_amdgcn_exp2f((m - mx) * LOG2E) * recip(L);
      side = asu((la & 1) ? L : mx) >> (16 * (c & 1));
    }
    asm volatile("" : "+v"(po), "+v"(fw), "+v"(side));
    if (w >= S && hw < NH - RSVD) {
      asm volatile("s_waitcnt vmcnt(0)" ::: "memory");
      flag_add(lds, F_AR2, lane);
    }
    pv(bA, sl[0]);
    pv(bB, sl[1]);
    {
      f32x4 res;
#pragma unroll
      for (int r = 0; r < 4; ++r) {
        const float own = tb ? acc[1][1][r] : acc[0][0][r], snd = tb ? acc[1][0][r] : acc[0][1][r];
        res[r] = (own + dppf<ROR8>(snd)) * fw;
      }
      *(f32x4*)(lds + L_PART + w * 1024 + lane * 16) = res;
      asm volatile("" ::: "memory");
      unsigned old = 0;
      if (lane == 0) old = __hip_atomic_fetch_add((unsigned*)(lds + L_FLAG) + F_WP + vzero(), 1u, __ATOMIC_RELAXED, __HIP_MEMORY_SCOPE_WORKGROUP);
      asm volatile("" ::: "memory");
      f32x4 pp[NWAVE];
#pragma unroll
      for (int k = 0; k < NWAVE; ++k) pp[k] = *(const f32x4*)(lds + L_PART + k * 1024 + lane * 16);
      __builtin_amdgcn_sched_barrier(0);
      if (__builtin_amdgcn_readfirstlane(old) == NWAVE - 1) {
        const f32x4 sum = ((pp[0] + pp[1]) + (pp[2] + pp[3])) + ((pp[4] + pp[5]) + (pp[6] + pp[7]));
        __builtin_amdgcn_raw_buffer_store_b128(
            u32x4{put_lo(sum[0], tagA), put_lo(sum[1], side), put_lo(sum[2], tagA >> 8), put_lo(sum[3], side >> 8)}, rws, po, 0, AUX_SC1);
        flag_add(lds, F_END, lane);
      }
    }
    if (w < S) {
      flag_wait<1>(lds, F_WP, WOL);
      const rsrc_t ro = mkrsrc(wo + (size_t)(kvh * 256 + qh * 128 + w * 64) * DM, 64 * DM * 2);
      u32x4 wt[24];
#pragma unroll
      for (int i = 0; i < 24; ++i)
        wt[i] = __builtin_amdgcn_raw_buffer_load_b128(
            ro, 2 * (i % 3) < nwk ? ((i / 3) * 8 + (lane >> 3)) * (DM * 2) + (s & 3) * 256 + (i % 3) * 128 + (lane & 7) * 16 : OOB, 0, AUX_NT);
#pragma unroll
      for (int i = 0; i < 24; ++i)
        *(u32x4*)(lds + L_WO + w * 24576 + (i % 3) * 8192 + ((i / 3) * 8 + ((lane >> 3) & 4) + ((lane >> 2) & 2) + ((lane >> 4) & 1)) * 128 +
                  (((lane & 7) * 16) ^ (((i / 3) & 3) * 32))) = wt[i];
      flag_add(lds, F_WO, lane);
    } else if (hw < nwk) {
      const int n = lane & 15;
      int oo = n < 2 ? WS_OX + (b * 18 + (s & 3) * 4 + hw) * 4096 + (kvh * 2 + qh) * 128 + n * 64 + la * 16 : OOB;
      asm volatile("" : "+v"(oo));
      flag_wait<1>(lds, F_WO, S);
      u32x4 wof[2][4];
      {
        typedef __attribute__((address_space(3))) s16x4* lds_s16x4;
        unsigned char* img = lds + L_WO + (hw >> 1) * 8192 + ((n >> 2) * 8 + (la >> 1) * 4 + (la & 1)) * 128 + (lane & 3) * 8;
#pragma unroll
        for (int t = 0; t < 2; ++t)
#pragma unroll
          for (int ks = 0; ks < 4; ++ks) {
            unsigned char* p = img + (ks >> 1) * 24576 + (ks & 1) * 4096 + (((hw & 1) * 2 + t) ^ (n >> 2)) * 32;
            const u32x2 lo = __builtin_bit_cast(u32x2, __builtin_amdgcn_ds_read_tr16_b64_v4i16((lds_s16x4)p));
            const u32x2 hi = __builtin_bit_cast(u32x2, __builtin_amdgcn_ds_read_tr16_b64_v4i16((lds_s16x4)(p + 256)));
            wof[t][ks] = u32x4{lo[0], lo[1], hi[0], hi[1]};
          }
      }
      u32x4 ob[4];
#pragma unroll 1
      for (unsigned spin = 0; spin < (SPIN_CAP << 4); ++spin) {
        asm volatile("" ::: "memory");
        const unsigned f = __hip_atomic_load((unsigned*)(lds + L_FLAG) + F_O, __ATOMIC_RELAXED, __HIP_MEMORY_SCOPE_WORKGROUP);
        asm volatile("" ::: "memory");
#pragma unroll
        for (int ks = 0; ks < 4; ++ks) ob[ks] = *(const u32x4*)(lds + L_OA + ks * 64 + la * 16);
        __builtin_amdgcn_sched_barrier(0);
        if (__builtin_amdgcn_readfirstlane(f) != 0u) break;
      }
      f32x4 d0 = {0.f, 0.f, 0.f, 0.f}, d1 = {0.f, 0.f, 0.f, 0.f};
#pragma unroll
      for (int ks = 0; ks < 4; ++ks) { d0 = mfma32(wof[0][ks], ob[ks], d0); d1 = mfma32(wof[1][ks], ob[ks], d1); }
      const f32x4 d = n ? d1 : d0;
      __builtin_amdgcn_raw_buffer_store_b128(
          u32x4{put_lo(d[0], tagA), put_lo(d[1], tagA >> 8), put_lo(d[2], tagA >> 16), put_lo(d[3], tagA >> 24)}, rws, oo, 0, AUX_SC1);
    }
  }
}

}

extern "C" void kernel_launch(void* const* d_in, const int* in_sizes, int n_in, void* d_out, int out_size, void* d_ws,
                              size_t ws_size, hipStream_t stream) {
  (void)in_sizes; (void)n_in; (void)out_size; (void)ws_size;
  fused_decode_kernel<<<dim3(NWG), dim3(NTHR), 0, stream>>>(
      (const unsigned short*)d_in[0], (const unsigned char*)d_in[1], (const unsigned char*)d_in[2],
      (const unsigned short*)d_in[3], (const unsigned short*)d_in[4], (const unsigned short*)d_in[6],
      (const unsigned short*)d_in[7], (const unsigned short*)d_in[8], (const unsigned short*)d_in[9],
      (unsigned short*)d_out, (unsigned char*)d_ws);
}
```

```cpp
#include <hip/hip_runtime.h>
#include <hip/hip_bf16.h>
#include <stdint.h>

namespace {

typedef unsigned u32x2 __attribute__((ext_vector_type(2)));
typedef unsigned u32x4 __attribute__((ext_vector_type(4)));
typedef float f32x2 __attribute__((ext_vector_type(2)));
typedef float f32x4 __attribute__((ext_vector_type(4)));
typedef short s16x4 __attribute__((ext_vector_type(4)));
typedef short s16x8 __attribute__((ext_vector_type(8)));
typedef __bf16 bf16x2 __attribute__((ext_vector_type(2)));
typedef __amdgpu_buffer_rsrc_t rsrc_t;

constexpr int DM = 576, SK = 32768, DH = 128, FF = 4608;
constexpr int NSPLIT = 8, NWG = 256, NWAVE = 8, NTHR = (NWAVE + 1) * 64;
constexpr int KEYS_WG = SK / NSPLIT;
constexpr int TILE_BYTES = 32 * DH * 2;
constexpr int NT = KEYS_WG / 32;
constexpr int SPAN = 2 * KEYS_WG * DH * 2;
#define NSTR 4
constexpr int S = NSTR, NH = NWAVE - S;
constexpr int NPAIR = (NT / 2 - NH) / S;
constexpr int NITER = 2 * NPAIR - 1;
static_assert(2 * (NPAIR * S + NH) == NT, "every tile has one owner");

#define SCA 170
#define SCT 424
#define SLT 30
#define SLS 0
#define SLI 6
#define MIND 0
#define HOLDA 1
#define RSH 0
#define GAP 40
#define GAPI 0
#define CBASE 1
#define CBETA 6
#define LGAIN 0
#define WOT 1
#define GLA 20
#define PGL 40
#define PFD 3
#define DLY0 8
#define DASY 0
#define DLYMIN 0
#define GLT 80
#define DLYMAX 12
#define GAIN 3
#define TAU16 1704
#define REF0 90
#define RSV0 0
#define RSVH 0
#define RSVB 400
#define RSVD 6
#define WOL 6
#define PSL 6
#define QPS 2
#define T1D 7
#define T2D 18

constexpr int PGAP = 2 * NWAVE + 2 * S * (NPAIR + GAPI);
constexpr int AUX_NT = 2, AUX_SC1 = 16;
constexpr unsigned RSRC3 = 0x00020000u;
constexpr unsigned SPIN_CAP = 1u << 19;
constexpr int OOB = 0x7ff00000;

constexpr int WS_EPOCH = 0;
constexpr int WS_QX = 4096;
constexpr int WS_PX = WS_QX + 32 * 4096;
constexpr int WS_OX = WS_PX + NWG * 1024;
constexpr int WS_END = WS_OX + 2 * 18 * 4096;

constexpr int L_FLAG = 0;
constexpr int L_TAG = 64;
constexpr int L_XN = 80;
constexpr int L_QPART = L_XN + 1280;
constexpr int L_QS = L_QPART + 8 * 64 * 4;
constexpr int L_OA = L_QS + 512;
constexpr int L_YP = L_OA + 256;
constexpr int L_ML = L_YP + 4 * 8 * 4;
constexpr int L_PART = L_ML + NWAVE * 16;
constexpr int L_WO = L_PART + NWAVE * 1024;
constexpr int L_SC = L_WO + 128 * 384;
constexpr int L_END = L_SC + S * NPAIR * 512;
constexpr int F_QP = 0, F_Q = 1, F_WP = 2, F_END = 3, F_O = 4, F_Y = 5, F_ARR = 6, F_AR2 = 7, F_WO = 8, F_ML = 9, F_SH = 10, F_P1 = 12;

constexpr float LOG2E = 1.4426950408889634f;
constexpr float SCALE = 0.08838834764831845f;
constexpr float LN_EPS = 1e-5f;

__device__ __forceinline__ float asf(unsigned u) { return __builtin_bit_cast(float, u); }
__device__ __forceinline__ unsigned asu(float f) { return __builtin_bit_cast(unsigned, f); }
__device__ __forceinline__ float bflo(unsigned u) { return asf(u << 16); }
__device__ __forceinline__ float bfhi(unsigned u) { return asf(u & 0xffff0000u); }
__device__ __forceinline__ unsigned pack_bf16(float lo, float hi) {
  f32x2 v = {lo, hi};
  return __builtin_bit_cast(unsigned, __builtin_convertvector(v, bf16x2));
}
__device__ __forceinline__ float rbf(float f) { return (float)(__bf16)f; }
template <int CTRL> __device__ __forceinline__ unsigned dppu(unsigned x) {
  return (unsigned)__builtin_amdgcn_mov_dpp((int)x, CTRL, 0xf, 0xf, true);
}
template <int CTRL> __device__ __forceinline__ float dppf(float x) { return asf(dppu<CTRL>(asu(x))); }
constexpr int XOR1 = 0xB1, XOR2 = 0x4E, XOR7 = 0x141, ROR4 = 0x124, ROR8 = 0x128;
constexpr int QB0 = 0x00, QB1 = 0x55, QB2 = 0xAA, QB3 = 0xFF;
__device__ __forceinline__ float rows_sum(float x) {
  auto s = __builtin_amdgcn_permlane16_swap(asu(x), asu(x), false, false);
  x = asf(s[0]) + asf(s[1]);
  auto t = __builtin_amdgcn_permlane32_swap(asu(x), asu(x), false, false);
  return asf(t[0]) + asf(t[1]);
}
__device__ __forceinline__ float rows_max(float x) {
  auto s = __builtin_amdgcn_permlane16_swap(asu(x), asu(x), false, false);
  x = fmaxf(asf(s[0]), asf(s[1]));
  auto t = __builtin_amdgcn_permlane32_swap(asu(x), asu(x), false, false);
  return fmaxf(asf(t[0]), asf(t[1]));
}
__device__ __forceinline__ float rows_sum_t(float v0, float v1, float v2, float v3) {
  const auto p0 = __builtin_amdgcn_permlane32_swap(asu(v0), asu(v2), false, false);
  const auto p1 = __builtin_amdgcn_permlane32_swap(asu(v1), asu(v3), false, false);
  const auto p2 = __builtin_amdgcn_permlane16_swap(asu(asf(p0[0]) + asf(p0[1])), asu(asf(p1[0]) + asf(p1[1])), false, false);
  return asf(p2[0]) + asf(p2[1]);
}
__device__ __forceinline__ float wave_sum(float x) {
  x += dppf<XOR1>(x); x += dppf<XOR2>(x); x += dppf<ROR4>(x); x += dppf<ROR8>(x);
  return rows_sum(x);
}
__device__ __forceinline__ float recip(float x) {
  const float r = __builtin_amdgcn_rcpf(x);
  return fmaf(fmaf(-x, r, 1.0f), r, r);
}
__device__ __forceinline__ rsrc_t mkrsrc(const void* p, int bytes) {
  return __builtin_amdgcn_make_buffer_rsrc(const_cast<void*>(p), 0, bytes, RSRC3);
}
__device__ __forceinline__ f32x4 mfma32(u32x4 a, u32x4 b, f32x4 c) {
  return __builtin_amdgcn_mfma_f32_16x16x32_bf16(__builtin_bit_cast(s16x8, a), __builtin_bit_cast(s16x8, b), c, 0, 0, 0);
}
__device__ __forceinline__ f32x4 mfma16(unsigned a0, unsigned a1, unsigned b0, unsigned b1, f32x4 c) {
  u32x2 a = {a0, a1}, b = {b0, b1};
  return __builtin_amdgcn_mfma_f32_16x16x16bf16_1k(__builtin_bit_cast(s16x4, a), __builtin_bit_cast(s16x4, b), c, 0, 0, 0);
}
__device__ __forceinline__ unsigned put_lo(float v, unsigned byte) { return (asu(v) & 0xffffff00u) | (byte & 0xffu); }
__device__ __forceinline__ float cut_lo(unsigned u) { return asf(u & 0xffffff00u); }
__device__ __forceinline__ unsigned now32() { return (unsigned)__builtin_amdgcn_s_memrealtime(); }
__device__ __forceinline__ void wait_until(unsigned t) {
  while ((int)(now32() - t) < 0) __builtin_amdgcn_s_sleep(1);
}
__device__ __forceinline__ int vzero() { int z = 0; asm("" : "+v"(z)); return z; }
__device__ __forceinline__ void flag_add(unsigned char* lds, int f, int lane) {
  asm volatile("" ::: "memory");
  if (lane == 0) __hip_atomic_fetch_add((unsigned*)(lds + L_FLAG) + f + vzero(), 1u, __ATOMIC_RELAXED, __HIP_MEMORY_SCOPE_WORKGROUP);
  asm volatile("" ::: "memory");
}
__device__ __forceinline__ unsigned flag_get(unsigned char* lds, int f) {
  return __builtin_amdgcn_readfirstlane(__hip_atomic_load((unsigned*)(lds + L_FLAG) + f, __ATOMIC_RELAXED, __HIP_MEMORY_SCOPE_WORKGROUP));
}
template <int SLEEP> __device__ __forceinline__ void flag_wait(unsigned char* lds, int f, unsigned n) {
#pragma unroll 1
  for (unsigned spin = 0; spin < (SPIN_CAP << 4); ++spin) {
    if (flag_get(lds, f) >= n) break;
    if (SLEEP) __builtin_amdgcn_s_sleep(SLEEP);
  }
  asm volatile("" ::: "memory");
}
__device__ __forceinline__ void ln_stats(const float (&v)[10], bool last_valid, float& mean_b, float& den_b) {
  float s = 0.f;
#pragma unroll
  for (int i = 0; i < 8; ++i) s += v[i];
  s += last_valid ? (v[8] + v[9]) : 0.f;
  const float mean = wave_sum(s) * (1.0f / DM);
  float q = 0.f;
#pragma unroll
  for (int i = 0; i < 8; ++i) { const float d = v[i] - mean; q = fmaf(d, d, q); }
  { const float d8 = v[8] - mean, d9 = v[9] - mean; q += last_valid ? fmaf(d8, d8, d9 * d9) : 0.f; }
  const float var = wave_sum(q) * (1.0f / DM);
  mean_b = rbf(mean);
  den_b = rbf(__builtin_amdgcn_sqrtf(rbf(rbf(var) + LN_EPS)));
}
__device__ __forceinline__ float ln_apply(float x, float mean_b, float den_b) {
  const float d = rbf(x - mean_b), r = __builtin_amdgcn_rcpf(den_b), q = d * r;
  return rbf(fmaf(fmaf(-q, den_b, d), r, q));
}

#define STAMP(i) do { } while (0)

__global__ void __launch_bounds__(NTHR, 3)
fused_decode_kernel(const unsigned short* __restrict__ x, const unsigned char* __restrict__ kc, const unsigned char* __restrict__ vc,
                    const unsigned short* __restrict__ wq, const unsigned short* __restrict__ wo,
                    const unsigned short* __restrict__ w1, const unsigned short* __restrict__ b1,
                    const unsigned short* __restrict__ w2, const unsigned short* __restrict__ b2,
                    unsigned short* __restrict__ out, unsigned char* ws) {
  __shared__ __attribute__((aligned(16))) unsigned char lds[L_END];
  const int lane = threadIdx.x & 63, wv = __builtin_amdgcn_readfirstlane(threadIdx.x >> 6), w = wv < 8 ? wv ^ 4 : wv;
  const unsigned tst = now32();
  const int g = blockIdx.x >> 3, s = (blockIdx.x + 3 * g) & 7, id = g * 8 + s, b = g >> 4, kvh = g & 15;
  STAMP(0);
  const rsrc_t rws = mkrsrc(ws, WS_END);
  const rsrc_t rx = mkrsrc(x + b * DM, DM * 2);
  const int qh = s >> 2, cb = (s + (kvh >> 2)) & 3;
  const int la = lane >> 4;
  const int red = kvh * 8 + s;

  u32x2 ep = {0u, 0u};
  if (w == NWAVE) {
    ep = __builtin_amdgcn_raw_buffer_load_b64(rws, WS_EPOCH + b * 256, 0, 0);
    if (lane < 16) *((unsigned*)(lds + L_FLAG) + lane) = 0u;
    asm volatile("s_waitcnt lgkmcnt(0)\n\ts_barrier" ::: "memory");
  }
  if (w >= NWAVE) {
    __builtin_amdgcn_s_setprio(3);
    const unsigned e0 = __builtin_amdgcn_readfirstlane(ep[0]);
    const unsigned e1 = ((e0 & 0x7f7f7f7fu) + 0x01010101u) ^ (e0 & 0x80808080u);
    const unsigned tagA = e1 ^ 0xb77a3d00u;
    const unsigned tagB = __builtin_amdgcn_readfirstlane(ep[1]) + 1u;
    if (lane == 0) *(unsigned*)(lds + L_TAG) = tagA;
    flag_wait<0>(lds, F_QP, 4);
    {
      f32x2 q2 = *(const f32x2*)(lds + L_QPART + (lane & 31) * 8);
#pragma unroll
      for (int ww = 1; ww < 8; ++ww) q2 += *(const f32x2*)(lds + L_QPART + ww * 256 + (lane & 31) * 8);
      __builtin_amdgcn_raw_buffer_store_b128(u32x4{tagA, asu(q2[0]), (tagB & 0xffffu) | (tst << 16), asu(q2[1])}, rws,
                                             lane < 32 ? WS_QX + g * 4096 + qh * 2048 + cb * 512 + lane * 16 : OOB, 0, AUX_SC1);
      STAMP(8);
    }
    __builtin_amdgcn_s_sleep(PSL);
    {
      float qa[4], qb[4];
      int ts[4];
      int of[4];
#pragma unroll
      for (int i = 0; i < 4; ++i) { qa[i] = 0.f; qb[i] = 0.f; ts[i] = 0; of[i] = WS_QX + g * 4096 + i * 1024 + lane * 16; }
#pragma unroll 1
      for (unsigned spin = 0; spin < SPIN_CAP; ++spin) {
        asm volatile("" ::: "memory");
        u32x4 gr[4];
#pragma unroll
        for (int i = 0; i < 4; ++i) gr[i] = __builtin_amdgcn_raw_buffer_load_b128(rws, of[i], 0, AUX_SC1);
        bool done = true;
#pragma unroll
        for (int i = 0; i < 4; ++i) {
          const bool fresh = (gr[i][0] == tagA) & (((gr[i][2] ^ tagB) & 0xffffu) == 0u) & (of[i] != OOB);
          ts[i] = fresh ? (int)(gr[i][2] - (tst << 16)) >> 16 : ts[i];
          qa[i] = fresh ? asf(gr[i][1]) : qa[i]; qb[i] = fresh ? asf(gr[i][3]) : qb[i];
          of[i] = fresh ? OOB : of[i];
          done &= of[i] == OOB;
        }
        if (__all(done)) break;
        __builtin_amdgcn_s_sleep(QPS);
      }
      *(unsigned*)(lds + L_QS + lane * 4) = pack_bf16(qa[0] + qa[2], qb[0] + qb[2]);
      *(unsigned*)(lds + L_QS + 256 + lane * 4) = pack_bf16(qa[1] + qa[3], qb[1] + qb[3]);
      {
        int sm = 0, mx = -32768;
#pragma unroll
        for (int i = 0; i < 4; ++i) {
          const int t0 = __builtin_amdgcn_readlane(ts[i], 0), t1 = __builtin_amdgcn_readlane(ts[i], 32);
          sm += t0 + t1; mx = max(mx, max(t0, t1));
        }
        sm >>= 3;
        if (lane == 0) *(unsigned*)(lds + L_TAG + 4) = tst + (unsigned)(CBASE ? sm + ((mx - sm) * CBETA >> 4) : 0);
      }
      flag_add(lds, F_Q, lane);
    }
    const int fc = (lane & 1) * 4 + la;
    const unsigned xraw = __builtin_amdgcn_raw_buffer_load_b32(rx, red * 16 + (fc >> 1) * 4, 0, 0);
    const unsigned b2v = __builtin_amdgcn_raw_buffer_load_b16(mkrsrc(b2, 4), 0, 0, 0);
    if (red == 127) {
      wait_until(tst + 2000u);
#pragma unroll 1
      for (unsigned spin = 0; spin < SPIN_CAP; ++spin) {
        asm volatile("" ::: "memory");
        const u32x4 v0 = __builtin_amdgcn_raw_buffer_load_b128(rws, WS_QX + b * 65536 + lane * 512, 0, AUX_SC1);
        const u32x4 v1 = __builtin_amdgcn_raw_buffer_load_b128(rws, WS_QX + b * 65536 + 32768 + lane * 512, 0, AUX_SC1);
        if (__all((v0[0] == tagA) & (v1[0] == tagA) & ((((v0[2] ^ tagB) | (v1[2] ^ tagB)) & 0xffffu) == 0u))) break;
        __builtin_amdgcn_s_sleep(16);
      }
      __builtin_amdgcn_raw_buffer_store_b64(u32x2{e1, tagB}, rws, lane == 0 ? WS_EPOCH + b * 256 : OOB, 0, AUX_SC1);
    }
    float yv = 0.f, xfin = (fc & 1) ? bfhi(xraw) : bflo(xraw);
    const rsrc_t rout = mkrsrc(out + b * DM, DM * 2 + 2);
    int oout = (lane & 14) == 0 ? (red * 8 + fc) * 2 : OOB;
    if (red < 72) {
      flag_wait<16>(lds, F_Y, 4);
#pragma unroll
      for (int ww = 0; ww < 4; ++ww) yv += *(const float*)(lds + L_YP + ww * 32 + fc * 4);
      yv = rbf(rbf(yv) + bflo(b2v));
      __builtin_amdgcn_raw_buffer_store_b16((unsigned short)0, rout, oout, 0, 0);
    }
    int oa = (lane & 8) ? L_QPART + lane * 8 : L_OA + la * 64 + (lane & 7) * 8;
    asm volatile("" : "+v"(yv), "+v"(xfin), "+v"(oout), "+v"(oa));
    flag_wait<16>(lds, F_WP, 1);
    u32x4 ga[4];
#pragma unroll
    for (int i = 0; i < 4; ++i) ga[i] = u32x4{0u, 0u, 0u, 0u};
    int of1 = WS_PX + (g * 8 + (s & 4)) * 1024 + lane * 16;
    asm volatile("" : "+v"(of1), "+v"(ga[0]), "+v"(ga[1]), "+v"(ga[2]), "+v"(ga[3]));
    flag_wait<0>(lds, F_END, 1);
    STAMP(5);
    __builtin_amdgcn_s_sleep(T1D);
    {
      unsigned bad = 1u;
#pragma unroll 1
      for (unsigned spin = 0; spin < SPIN_CAP; ++spin) {
        asm volatile("" ::: "memory");
        if (bad != 0u) {
#pragma unroll
          for (int i = 0; i < 4; ++i) ga[i] = __builtin_amdgcn_raw_buffer_load_b128(rws, of1 + i * 1024, 0, AUX_SC1);
        }
        bad = 0u;
#pragma unroll
        for (int i = 0; i < 4; ++i) bad |= (ga[i][0] ^ tagA) | (ga[i][2] ^ (tagA >> 8));
        bad &= 0xffu;
        if (__all(bad == 0u)) break;
        bad |= dppu<XOR1>(bad); bad |= dppu<XOR2>(bad); bad |= dppu<XOR7>(bad);
        __builtin_amdgcn_s_sleep(1);
      }
      const unsigned hm = __builtin_amdgcn_perm(ga[0][3], ga[0][1], 0x0c0c0400u) << (16 * (lane & 1));
      const unsigned hl = __builtin_amdgcn_perm(ga[1][3], ga[1][1], 0x0c0c0400u) << (16 * (lane & 1));
      const float pm = asf(hm | dppu<XOR1>(hm)), pl = asf(hl | dppu<XOR1>(hl));
      const float M = rows_max(fmaxf(pm, dppf<ROR8>(pm)));
      const float w0 = __builtin_amdgcn_exp2f((pm - M) * LOG2E) * pl;
      const float inv = recip(rows_sum(w0 + dppf<ROR8>(w0)));
      float v[4];
#pragma unroll
      for (int r = 0; r < 4; ++r) {
        const float t = rows_sum_t(w0 * cut_lo(ga[0][r]), w0 * cut_lo(ga[1][r]), w0 * cut_lo(ga[2][r]), w0 * cut_lo(ga[3][r]));
        v[r] = (t + dppf<ROR8>(t)) * inv;
      }
      *(u32x2*)(lds + oa) = u32x2{pack_bf16(v[0], v[1]), pack_bf16(v[2], v[3])};
      flag_add(lds, F_O, lane);
    }
    if (red < 72) {
      __builtin_amdgcn_s_sleep(T2D);
      u32x4 gb = {0u, 0u, 0u, 0u};
      const int of2 = WS_OX + (b * 18 + (red >> 2)) * 4096 + (lane >> 1) * 128 + (red & 3) * 32 + (lane & 1) * 16;
      unsigned bad = 1u;
#pragma unroll 1
      for (unsigned spin = 0; spin < SPIN_CAP; ++spin) {
        asm volatile("" ::: "memory");
        if (bad != 0u) gb = __builtin_amdgcn_raw_buffer_load_b128(rws, of2, 0, AUX_SC1);
        bad = (__builtin_amdgcn_perm(gb[1], gb[0], 0x0c0c0400u) | __builtin_amdgcn_perm(gb[3], gb[2], 0x04000c0cu)) ^ tagA;
        if (__all(bad == 0u)) break;
        bad |= dppu<XOR1>(bad);
        __builtin_amdgcn_s_sleep(1);
      }
      STAMP(13);
      float t = rows_sum_t(cut_lo(gb[0]), cut_lo(gb[1]), cut_lo(gb[2]), cut_lo(gb[3]));
      t += dppf<XOR2>(t); t += dppf<ROR4>(t); t += dppf<ROR8>(t);
      __builtin_amdgcn_raw_buffer_store_b16((unsigned short)pack_bf16(rbf(xfin + rbf(t)) + yv, 0.f), rout, oout, 0, 0);
    }
  } else {
    const rsrc_t rk = mkrsrc(kc + (size_t)(id >> 1) * SPAN, SPAN);
    const rsrc_t rv = mkrsrc(vc + (size_t)(id >> 1) * SPAN, SPAN);
    auto poff = [&](int d, int h) {
      const int t = (d % S) + S * h + 2 * S * (d / S);
      return ((t & 7) | ((id & 1) << 3) | ((t >> 3) << 4)) * TILE_BYTES;
    };
    const int voffK = ((lane & 15) >> 2) * 256 + la * 64 + (lane & 3) * 16;
    const int voffV = lane * 16;
    u32x4 bA[8], bB[8];
    auto request = [&](u32x4 (&x)[8], rsrc_t r, int voff, int so) {
      asm volatile("" : "+v"(voff));
#pragma unroll
      for (int u = 0; u < 8; ++u) x[u] = __builtin_amdgcn_raw_buffer_load_b128(r, voff + (u & 3) * 1024, so + (u >> 2) * 4096, AUX_NT);
      __builtin_amdgcn_sched_barrier(0);
    };
    if (w >= 4) {
      const int rg = (w - 4) * 8 + (lane >> 3), c8 = lane & 7;
      unsigned xs[5];
      u32x4 wqv[9];
#pragma unroll
      for (int i = 0; i < 5; ++i) xs[i] = __builtin_amdgcn_raw_buffer_load_b32(rx, (lane + 64 * i) * 4, 0, 0);
      __builtin_amdgcn_sched_barrier(0);
      const rsrc_t rq = mkrsrc(wq + qh * 288 * 4096 + kvh * 256 + cb * 64, (287 * 4096 + 64) * 2);
#pragma unroll
      for (int i = 0; i < 9; ++i) wqv[i] = __builtin_amdgcn_raw_buffer_load_b128(rq, (rg + 32 * i) * 8192 + c8 * 16, 0, AUX_NT);
      __builtin_amdgcn_sched_barrier(0);
      __builtin_amdgcn_s_barrier();
      __builtin_amdgcn_sched_barrier(0);
      float xv[10], mean1, den1;
#pragma unroll
      for (int i = 0; i < 5; ++i) { xv[2 * i] = bflo(xs[i]); xv[2 * i + 1] = bfhi(xs[i]); }
      ln_stats(xv, lane < 32, mean1, den1);
#pragma unroll
      for (int i = 0; i < 5; ++i)
        *(unsigned*)(lds + L_XN + (lane + 64 * i) * 4) = pack_bf16(ln_apply(xv[2 * i], mean1, den1), ln_apply(xv[2 * i + 1], mean1, den1));
      asm volatile("" ::: "memory");
      STAMP(9);
      f32x4 qa = {0.f, 0.f, 0.f, 0.f};
#pragma unroll
      for (int i = 0; i < 9; ++i) {
        const unsigned t = (unsigned)*(const unsigned short*)(lds + L_XN + (qh * 288 + rg + 32 * i) * 2) << (16 * (lane & 1));
        const int jp = c8 >> 1;
        qa = mfma32(wqv[i], u32x4{jp == 0 ? t : 0u, jp == 1 ? t : 0u, jp == 2 ? t : 0u, jp == 3 ? t : 0u}, qa);
      }
      if ((lane >> 5) == ((lane >> 3) & 1)) {
        float* qp = (float*)(lds + L_QPART) + ((w - 4) * 2 + (lane >> 5)) * 64 + (la & 1) * 32 + c8;
#pragma unroll
        for (int r = 0; r < 4; ++r) qp[r * 8] = qa[r];
      }
      flag_add(lds, F_QP, lane);
      STAMP(1);
    }
    if (w < 4) asm volatile("s_barrier" ::: "memory");
    if (w >= PFD) flag_wait<1>(lds, F_ARR, w - PFD + 1);
    __builtin_amdgcn_sched_barrier(0);
    STAMP(14);
    request(bA, rk, voffK, poff(w, 0));
    request(bB, rk, voffK, poff(w, 1));
    asm volatile("s_waitcnt vmcnt(0)" ::: "memory");
    flag_add(lds, F_ARR, lane);
    STAMP(15);
    flag_wait<1>(lds, F_Q, 1);
    if (w < S) STAMP(2);

    const int c = lane & 3, hb = (lane >> 2) & 1, tb = (lane >> 3) & 1;
    const unsigned tagA = __builtin_amdgcn_readfirstlane(*(const unsigned*)(lds + L_TAG));
    const unsigned tb0 = __builtin_amdgcn_readfirstlane(*(const unsigned*)(lds + L_TAG + 4));
    float m;
    f32x4 acc[2][2];
#pragma unroll
    for (int i = 0; i < 2; ++i)
#pragma unroll
      for (int j = 0; j < 2; ++j) acc[i][j] = f32x4{0.f, 0.f, 0.f, 0.f};
    const u32x4 qf = *(const u32x4*)(lds + L_QS + (hb * 128 + la * 32 + c * 8) * 2);
    const u32x4 zero4 = {0u, 0u, 0u, 0u};
    const u32x4 q0 = tb ? zero4 : qf, q1 = tb ? qf : zero4;
    const unsigned mask0 = c == 0 ? 0x0000ffffu : c == 1 ? 0xffff0000u : 0u;
    const unsigned mask1 = c == 2 ? 0x0000ffffu : c == 3 ? 0xffff0000u : 0u;
    auto qk = [&](const u32x4 (&x)[8], f32x4 (&sc)[4]) {
#pragma unroll
      for (int i = 0; i < 4; ++i) sc[i] = mfma32(x[i], q0, f32x4{0.f, 0.f, 0.f, 0.f});
#pragma unroll
      for (int i = 0; i < 4; ++i) sc[i] = mfma32(x[4 + i], q1, sc[i]);
    };
    auto score = [&](const f32x4 (&sc)[4]) {
      float t4[4];
#pragma unroll
      for (int i = 0; i < 4; ++i) t4[i] = (dppf<QB0>(sc[i][0]) + dppf<QB1>(sc[i][1])) + (dppf<QB2>(sc[i][2]) + dppf<QB3>(sc[i][3]));
      const float raw = (c & 2) ? ((c & 1) ? t4[3] : t4[2]) : ((c & 1) ? t4[1] : t4[0]);
      float sv = rbf(rbf(raw) * SCALE);
      asm volatile("" : "+v"(sv));
      return sv;
    };
    auto head_max = [&](float v) {
      v = fmaxf(v, dppf<XOR1>(v));
      v = fmaxf(v, dppf<XOR2>(v));
      v = fmaxf(v, dppf<ROR8>(v));
      return rows_max(v);
    };
    auto pv = [&](const u32x4 (&x)[8], float sv) {
      const float p = __builtin_amdgcn_exp2f((sv - m) * LOG2E);
      const unsigned pk = pack_bf16(p, p);
      unsigned pb0[4], pb1[4];
      pb0[0] = dppu<QB0>(pk) & mask0; pb1[0] = dppu<QB0>(pk) & mask1;
      pb0[1] = dppu<QB1>(pk) & mask0; pb1[1] = dppu<QB1>(pk) & mask1;
      pb0[2] = dppu<QB2>(pk) & mask0; pb1[2] = dppu<QB2>(pk) & mask1;
      pb0[3] = dppu<QB3>(pk) & mask0; pb1[3] = dppu<QB3>(pk) & mask1;
#pragma unroll
      for (int u = 0; u < 8; ++u) {
        acc[u >> 2][0] = mfma16(x[u][0], x[u][1], pb0[u & 3], pb1[u & 3], acc[u >> 2][0]);
        acc[u >> 2][1] = mfma16(x[u][2], x[u][3], pb0[u & 3], pb1[u & 3], acc[u >> 2][1]);
      }
    };
    auto put_ml = [&](int k, float mk, float lk) {
      lk += dppf<XOR1>(lk);
      lk += dppf<XOR2>(lk);
      lk += dppf<ROR8>(lk);
      lk = rows_sum(lk);
      if ((lane & ~4) == 0) *(f32x2*)(lds + L_ML + (k * 2 + hb) * 8) = f32x2{mk, lk};
      flag_add(lds, F_ML, lane);
    };
    auto hold = [&](int dly) {
      __builtin_amdgcn_sched_barrier(0);
      for (int i = 0; i < dly; ++i) __builtin_amdgcn_s_sleep(1);
      __builtin_amdgcn_sched_barrier(0);
    };
    const int hw = w - S, wk = w - 2, nwk = (s & 3) == 3 ? 6 : 4;
    int lprev = 0x40000000;
    auto slot = [&](int p) {
      __builtin_amdgcn_sched_barrier(0);
      const int shift = flag_get(lds, F_SH);
      const unsigned due = tb0 + SCA + shift + (p >= PGAP ? GAP : 0) + (unsigned)(p * SCT >> 4);
      const int late = (int)(now32() - due);
      if (late - lprev > GLT && lane == 0)
        __hip_atomic_fetch_max((unsigned*)(lds + L_FLAG) + F_SH + vzero(), (unsigned)(shift + late - lprev + GLA), __ATOMIC_RELAXED, __HIP_MEMORY_SCOPE_WORKGROUP);
      lprev = p >= PGL ? max(late, 0) : 0x40000000;
      while ((int)(now32() - due) < 0) {}
      __builtin_amdgcn_sched_barrier(0);
    };
    f32x2 sl;
    if (w < S) {
      __builtin_amdgcn_s_setprio(2);
      unsigned char* sco = lds + L_SC + w * (NPAIR * 512) + lane * 8;
      if (HOLDA) flag_wait<1>(lds, F_ARR, NWAVE - PFD + 1 + (w * 2 >= S));
      float mx = -INFINITY;
      for (int j = 0; j < NPAIR; ++j) {
        const bool k = j + 1 < NPAIR;
        const rsrc_t r = k ? rk : rv;
        const int vo = k ? voffK : voffV, d = NWAVE + S * (k ? j : j - 1) + w;
        f32x4 sc[4];
        qk(bA, sc);
        slot(2 * NWAVE + 2 * S * j + w);
        request(bA, r, vo, poff(d, 0));
        const float s0 = score(sc);
        qk(bB, sc);
        slot(2 * NWAVE + 2 * S * j + S + w);
        request(bB, r, vo, poff(d, 1));
        const float s1 = score(sc);
        mx = fmaxf(mx, fmaxf(s0, s1));
        *(f32x2*)(sco + j * 512) = f32x2{s0, s1};
      }
      m = head_max(mx);
      if ((lane & ~4) == 0) *(float*)(lds + L_ML + (w * 2 + hb) * 8) = m;
      flag_add(lds, F_P1 + w, lane);
      for (int j = 0; j + 1 < NPAIR; ++j) {
        const f32x2 sv = *(const f32x2*)(sco + (NPAIR - 1 - j) * 512);
        const int d = j + 2 < NPAIR ? NWAVE + S * (NPAIR - 3 - j) + w : w;
        pv(bA, sv[0]);
        slot(2 * NWAVE + 2 * S * (NPAIR + j) + w);
        request(bA, rv, voffV, poff(d, 0));
        pv(bB, sv[1]);
        slot(2 * NWAVE + 2 * S * (NPAIR + j) + S + w);
        request(bB, rv, voffV, poff(d, 1));
      }
      sl = *(const f32x2*)sco;
    }
    asm volatile("" ::: "memory");
    if (w >= S) {
      {
        f32x4 sc[4];
        qk(bA, sc);
        sl[0] = score(sc);
        qk(bB, sc);
        sl[1] = score(sc);
        m = head_max(fmaxf(sl[0], sl[1]));
        put_ml(w, m, __builtin_amdgcn_exp2f((sl[0] - m) * LOG2E) + __builtin_amdgcn_exp2f((sl[1] - m) * LOG2E));
      }
      STAMP(12);
      if (w >= 4 && red < 72) {
        const int ht = (w - 4) * 64 + lane;
        unsigned a1[9], ab[9], a2[9];
        const rsrc_t r1 = mkrsrc(w1, FF * 2), rb = mkrsrc(b1, FF * 2), r2 = mkrsrc(w2, FF * 2);
#pragma unroll
        for (int i = 0; i < 9; ++i) {
          a1[i] = __builtin_amdgcn_raw_buffer_load_b32(r1, (ht + 256 * i) * 4, 0, 0);
          ab[i] = __builtin_amdgcn_raw_buffer_load_b32(rb, (ht + 256 * i) * 4, 0, 0);
          a2[i] = __builtin_amdgcn_raw_buffer_load_b32(r2, (ht + 256 * i) * 4, 0, 0);
        }
        __builtin_amdgcn_sched_barrier(0);
        float xn[10];
#pragma unroll
        for (int i = 0; i < 5; ++i) { const unsigned v = *(const unsigned*)(lds + L_XN + (lane + 64 * i) * 4); xn[2 * i] = bflo(v); xn[2 * i + 1] = bfhi(v); }
        float mean2, den2;
        ln_stats(xn, lane < 32, mean2, den2);
        constexpr float GC0 = -2.0f * 0.7978845608028654f * LOG2E, GC1 = GC0 * 0.044715f;
#pragma unroll 1
        for (int j = 0; j < 8; ++j) {
          const float hv = ln_apply(bflo(*(const unsigned short*)(lds + L_XN + (red * 8 + j) * 2)), mean2, den2);
          float y = 0.f;
#pragma unroll
          for (int i = 0; i < 9; ++i) {
            asm volatile("" : "+v"(a1[i]), "+v"(ab[i]), "+v"(a2[i]));
            const unsigned pr = pack_bf16(hv * bflo(a1[i]), hv * bfhi(a1[i]));
            const unsigned uu = pack_bf16(bflo(pr) + bflo(ab[i]), bfhi(pr) + bfhi(ab[i]));
            const float u0 = bflo(uu), u1 = bfhi(uu);
            const float e0 = __builtin_amdgcn_exp2f(u0 * fmaf(u0 * u0, GC1, GC0));
            const float e1 = __builtin_amdgcn_exp2f(u1 * fmaf(u1 * u1, GC1, GC0));
            const unsigned gg = pack_bf16(u0 * __builtin_amdgcn_rcpf(1.0f + e0), u1 * __builtin_amdgcn_rcpf(1.0f + e1));
            y = fmaf(bflo(gg), bflo(a2[i]), y);
            y = fmaf(bfhi(gg), bfhi(a2[i]), y);
          }
          y = wave_sum(y);
          if (lane == 0) *(float*)(lds + L_YP + (w - 4) * 32 + j * 4) = y;
        }
        flag_add(lds, F_Y, lane);
      }
      if (hw < S) {
        flag_wait<16>(lds, F_P1 + hw, 1);
        const float ms = *(const float*)(lds + L_ML + (hw * 2 + hb) * 8);
        float ls = 0.f;
#pragma unroll 1
        for (int j = 0; j < NPAIR; ++j) {
          const f32x2 sv = *(const f32x2*)(lds + L_SC + hw * (NPAIR * 512) + lane * 8 + j * 512);
          ls += __builtin_amdgcn_exp2f((sv[0] - ms) * LOG2E) + __builtin_amdgcn_exp2f((sv[1] - ms) * LOG2E);
        }
        put_ml(hw, ms, ls);
      }
      constexpr int PEND = 2 * NWAVE + 2 * S * NITER;
      wait_until(tb0 + SCA + GAP + (unsigned)(PEND * SCT >> 4) - 60u);
      __builtin_amdgcn_s_setprio(2);
      const int p0 = PEND + RSV0 + 2 * S * (hw / S) + hw % S;
      slot(p0);
      request(bA, rv, voffV, poff(w, 0));
      slot(p0 + S);
      request(bB, rv, voffV, poff(w, 1));
    }
    int po = WS_PX + ((g * 8 + hb * 4 + la) * 8 + s) * 128 + (tb * 4 + c) * 16;
    float fw;
    unsigned side;
    {
      flag_wait<1>(lds, F_ML, NWAVE);
      f32x2 ml[NWAVE];
#pragma unroll
      for (int k = 0; k < NWAVE; ++k) ml[k] = *(const f32x2*)(lds + L_ML + (k * 2 + hb) * 8);
      float mx = ml[0][0], L = 0.f;
#pragma unroll
      for (int k = 1; k < NWAVE; ++k) mx = fmaxf(mx, ml[k][0]);
#pragma unroll
      for (int k = 0; k < NWAVE; ++k) L = fmaf(__builtin_amdgcn_exp2f((ml[k][0] - mx) * LOG2E), ml[k][1], L);
      fw = __builtin_amdgcn_exp2f((m - mx) * LOG2E) * recip(L);
      side = asu((la & 1) ? L : mx) >> (16 * (c & 1));
    }
    asm volatile("" : "+v"(po), "+v"(fw), "+v"(side));
    pv(bA, sl[0]);
    pv(bB, sl[1]);
    STAMP(3);
    {
      f32x4 res;
#pragma unroll
      for (int r = 0; r < 4; ++r) {
        const float own = tb ? acc[1][1][r] : acc[0][0][r], snd = tb ? acc[1][0][r] : acc[0][1][r];
        res[r] = (own + dppf<ROR8>(snd)) * fw;
      }
      *(f32x4*)(lds + L_PART + w * 1024 + lane * 16) = res;
      asm volatile("" ::: "memory");
      unsigned old = 0;
      if (lane == 0) old = __hip_atomic_fetch_add((unsigned*)(lds + L_FLAG) + F_WP + vzero(), 1u, __ATOMIC_RELAXED, __HIP_MEMORY_SCOPE_WORKGROUP);
      asm volatile("" ::: "memory");
      f32x4 pp[NWAVE];
#pragma unroll
      for (int k = 0; k < NWAVE; ++k) pp[k] = *(const f32x4*)(lds + L_PART + k * 1024 + lane * 16);
      __builtin_amdgcn_sched_barrier(0);
      if (__builtin_amdgcn_readfirstlane(old) == NWAVE - 1) {
        const f32x4 sum = ((pp[0] + pp[1]) + (pp[2] + pp[3])) + ((pp[4] + pp[5]) + (pp[6] + pp[7]));
        __builtin_amdgcn_raw_buffer_store_b128(
            u32x4{put_lo(sum[0], tagA), put_lo(sum[1], side), put_lo(sum[2], tagA >> 8), put_lo(sum[3], side >> 8)}, rws, po, 0, AUX_SC1);
        flag_add(lds, F_END, lane);
        STAMP(10);
      }
    }
    if (w < 2) {
      if (WOT) slot(2 * NT + WOT - 1 + 2 * w); else flag_wait<1>(lds, F_WP, WOL);
      const rsrc_t ro = mkrsrc(wo + (size_t)(kvh * 256 + qh * 128 + w * 64) * DM, 64 * DM * 2);
      u32x4 wt[24];
#pragma unroll
      for (int i = 0; i < 24; ++i)
        wt[i] = __builtin_amdgcn_raw_buffer_load_b128(
            ro, 2 * (i % 3) < nwk ? ((i / 3) * 8 + (lane >> 3)) * (DM * 2) + (s & 3) * 256 + (i % 3) * 128 + (lane & 7) * 16 : OOB, 0, AUX_NT);
#pragma unroll
      for (int i = 0; i < 24; ++i)
        *(u32x4*)(lds + L_WO + w * 24576 + (i % 3) * 8192 + ((i / 3) * 8 + ((lane >> 3) & 4) + ((lane >> 2) & 2) + ((lane >> 4) & 1)) * 128 +
                  (((lane & 7) * 16) ^ (((i / 3) & 3) * 32))) = wt[i];
      flag_add(lds, F_WO, lane);
      STAMP(4);
    } else if (wk < nwk) {
      const int n = lane & 15;
      int oo = n < 2 ? WS_OX + (b * 18 + (s & 3) * 4 + wk) * 4096 + (kvh * 2 + qh) * 128 + n * 64 + la * 16 : OOB;
      asm volatile("" : "+v"(oo));
      flag_wait<1>(lds, F_WO, 2);
      u32x4 wof[2][4];
      {
        typedef __attribute__((address_space(3))) s16x4* lds_s16x4;
        unsigned char* img = lds + L_WO + (wk >> 1) * 8192 + ((n >> 2) * 8 + (la >> 1) * 4 + (la & 1)) * 128 + (lane & 3) * 8;
#pragma unroll
        for (int t = 0; t < 2; ++t)
#pragma unroll
          for (int ks = 0; ks < 4; ++ks) {
            unsigned char* p = img + (ks >> 1) * 24576 + (ks & 1) * 4096 + (((wk & 1) * 2 + t) ^ (n >> 2)) * 32;
            const u32x2 lo = __builtin_bit_cast(u32x2, __builtin_amdgcn_ds_read_tr16_b64_v4i16((lds_s16x4)p));
            const u32x2 hi = __builtin_bit_cast(u32x2, __builtin_amdgcn_ds_read_tr16_b64_v4i16((lds_s16x4)(p + 256)));
            wof[t][ks] = u32x4{lo[0], lo[1], hi[0], hi[1]};
          }
      }
      u32x4 ob[4];
#pragma unroll 1
      for (unsigned spin = 0; spin < (SPIN_CAP << 4); ++spin) {
        asm volatile("" ::: "memory");
        const unsigned f = __hip_atomic_load((unsigned*)(lds + L_FLAG) + F_O, __ATOMIC_RELAXED, __HIP_MEMORY_SCOPE_WORKGROUP);
        asm volatile("" ::: "memory");
#pragma unroll
        for (int ks = 0; ks < 4; ++ks) ob[ks] = *(const u32x4*)(lds + L_OA + ks * 64 + la * 16);
        __builtin_amdgcn_sched_barrier(0);
        if (__builtin_amdgcn_readfirstlane(f) != 0u) break;
      }
      STAMP(6);
      f32x4 d0 = {0.f, 0.f, 0.f, 0.f}, d1 = {0.f, 0.f, 0.f, 0.f};
#pragma unroll
      for (int ks = 0; ks < 4; ++ks) { d0 = mfma32(wof[0][ks], ob[ks], d0); d1 = mfma32(wof[1][ks], ob[ks], d1); }
      const f32x4 d = n ? d1 : d0;
      __builtin_amdgcn_raw_buffer_store_b128(
          u32x4{put_lo(d[0], tagA), put_lo(d[1], tagA >> 8), put_lo(d[2], tagA >> 16), put_lo(d[3], tagA >> 24)}, rws, oo, 0, AUX_SC1);
      STAMP(11);
    }
  }
}

}

extern "C" void kernel_launch(void* const* d_in, const int* in_sizes, int n_in, void* d_out, int out_size, void* d_ws,
                              size_t ws_size, hipStream_t stream) {
  (void)in_sizes; (void)n_in; (void)out_size; (void)ws_size;
  fused_decode_kernel<<<dim3(NWG), dim3(NTHR), 0, stream>>>(
      (const unsigned short*)d_in[0], (const unsigned char*)d_in[1], (const unsigned char*)d_in[2],
      (const unsigned short*)d_in[3], (const unsigned short*)d_in[4], (const unsigned short*)d_in[6],
      (const unsigned short*)d_in[7], (const unsigned short*)d_in[8], (const unsigned short*)d_in[9],
      (unsigned short*)d_out, (unsigned char*)d_ws);
}
```

```cpp
#include <hip/hip_runtime.h>
#include <hip/hip_bf16.h>
#include <stdint.h>

namespace {

typedef unsigned u32x2 __attribute__((ext_vector_type(2)));
typedef unsigned u32x4 __attribute__((ext_vector_type(4)));
typedef float f32x2 __attribute__((ext_vector_type(2)));
typedef float f32x4 __attribute__((ext_vector_type(4)));
typedef short s16x4 __attribute__((ext_vector_type(4)));
typedef short s16x8 __attribute__((ext_vector_type(8)));
typedef __bf16 bf16x2 __attribute__((ext_vector_type(2)));
typedef __amdgpu_buffer_rsrc_t rsrc_t;

constexpr int DM = 576, SK = 32768, DH = 128, FF = 4608;
constexpr int NSPLIT = 8, NWG = 256, NWAVE = 8, NTHR = (NWAVE + 1) * 64;
constexpr int KEYS_WG = SK / NSPLIT;
constexpr int TILE_BYTES = 32 * DH * 2;
constexpr int NT = KEYS_WG / 32;
constexpr int SPAN = 2 * KEYS_WG * DH * 2;
constexpr int S = 4, NH = NWAVE - S;
constexpr int NPAIR = (NT / 2 - NH) / S;
static_assert(2 * (NPAIR * S + NH) == NT && NH == S, "every tile has one owner, every stream one helper");

constexpr int SCA = 160;
constexpr int SCT = 427;
constexpr int GAP = 25;
constexpr int CBETA = 6;
constexpr int GLT = 125;
constexpr int GLQ = 210;
constexpr int PFD = 3;
constexpr int PSL = 6;
constexpr int QPS = 2;
constexpr int T1D = 17;
constexpr int T2D = 22;
constexpr int PGAP = 2 * NWAVE + 2 * S * NPAIR, PEND = 2 * NT - 2 * NH;

constexpr int AUX_NT = 2, AUX_SC1 = 16;
constexpr unsigned RSRC3 = 0x00020000u;
constexpr unsigned SPIN_CAP = 1u << 19;
constexpr int OOB = 0x7ff00000;

constexpr int WS_EPOCH = 0;
constexpr int WS_QX = 4096;
constexpr int WS_PX = WS_QX + 32 * 4096;
constexpr int WS_OX = WS_PX + NWG * 1024;
constexpr int WS_END = WS_OX + 2 * 18 * 4096;

constexpr int L_FLAG = 0;
constexpr int L_TAG = 64;
constexpr int L_XN = 80;
constexpr int L_QPART = L_XN + 1280;
constexpr int L_QS = L_QPART + 8 * 64 * 4;
constexpr int L_OA = L_QS + 512;
constexpr int L_YP = L_OA + 256;
constexpr int L_ML = L_YP + 4 * 8 * 4;
constexpr int L_PART = L_ML + NWAVE * 16;
constexpr int L_WO = L_PART + NWAVE * 1024;
constexpr int L_SC = L_WO + 128 * 384;
constexpr int L_END = L_SC + S * NPAIR * 512;
constexpr int F_QP = 0, F_Q = 1, F_WP = 2, F_END = 3, F_O = 4, F_Y = 5, F_ARR = 6, F_WO = 8, F_ML = 9, F_SH = 10, F_T = 11, F_P1 = 12;

constexpr float LOG2E = 1.4426950408889634f;
constexpr float SCALE = 0.08838834764831845f;
constexpr float LN_EPS = 1e-5f;

__device__ __forceinline__ float asf(unsigned u) { return __builtin_bit_cast(float, u); }
__device__ __forceinline__ unsigned asu(float f) { return __builtin_bit_cast(unsigned, f); }
__device__ __forceinline__ float bflo(unsigned u) { return asf(u << 16); }
__device__ __forceinline__ float bfhi(unsigned u) { return asf(u & 0xffff0000u); }
__device__ __forceinline__ unsigned pack_bf16(float lo, float hi) {
  f32x2 v = {lo, hi};
  return __builtin_bit_cast(unsigned, __builtin_convertvector(v, bf16x2));
}
__device__ __forceinline__ float rbf(float f) { return (float)(__bf16)f; }
template <int CTRL> __device__ __forceinline__ unsigned dppu(unsigned x) {
  return (unsigned)__builtin_amdgcn_mov_dpp((int)x, CTRL, 0xf, 0xf, true);
}
template <int CTRL> __device__ __forceinline__ float dppf(float x) { return asf(dppu<CTRL>(asu(x))); }
constexpr int XOR1 = 0xB1, XOR2 = 0x4E, XOR7 = 0x141, ROR4 = 0x124, ROR8 = 0x128;
constexpr int QB0 = 0x00, QB1 = 0x55, QB2 = 0xAA, QB3 = 0xFF;
__device__ __forceinline__ float rows_sum(float x) {
  auto s = __builtin_amdgcn_permlane16_swap(asu(x), asu(x), false, false);
  x = asf(s[0]) + asf(s[1]);
  auto t = __builtin_amdgcn_permlane32_swap(asu(x), asu(x), false, false);
  return asf(t[0]) + asf(t[1]);
}
__device__ __forceinline__ float rows_max(float x) {
  auto s = __builtin_amdgcn_permlane16_swap(asu(x), asu(x), false, false);
  x = fmaxf(asf(s[0]), asf(s[1]));
  auto t = __builtin_amdgcn_permlane32_swap(asu(x), asu(x), false, false);
  return fmaxf(asf(t[0]), asf(t[1]));
}
__device__ __forceinline__ float rows_sum_t(float v0, float v1, float v2, float v3) {
  const auto p0 = __builtin_amdgcn_permlane32_swap(asu(v0), asu(v2), false, false);
  const auto p1 = __builtin_amdgcn_permlane32_swap(asu(v1), asu(v3), false, false);
  const auto p2 = __builtin_amdgcn_permlane16_swap(asu(asf(p0[0]) + asf(p0[1])), asu(asf(p1[0]) + asf(p1[1])), false, false);
  return asf(p2[0]) + asf(p2[1]);
}
__device__ __forceinline__ float wave_sum(float x) {
  x += dppf<XOR1>(x); x += dppf<XOR2>(x); x += dppf<ROR4>(x); x += dppf<ROR8>(x);
  return rows_sum(x);
}
__device__ __forceinline__ float recip(float x) {
  const float r = __builtin_amdgcn_rcpf(x);
  return fmaf(fmaf(-x, r, 1.0f), r, r);
}
__device__ __forceinline__ rsrc_t mkrsrc(const void* p, int bytes) {
  return __builtin_amdgcn_make_buffer_rsrc(const_cast<void*>(p), 0, bytes, RSRC3);
}
__device__ __forceinline__ f32x4 mfma32(u32x4 a, u32x4 b, f32x4 c) {
  return __builtin_amdgcn_mfma_f32_16x16x32_bf16(__builtin_bit_cast(s16x8, a), __builtin_bit_cast(s16x8, b), c, 0, 0, 0);
}
__device__ __forceinline__ f32x4 mfma16(unsigned a0, unsigned a1, unsigned b0, unsigned b1, f32x4 c) {
  u32x2 a = {a0, a1}, b = {b0, b1};
  return __builtin_amdgcn_mfma_f32_16x16x16bf16_1k(__builtin_bit_cast(s16x4, a), __builtin_bit_cast(s16x4, b), c, 0, 0, 0);
}
__device__ __forceinline__ unsigned put_lo(float v, unsigned byte) { return (asu(v) & 0xffffff00u) | (byte & 0xffu); }
__device__ __forceinline__ float cut_lo(unsigned u) { return asf(u & 0xffffff00u); }
__device__ __forceinline__ unsigned now32() { return (unsigned)__builtin_amdgcn_s_memrealtime(); }
__device__ __forceinline__ void wait_until(unsigned t) {
  while ((int)(now32() - t) < 0) __builtin_amdgcn_s_sleep(1);
}
__device__ __forceinline__ int vzero() { int z = 0; asm("" : "+v"(z)); return z; }
__device__ __forceinline__ void flag_add(unsigned char* lds, int f, int lane) {
  asm volatile("" ::: "memory");
  if (lane == 0) __hip_atomic_fetch_add((unsigned*)(lds + L_FLAG) + f + vzero(), 1u, __ATOMIC_RELAXED, __HIP_MEMORY_SCOPE_WORKGROUP);
  asm volatile("" ::: "memory");
}
__device__ __forceinline__ unsigned flag_get(unsigned char* lds, int f) {
  return __builtin_amdgcn_readfirstlane(__hip_atomic_load((unsigned*)(lds + L_FLAG) + f, __ATOMIC_RELAXED, __HIP_MEMORY_SCOPE_WORKGROUP));
}
template <int SLEEP> __device__ __forceinline__ void flag_wait(unsigned char* lds, int f, unsigned n) {
#pragma unroll 1
  for (unsigned spin = 0; spin < (SPIN_CAP << 4); ++spin) {
    if (flag_get(lds, f) >= n) break;
    if (SLEEP) __builtin_amdgcn_s_sleep(SLEEP);
  }
  asm volatile("" ::: "memory");
}
__device__ __forceinline__ void ln_stats(const float (&v)[10], bool last_valid, float& mean_b, float& den_b) {
  float s = 0.f;
#pragma unroll
  for (int i = 0; i < 8; ++i) s += v[i];
  s += last_valid ? (v[8] + v[9]) : 0.f;
  const float mean = wave_sum(s) * (1.0f / DM);
  float q = 0.f;
#pragma unroll
  for (int i = 0; i < 8; ++i) { const float d = v[i] - mean; q = fmaf(d, d, q); }
  { const float d8 = v[8] - mean, d9 = v[9] - mean; q += last_valid ? fmaf(d8, d8, d9 * d9) : 0.f; }
  const float var = wave_sum(q) * (1.0f / DM);
  mean_b = rbf(mean);
  den_b = rbf(__builtin_amdgcn_sqrtf(rbf(rbf(var) + LN_EPS)));
}
__device__ __forceinline__ float ln_apply(float x, float mean_b, float den_b) {
  const float d = rbf(x - mean_b), r = __builtin_amdgcn_rcpf(den_b), q = d * r;
  return rbf(fmaf(fmaf(-q, den_b, d), r, q));
}

__global__ void __launch_bounds__(NTHR, 3)
fused_decode_kernel(const unsigned short* __restrict__ x, const unsigned char* __restrict__ kc, const unsigned char* __restrict__ vc,
                    const unsigned short* __restrict__ wq, const unsigned short* __restrict__ wo,
                    const unsigned short* __restrict__ w1, const unsigned short* __restrict__ b1,
                    const unsigned short* __restrict__ w2, const unsigned short* __restrict__ b2,
                    unsigned short* __restrict__ out, unsigned char* ws) {
  __shared__ __attribute__((aligned(16))) unsigned char lds[L_END];
  const int lane = threadIdx.x & 63, wv = __builtin_amdgcn_readfirstlane(threadIdx.x >> 6), w = wv < 8 ? wv ^ 4 : wv;
  const unsigned tst = now32();
  const int g = blockIdx.x >> 3, s = (blockIdx.x + 3 * g) & 7, id = g * 8 + s, b = g >> 4, kvh = g & 15;
  const rsrc_t rws = mkrsrc(ws, WS_END);
  const rsrc_t rx = mkrsrc(x + b * DM, DM * 2);
  const int qh = s >> 2, cb = (s + (kvh >> 2)) & 3;
  const int la = lane >> 4;
  const int red = kvh * 8 + s;

  u32x2 ep = {0u, 0u};
  if (w == NWAVE) {
    ep = __builtin_amdgcn_raw_buffer_load_b64(rws, WS_EPOCH + b * 256, 0, 0);
    if (lane < 16) *((unsigned*)(lds + L_FLAG) + lane) = 0u;
    asm volatile("s_waitcnt lgkmcnt(0)\n\ts_barrier" ::: "memory");
  }
  if (w >= NWAVE) {
    __builtin_amdgcn_s_setprio(3);
    const unsigned e0 = __builtin_amdgcn_readfirstlane(ep[0]);
    const unsigned e1 = ((e0 & 0x7f7f7f7fu) + 0x01010101u) ^ (e0 & 0x80808080u);
    const unsigned tagA = e1 ^ 0xb77a3d00u;
    const unsigned tagB = __builtin_amdgcn_readfirstlane(ep[1]) + 1u;
    if (lane == 0) *(unsigned*)(lds + L_TAG) = tagA;
    flag_wait<0>(lds, F_QP, 4);
    {
      f32x2 q2 = *(const f32x2*)(lds + L_QPART + (lane & 31) * 8);
#pragma unroll
      for (int ww = 1; ww < 8; ++ww) q2 += *(const f32x2*)(lds + L_QPART + ww * 256 + (lane & 31) * 8);
      __builtin_amdgcn_raw_buffer_store_b128(u32x4{tagA, asu(q2[0]), (tagB & 0xffffu) | (tst << 16), asu(q2[1])}, rws,
                                             lane < 32 ? WS_QX + g * 4096 + qh * 2048 + cb * 512 + lane * 16 : OOB, 0, AUX_SC1);
    }
    __builtin_amdgcn_s_sleep(PSL);
    {
      float qa[4], qb[4];
      int ts[4];
      int of[4];
#pragma unroll
      for (int i = 0; i < 4; ++i) { qa[i] = 0.f; qb[i] = 0.f; ts[i] = 0; of[i] = WS_QX + g * 4096 + i * 1024 + lane * 16; }
#pragma unroll 1
      for (unsigned spin = 0; spin < SPIN_CAP; ++spin) {
        asm volatile("" ::: "memory");
        u32x4 gr[4];
#pragma unroll
        for (int i = 0; i < 4; ++i) gr[i] = __builtin_amdgcn_raw_buffer_load_b128(rws, of[i], 0, AUX_SC1);
        bool done = true;
#pragma unroll
        for (int i = 0; i < 4; ++i) {
          const bool fresh = (gr[i][0] == tagA) & (((gr[i][2] ^ tagB) & 0xffffu) == 0u) & (of[i] != OOB);
          ts[i] = fresh ? (int)(gr[i][2] - (tst << 16)) >> 16 : ts[i];
          qa[i] = fresh ? asf(gr[i][1]) : qa[i]; qb[i] = fresh ? asf(gr[i][3]) : qb[i];
          of[i] = fresh ? OOB : of[i];
          done &= of[i] == OOB;
        }
        if (__all(done)) break;
        __builtin_amdgcn_s_sleep(QPS);
      }
      *(unsigned*)(lds + L_QS + lane * 4) = pack_bf16(qa[0] + qa[2], qb[0] + qb[2]);
      *(unsigned*)(lds + L_QS + 256 + lane * 4) = pack_bf16(qa[1] + qa[3], qb[1] + qb[3]);
      {
        int sm = 0, mx = -32768;
#pragma unroll
        for (int i = 0; i < 4; ++i) {
          const int t0 = __builtin_amdgcn_readlane(ts[i], 0), t1 = __builtin_amdgcn_readlane(ts[i], 32);
          sm += t0 + t1; mx = max(mx, max(t0, t1));
        }
        sm >>= 3;
        if (lane == 0) *(unsigned*)(lds + L_TAG + 4) = tst + (unsigned)(sm + ((mx - sm) * CBETA >> 4));
      }
      flag_add(lds, F_Q, lane);
    }
    const int fc = (lane & 1) * 4 + la;
    const unsigned xraw = __builtin_amdgcn_raw_buffer_load_b32(rx, red * 16 + (fc >> 1) * 4, 0, 0);
    const unsigned b2v = __builtin_amdgcn_raw_buffer_load_b16(mkrsrc(b2, 4), 0, 0, 0);
    if (red == 127) {
      wait_until(tst + 2000u);
#pragma unroll 1
      for (unsigned spin = 0; spin < SPIN_CAP; ++spin) {
        asm volatile("" ::: "memory");
        const u32x4 v0 = __builtin_amdgcn_raw_buffer_load_b128(rws, WS_QX + b * 65536 + lane * 512, 0, AUX_SC1);
        const u32x4 v1 = __builtin_amdgcn_raw_buffer_load_b128(rws, WS_QX + b * 65536 + 32768 + lane * 512, 0, AUX_SC1);
        if (__all((v0[0] == tagA) & (v1[0] == tagA) & ((((v0[2] ^ tagB) | (v1[2] ^ tagB)) & 0xffffu) == 0u))) break;
        __builtin_amdgcn_s_sleep(16);
      }
      __builtin_amdgcn_raw_buffer_store_b64(u32x2{e1, tagB}, rws, lane == 0 ? WS_EPOCH + b * 256 : OOB, 0, AUX_SC1);
    }
    float yv = 0.f, xfin = (fc & 1) ? bfhi(xraw) : bflo(xraw);
    const rsrc_t rout = mkrsrc(out + b * DM, DM * 2 + 2);
    int oout = (lane & 14) == 0 ? (red * 8 + fc) * 2 : OOB;
    if (red < 72) {
      flag_wait<16>(lds, F_Y, 4);
#pragma unroll
      for (int ww = 0; ww < 4; ++ww) yv += *(const float*)(lds + L_YP + ww * 32 + fc * 4);
      yv = rbf(rbf(yv) + bflo(b2v));
      __builtin_amdgcn_raw_buffer_store_b16((unsigned short)0, rout, oout, 0, 0);
    }
    int oa = (lane & 8) ? L_QPART + lane * 8 : L_OA + la * 64 + (lane & 7) * 8;
    asm volatile("" : "+v"(yv), "+v"(xfin), "+v"(oout), "+v"(oa));
    flag_wait<16>(lds, F_WP, 1);
    u32x4 ga[4];
#pragma unroll
    for (int i = 0; i < 4; ++i) ga[i] = u32x4{0u, 0u, 0u, 0u};
    int of1 = WS_PX + (g * 8 + (s & 4)) * 1024 + lane * 16;
    asm volatile("" : "+v"(of1), "+v"(ga[0]), "+v"(ga[1]), "+v"(ga[2]), "+v"(ga[3]));
    flag_wait<0>(lds, F_END, 1);
    __builtin_amdgcn_s_sleep(T1D);
    {
      unsigned bad = 1u;
#pragma unroll 1
      for (unsigned spin = 0; spin < SPIN_CAP; ++spin) {
        asm volatile("" ::: "memory");
        if (bad != 0u) {
#pragma unroll
          for (int i = 0; i < 4; ++i) ga[i] = __builtin_amdgcn_raw_buffer_load_b128(rws, of1 + i * 1024, 0, AUX_SC1);
        }
        bad = 0u;
#pragma unroll
        for (int i = 0; i < 4; ++i) bad |= (ga[i][0] ^ tagA) | (ga[i][2] ^ (tagA >> 8));
        bad &= 0xffu;
        if (__all(bad == 0u)) break;
        bad |= dppu<XOR1>(bad); bad |= dppu<XOR2>(bad); bad |= dppu<XOR7>(bad);
        __builtin_amdgcn_s_sleep(1);
      }
      const unsigned hm = __builtin_amdgcn_perm(ga[0][3], ga[0][1], 0x0c0c0400u) << (16 * (lane & 1));
      const unsigned hl = __builtin_amdgcn_perm(ga[1][3], ga[1][1], 0x0c0c0400u) << (16 * (lane & 1));
      const float pm = asf(hm | dppu<XOR1>(hm)), pl = asf(hl | dppu<XOR1>(hl));
      const float M = rows_max(fmaxf(pm, dppf<ROR8>(pm)));
      const float w0 = __builtin_amdgcn_exp2f((pm - M) * LOG2E) * pl;
      const float inv = recip(rows_sum(w0 + dppf<ROR8>(w0)));
      float v[4];
#pragma unroll
      for (int r = 0; r < 4; ++r) {
        const float t = rows_sum_t(w0 * cut_lo(ga[0][r]), w0 * cut_lo(ga[1][r]), w0 * cut_lo(ga[2][r]), w0 * cut_lo(ga[3][r]));
        v[r] = (t + dppf<ROR8>(t)) * inv;
      }
      *(u32x2*)(lds + oa) = u32x2{pack_bf16(v[0], v[1]), pack_bf16(v[2], v[3])};
      flag_add(lds, F_O, lane);
    }
    if (red < 72) {
      __builtin_amdgcn_s_sleep(T2D);
      u32x4 gb = {0u, 0u, 0u, 0u};
      const int of2 = WS_OX + (b * 18 + (red >> 2)) * 4096 + (lane >> 1) * 128 + (red & 3) * 32 + (lane & 1) * 16;
      unsigned bad = 1u;
#pragma unroll 1
      for (unsigned spin = 0; spin < SPIN_CAP; ++spin) {
        asm volatile("" ::: "memory");
        if (bad != 0u) gb = __builtin_amdgcn_raw_buffer_load_b128(rws, of2, 0, AUX_SC1);
        bad = (__builtin_amdgcn_perm(gb[1], gb[0], 0x0c0c0400u) | __builtin_amdgcn_perm(gb[3], gb[2], 0x04000c0cu)) ^ tagA;
        if (__all(bad == 0u)) break;
        bad |= dppu<XOR1>(bad);
        __builtin_amdgcn_s_sleep(1);
      }
      float t = rows_sum_t(cut_lo(gb[0]), cut_lo(gb[1]), cut_lo(gb[2]), cut_lo(gb[3]));
      t += dppf<XOR2>(t); t += dppf<ROR4>(t); t += dppf<ROR8>(t);
      __builtin_amdgcn_raw_buffer_store_b16((unsigned short)pack_bf16(rbf(xfin + rbf(t)) + yv, 0.f), rout, oout, 0, 0);
    }
  } else {
    const rsrc_t rk = mkrsrc(kc + (size_t)(id >> 1) * SPAN, SPAN);
    const rsrc_t rv = mkrsrc(vc + (size_t)(id >> 1) * SPAN, SPAN);
    auto poff = [&](int d, int h) {
      return ((d & 3) | (h << 2) | ((id & 1) << 3) | ((d >> 2) << 4)) * TILE_BYTES;
    };
    const int voffK = ((lane & 15) >> 2) * 256 + la * 64 + (lane & 3) * 16;
    const int voffV = lane * 16;
    u32x4 bA[8], bB[8];
    auto request = [&](u32x4 (&x)[8], rsrc_t r, int voff, int so) {
      asm volatile("" : "+v"(voff));
#pragma unroll
      for (int u = 0; u < 8; ++u) x[u] = __builtin_amdgcn_raw_buffer_load_b128(r, voff + (u & 3) * 1024, so + (u >> 2) * 4096, AUX_NT);
      __builtin_amdgcn_sched_barrier(0);
    };
    if (w >= 4) {
      const int rg = (w - 4) * 8 + (lane >> 3), c8 = lane & 7;
      unsigned xs[5];
      u32x4 wqv[9];
#pragma unroll
      for (int i = 0; i < 5; ++i) xs[i] = __builtin_amdgcn_raw_buffer_load_b32(rx, (lane + 64 * i) * 4, 0, 0);
      __builtin_amdgcn_sched_barrier(0);
      const rsrc_t rq = mkrsrc(wq + qh * 288 * 4096 + kvh * 256 + cb * 64, (287 * 4096 + 64) * 2);
#pragma unroll
      for (int i = 0; i < 9; ++i) wqv[i] = __builtin_amdgcn_raw_buffer_load_b128(rq, (rg + 32 * i) * 8192 + c8 * 16, 0, AUX_NT);
      __builtin_amdgcn_sched_barrier(0);
      __builtin_amdgcn_s_barrier();
      __builtin_amdgcn_sched_barrier(0);
      float xv[10], mean1, den1;
#pragma unroll
      for (int i = 0; i < 5; ++i) { xv[2 * i] = bflo(xs[i]); xv[2 * i + 1] = bfhi(xs[i]); }
      ln_stats(xv, lane < 32, mean1, den1);
#pragma unroll
      for (int i = 0; i < 5; ++i)
        *(unsigned*)(lds + L_XN + (lane + 64 * i) * 4) = pack_bf16(ln_apply(xv[2 * i], mean1, den1), ln_apply(xv[2 * i + 1], mean1, den1));
      asm volatile("" ::: "memory");
      f32x4 qa = {0.f, 0.f, 0.f, 0.f};
#pragma unroll
      for (int i = 0; i < 9; ++i) {
        const unsigned t = (unsigned)*(const unsigned short*)(lds + L_XN + (qh * 288 + rg + 32 * i) * 2) << (16 * (lane & 1));
        const int jp = c8 >> 1;
        qa = mfma32(wqv[i], u32x4{jp == 0 ? t : 0u, jp == 1 ? t : 0u, jp == 2 ? t : 0u, jp == 3 ? t : 0u}, qa);
      }
      if ((lane >> 5) == ((lane >> 3) & 1)) {
        float* qp = (float*)(lds + L_QPART) + ((w - 4) * 2 + (lane >> 5)) * 64 + (la & 1) * 32 + c8;
#pragma unroll
        for (int r = 0; r < 4; ++r) qp[r * 8] = qa[r];
      }
      flag_add(lds, F_QP, lane);
    }
    if (w < 4) asm volatile("s_barrier" ::: "memory");
    if (w >= PFD) flag_wait<1>(lds, F_ARR, w - PFD + 1);
    __builtin_amdgcn_sched_barrier(0);
    request(bA, rk, voffK, poff(w, 0));
    request(bB, rk, voffK, poff(w, 1));
    asm volatile("s_waitcnt vmcnt(0)" ::: "memory");
    flag_add(lds, F_ARR, lane);
    flag_wait<1>(lds, F_Q, 1);

    const int c = lane & 3, hb = (lane >> 2) & 1, tb = (lane >> 3) & 1;
    const unsigned tagA = __builtin_amdgcn_readfirstlane(*(const unsigned*)(lds + L_TAG));
    const unsigned tb0 = __builtin_amdgcn_readfirstlane(*(const unsigned*)(lds + L_TAG + 4));
    float m;
    f32x4 acc[2][2];
#pragma unroll
    for (int i = 0; i < 2; ++i)
#pragma unroll
      for (int j = 0; j < 2; ++j) acc[i][j] = f32x4{0.f, 0.f, 0.f, 0.f};
    const u32x4 qf = *(const u32x4*)(lds + L_QS + (hb * 128 + la * 32 + c * 8) * 2);
    const u32x4 zero4 = {0u, 0u, 0u, 0u};
    const u32x4 q0 = tb ? zero4 : qf, q1 = tb ? qf : zero4;
    const unsigned mask0 = c == 0 ? 0x0000ffffu : c == 1 ? 0xffff0000u : 0u;
    const unsigned mask1 = c == 2 ? 0x0000ffffu : c == 3 ? 0xffff0000u : 0u;
    auto qk = [&](const u32x4 (&x)[8], f32x4 (&sc)[4]) {
#pragma unroll
      for (int i = 0; i < 4; ++i) sc[i] = mfma32(x[i], q0, f32x4{0.f, 0.f, 0.f, 0.f});
#pragma unroll
      for (int i = 0; i < 4; ++i) sc[i] = mfma32(x[4 + i], q1, sc[i]);
    };
    auto score = [&](const f32x4 (&sc)[4]) {
      float t4[4];
#pragma unroll
      for (int i = 0; i < 4; ++i) t4[i] = (dppf<QB0>(sc[i][0]) + dppf<QB1>(sc[i][1])) + (dppf<QB2>(sc[i][2]) + dppf<QB3>(sc[i][3]));
      const float raw = (c & 2) ? ((c & 1) ? t4[3] : t4[2]) : ((c & 1) ? t4[1] : t4[0]);
      float sv = rbf(rbf(raw) * SCALE);
      asm volatile("" : "+v"(sv));
      return sv;
    };
    auto head_max = [&](float v) {
      v = fmaxf(v, dppf<XOR1>(v));
      v = fmaxf(v, dppf<XOR2>(v));
      v = fmaxf(v, dppf<ROR8>(v));
      return rows_max(v);
    };
    auto pv = [&](const u32x4 (&x)[8], float sv) {
      const float p = __builtin_amdgcn_exp2f((sv - m) * LOG2E);
      const unsigned pk = pack_bf16(p, p);
      unsigned pb0[4], pb1[4];
      pb0[0] = dppu<QB0>(pk) & mask0; pb1[0] = dppu<QB0>(pk) & mask1;
      pb0[1] = dppu<QB1>(pk) & mask0; pb1[1] = dppu<QB1>(pk) & mask1;
      pb0[2] = dppu<QB2>(pk) & mask0; pb1[2] = dppu<QB2>(pk) & mask1;
      pb0[3] = dppu<QB3>(pk) & mask0; pb1[3] = dppu<QB3>(pk) & mask1;
#pragma unroll
      for (int u = 0; u < 8; ++u) {
        acc[u >> 2][0] = mfma16(x[u][0], x[u][1], pb0[u & 3], pb1[u & 3], acc[u >> 2][0]);
        acc[u >> 2][1] = mfma16(x[u][2], x[u][3], pb0[u & 3], pb1[u & 3], acc[u >> 2][1]);
      }
    };
    auto put_ml = [&](int k, float mk, float lk) {
      lk += dppf<XOR1>(lk);
      lk += dppf<XOR2>(lk);
      lk += dppf<ROR8>(lk);
      lk = rows_sum(lk);
      if ((lane & ~4) == 0) *(f32x2*)(lds + L_ML + (k * 2 + hb) * 8) = f32x2{mk, lk};
      flag_add(lds, F_ML, lane);
    };
    const int hw = w - S, wk = w - 2, nwk = (s & 3) == 3 ? 6 : 4;
    int ontime = 0;
    auto slot = [&](int p, bool watch) {
      __builtin_amdgcn_sched_barrier(0);
      if (watch && lane == 0) {
        unsigned* fl = (unsigned*)(lds + L_FLAG) + vzero();
        const unsigned t = now32() - tb0;
        const int gap = (int)(t - __hip_atomic_fetch_max(fl + F_T, t, __ATOMIC_RELAXED, __HIP_MEMORY_SCOPE_WORKGROUP));
        if (gap > GLT && ontime >= 3) {
          __hip_atomic_fetch_max(fl + F_T, t + GLQ, __ATOMIC_RELAXED, __HIP_MEMORY_SCOPE_WORKGROUP);
          __hip_atomic_fetch_add(fl + F_SH, (unsigned)GLQ, __ATOMIC_RELAXED, __HIP_MEMORY_SCOPE_WORKGROUP);
        }
      }
      const unsigned due = tb0 + SCA + (p >= PGAP ? GAP : 0) + (unsigned)(p * SCT >> 4);
      ontime = (int)(now32() - due - flag_get(lds, F_SH)) < 0 ? ontime + 1 : 0;
      while ((int)(now32() - due - flag_get(lds, F_SH)) < 0) {}
      __builtin_amdgcn_sched_barrier(0);
    };
    f32x2 sl;
    if (w < S) {
      __builtin_amdgcn_s_setprio(2);
      unsigned char* sco = lds + L_SC + w * (NPAIR * 512) + lane * 8;
      float mx = -INFINITY;
      for (int j = 0; j < NPAIR; ++j) {
        const bool k = j + 1 < NPAIR;
        const rsrc_t r = k ? rk : rv;
        const int vo = k ? voffK : voffV, d = NWAVE + S * (k ? j : j - 1) + w;
        f32x4 sc[4];
        qk(bA, sc);
        slot(2 * NWAVE + 2 * S * j + w, true);
        request(bA, r, vo, poff(d, 0));
        const float s0 = score(sc);
        qk(bB, sc);
        slot(2 * NWAVE + 2 * S * j + S + w, true);
        request(bB, r, vo, poff(d, 1));
        const float s1 = score(sc);
        mx = fmaxf(mx, fmaxf(s0, s1));
        *(f32x2*)(sco + j * 512) = f32x2{s0, s1};
      }
      m = head_max(mx);
      if ((lane & ~4) == 0) *(float*)(lds + L_ML + (w * 2 + hb) * 8) = m;
      flag_add(lds, F_P1 + w, lane);
      for (int j = 0; j + 1 < NPAIR; ++j) {
        const f32x2 sv = *(const f32x2*)(sco + (NPAIR - 1 - j) * 512);
        const int d = j + 2 < NPAIR ? NWAVE + S * (NPAIR - 3 - j) + w : w;
        pv(bA, sv[0]);
        slot(2 * NWAVE + 2 * S * (NPAIR + j) + w, true);
        request(bA, rv, voffV, poff(d, 0));
        pv(bB, sv[1]);
        slot(2 * NWAVE + 2 * S * (NPAIR + j) + S + w, true);
        request(bB, rv, voffV, poff(d, 1));
      }
      sl = *(const f32x2*)sco;
    }
    asm volatile("" ::: "memory");
    if (w >= S) {
      {
        f32x4 sc[4];
        qk(bA, sc);
        sl[0] = score(sc);
        qk(bB, sc);
        sl[1] = score(sc);
        m = head_max(fmaxf(sl[0], sl[1]));
        put_ml(w, m, __builtin_amdgcn_exp2f((sl[0] - m) * LOG2E) + __builtin_amdgcn_exp2f((sl[1] - m) * LOG2E));
      }
      if (red < 72) {
        const int ht = (w - 4) * 64 + lane;
        unsigned a1[9], ab[9], a2[9];
        const rsrc_t r1 = mkrsrc(w1, FF * 2), rb = mkrsrc(b1, FF * 2), r2 = mkrsrc(w2, FF * 2);
#pragma unroll
        for (int i = 0; i < 9; ++i) {
          a1[i] = __builtin_amdgcn_raw_buffer_load_b32(r1, (ht + 256 * i) * 4, 0, 0);
          ab[i] = __builtin_amdgcn_raw_buffer_load_b32(rb, (ht + 256 * i) * 4, 0, 0);
          a2[i] = __builtin_amdgcn_raw_buffer_load_b32(r2, (ht + 256 * i) * 4, 0, 0);
        }
        __builtin_amdgcn_sched_barrier(0);
        float xn[10];
#pragma unroll
        for (int i = 0; i < 5; ++i) { const unsigned v = *(const unsigned*)(lds + L_XN + (lane + 64 * i) * 4); xn[2 * i] = bflo(v); xn[2 * i + 1] = bfhi(v); }
        float mean2, den2;
        ln_stats(xn, lane < 32, mean2, den2);
        constexpr float GC0 = -2.0f * 0.7978845608028654f * LOG2E, GC1 = GC0 * 0.044715f;
#pragma unroll 1
        for (int j = 0; j < 8; ++j) {
          const float hv = ln_apply(bflo(*(const unsigned short*)(lds + L_XN + (red * 8 + j) * 2)), mean2, den2);
          float y = 0.f;
#pragma unroll
          for (int i = 0; i < 9; ++i) {
            asm volatile("" : "+v"(a1[i]), "+v"(ab[i]), "+v"(a2[i]));
            const unsigned pr = pack_bf16(hv * bflo(a1[i]), hv * bfhi(a1[i]));
            const unsigned uu = pack_bf16(bflo(pr) + bflo(ab[i]), bfhi(pr) + bfhi(ab[i]));
            const float u0 = bflo(uu), u1 = bfhi(uu);
            const float e0 = __builtin_amdgcn_exp2f(u0 * fmaf(u0 * u0, GC1, GC0));
            const float e1 = __builtin_amdgcn_exp2f(u1 * fmaf(u1 * u1, GC1, GC0));
            const unsigned gg = pack_bf16(u0 * __builtin_amdgcn_rcpf(1.0f + e0), u1 * __builtin_amdgcn_rcpf(1.0f + e1));
            y = fmaf(bflo(gg), bflo(a2[i]), y);
            y = fmaf(bfhi(gg), bfhi(a2[i]), y);
          }
          y = wave_sum(y);
          if (lane == 0) *(float*)(lds + L_YP + (w - 4) * 32 + j * 4) = y;
        }
        flag_add(lds, F_Y, lane);
      }
      {
        flag_wait<16>(lds, F_P1 + hw, 1);
        const float ms = *(const float*)(lds + L_ML + (hw * 2 + hb) * 8);
        float ls = 0.f;
#pragma unroll 1
        for (int j = 0; j < NPAIR; ++j) {
          const f32x2 sv = *(const f32x2*)(lds + L_SC + hw * (NPAIR * 512) + lane * 8 + j * 512);
          ls += __builtin_amdgcn_exp2f((sv[0] - ms) * LOG2E) + __builtin_amdgcn_exp2f((sv[1] - ms) * LOG2E);
        }
        put_ml(hw, ms, ls);
      }
      wait_until(tb0 + SCA + (unsigned)(PEND * SCT >> 4) - 60u);
      __builtin_amdgcn_s_setprio(2);
      slot(PEND + hw, false);
      request(bA, rv, voffV, poff(w, 0));
      slot(PEND + NH + hw, false);
      request(bB, rv, voffV, poff(w, 1));
    }
    int po = WS_PX + ((g * 8 + hb * 4 + la) * 8 + s) * 128 + (tb * 4 + c) * 16;
    float fw;
    unsigned side;
    {
      flag_wait<1>(lds, F_ML, NWAVE);
      f32x2 ml[NWAVE];
#pragma unroll
      for (int k = 0; k < NWAVE; ++k) ml[k] = *(const f32x2*)(lds + L_ML + (k * 2 + hb) * 8);
      float mx = ml[0][0], L = 0.f;
#pragma unroll
      for (int k = 1; k < NWAVE; ++k) mx = fmaxf(mx, ml[k][0]);
#pragma unroll
      for (int k = 0; k < NWAVE; ++k) L = fmaf(__builtin_amdgcn_exp2f((ml[k][0] - mx) * LOG2E), ml[k][1], L);
      fw = __builtin_amdgcn_exp2f((m - mx) * LOG2E) * recip(L);
      side = asu((la & 1) ? L : mx) >> (16 * (c & 1));
    }
    asm volatile("" : "+v"(po), "+v"(fw), "+v"(side));
    pv(bA, sl[0]);
    pv(bB, sl[1]);
    {
      f32x4 res;
#pragma unroll
      for (int r = 0; r < 4; ++r) {
        const float own = tb ? acc[1][1][r] : acc[0][0][r], snd = tb ? acc[1][0][r] : acc[0][1][r];
        res[r] = (own + dppf<ROR8>(snd)) * fw;
      }
      *(f32x4*)(lds + L_PART + w * 1024 + lane * 16) = res;
      asm volatile("" ::: "memory");
      unsigned old = 0;
      if (lane == 0) old = __hip_atomic_fetch_add((unsigned*)(lds + L_FLAG) + F_WP + vzero(), 1u, __ATOMIC_RELAXED, __HIP_MEMORY_SCOPE_WORKGROUP);
      asm volatile("" ::: "memory");
      f32x4 pp[NWAVE];
#pragma unroll
      for (int k = 0; k < NWAVE; ++k) pp[k] = *(const f32x4*)(lds + L_PART + k * 1024 + lane * 16);
      __builtin_amdgcn_sched_barrier(0);
      if (__builtin_amdgcn_readfirstlane(old) == NWAVE - 1) {
        const f32x4 sum = ((pp[0] + pp[1]) + (pp[2] + pp[3])) + ((pp[4] + pp[5]) + (pp[6] + pp[7]));
        __builtin_amdgcn_raw_buffer_store_b128(
            u32x4{put_lo(sum[0], tagA), put_lo(sum[1], side), put_lo(sum[2], tagA >> 8), put_lo(sum[3], side >> 8)}, rws, po, 0, AUX_SC1);
        flag_add(lds, F_END, lane);
      }
    }
    if (w < 2) {
      const rsrc_t ro = mkrsrc(wo + (size_t)(kvh * 256 + qh * 128 + w * 64) * DM, 64 * DM * 2);
      u32x4 wt[24];
#pragma unroll
      for (int i = 0; i < 24; ++i) {
        if (i % 12 == 0) slot(2 * NT + w + 2 * (i / 12), false);
        wt[i] = __builtin_amdgcn_raw_buffer_load_b128(
            ro, 2 * (i % 3) < nwk ? ((i / 3) * 8 + (lane >> 3)) * (DM * 2) + (s & 3) * 256 + (i % 3) * 128 + (lane & 7) * 16 : OOB, 0, AUX_NT);
      }
#pragma unroll
      for (int i = 0; i < 24; ++i)
        *(u32x4*)(lds + L_WO + w * 24576 + (i % 3) * 8192 + ((i / 3) * 8 + ((lane >> 3) & 4) + ((lane >> 2) & 2) + ((lane >> 4) & 1)) * 128 +
                  (((lane & 7) * 16) ^ (((i / 3) & 3) * 32))) = wt[i];
      flag_add(lds, F_WO, lane);
    } else if (wk < nwk) {
      const int n = lane & 15;
      int oo = n < 2 ? WS_OX + (b * 18 + (s & 3) * 4 + wk) * 4096 + (kvh * 2 + qh) * 128 + n * 64 + la * 16 : OOB;
      asm volatile("" : "+v"(oo));
      flag_wait<1>(lds, F_WO, 2);
      u32x4 wof[2][4];
      {
        typedef __attribute__((address_space(3))) s16x4* lds_s16x4;
        unsigned char* img = lds + L_WO + (wk >> 1) * 8192 + ((n >> 2) * 8 + (la >> 1) * 4 + (la & 1)) * 128 + (lane & 3) * 8;
#pragma unroll
        for (int t = 0; t < 2; ++t)
#pragma unroll
          for (int ks = 0; ks < 4; ++ks) {
            unsigned char* p = img + (ks >> 1) * 24576 + (ks & 1) * 4096 + (((wk & 1) * 2 + t) ^ (n >> 2)) * 32;
            const u32x2 lo = __builtin_bit_cast(u32x2, __builtin_amdgcn_ds_read_tr16_b64_v4i16((lds_s16x4)p));
            const u32x2 hi = __builtin_bit_cast(u32x2, __builtin_amdgcn_ds_read_tr16_b64_v4i16((lds_s16x4)(p + 256)));
            wof[t][ks] = u32x4{lo[0], lo[1], hi[0], hi[1]};
          }
      }
      u32x4 ob[4];
#pragma unroll 1
      for (unsigned spin = 0; spin < (SPIN_CAP << 4); ++spin) {
        asm volatile("" ::: "memory");
        const unsigned f = __hip_atomic_load((unsigned*)(lds + L_FLAG) + F_O, __ATOMIC_RELAXED, __HIP_MEMORY_SCOPE_WORKGROUP);
        asm volatile("" ::: "memory");
#pragma unroll
        for (int ks = 0; ks < 4; ++ks) ob[ks] = *(const u32x4*)(lds + L_OA + ks * 64 + la * 16);
        __builtin_amdgcn_sched_barrier(0);
        if (__builtin_amdgcn_readfirstlane(f) != 0u) break;
      }
      f32x4 d0 = {0.f, 0.f, 0.f, 0.f}, d1 = {0.f, 0.f, 0.f, 0.f};
#pragma unroll
      for (int ks = 0; ks < 4; ++ks) { d0 = mfma32(wof[0][ks], ob[ks], d0); d1 = mfma32(wof[1][ks], ob[ks], d1); }
      const f32x4 d = n ? d1 : d0;
      __builtin_amdgcn_raw_buffer_store_b128(
          u32x4{put_lo(d[0], tagA), put_lo(d[1], tagA >> 8), put_lo(d[2], tagA >> 16), put_lo(d[3], tagA >> 24)}, rws, oo, 0, AUX_SC1);
    }
  }
}

}

extern "C" void kernel_launch(void* const* d_in, const int* in_sizes, int n_in, void* d_out, int out_size, void* d_ws,
                              size_t ws_size, hipStream_t stream) {
  (void)in_sizes; (void)n_in; (void)out_size; (void)ws_size;
  fused_decode_kernel<<<dim3(NWG), dim3(NTHR), 0, stream>>>(
      (const unsigned short*)d_in[0], (const unsigned char*)d_in[1], (const unsigned char*)d_in[2],
      (const unsigned short*)d_in[3], (const unsigned short*)d_in[4], (const unsigned short*)d_in[6],
      (const unsigned short*)d_in[7], (const unsigned short*)d_in[8], (const unsigned short*)d_in[9],
      (unsigned short*)d_out, (unsigned char*)d_ws);
}
```

```cpp
#include <hip/hip_runtime.h>
#include <hip/hip_bf16.h>
#include <stdint.h>

#define DIAG_M 2
namespace {

typedef unsigned u32x2 __attribute__((ext_vector_type(2)));
typedef unsigned u32x4 __attribute__((ext_vector_type(4)));
typedef float f32x2 __attribute__((ext_vector_type(2)));
typedef float f32x4 __attribute__((ext_vector_type(4)));
typedef short s16x4 __attribute__((ext_vector_type(4)));
typedef short s16x8 __attribute__((ext_vector_type(8)));
typedef __bf16 bf16x2 __attribute__((ext_vector_type(2)));
typedef __amdgpu_buffer_rsrc_t rsrc_t;

constexpr int DM = 576, SK = 32768, DH = 128, FF = 4608;
constexpr int NSPLIT = 8, NWG = 256, NWAVE = 8, NTHR = (NWAVE + 1) * 64;
constexpr int KEYS_WG = SK / NSPLIT;
constexpr int TILE_BYTES = 32 * DH * 2;
constexpr int NT = KEYS_WG / 32;
constexpr int SPAN = 2 * KEYS_WG * DH * 2;
constexpr int S = 4, NH = NWAVE - S;
constexpr int NPAIR = (NT / 2 - NH) / S;
static_assert(2 * (NPAIR * S + NH) == NT && NH == S, "every tile has one owner, every stream one helper");

constexpr int SCA = 160;
constexpr int SCT = 427;
constexpr int GAP = 25;
constexpr int CBETA = 6;
constexpr int GLT = 125;
constexpr int GLQ = 210;
constexpr int PFD = 3;
constexpr int PSL = 6;
constexpr int QPS = 2;
constexpr int T1D = 17;
constexpr int T2D = 22;
constexpr int PGAP = 2 * NWAVE + 2 * S * NPAIR, PEND = 2 * NT - 2 * NH;

constexpr int AUX_NT = 2, AUX_SC1 = 16;
constexpr unsigned RSRC3 = 0x00020000u;
constexpr unsigned SPIN_CAP = 1u << 19;
constexpr int OOB = 0x7ff00000;

constexpr int WS_EPOCH = 0;
constexpr int WS_QX = 4096;
constexpr int WS_PX = WS_QX + 32 * 4096;
constexpr int WS_OX = WS_PX + NWG * 1024;
constexpr int WS_END = WS_OX + 2 * 18 * 4096;

constexpr int L_FLAG = 0;
constexpr int L_TAG = 64;
constexpr int L_XN = 80;
constexpr int L_QPART = L_XN + 1280;
constexpr int L_QS = L_QPART + 8 * 64 * 4;
constexpr int L_OA = L_QS + 512;
constexpr int L_YP = L_OA + 256;
constexpr int L_ML = L_YP + 4 * 8 * 4;
constexpr int L_PART = L_ML + NWAVE * 16;
constexpr int L_WO = L_PART + NWAVE * 1024;
constexpr int L_SC = L_WO + 128 * 384;
constexpr int L_END = L_SC + S * NPAIR * 512;
constexpr int F_QP = 0, F_Q = 1, F_WP = 2, F_END = 3, F_O = 4, F_Y = 5, F_ARR = 6, F_WO = 8, F_ML = 9, F_SH = 10, F_T = 11, F_P1 = 12;

constexpr float LOG2E = 1.4426950408889634f;
constexpr float SCALE = 0.08838834764831845f;
constexpr float LN_EPS = 1e-5f;

__device__ __forceinline__ float asf(unsigned u) { return __builtin_bit_cast(float, u); }
__device__ __forceinline__ unsigned asu(float f) { return __builtin_bit_cast(unsigned, f); }
__device__ __forceinline__ float bflo(unsigned u) { return asf(u << 16); }
__device__ __forceinline__ float bfhi(unsigned u) { return asf(u & 0xffff0000u); }
__device__ __forceinline__ unsigned pack_bf16(float lo, float hi) {
  f32x2 v = {lo, hi};
  return __builtin_bit_cast(unsigned, __builtin_convertvector(v, bf16x2));
}
__device__ __forceinline__ float rbf(float f) { return (float)(__bf16)f; }
template <int CTRL> __device__ __forceinline__ unsigned dppu(unsigned x) {
  return (unsigned)__builtin_amdgcn_mov_dpp((int)x, CTRL, 0xf, 0xf, true);
}
template <int CTRL> __device__ __forceinline__ float dppf(float x) { return asf(dppu<CTRL>(asu(x))); }
constexpr int XOR1 = 0xB1, XOR2 = 0x4E, XOR7 = 0x141, ROR4 = 0x124, ROR8 = 0x128;
constexpr int QB0 = 0x00, QB1 = 0x55, QB2 = 0xAA, QB3 = 0xFF;
__device__ __forceinline__ float rows_sum(float x) {
  auto s = __builtin_amdgcn_permlane16_swap(asu(x), asu(x), false, false);
  x = asf(s[0]) + asf(s[1]);
  auto t = __builtin_amdgcn_permlane32_swap(asu(x), asu(x), false, false);
  return asf(t[0]) + asf(t[1]);
}
__device__ __forceinline__ float rows_max(float x) {
  auto s = __builtin_amdgcn_permlane16_swap(asu(x), asu(x), false, false);
  x = fmaxf(asf(s[0]), asf(s[1]));
  auto t = __builtin_amdgcn_permlane32_swap(asu(x), asu(x), false, false);
  return fmaxf(asf(t[0]), asf(t[1]));
}
__device__ __forceinline__ float rows_sum_t(float v0, float v1, float v2, float v3) {
  const auto p0 = __builtin_amdgcn_permlane32_swap(asu(v0), asu(v2), false, false);
  const auto p1 = __builtin_amdgcn_permlane32_swap(asu(v1), asu(v3), false, false);
  const auto p2 = __builtin_amdgcn_permlane16_swap(asu(asf(p0[0]) + asf(p0[1])), asu(asf(p1[0]) + asf(p1[1])), false, false);
  return asf(p2[0]) + asf(p2[1]);
}
__device__ __forceinline__ float wave_sum(float x) {
  x += dppf<XOR1>(x); x += dppf<XOR2>(x); x += dppf<ROR4>(x); x += dppf<ROR8>(x);
  return rows_sum(x);
}
__device__ __forceinline__ float recip(float x) {
  const float r = __builtin_amdgcn_rcpf(x);
  return fmaf(fmaf(-x, r, 1.0f), r, r);
}
__device__ __forceinline__ rsrc_t mkrsrc(const void* p, int bytes) {
  return __builtin_amdgcn_make_buffer_rsrc(const_cast<void*>(p), 0, bytes, RSRC3);
}
__device__ __forceinline__ f32x4 mfma32(u32x4 a, u32x4 b, f32x4 c) {
  return __builtin_amdgcn_mfma_f32_16x16x32_bf16(__builtin_bit_cast(s16x8, a), __builtin_bit_cast(s16x8, b), c, 0, 0, 0);
}
__device__ __forceinline__ f32x4 mfma16(unsigned a0, unsigned a1, unsigned b0, unsigned b1, f32x4 c) {
  u32x2 a = {a0, a1}, b = {b0, b1};
  return __builtin_amdgcn_mfma_f32_16x16x16bf16_1k(__builtin_bit_cast(s16x4, a), __builtin_bit_cast(s16x4, b), c, 0, 0, 0);
}
__device__ __forceinline__ unsigned put_lo(float v, unsigned byte) { return (asu(v) & 0xffffff00u) | (byte & 0xffu); }
__device__ __forceinline__ float cut_lo(unsigned u) { return asf(u & 0xffffff00u); }
__device__ __forceinline__ unsigned now32() { return (unsigned)__builtin_amdgcn_s_memrealtime(); }
__device__ __forceinline__ void wait_until(unsigned t) {
  while ((int)(now32() - t) < 0) __builtin_amdgcn_s_sleep(1);
}
__device__ __forceinline__ int vzero() { int z = 0; asm("" : "+v"(z)); return z; }
__device__ __forceinline__ void flag_add(unsigned char* lds, int f, int lane) {
  asm volatile("" ::: "memory");
  if (lane == 0) __hip_atomic_fetch_add((unsigned*)(lds + L_FLAG) + f + vzero(), 1u, __ATOMIC_RELAXED, __HIP_MEMORY_SCOPE_WORKGROUP);
  asm volatile("" ::: "memory");
}
__device__ __forceinline__ unsigned flag_get(unsigned char* lds, int f) {
  return __builtin_amdgcn_readfirstlane(__hip_atomic_load((unsigned*)(lds + L_FLAG) + f, __ATOMIC_RELAXED, __HIP_MEMORY_SCOPE_WORKGROUP));
}
template <int SLEEP> __device__ __forceinline__ void flag_wait(unsigned char* lds, int f, unsigned n) {
#pragma unroll 1
  for (unsigned spin = 0; spin < (SPIN_CAP << 4); ++spin) {
    if (flag_get(lds, f) >= n) break;
    if (SLEEP) __builtin_amdgcn_s_sleep(SLEEP);
  }
  asm volatile("" ::: "memory");
}
__device__ __forceinline__ void ln_stats(const float (&v)[10], bool last_valid, float& mean_b, float& den_b) {
  float s = 0.f;
#pragma unroll
  for (int i = 0; i < 8; ++i) s += v[i];
  s += last_valid ? (v[8] + v[9]) : 0.f;
  const float mean = wave_sum(s) * (1.0f / DM);
  float q = 0.f;
#pragma unroll
  for (int i = 0; i < 8; ++i) { const float d = v[i] - mean; q = fmaf(d, d, q); }
  { const float d8 = v[8] - mean, d9 = v[9] - mean; q += last_valid ? fmaf(d8, d8, d9 * d9) : 0.f; }
  const float var = wave_sum(q) * (1.0f / DM);
  mean_b = rbf(mean);
  den_b = rbf(__builtin_amdgcn_sqrtf(rbf(rbf(var) + LN_EPS)));
}
__device__ __forceinline__ float ln_apply(float x, float mean_b, float den_b) {
  const float d = rbf(x - mean_b), r = __builtin_amdgcn_rcpf(den_b), q = d * r;
  return rbf(fmaf(fmaf(-q, den_b, d), r, q));
}

__global__ void __launch_bounds__(NTHR, 3)
fused_decode_kernel(const unsigned short* __restrict__ x, const unsigned char* __restrict__ kc, const unsigned char* __restrict__ vc,
                    const unsigned short* __restrict__ wq, const unsigned short* __restrict__ wo,
                    const unsigned short* __restrict__ w1, const unsigned short* __restrict__ b1,
                    const unsigned short* __restrict__ w2, const unsigned short* __restrict__ b2,
                    unsigned short* __restrict__ out, unsigned char* ws) {
  __shared__ __attribute__((aligned(16))) unsigned char lds[L_END];
  const int lane = threadIdx.x & 63, wv = __builtin_amdgcn_readfirstlane(threadIdx.x >> 6), w = wv < 8 ? wv ^ 4 : wv;
  const unsigned tst = now32();
  const int g = blockIdx.x >> 3, s = (blockIdx.x + 3 * g) & 7, id = g * 8 + s, b = g >> 4, kvh = g & 15;
  const rsrc_t rws = mkrsrc(ws, WS_END);
  const rsrc_t rx = mkrsrc(x + b * DM, DM * 2);
  const int qh = s >> 2, cb = (s + (kvh >> 2)) & 3;
  const int la = lane >> 4;
  const int red = kvh * 8 + s;

  u32x2 ep = {0u, 0u};
  if (w == NWAVE) {
    ep = __builtin_amdgcn_raw_buffer_load_b64(rws, WS_EPOCH + b * 256, 0, 0);
    if (lane < 16) *((unsigned*)(lds + L_FLAG) + lane) = 0u;
    asm volatile("s_waitcnt lgkmcnt(0)\n\ts_barrier" ::: "memory");
  }
  if (w >= NWAVE) {
    __builtin_amdgcn_s_setprio(3);
    const unsigned e0 = __builtin_amdgcn_readfirstlane(ep[0]);
    const unsigned e1 = ((e0 & 0x7f7f7f7fu) + 0x01010101u) ^ (e0 & 0x80808080u);
    const unsigned tagA = e1 ^ 0xb77a3d00u;
    const unsigned tagB = __builtin_amdgcn_readfirstlane(ep[1]) + 1u;
    if (lane == 0) *(unsigned*)(lds + L_TAG) = tagA;
    flag_wait<0>(lds, F_QP, 4);
    {
      f32x2 q2 = *(const f32x2*)(lds + L_QPART + (lane & 31) * 8);
#pragma unroll
      for (int ww = 1; ww < 8; ++ww) q2 += *(const f32x2*)(lds + L_QPART + ww * 256 + (lane & 31) * 8);
      __builtin_amdgcn_raw_buffer_store_b128(u32x4{tagA, asu(q2[0]), (tagB & 0xffffu) | (tst << 16), asu(q2[1])}, rws,
                                             lane < 32 ? WS_QX + g * 4096 + qh * 2048 + cb * 512 + lane * 16 : OOB, 0, AUX_SC1);
    }
    const unsigned t_pub = now32();
    __builtin_amdgcn_s_sleep(PSL);
    {
      float qa[4], qb[4];
      int ts[4];
      int of[4];
#pragma unroll
      for (int i = 0; i < 4; ++i) { qa[i] = 0.f; qb[i] = 0.f; ts[i] = 0; of[i] = WS_QX + g * 4096 + i * 1024 + lane * 16; }
#pragma unroll 1
      for (unsigned spin = 0; spin < SPIN_CAP; ++spin) {
        asm volatile("" ::: "memory");
        u32x4 gr[4];
#pragma unroll
        for (int i = 0; i < 4; ++i) gr[i] = __builtin_amdgcn_raw_buffer_load_b128(rws, of[i], 0, AUX_SC1);
        bool done = true;
#pragma unroll
        for (int i = 0; i < 4; ++i) {
          const bool fresh = (gr[i][0] == tagA) & (((gr[i][2] ^ tagB) & 0xffffu) == 0u) & (of[i] != OOB);
          ts[i] = fresh ? (int)(gr[i][2] - (tst << 16)) >> 16 : ts[i];
          qa[i] = fresh ? asf(gr[i][1]) : qa[i]; qb[i] = fresh ? asf(gr[i][3]) : qb[i];
          of[i] = fresh ? OOB : of[i];
          done &= of[i] == OOB;
        }
        if (__all(done)) break;
        __builtin_amdgcn_s_sleep(QPS);
      }
      *(unsigned*)(lds + L_QS + lane * 4) = pack_bf16(qa[0] + qa[2], qb[0] + qb[2]);
      *(unsigned*)(lds + L_QS + 256 + lane * 4) = pack_bf16(qa[1] + qa[3], qb[1] + qb[3]);
      {
        int sm = 0, mx = -32768;
#pragma unroll
        for (int i = 0; i < 4; ++i) {
          const int t0 = __builtin_amdgcn_readlane(ts[i], 0), t1 = __builtin_amdgcn_readlane(ts[i], 32);
          sm += t0 + t1; mx = max(mx, max(t0, t1));
        }
        sm >>= 3;
        if (lane == 0) *(unsigned*)(lds + L_TAG + 4) = tst + (unsigned)(sm + ((mx - sm) * CBETA >> 4));
      }
      flag_add(lds, F_Q, lane);
    }
    const unsigned t_q = now32();
    const int fc = (lane & 1) * 4 + la;
    const unsigned xraw = __builtin_amdgcn_raw_buffer_load_b32(rx, red * 16 + (fc >> 1) * 4, 0, 0);
    const unsigned b2v = __builtin_amdgcn_raw_buffer_load_b16(mkrsrc(b2, 4), 0, 0, 0);
    if (red == 127) {
      wait_until(tst + 2000u);
#pragma unroll 1
      for (unsigned spin = 0; spin < SPIN_CAP; ++spin) {
        asm volatile("" ::: "memory");
        const u32x4 v0 = __builtin_amdgcn_raw_buffer_load_b128(rws, WS_QX + b * 65536 + lane * 512, 0, AUX_SC1);
        const u32x4 v1 = __builtin_amdgcn_raw_buffer_load_b128(rws, WS_QX + b * 65536 + 32768 + lane * 512, 0, AUX_SC1);
        if (__all((v0[0] == tagA) & (v1[0] == tagA) & ((((v0[2] ^ tagB) | (v1[2] ^ tagB)) & 0xffffu) == 0u))) break;
        __builtin_amdgcn_s_sleep(16);
      }
      __builtin_amdgcn_raw_buffer_store_b64(u32x2{e1, tagB}, rws, lane == 0 ? WS_EPOCH + b * 256 : OOB, 0, AUX_SC1);
    }
    float yv = 0.f, xfin = (fc & 1) ? bfhi(xraw) : bflo(xraw);
    const rsrc_t rout = mkrsrc(out + b * DM, DM * 2 + 2);
    int oout = (lane & 14) == 0 ? (red * 8 + fc) * 2 : OOB;
    if (red < 72) {
      flag_wait<16>(lds, F_Y, 4);
#pragma unroll
      for (int ww = 0; ww < 4; ++ww) yv += *(const float*)(lds + L_YP + ww * 32 + fc * 4);
      yv = rbf(rbf(yv) + bflo(b2v));
      __builtin_amdgcn_raw_buffer_store_b16((unsigned short)0, rout, oout, 0, 0);
    }
    int oa = (lane & 8) ? L_QPART + lane * 8 : L_OA + la * 64 + (lane & 7) * 8;
    asm volatile("" : "+v"(yv), "+v"(xfin), "+v"(oout), "+v"(oa));
    flag_wait<16>(lds, F_WP, 1);
    u32x4 ga[4];
#pragma unroll
    for (int i = 0; i < 4; ++i) ga[i] = u32x4{0u, 0u, 0u, 0u};
    int of1 = WS_PX + (g * 8 + (s & 4)) * 1024 + lane * 16;
    asm volatile("" : "+v"(of1), "+v"(ga[0]), "+v"(ga[1]), "+v"(ga[2]), "+v"(ga[3]));
    flag_wait<0>(lds, F_END, 1);
    __builtin_amdgcn_s_sleep(T1D);
    {
      unsigned bad = 1u;
#pragma unroll 1
      for (unsigned spin = 0; spin < SPIN_CAP; ++spin) {
        asm volatile("" ::: "memory");
        if (bad != 0u) {
#pragma unroll
          for (int i = 0; i < 4; ++i) ga[i] = __builtin_amdgcn_raw_buffer_load_b128(rws, of1 + i * 1024, 0, AUX_SC1);
        }
        bad = 0u;
#pragma unroll
        for (int i = 0; i < 4; ++i) bad |= (ga[i][0] ^ tagA) | (ga[i][2] ^ (tagA >> 8));
        bad &= 0xffu;
        if (__all(bad == 0u)) break;
        bad |= dppu<XOR1>(bad); bad |= dppu<XOR2>(bad); bad |= dppu<XOR7>(bad);
        __builtin_amdgcn_s_sleep(1);
      }
      const unsigned hm = __builtin_amdgcn_perm(ga[0][3], ga[0][1], 0x0c0c0400u) << (16 * (lane & 1));
      const unsigned hl = __builtin_amdgcn_perm(ga[1][3], ga[1][1], 0x0c0c0400u) << (16 * (lane & 1));
      const float pm = asf(hm | dppu<XOR1>(hm)), pl = asf(hl | dppu<XOR1>(hl));
      const float M = rows_max(fmaxf(pm, dppf<ROR8>(pm)));
      const float w0 = __builtin_amdgcn_exp2f((pm - M) * LOG2E) * pl;
      const float inv = recip(rows_sum(w0 + dppf<ROR8>(w0)));
      float v[4];
#pragma unroll
      for (int r = 0; r < 4; ++r) {
        const float t = rows_sum_t(w0 * cut_lo(ga[0][r]), w0 * cut_lo(ga[1][r]), w0 * cut_lo(ga[2][r]), w0 * cut_lo(ga[3][r]));
        v[r] = (t + dppf<ROR8>(t)) * inv;
      }
      *(u32x2*)(lds + oa) = u32x2{pack_bf16(v[0], v[1]), pack_bf16(v[2], v[3])};
      flag_add(lds, F_O, lane);
    }
    if (red < 72) {
      __builtin_amdgcn_s_sleep(T2D);
      u32x4 gb = {0u, 0u, 0u, 0u};
      const int of2 = WS_OX + (b * 18 + (red >> 2)) * 4096 + (lane >> 1) * 128 + (red & 3) * 32 + (lane & 1) * 16;
      unsigned bad = 1u;
#pragma unroll 1
      for (unsigned spin = 0; spin < SPIN_CAP; ++spin) {
        asm volatile("" ::: "memory");
        if (bad != 0u) gb = __builtin_amdgcn_raw_buffer_load_b128(rws, of2, 0, AUX_SC1);
        bad = (__builtin_amdgcn_perm(gb[1], gb[0], 0x0c0c0400u) | __builtin_amdgcn_perm(gb[3], gb[2], 0x04000c0cu)) ^ tagA;
        if (__all(bad == 0u)) break;
        bad |= dppu<XOR1>(bad);
        __builtin_amdgcn_s_sleep(1);
      }
      float t = rows_sum_t(cut_lo(gb[0]), cut_lo(gb[1]), cut_lo(gb[2]), cut_lo(gb[3]));
      t += dppf<XOR2>(t); t += dppf<ROR4>(t); t += dppf<ROR8>(t);
      __builtin_amdgcn_raw_buffer_store_b16((unsigned short)pack_bf16(rbf(xfin + rbf(t)) + yv, 0.f), rout, oout, 0, 0);
    }
    if (id == 0) {
      const unsigned tb = __builtin_amdgcn_readfirstlane(*(const unsigned*)(lds + L_TAG + 4));
      const unsigned met = DIAG_M == 0 ? now32() - tb : DIAG_M == 1 ? t_pub - tst : t_q - tb;
      wait_until(tst + 15000u + 10u * (met < 20000u ? met : 20000u));
    }
  } else {
    const rsrc_t rk = mkrsrc(kc + (size_t)(id >> 1) * SPAN, SPAN);
    const rsrc_t rv = mkrsrc(vc + (size_t)(id >> 1) * SPAN, SPAN);
    auto poff = [&](int d, int h) {
      return ((d & 3) | (h << 2) | ((id & 1) << 3) | ((d >> 2) << 4)) * TILE_BYTES;
    };
    const int voffK = ((lane & 15) >> 2) * 256 + la * 64 + (lane & 3) * 16;
    const int voffV = lane * 16;
    u32x4 bA[8], bB[8];
    auto request = [&](u32x4 (&x)[8], rsrc_t r, int voff, int so) {
      asm volatile("" : "+v"(voff));
#pragma unroll
      for (int u = 0; u < 8; ++u) x[u] = __builtin_amdgcn_raw_buffer_load_b128(r, voff + (u & 3) * 1024, so + (u >> 2) * 4096, AUX_NT);
      __builtin_amdgcn_sched_barrier(0);
    };
    if (w >= 4) {
      const int rg = (w - 4) * 8 + (lane >> 3), c8 = lane & 7;
      unsigned xs[5];
      u32x4 wqv[9];
#pragma unroll
      for (int i = 0; i < 5; ++i) xs[i] = __builtin_amdgcn_raw_buffer_load_b32(rx, (lane + 64 * i) * 4, 0, 0);
      __builtin_amdgcn_sched_barrier(0);
      const rsrc_t rq = mkrsrc(wq + qh * 288 * 4096 + kvh * 256 + cb * 64, (287 * 4096 + 64) * 2);
#pragma unroll
      for (int i = 0; i < 9; ++i) wqv[i] = __builtin_amdgcn_raw_buffer_load_b128(rq, (rg + 32 * i) * 8192 + c8 * 16, 0, AUX_NT);
      __builtin_amdgcn_sched_barrier(0);
      __builtin_amdgcn_s_barrier();
      __builtin_amdgcn_sched_barrier(0);
      float xv[10], mean1, den1;
#pragma unroll
      for (int i = 0; i < 5; ++i) { xv[2 * i] = bflo(xs[i]); xv[2 * i + 1] = bfhi(xs[i]); }
      ln_stats(xv, lane < 32, mean1, den1);
#pragma unroll
      for (int i = 0; i < 5; ++i)
        *(unsigned*)(lds + L_XN + (lane + 64 * i) * 4) = pack_bf16(ln_apply(xv[2 * i], mean1, den1), ln_apply(xv[2 * i + 1], mean1, den1));
      asm volatile("" ::: "memory");
      f32x4 qa = {0.f, 0.f, 0.f, 0.f};
#pragma unroll
      for (int i = 0; i < 9; ++i) {
        const unsigned t = (unsigned)*(const unsigned short*)(lds + L_XN + (qh * 288 + rg + 32 * i) * 2) << (16 * (lane & 1));
        const int jp = c8 >> 1;
        qa = mfma32(wqv[i], u32x4{jp == 0 ? t : 0u, jp == 1 ? t : 0u, jp == 2 ? t : 0u, jp == 3 ? t : 0u}, qa);
      }
      if ((lane >> 5) == ((lane >> 3) & 1)) {
        float* qp = (float*)(lds + L_QPART) + ((w - 4) * 2 + (lane >> 5)) * 64 + (la & 1) * 32 + c8;
#pragma unroll
        for (int r = 0; r < 4; ++r) qp[r * 8] = qa[r];
      }
      flag_add(lds, F_QP, lane);
    }
    if (w < 4) asm volatile("s_barrier" ::: "memory");
    if (w >= PFD) flag_wait<1>(lds, F_ARR, w - PFD + 1);
    __builtin_amdgcn_sched_barrier(0);
    request(bA, rk, voffK, poff(w, 0));
    request(bB, rk, voffK, poff(w, 1));
    asm volatile("s_waitcnt vmcnt(0)" ::: "memory");
    flag_add(lds, F_ARR, lane);
    flag_wait<1>(lds, F_Q, 1);

    const int c = lane & 3, hb = (lane >> 2) & 1, tb = (lane >> 3) & 1;
    const unsigned tagA = __builtin_amdgcn_readfirstlane(*(const unsigned*)(lds + L_TAG));
    const unsigned tb0 = __builtin_amdgcn_readfirstlane(*(const unsigned*)(lds + L_TAG + 4));
    float m;
    f32x4 acc[2][2];
#pragma unroll
    for (int i = 0; i < 2; ++i)
#pragma unroll
      for (int j = 0; j < 2; ++j) acc[i][j] = f32x4{0.f, 0.f, 0.f, 0.f};
    const u32x4 qf = *(const u32x4*)(lds + L_QS + (hb * 128 + la * 32 + c * 8) * 2);
    const u32x4 zero4 = {0u, 0u, 0u, 0u};
    const u32x4 q0 = tb ? zero4 : qf, q1 = tb ? qf : zero4;
    const unsigned mask0 = c == 0 ? 0x0000ffffu : c == 1 ? 0xffff0000u : 0u;
    const unsigned mask1 = c == 2 ? 0x0000ffffu : c == 3 ? 0xffff0000u : 0u;
    auto qk = [&](const u32x4 (&x)[8], f32x4 (&sc)[4]) {
#pragma unroll
      for (int i = 0; i < 4; ++i) sc[i] = mfma32(x[i], q0, f32x4{0.f, 0.f, 0.f, 0.f});
#pragma unroll
      for (int i = 0; i < 4; ++i) sc[i] = mfma32(x[4 + i], q1, sc[i]);
    };
    auto score = [&](const f32x4 (&sc)[4]) {
      float t4[4];
#pragma unroll
      for (int i = 0; i < 4; ++i) t4[i] = (dppf<QB0>(sc[i][0]) + dppf<QB1>(sc[i][1])) + (dppf<QB2>(sc[i][2]) + dppf<QB3>(sc[i][3]));
      const float raw = (c & 2) ? ((c & 1) ? t4[3] : t4[2]) : ((c & 1) ? t4[1] : t4[0]);
      float sv = rbf(rbf(raw) * SCALE);
      asm volatile("" : "+v"(sv));
      return sv;
    };
    auto head_max = [&](float v) {
      v = fmaxf(v, dppf<XOR1>(v));
      v = fmaxf(v, dppf<XOR2>(v));
      v = fmaxf(v, dppf<ROR8>(v));
      return rows_max(v);
    };
    auto pv = [&](const u32x4 (&x)[8], float sv) {
      const float p = __builtin_amdgcn_exp2f((sv - m) * LOG2E);
      const unsigned pk = pack_bf16(p, p);
      unsigned pb0[4], pb1[4];
      pb0[0] = dppu<QB0>(pk) & mask0; pb1[0] = dppu<QB0>(pk) & mask1;
      pb0[1] = dppu<QB1>(pk) & mask0; pb1[1] = dppu<QB1>(pk) & mask1;
      pb0[2] = dppu<QB2>(pk) & mask0; pb1[2] = dppu<QB2>(pk) & mask1;
      pb0[3] = dppu<QB3>(pk) & mask0; pb1[3] = dppu<QB3>(pk) & mask1;
#pragma unroll
      for (int u = 0; u < 8; ++u) {
        acc[u >> 2][0] = mfma16(x[u][0], x[u][1], pb0[u & 3], pb1[u & 3], acc[u >> 2][0]);
        acc[u >> 2][1] = mfma16(x[u][2], x[u][3], pb0[u & 3], pb1[u & 3], acc[u >> 2][1]);
      }
    };
    auto put_ml = [&](int k, float mk, float lk) {
      lk += dppf<XOR1>(lk);
      lk += dppf<XOR2>(lk);
      lk += dppf<ROR8>(lk);
      lk = rows_sum(lk);
      if ((lane & ~4) == 0) *(f32x2*)(lds + L_ML + (k * 2 + hb) * 8) = f32x2{mk, lk};
      flag_add(lds, F_ML, lane);
    };
    const int hw = w - S, wk = w - 2, nwk = (s & 3) == 3 ? 6 : 4;
    int ontime = 0;
    auto slot = [&](int p, bool watch) {
      __builtin_amdgcn_sched_barrier(0);
      if (watch && lane == 0) {
        unsigned* fl = (unsigned*)(lds + L_FLAG) + vzero();
        const unsigned t = now32() - tb0;
        const int gap = (int)(t - __hip_atomic_fetch_max(fl + F_T, t, __ATOMIC_RELAXED, __HIP_MEMORY_SCOPE_WORKGROUP));
        if (gap > GLT && ontime >= 3) {
          __hip_atomic_fetch_max(fl + F_T, t + GLQ, __ATOMIC_RELAXED, __HIP_MEMORY_SCOPE_WORKGROUP);
          __hip_atomic_fetch_add(fl + F_SH, (unsigned)GLQ, __ATOMIC_RELAXED, __HIP_MEMORY_SCOPE_WORKGROUP);
        }
      }
      const unsigned due = tb0 + SCA + (p >= PGAP ? GAP : 0) + (unsigned)(p * SCT >> 4);
      ontime = (int)(now32() - due - flag_get(lds, F_SH)) < 0 ? ontime + 1 : 0;
      while ((int)(now32() - due - flag_get(lds, F_SH)) < 0) {}
      __builtin_amdgcn_sched_barrier(0);
    };
    f32x2 sl;
    if (w < S) {
      __builtin_amdgcn_s_setprio(2);
      unsigned char* sco = lds + L_SC + w * (NPAIR * 512) + lane * 8;
      float mx = -INFINITY;
      for (int j = 0; j < NPAIR; ++j) {
        const bool k = j + 1 < NPAIR;
        const rsrc_t r = k ? rk : rv;
        const int vo = k ? voffK : voffV, d = NWAVE + S * (k ? j : j - 1) + w;
        f32x4 sc[4];
        qk(bA, sc);
        slot(2 * NWAVE + 2 * S * j + w, true);
        request(bA, r, vo, poff(d, 0));
        const float s0 = score(sc);
        qk(bB, sc);
        slot(2 * NWAVE + 2 * S * j + S + w, true);
        request(bB, r, vo, poff(d, 1));
        const float s1 = score(sc);
        mx = fmaxf(mx, fmaxf(s0, s1));
        *(f32x2*)(sco + j * 512) = f32x2{s0, s1};
      }
      m = head_max(mx);
      if ((lane & ~4) == 0) *(float*)(lds + L_ML + (w * 2 + hb) * 8) = m;
      flag_add(lds, F_P1 + w, lane);
      for (int j = 0; j + 1 < NPAIR; ++j) {
        const f32x2 sv = *(const f32x2*)(sco + (NPAIR - 1 - j) * 512);
        const int d = j + 2 < NPAIR ? NWAVE + S * (NPAIR - 3 - j) + w : w;
        pv(bA, sv[0]);
        slot(2 * NWAVE + 2 * S * (NPAIR + j) + w, true);
        request(bA, rv, voffV, poff(d, 0));
        pv(bB, sv[1]);
        slot(2 * NWAVE + 2 * S * (NPAIR + j) + S + w, true);
        request(bB, rv, voffV, poff(d, 1));
      }
      sl = *(const f32x2*)sco;
    }
    asm volatile("" ::: "memory");
    if (w >= S) {
      {
        f32x4 sc[4];
        qk(bA, sc);
        sl[0] = score(sc);
        qk(bB, sc);
        sl[1] = score(sc);
        m = head_max(fmaxf(sl[0], sl[1]));
        put_ml(w, m, __builtin_amdgcn_exp2f((sl[0] - m) * LOG2E) + __builtin_amdgcn_exp2f((sl[1] - m) * LOG2E));
      }
      if (red < 72) {
        const int ht = (w - 4) * 64 + lane;
        unsigned a1[9], ab[9], a2[9];
        const rsrc_t r1 = mkrsrc(w1, FF * 2), rb = mkrsrc(b1, FF * 2), r2 = mkrsrc(w2, FF * 2);
#pragma unroll
        for (int i = 0; i < 9; ++i) {
          a1[i] = __builtin_amdgcn_raw_buffer_load_b32(r1, (ht + 256 * i) * 4, 0, 0);
          ab[i] = __builtin_amdgcn_raw_buffer_load_b32(rb, (ht + 256 * i) * 4, 0, 0);
          a2[i] = __builtin_amdgcn_raw_buffer_load_b32(r2, (ht + 256 * i) * 4, 0, 0);
        }
        __builtin_amdgcn_sched_barrier(0);
        float xn[10];
#pragma unroll
        for (int i = 0; i < 5; ++i) { const unsigned v = *(const unsigned*)(lds + L_XN + (lane + 64 * i) * 4); xn[2 * i] = bflo(v); xn[2 * i + 1] = bfhi(v); }
        float mean2, den2;
        ln_stats(xn, lane < 32, mean2, den2);
        constexpr float GC0 = -2.0f * 0.7978845608028654f * LOG2E, GC1 = GC0 * 0.044715f;
#pragma unroll 1
        for (int j = 0; j < 8; ++j) {
          const float hv = ln_apply(bflo(*(const unsigned short*)(lds + L_XN + (red * 8 + j) * 2)), mean2, den2);
          float y = 0.f;
#pragma unroll
          for (int i = 0; i < 9; ++i) {
            asm volatile("" : "+v"(a1[i]), "+v"(ab[i]), "+v"(a2[i]));
            const unsigned pr = pack_bf16(hv * bflo(a1[i]), hv * bfhi(a1[i]));
            const unsigned uu = pack_bf16(bflo(pr) + bflo(ab[i]), bfhi(pr) + bfhi(ab[i]));
            const float u0 = bflo(uu), u1 = bfhi(uu);
            const float e0 = __builtin_amdgcn_exp2f(u0 * fmaf(u0 * u0, GC1, GC0));
            const float e1 = __builtin_amdgcn_exp2f(u1 * fmaf(u1 * u1, GC1, GC0));
            const unsigned gg = pack_bf16(u0 * __builtin_amdgcn_rcpf(1.0f + e0), u1 * __builtin_amdgcn_rcpf(1.0f + e1));
            y = fmaf(bflo(gg), bflo(a2[i]), y);
            y = fmaf(bfhi(gg), bfhi(a2[i]), y);
          }
          y = wave_sum(y);
          if (lane == 0) *(float*)(lds + L_YP + (w - 4) * 32 + j * 4) = y;
        }
        flag_add(lds, F_Y, lane);
      }
      {
        flag_wait<16>(lds, F_P1 + hw, 1);
        const float ms = *(const float*)(lds + L_ML + (hw * 2 + hb) * 8);
        float ls = 0.f;
#pragma unroll 1
        for (int j = 0; j < NPAIR; ++j) {
          const f32x2 sv = *(const f32x2*)(lds + L_SC + hw * (NPAIR * 512) + lane * 8 + j * 512);
          ls += __builtin_amdgcn_exp2f((sv[0] - ms) * LOG2E) + __builtin_amdgcn_exp2f((sv[1] - ms) * LOG2E);
        }
        put_ml(hw, ms, ls);
      }
      wait_until(tb0 + SCA + (unsigned)(PEND * SCT >> 4) - 60u);
      __builtin_amdgcn_s_setprio(2);
      slot(PEND + hw, false);
      request(bA, rv, voffV, poff(w, 0));
      slot(PEND + NH + hw, false);
      request(bB, rv, voffV, poff(w, 1));
    }
    int po = WS_PX + ((g * 8 + hb * 4 + la) * 8 + s) * 128 + (tb * 4 + c) * 16;
    float fw;
    unsigned side;
    {
      flag_wait<1>(lds, F_ML, NWAVE);
      f32x2 ml[NWAVE];
#pragma unroll
      for (int k = 0; k < NWAVE; ++k) ml[k] = *(const f32x2*)(lds + L_ML + (k * 2 + hb) * 8);
      float mx = ml[0][0], L = 0.f;
#pragma unroll
      for (int k = 1; k < NWAVE; ++k) mx = fmaxf(mx, ml[k][0]);
#pragma unroll
      for (int k = 0; k < NWAVE; ++k) L = fmaf(__builtin_amdgcn_exp2f((ml[k][0] - mx) * LOG2E), ml[k][1], L);
      fw = __builtin_amdgcn_exp2f((m - mx) * LOG2E) * recip(L);
      side = asu((la & 1) ? L : mx) >> (16 * (c & 1));
    }
    asm volatile("" : "+v"(po), "+v"(fw), "+v"(side));
    pv(bA, sl[0]);
    pv(bB, sl[1]);
    {
      f32x4 res;
#pragma unroll
      for (int r = 0; r < 4; ++r) {
        const float own = tb ? acc[1][1][r] : acc[0][0][r], snd = tb ? acc[1][0][r] : acc[0][1][r];
        res[r] = (own + dppf<ROR8>(snd)) * fw;
      }
      *(f32x4*)(lds + L_PART + w * 1024 + lane * 16) = res;
      asm volatile("" ::: "memory");
      unsigned old = 0;
      if (lane == 0) old = __hip_atomic_fetch_add((unsigned*)(lds + L_FLAG) + F_WP + vzero(), 1u, __ATOMIC_RELAXED, __HIP_MEMORY_SCOPE_WORKGROUP);
      asm volatile("" ::: "memory");
      f32x4 pp[NWAVE];
#pragma unroll
      for (int k = 0; k < NWAVE; ++k) pp[k] = *(const f32x4*)(lds + L_PART + k * 1024 + lane * 16);
      __builtin_amdgcn_sched_barrier(0);
      if (__builtin_amdgcn_readfirstlane(old) == NWAVE - 1) {
        const f32x4 sum = ((pp[0] + pp[1]) + (pp[2] + pp[3])) + ((pp[4] + pp[5]) + (pp[6] + pp[7]));
        __builtin_amdgcn_raw_buffer_store_b128(
            u32x4{put_lo(sum[0], tagA), put_lo(sum[1], side), put_lo(sum[2], tagA >> 8), put_lo(sum[3], side >> 8)}, rws, po, 0, AUX_SC1);
        flag_add(lds, F_END, lane);
      }
    }
    if (w < 2) {
      const rsrc_t ro = mkrsrc(wo + (size_t)(kvh * 256 + qh * 128 + w * 64) * DM, 64 * DM * 2);
      u32x4 wt[24];
#pragma unroll
      for (int i = 0; i < 24; ++i) {
        if (i % 12 == 0) slot(2 * NT + w + 2 * (i / 12), false);
        wt[i] = __builtin_amdgcn_raw_buffer_load_b128(
            ro, 2 * (i % 3) < nwk ? ((i / 3) * 8 + (lane >> 3)) * (DM * 2) + (s & 3) * 256 + (i % 3) * 128 + (lane & 7) * 16 : OOB, 0, AUX_NT);
      }
#pragma unroll
      for (int i = 0; i < 24; ++i)
        *(u32x4*)(lds + L_WO + w * 24576 + (i % 3) * 8192 + ((i / 3) * 8 + ((lane >> 3) & 4) + ((lane >> 2) & 2) + ((lane >> 4) & 1)) * 128 +
                  (((lane & 7) * 16) ^ (((i / 3) & 3) * 32))) = wt[i];
      flag_add(lds, F_WO, lane);
    } else if (wk < nwk) {
      const int n = lane & 15;
      int oo = n < 2 ? WS_OX + (b * 18 + (s & 3) * 4 + wk) * 4096 + (kvh * 2 + qh) * 128 + n * 64 + la * 16 : OOB;
      asm volatile("" : "+v"(oo));
      flag_wait<1>(lds, F_WO, 2);
      u32x4 wof[2][4];
      {
        typedef __attribute__((address_space(3))) s16x4* lds_s16x4;
        unsigned char* img = lds + L_WO + (wk >> 1) * 8192 + ((n >> 2) * 8 + (la >> 1) * 4 + (la & 1)) * 128 + (lane & 3) * 8;
#pragma unroll
        for (int t = 0; t < 2; ++t)
#pragma unroll
          for (int ks = 0; ks < 4; ++ks) {
            unsigned char* p = img + (ks >> 1) * 24576 + (ks & 1) * 4096 + (((wk & 1) * 2 + t) ^ (n >> 2)) * 32;
            const u32x2 lo = __builtin_bit_cast(u32x2, __builtin_amdgcn_ds_read_tr16_b64_v4i16((lds_s16x4)p));
            const u32x2 hi = __builtin_bit_cast(u32x2, __builtin_amdgcn_ds_read_tr16_b64_v4i16((lds_s16x4)(p + 256)));
            wof[t][ks] = u32x4{lo[0], lo[1], hi[0], hi[1]};
          }
      }
      u32x4 ob[4];
#pragma unroll 1
      for (unsigned spin = 0; spin < (SPIN_CAP << 4); ++spin) {
        asm volatile("" ::: "memory");
        const unsigned f = __hip_atomic_load((unsigned*)(lds + L_FLAG) + F_O, __ATOMIC_RELAXED, __HIP_MEMORY_SCOPE_WORKGROUP);
        asm volatile("" ::: "memory");
#pragma unroll
        for (int ks = 0; ks < 4; ++ks) ob[ks] = *(const u32x4*)(lds + L_OA + ks * 64 + la * 16);
        __builtin_amdgcn_sched_barrier(0);
        if (__builtin_amdgcn_readfirstlane(f) != 0u) break;
      }
      f32x4 d0 = {0.f, 0.f, 0.f, 0.f}, d1 = {0.f, 0.f, 0.f, 0.f};
#pragma unroll
      for (int ks = 0; ks < 4; ++ks) { d0 = mfma32(wof[0][ks], ob[ks], d0); d1 = mfma32(wof[1][ks], ob[ks], d1); }
      const f32x4 d = n ? d1 : d0;
      __builtin_amdgcn_raw_buffer_store_b128(
          u32x4{put_lo(d[0], tagA), put_lo(d[1], tagA >> 8), put_lo(d[2], tagA >> 16), put_lo(d[3], tagA >> 24)}, rws, oo, 0, AUX_SC1);
    }
  }
}

}

extern "C" void kernel_launch(void* const* d_in, const int* in_sizes, int n_in, void* d_out, int out_size, void* d_ws,
                              size_t ws_size, hipStream_t stream) {
  (void)in_sizes; (void)n_in; (void)out_size; (void)ws_size;
  fused_decode_kernel<<<dim3(NWG), dim3(NTHR), 0, stream>>>(
      (const unsigned short*)d_in[0], (const unsigned char*)d_in[1], (const unsigned char*)d_in[2],
      (const unsigned short*)d_in[3], (const unsigned short*)d_in[4], (const unsigned short*)d_in[6],
      (const unsigned short*)d_in[7], (const unsigned short*)d_in[8], (const unsigned short*)d_in[9],
      (unsigned short*)d_out, (unsigned char*)d_ws);
}
```

```cpp
#include <hip/hip_runtime.h>
#include <hip/hip_bf16.h>
#include <stdint.h>

namespace {

typedef unsigned u32x2 __attribute__((ext_vector_type(2)));
typedef unsigned u32x4 __attribute__((ext_vector_type(4)));
typedef float f32x2 __attribute__((ext_vector_type(2)));
typedef float f32x4 __attribute__((ext_vector_type(4)));
typedef short s16x4 __attribute__((ext_vector_type(4)));
typedef short s16x8 __attribute__((ext_vector_type(8)));
typedef __bf16 bf16x2 __attribute__((ext_vector_type(2)));
typedef __amdgpu_buffer_rsrc_t rsrc_t;

constexpr int DM = 576, SK = 32768, DH = 128, FF = 4608;
constexpr int NSPLIT = 8, NWG = 256, NWAVE = 8, NTHR = (NWAVE + 1) * 64;
constexpr int KEYS_WG = SK / NSPLIT;
constexpr int TILE_BYTES = 32 * DH * 2;
constexpr int NT = KEYS_WG / 32;
constexpr int SPAN = 2 * KEYS_WG * DH * 2;
constexpr int S = 2, NH = NWAVE - S;
constexpr int NPAIR = (NT / 2 - NH) / S;
constexpr int NITER = 2 * NPAIR - 1;
static_assert(2 * (NPAIR * S + NH) == NT, "every tile has one owner");

#define PFD 3
#define DLY0 8
#define DASY 0
#define DLYMIN 0
#define GLT 130
#define DLYMAX 12
#define GAIN 3
#define TAU16 1704
#define REF0 90
#define RSV0 180
#define RSVH 25
#define RSVB 70
#define RSVD 3
#define WOL 8
#define PSL 6
#define QPS 2
#define T1D 7
#define T2D 18

constexpr int AUX_NT = 2, AUX_SC1 = 16;
constexpr unsigned RSRC3 = 0x00020000u;
constexpr unsigned SPIN_CAP = 1u << 19;
constexpr int OOB = 0x7ff00000;

constexpr int WS_EPOCH = 0;
constexpr int WS_QX = 4096;
constexpr int WS_PX = WS_QX + 32 * 4096;
constexpr int WS_OX = WS_PX + NWG * 1024;
constexpr int WS_END = WS_OX + 2 * 18 * 4096;

constexpr int L_FLAG = 0;
constexpr int L_TAG = 64;
constexpr int L_XN = 80;
constexpr int L_QPART = L_XN + 1280;
constexpr int L_QS = L_QPART + 8 * 64 * 4;
constexpr int L_OA = L_QS + 512;
constexpr int L_YP = L_OA + 256;
constexpr int L_ML = L_YP + 4 * 8 * 4;
constexpr int L_PART = L_ML + NWAVE * 16;
constexpr int L_WO = L_PART + NWAVE * 1024;
constexpr int L_END = L_WO + 128 * 384;
static_assert(NPAIR * 512 <= 64 * 384, "a stream's scores fit its half of the wo image");
constexpr int F_QP = 0, F_Q = 1, F_WP = 2, F_END = 3, F_O = 4, F_Y = 5, F_ARR = 6, F_AR2 = 7, F_WO = 8, F_ML = 9, F_LAST = 10, F_P1 = 12,
              F_SH = 14;

constexpr float LOG2E = 1.4426950408889634f;
constexpr float SCALE = 0.08838834764831845f;
constexpr float LN_EPS = 1e-5f;

__device__ __forceinline__ float asf(unsigned u) { return __builtin_bit_cast(float, u); }
__device__ __forceinline__ unsigned asu(float f) { return __builtin_bit_cast(unsigned, f); }
__device__ __forceinline__ float bflo(unsigned u) { return asf(u << 16); }
__device__ __forceinline__ float bfhi(unsigned u) { return asf(u & 0xffff0000u); }
__device__ __forceinline__ unsigned pack_bf16(float lo, float hi) {
  f32x2 v = {lo, hi};
  return __builtin_bit_cast(unsigned, __builtin_convertvector(v, bf16x2));
}
__device__ __forceinline__ float rbf(float f) { return (float)(__bf16)f; }
template <int CTRL> __device__ __forceinline__ unsigned dppu(unsigned x) {
  return (unsigned)__builtin_amdgcn_mov_dpp((int)x, CTRL, 0xf, 0xf, true);
}
template <int CTRL> __device__ __forceinline__ float dppf(float x) { return asf(dppu<CTRL>(asu(x))); }
constexpr int XOR1 = 0xB1, XOR2 = 0x4E, XOR7 = 0x141, ROR4 = 0x124, ROR8 = 0x128;
constexpr int QB0 = 0x00, QB1 = 0x55, QB2 = 0xAA, QB3 = 0xFF;
__device__ __forceinline__ float rows_sum(float x) {
  auto s = __builtin_amdgcn_permlane16_swap(asu(x), asu(x), false, false);
  x = asf(s[0]) + asf(s[1]);
  auto t = __builtin_amdgcn_permlane32_swap(asu(x), asu(x), false, false);
  return asf(t[0]) + asf(t[1]);
}
__device__ __forceinline__ float rows_max(float x) {
  auto s = __builtin_amdgcn_permlane16_swap(asu(x), asu(x), false, false);
  x = fmaxf(asf(s[0]), asf(s[1]));
  auto t = __builtin_amdgcn_permlane32_swap(asu(x), asu(x), false, false);
  return fmaxf(asf(t[0]), asf(t[1]));
}
__device__ __forceinline__ float rows_sum_t(float v0, float v1, float v2, float v3) {
  const auto p0 = __builtin_amdgcn_permlane32_swap(asu(v0), asu(v2), false, false);
  const auto p1 = __builtin_amdgcn_permlane32_swap(asu(v1), asu(v3), false, false);
  const auto p2 = __builtin_amdgcn_permlane16_swap(asu(asf(p0[0]) + asf(p0[1])), asu(asf(p1[0]) + asf(p1[1])), false, false);
  return asf(p2[0]) + asf(p2[1]);
}
__device__ __forceinline__ float wave_sum(float x) {
  x += dppf<XOR1>(x); x += dppf<XOR2>(x); x += dppf<ROR4>(x); x += dppf<ROR8>(x);
  return rows_sum(x);
}
__device__ __forceinline__ float recip(float x) {
  const float r = __builtin_amdgcn_rcpf(x);
  return fmaf(fmaf(-x, r, 1.0f), r, r);
}
__device__ __forceinline__ rsrc_t mkrsrc(const void* p, int bytes) {
  return __builtin_amdgcn_make_buffer_rsrc(const_cast<void*>(p), 0, bytes, RSRC3);
}
__device__ __forceinline__ f32x4 mfma32(u32x4 a, u32x4 b, f32x4 c) {
  return __builtin_amdgcn_mfma_f32_16x16x32_bf16(__builtin_bit_cast(s16x8, a), __builtin_bit_cast(s16x8, b), c, 0, 0, 0);
}
__device__ __forceinline__ f32x4 mfma16(unsigned a0, unsigned a1, unsigned b0, unsigned b1, f32x4 c) {
  u32x2 a = {a0, a1}, b = {b0, b1};
  return __builtin_amdgcn_mfma_f32_16x16x16bf16_1k(__builtin_bit_cast(s16x4, a), __builtin_bit_cast(s16x4, b), c, 0, 0, 0);
}
__device__ __forceinline__ unsigned put_lo(float v, unsigned byte) { return (asu(v) & 0xffffff00u) | (byte & 0xffu); }
__device__ __forceinline__ float cut_lo(unsigned u) { return asf(u & 0xffffff00u); }
__device__ __forceinline__ unsigned now32() { return (unsigned)__builtin_amdgcn_s_memrealtime(); }
__device__ __forceinline__ void wait_until(unsigned t) {
  while ((int)(now32() - t) < 0) __builtin_amdgcn_s_sleep(1);
}
__device__ __forceinline__ int vzero() { int z = 0; asm("" : "+v"(z)); return z; }
__device__ __forceinline__ void flag_add(unsigned char* lds, int f, int lane) {
  asm volatile("" ::: "memory");
  if (lane == 0) __hip_atomic_fetch_add((unsigned*)(lds + L_FLAG) + f + vzero(), 1u, __ATOMIC_RELAXED, __HIP_MEMORY_SCOPE_WORKGROUP);
  asm volatile("" ::: "memory");
}
__device__ __forceinline__ unsigned flag_get(unsigned char* lds, int f) {
  return __builtin_amdgcn_readfirstlane(__hip_atomic_load((unsigned*)(lds + L_FLAG) + f, __ATOMIC_RELAXED, __HIP_MEMORY_SCOPE_WORKGROUP));
}
template <int SLEEP> __device__ __forceinline__ void flag_wait(unsigned char* lds, int f, unsigned n) {
#pragma unroll 1
  for (unsigned spin = 0; spin < (SPIN_CAP << 4); ++spin) {
    if (flag_get(lds, f) >= n) break;
    if (SLEEP) __builtin_amdgcn_s_sleep(SLEEP);
  }
  asm volatile("" ::: "memory");
}
__device__ __forceinline__ void ln_stats(const float (&v)[10], bool last_valid, float& mean_b, float& den_b) {
  float s = 0.f;
#pragma unroll
  for (int i = 0; i < 8; ++i) s += v[i];
  s += last_valid ? (v[8] + v[9]) : 0.f;
  const float mean = wave_sum(s) * (1.0f / DM);
  float q = 0.f;
#pragma unroll
  for (int i = 0; i < 8; ++i) { const float d = v[i] - mean; q = fmaf(d, d, q); }
  { const float d8 = v[8] - mean, d9 = v[9] - mean; q += last_valid ? fmaf(d8, d8, d9 * d9) : 0.f; }
  const float var = wave_sum(q) * (1.0f / DM);
  mean_b = rbf(mean);
  den_b = rbf(__builtin_amdgcn_sqrtf(rbf(rbf(var) + LN_EPS)));
}
__device__ __forceinline__ float ln_apply(float x, float mean_b, float den_b) {
  const float d = rbf(x - mean_b), r = __builtin_amdgcn_rcpf(den_b), q = d * r;
  return rbf(fmaf(fmaf(-q, den_b, d), r, q));
}

#define STAMP(i) do { } while (0)

__global__ void __launch_bounds__(NTHR, 3)
fused_decode_kernel(const unsigned short* __restrict__ x, const unsigned char* __restrict__ kc, const unsigned char* __restrict__ vc,
                    const unsigned short* __restrict__ wq, const unsigned short* __restrict__ wo,
                    const unsigned short* __restrict__ w1, const unsigned short* __restrict__ b1,
                    const unsigned short* __restrict__ w2, const unsigned short* __restrict__ b2,
                    unsigned short* __restrict__ out, unsigned char* ws) {
  __shared__ __attribute__((aligned(16))) unsigned char lds[L_END];
  const int lane = threadIdx.x & 63, wv = __builtin_amdgcn_readfirstlane(threadIdx.x >> 6), w = wv < 8 ? wv ^ 4 : wv;
  const unsigned tst = now32();
  const int g = blockIdx.x >> 3, s = (blockIdx.x + 3 * g) & 7, id = g * 8 + s, b = g >> 4, kvh = g & 15;
  STAMP(0);
  const rsrc_t rws = mkrsrc(ws, WS_END);
  const rsrc_t rx = mkrsrc(x + b * DM, DM * 2);
  const int qh = s >> 2, cb = (s + (kvh >> 2)) & 3;
  const int la = lane >> 4;
  const int red = kvh * 8 + s;

  u32x2 ep = {0u, 0u};
  if (w == NWAVE) {
    ep = __builtin_amdgcn_raw_buffer_load_b64(rws, WS_EPOCH + b * 256, 0, 0);
    if (lane < 16) *((unsigned*)(lds + L_FLAG) + lane) = 0u;
    asm volatile("s_waitcnt lgkmcnt(0)\n\ts_barrier" ::: "memory");
  }
  if (w >= NWAVE) {
    __builtin_amdgcn_s_setprio(3);
    const unsigned e0 = __builtin_amdgcn_readfirstlane(ep[0]);
    const unsigned e1 = ((e0 & 0x7f7f7f7fu) + 0x01010101u) ^ (e0 & 0x80808080u);
    const unsigned tagA = e1 ^ 0xb77a3d00u;
    const unsigned tagB = __builtin_amdgcn_readfirstlane(ep[1]) + 1u;
    if (lane == 0) *(unsigned*)(lds + L_TAG) = tagA;
    flag_wait<0>(lds, F_QP, 4);
    {
      f32x2 q2 = *(const f32x2*)(lds + L_QPART + (lane & 31) * 8);
#pragma unroll
      for (int ww = 1; ww < 8; ++ww) q2 += *(const f32x2*)(lds + L_QPART + ww * 256 + (lane & 31) * 8);
      __builtin_amdgcn_raw_buffer_store_b128(u32x4{tagA, asu(q2[0]), tagB, asu(q2[1])}, rws,
                                             lane < 32 ? WS_QX + g * 4096 + qh * 2048 + cb * 512 + lane * 16 : OOB, 0, AUX_SC1);
      STAMP(8);
    }
    __builtin_amdgcn_s_sleep(PSL);
    {
      float qa[4], qb[4];
      int of[4];
#pragma unroll
      for (int i = 0; i < 4; ++i) { qa[i] = 0.f; qb[i] = 0.f; of[i] = WS_QX + g * 4096 + i * 1024 + lane * 16; }
#pragma unroll 1
      for (unsigned spin = 0; spin < SPIN_CAP; ++spin) {
        asm volatile("" ::: "memory");
        u32x4 gr[4];
#pragma unroll
        for (int i = 0; i < 4; ++i) gr[i] = __builtin_amdgcn_raw_buffer_load_b128(rws, of[i], 0, AUX_SC1);
        bool done = true;
#pragma unroll
        for (int i = 0; i < 4; ++i) {
          const bool fresh = (gr[i][0] == tagA) & (gr[i][2] == tagB) & (of[i] != OOB);
          qa[i] = fresh ? asf(gr[i][1]) : qa[i]; qb[i] = fresh ? asf(gr[i][3]) : qb[i];
          of[i] = fresh ? OOB : of[i];
          done &= of[i] == OOB;
        }
        if (__all(done)) break;
        __builtin_amdgcn_s_sleep(QPS);
      }
      *(unsigned*)(lds + L_QS + lane * 4) = pack_bf16(qa[0] + qa[2], qb[0] + qb[2]);
      *(unsigned*)(lds + L_QS + 256 + lane * 4) = pack_bf16(qa[1] + qa[3], qb[1] + qb[3]);
      flag_add(lds, F_Q, lane);
    }
    const int fc = (lane & 1) * 4 + la;
    const unsigned xraw = __builtin_amdgcn_raw_buffer_load_b32(rx, red * 16 + (fc >> 1) * 4, 0, 0);
    const unsigned b2v = __builtin_amdgcn_raw_buffer_load_b16(mkrsrc(b2, 4), 0, 0, 0);
    if (red == 127) {
      wait_until(tst + 2000u);
#pragma unroll 1
      for (unsigned spin = 0; spin < SPIN_CAP; ++spin) {
        asm volatile("" ::: "memory");
        const u32x4 v0 = __builtin_amdgcn_raw_buffer_load_b128(rws, WS_QX + b * 65536 + lane * 512, 0, AUX_SC1);
        const u32x4 v1 = __builtin_amdgcn_raw_buffer_load_b128(rws, WS_QX + b * 65536 + 32768 + lane * 512, 0, AUX_SC1);
        if (__all((v0[0] == tagA) & (v0[2] == tagB) & (v1[0] == tagA) & (v1[2] == tagB))) break;
        __builtin_amdgcn_s_sleep(16);
      }
      __builtin_amdgcn_raw_buffer_store_b64(u32x2{e1, tagB}, rws, lane == 0 ? WS_EPOCH + b * 256 : OOB, 0, AUX_SC1);
    }
    float yv = 0.f, xfin = (fc & 1) ? bfhi(xraw) : bflo(xraw);
    const rsrc_t rout = mkrsrc(out + b * DM, DM * 2 + 2);
    int oout = (lane & 14) == 0 ? (red * 8 + fc) * 2 : OOB;
    if (red < 72) {
      flag_wait<16>(lds, F_Y, 4);
#pragma unroll
      for (int ww = 0; ww < 4; ++ww) yv += *(const float*)(lds + L_YP + ww * 32 + fc * 4);
      yv = rbf(rbf(yv) + bflo(b2v));
      __builtin_amdgcn_raw_buffer_store_b16((unsigned short)0, rout, oout, 0, 0);
    }
    int oa = (lane & 8) ? L_QPART + lane * 8 : L_OA + la * 64 + (lane & 7) * 8;
    asm volatile("" : "+v"(yv), "+v"(xfin), "+v"(oout), "+v"(oa));
    flag_wait<16>(lds, F_WP, 1);
    u32x4 ga[4];
#pragma unroll
    for (int i = 0; i < 4; ++i) ga[i] = u32x4{0u, 0u, 0u, 0u};
    int of1 = WS_PX + (g * 8 + (s & 4)) * 1024 + lane * 16;
    asm volatile("" : "+v"(of1), "+v"(ga[0]), "+v"(ga[1]), "+v"(ga[2]), "+v"(ga[3]));
    flag_wait<0>(lds, F_END, 1);
    STAMP(5);
    __builtin_amdgcn_s_sleep(T1D);
    {
      unsigned bad = 1u;
#pragma unroll 1
      for (unsigned spin = 0; spin < SPIN_CAP; ++spin) {
        asm volatile("" ::: "memory");
        if (bad != 0u) {
#pragma unroll
          for (int i = 0; i < 4; ++i) ga[i] = __builtin_amdgcn_raw_buffer_load_b128(rws, of1 + i * 1024, 0, AUX_SC1);
        }
        bad = 0u;
#pragma unroll
        for (int i = 0; i < 4; ++i) bad |= (ga[i][0] ^ tagA) | (ga[i][2] ^ (tagA >> 8));
        bad &= 0xffu;
        if (__all(bad == 0u)) break;
        bad |= dppu<XOR1>(bad); bad |= dppu<XOR2>(bad); bad |= dppu<XOR7>(bad);
        __builtin_amdgcn_s_sleep(1);
      }
      const unsigned hm = __builtin_amdgcn_perm(ga[0][3], ga[0][1], 0x0c0c0400u) << (16 * (lane & 1));
      const unsigned hl = __builtin_amdgcn_perm(ga[1][3], ga[1][1], 0x0c0c0400u) << (16 * (lane & 1));
      const float pm = asf(hm | dppu<XOR1>(hm)), pl = asf(hl | dppu<XOR1>(hl));
      const float M = rows_max(fmaxf(pm, dppf<ROR8>(pm)));
      const float w0 = __builtin_amdgcn_exp2f((pm - M) * LOG2E) * pl;
      const float inv = recip(rows_sum(w0 + dppf<ROR8>(w0)));
      float v[4];
#pragma unroll
      for (int r = 0; r < 4; ++r) {
        const float t = rows_sum_t(w0 * cut_lo(ga[0][r]), w0 * cut_lo(ga[1][r]), w0 * cut_lo(ga[2][r]), w0 * cut_lo(ga[3][r]));
        v[r] = (t + dppf<ROR8>(t)) * inv;
      }
      *(u32x2*)(lds + oa) = u32x2{pack_bf16(v[0], v[1]), pack_bf16(v[2], v[3])};
      flag_add(lds, F_O, lane);
    }
    if (red < 72) {
      __builtin_amdgcn_s_sleep(T2D);
      u32x4 gb = {0u, 0u, 0u, 0u};
      const int of2 = WS_OX + (b * 18 + (red >> 2)) * 4096 + (lane >> 1) * 128 + (red & 3) * 32 + (lane & 1) * 16;
      unsigned bad = 1u;
#pragma unroll 1
      for (unsigned spin = 0; spin < SPIN_CAP; ++spin) {
        asm volatile("" ::: "memory");
        if (bad != 0u) gb = __builtin_amdgcn_raw_buffer_load_b128(rws, of2, 0, AUX_SC1);
        bad = (__builtin_amdgcn_perm(gb[1], gb[0], 0x0c0c0400u) | __builtin_amdgcn_perm(gb[3], gb[2], 0x04000c0cu)) ^ tagA;
        if (__all(bad == 0u)) break;
        bad |= dppu<XOR1>(bad);
        __builtin_amdgcn_s_sleep(1);
      }
      STAMP(13);
      float t = rows_sum_t(cut_lo(gb[0]), cut_lo(gb[1]), cut_lo(gb[2]), cut_lo(gb[3]));
      t += dppf<XOR2>(t); t += dppf<ROR4>(t); t += dppf<ROR8>(t);
      __builtin_amdgcn_raw_buffer_store_b16((unsigned short)pack_bf16(rbf(xfin + rbf(t)) + yv, 0.f), rout, oout, 0, 0);
    }
  } else {
    const rsrc_t rk = mkrsrc(kc + (size_t)(id >> 1) * SPAN, SPAN);
    const rsrc_t rv = mkrsrc(vc + (size_t)(id >> 1) * SPAN, SPAN);
    auto poff = [&](int d, int h) {
      return ((d & 1) | (h << 1) | ((d & 2) << 1) | ((id & 1) << 3) | ((d >> 2) << 4)) * TILE_BYTES;
    };
    const int voffK = ((lane & 15) >> 2) * 256 + la * 64 + (lane & 3) * 16;
    const int voffV = lane * 16;
    u32x4 bA[8], bB[8];
    auto request = [&](u32x4 (&x)[8], rsrc_t r, int voff, int so) {
      asm volatile("" : "+v"(voff));
#pragma unroll
      for (int u = 0; u < 8; ++u) x[u] = __builtin_amdgcn_raw_buffer_load_b128(r, voff + (u & 3) * 1024, so + (u >> 2) * 4096, AUX_NT);
      __builtin_amdgcn_sched_barrier(0);
    };
    if (w >= 4) {
      const int rg = (w - 4) * 8 + (lane >> 3), c8 = lane & 7;
      unsigned xs[5];
      u32x4 wqv[9];
#pragma unroll
      for (int i = 0; i < 5; ++i) xs[i] = __builtin_amdgcn_raw_buffer_load_b32(rx, (lane + 64 * i) * 4, 0, 0);
      __builtin_amdgcn_sched_barrier(0);
      const rsrc_t rq = mkrsrc(wq + qh * 288 * 4096 + kvh * 256 + cb * 64, (287 * 4096 + 64) * 2);
#pragma unroll
      for (int i = 0; i < 9; ++i) wqv[i] = __builtin_amdgcn_raw_buffer_load_b128(rq, (rg + 32 * i) * 8192 + c8 * 16, 0, AUX_NT);
      __builtin_amdgcn_sched_barrier(0);
      __builtin_amdgcn_s_barrier();
      __builtin_amdgcn_sched_barrier(0);
      float xv[10], mean1, den1;
#pragma unroll
      for (int i = 0; i < 5; ++i) { xv[2 * i] = bflo(xs[i]); xv[2 * i + 1] = bfhi(xs[i]); }
      ln_stats(xv, lane < 32, mean1, den1);
#pragma unroll
      for (int i = 0; i < 5; ++i)
        *(unsigned*)(lds + L_XN + (lane + 64 * i) * 4) = pack_bf16(ln_apply(xv[2 * i], mean1, den1), ln_apply(xv[2 * i + 1], mean1, den1));
      asm volatile("" ::: "memory");
      STAMP(9);
      f32x4 qa = {0.f, 0.f, 0.f, 0.f};
#pragma unroll
      for (int i = 0; i < 9; ++i) {
        const unsigned t = (unsigned)*(const unsigned short*)(lds + L_XN + (qh * 288 + rg + 32 * i) * 2) << (16 * (lane & 1));
        const int jp = c8 >> 1;
        qa = mfma32(wqv[i], u32x4{jp == 0 ? t : 0u, jp == 1 ? t : 0u, jp == 2 ? t : 0u, jp == 3 ? t : 0u}, qa);
      }
      if ((lane >> 5) == ((lane >> 3) & 1)) {
        float* qp = (float*)(lds + L_QPART) + ((w - 4) * 2 + (lane >> 5)) * 64 + (la & 1) * 32 + c8;
#pragma unroll
        for (int r = 0; r < 4; ++r) qp[r * 8] = qa[r];
      }
      flag_add(lds, F_QP, lane);
      STAMP(1);
    }
    if (w < 4) asm volatile("s_barrier" ::: "memory");
    if (w >= PFD) flag_wait<1>(lds, F_ARR, w - PFD + 1);
    __builtin_amdgcn_sched_barrier(0);
    STAMP(14);
    request(bA, rk, voffK, poff(w, 0));
    request(bB, rk, voffK, poff(w, 1));
    asm volatile("s_waitcnt vmcnt(0)" ::: "memory");
    flag_add(lds, F_ARR, lane);
    STAMP(15);
    flag_wait<1>(lds, F_Q, 1);
    if (w < S) STAMP(2);

    const int c = lane & 3, hb = (lane >> 2) & 1, tb = (lane >> 3) & 1;
    const unsigned tagA = __builtin_amdgcn_readfirstlane(*(const unsigned*)(lds + L_TAG));
    float m;
    f32x4 acc[2][2];
#pragma unroll
    for (int i = 0; i < 2; ++i)
#pragma unroll
      for (int j = 0; j < 2; ++j) acc[i][j] = f32x4{0.f, 0.f, 0.f, 0.f};
    const u32x4 qf = *(const u32x4*)(lds + L_QS + (hb * 128 + la * 32 + c * 8) * 2);
    const u32x4 zero4 = {0u, 0u, 0u, 0u};
    const u32x4 q0 = tb ? zero4 : qf, q1 = tb ? qf : zero4;
    const unsigned mask0 = c == 0 ? 0x0000ffffu : c == 1 ? 0xffff0000u : 0u;
    const unsigned mask1 = c == 2 ? 0x0000ffffu : c == 3 ? 0xffff0000u : 0u;
    auto qk = [&](const u32x4 (&x)[8], f32x4 (&sc)[4]) {
#pragma unroll
      for (int i = 0; i < 4; ++i) sc[i] = mfma32(x[i], q0, f32x4{0.f, 0.f, 0.f, 0.f});
#pragma unroll
      for (int i = 0; i < 4; ++i) sc[i] = mfma32(x[4 + i], q1, sc[i]);
    };
    auto score = [&](const f32x4 (&sc)[4]) {
      float t4[4];
#pragma unroll
      for (int i = 0; i < 4; ++i) t4[i] = (dppf<QB0>(sc[i][0]) + dppf<QB1>(sc[i][1])) + (dppf<QB2>(sc[i][2]) + dppf<QB3>(sc[i][3]));
      const float raw = (c & 2) ? ((c & 1) ? t4[3] : t4[2]) : ((c & 1) ? t4[1] : t4[0]);
      float sv = rbf(rbf(raw) * SCALE);
      asm volatile("" : "+v"(sv));
      return sv;
    };
    auto head_max = [&](float v) {
      v = fmaxf(v, dppf<XOR1>(v));
      v = fmaxf(v, dppf<XOR2>(v));
      v = fmaxf(v, dppf<ROR8>(v));
      return rows_max(v);
    };
    auto pv = [&](const u32x4 (&x)[8], float sv) {
      const float p = __builtin_amdgcn_exp2f((sv - m) * LOG2E);
      const unsigned pk = pack_bf16(p, p);
      unsigned pb0[4], pb1[4];
      pb0[0] = dppu<QB0>(pk) & mask0; pb1[0] = dppu<QB0>(pk) & mask1;
      pb0[1] = dppu<QB1>(pk) & mask0; pb1[1] = dppu<QB1>(pk) & mask1;
      pb0[2] = dppu<QB2>(pk) & mask0; pb1[2] = dppu<QB2>(pk) & mask1;
      pb0[3] = dppu<QB3>(pk) & mask0; pb1[3] = dppu<QB3>(pk) & mask1;
#pragma unroll
      for (int u = 0; u < 8; ++u) {
        acc[u >> 2][0] = mfma16(x[u][0], x[u][1], pb0[u & 3], pb1[u & 3], acc[u >> 2][0]);
        acc[u >> 2][1] = mfma16(x[u][2], x[u][3], pb0[u & 3], pb1[u & 3], acc[u >> 2][1]);
      }
    };
    auto put_ml = [&](int k, float mk, float lk) {
      lk += dppf<XOR1>(lk);
      lk += dppf<XOR2>(lk);
      lk += dppf<ROR8>(lk);
      lk = rows_sum(lk);
      if ((lane & ~4) == 0) *(f32x2*)(lds + L_ML + (k * 2 + hb) * 8) = f32x2{mk, lk};
      flag_add(lds, F_ML, lane);
    };
    auto hold = [&](int dly) {
      __builtin_amdgcn_sched_barrier(0);
      for (int i = 0; i < dly; ++i) __builtin_amdgcn_s_sleep(1);
      __builtin_amdgcn_sched_barrier(0);
    };
    const int hw = w - S, nwk = (s & 3) == 3 ? 6 : 4;
    f32x2 sl;
    if (w < S) {
      __builtin_amdgcn_s_setprio(2);
      int shift = 0, lprev = -0x40000000;
      auto pace = [&](int it) {
        int lead = (int)(tst + REF0 + (unsigned)((NWAVE / 2 + it) * TAU16 >> 4) - now32()) + shift;
        if (GLT && lprev - lead > GLT) {
          shift += lprev - lead;
          lead = lprev;
          __hip_atomic_store((unsigned*)(lds + L_FLAG) + F_SH + w, (unsigned)shift, __ATOMIC_RELAXED, __HIP_MEMORY_SCOPE_WORKGROUP);
        }
        lprev = it ? lead : -0x40000000;
        return min(max(DLY0 + ((lead * GAIN + 128) >> 8), DLYMIN), DLYMAX);
      };
      unsigned char* sco = lds + L_WO + w * (64 * 384) + lane * 8;
      flag_wait<1>(lds, F_ARR, NWAVE + w - PFD + 1);
      float mx = -INFINITY;
      for (int j = 0; j < NPAIR; ++j) {
        const int dly = pace(j) + DASY;
        const bool k = j + 1 < NPAIR;
        const rsrc_t r = k ? rk : rv;
        const int vo = k ? voffK : voffV, d = NWAVE + S * (k ? j : j - 1) + w;
        f32x4 sc[4];
        qk(bA, sc);
        hold(dly);
        request(bA, r, vo, poff(d, 0));
        const float s0 = score(sc);
        qk(bB, sc);
        hold(dly);
        request(bB, r, vo, poff(d, 1));
        const float s1 = score(sc);
        mx = fmaxf(mx, fmaxf(s0, s1));
        *(f32x2*)(sco + j * 512) = f32x2{s0, s1};
      }
      m = head_max(mx);
      if ((lane & ~4) == 0) *(float*)(lds + L_ML + (w * 2 + hb) * 8) = m;
      flag_add(lds, F_P1 + w, lane);
      for (int j = 0; j + 1 < NPAIR; ++j) {
        const f32x2 sv = *(const f32x2*)(sco + (NPAIR - 1 - j) * 512);
        const int dly = pace(NPAIR + j) - DASY;
        const int d = j + 2 < NPAIR ? NWAVE + S * (NPAIR - 3 - j) + w : w;
        pv(bA, sv[0]);
        hold(dly);
        request(bA, rv, voffV, poff(d, 0));
        pv(bB, sv[1]);
        hold(dly);
        request(bB, rv, voffV, poff(d, 1));
      }
      flag_add(lds, F_LAST + w, lane);
      sl = *(const f32x2*)sco;
    }
    asm volatile("" ::: "memory");
    if (w >= S) {
      {
        f32x4 sc[4];
        qk(bA, sc);
        sl[0] = score(sc);
        qk(bB, sc);
        sl[1] = score(sc);
        m = head_max(fmaxf(sl[0], sl[1]));
        put_ml(w, m, __builtin_amdgcn_exp2f((sl[0] - m) * LOG2E) + __builtin_amdgcn_exp2f((sl[1] - m) * LOG2E));
      }
      STAMP(12);
      if (hw < S) {
        flag_wait<16>(lds, F_P1 + hw, 1);
        const float ms = *(const float*)(lds + L_ML + (hw * 2 + hb) * 8);
        float ls = 0.f;
#pragma unroll 1
        for (int j = 0; j < NPAIR; ++j) {
          const f32x2 sv = *(const f32x2*)(lds + L_WO + hw * (64 * 384) + lane * 8 + j * 512);
          ls += __builtin_amdgcn_exp2f((sv[0] - ms) * LOG2E) + __builtin_amdgcn_exp2f((sv[1] - ms) * LOG2E);
        }
        put_ml(hw, ms, ls);
      }
      if (w >= 4 && red < 72) {
        const int ht = (w - 4) * 64 + lane;
        unsigned a1[9], ab[9], a2[9];
        const rsrc_t r1 = mkrsrc(w1, FF * 2), rb = mkrsrc(b1, FF * 2), r2 = mkrsrc(w2, FF * 2);
#pragma unroll
        for (int i = 0; i < 9; ++i) {
          a1[i] = __builtin_amdgcn_raw_buffer_load_b32(r1, (ht + 256 * i) * 4, 0, 0);
          ab[i] = __builtin_amdgcn_raw_buffer_load_b32(rb, (ht + 256 * i) * 4, 0, 0);
          a2[i] = __builtin_amdgcn_raw_buffer_load_b32(r2, (ht + 256 * i) * 4, 0, 0);
        }
        __builtin_amdgcn_sched_barrier(0);
        float xn[10];
#pragma unroll
        for (int i = 0; i < 5; ++i) { const unsigned v = *(const unsigned*)(lds + L_XN + (lane + 64 * i) * 4); xn[2 * i] = bflo(v); xn[2 * i + 1] = bfhi(v); }
        float mean2, den2;
        ln_stats(xn, lane < 32, mean2, den2);
        constexpr float GC0 = -2.0f * 0.7978845608028654f * LOG2E, GC1 = GC0 * 0.044715f;
#pragma unroll 1
        for (int j = 0; j < 8; ++j) {
          const float hv = ln_apply(bflo(*(const unsigned short*)(lds + L_XN + (red * 8 + j) * 2)), mean2, den2);
          float y = 0.f;
#pragma unroll
          for (int i = 0; i < 9; ++i) {
            asm volatile("" : "+v"(a1[i]), "+v"(ab[i]), "+v"(a2[i]));
            const unsigned pr = pack_bf16(hv * bflo(a1[i]), hv * bfhi(a1[i]));
            const unsigned uu = pack_bf16(bflo(pr) + bflo(ab[i]), bfhi(pr) + bfhi(ab[i]));
            const float u0 = bflo(uu), u1 = bfhi(uu);
            const float e0 = __builtin_amdgcn_exp2f(u0 * fmaf(u0 * u0, GC1, GC0));
            const float e1 = __builtin_amdgcn_exp2f(u1 * fmaf(u1 * u1, GC1, GC0));
            const unsigned gg = pack_bf16(u0 * __builtin_amdgcn_rcpf(1.0f + e0), u1 * __builtin_amdgcn_rcpf(1.0f + e1));
            y = fmaf(bflo(gg), bflo(a2[i]), y);
            y = fmaf(bfhi(gg), bfhi(a2[i]), y);
          }
          y = wave_sum(y);
          if (lane == 0) *(float*)(lds + L_YP + (w - 4) * 32 + j * 4) = y;
        }
        flag_add(lds, F_Y, lane);
      }
      if (hw < RSVD) {
        const unsigned due = tst + REF0 + RSV0 + (unsigned)((NWAVE + 2 * NITER + hw) * TAU16 >> 5);
        bool seen = false;
        unsigned tseen = 0;
#pragma unroll 1
        for (unsigned spin = 0; spin < (SPIN_CAP << 2); ++spin) {
          const unsigned now = now32();
          const int late = (int)(now - due) - (GLT ? (int)flag_get(lds, F_SH + (hw & 1)) : 0);
          if (!seen && flag_get(lds, F_LAST + (hw & 1)) != 0u) { seen = true; tseen = now; }
          if (late >= 0 || (seen && (late >= -RSVH || (int)(now - tseen) >= RSVB))) break;
          __builtin_amdgcn_s_sleep(2);
        }
      } else {
        flag_wait<2>(lds, F_AR2, hw - RSVD + 1);
      }
      __builtin_amdgcn_s_setprio(2);
      __builtin_amdgcn_sched_barrier(0);
      request(bA, rv, voffV, poff(w, 0));
      request(bB, rv, voffV, poff(w, 1));
    }
    int po = WS_PX + ((g * 8 + hb * 4 + la) * 8 + s) * 128 + (tb * 4 + c) * 16;
    float fw;
    unsigned side;
    {
      flag_wait<1>(lds, F_ML, NWAVE);
      f32x2 ml[NWAVE];
#pragma unroll
      for (int k = 0; k < NWAVE; ++k) ml[k] = *(const f32x2*)(lds + L_ML + (k * 2 + hb) * 8);
      float mx = ml[0][0], L = 0.f;
#pragma unroll
      for (int k = 1; k < NWAVE; ++k) mx = fmaxf(mx, ml[k][0]);
#pragma unroll
      for (int k = 0; k < NWAVE; ++k) L = fmaf(__builtin_amdgcn_exp2f((ml[k][0] - mx) * LOG2E), ml[k][1], L);
      fw = __builtin_amdgcn_exp2f((m - mx) * LOG2E) * recip(L);
      side = asu((la & 1) ? L : mx) >> (16 * (c & 1));
    }
    asm volatile("" : "+v"(po), "+v"(fw), "+v"(side));
    if (w >= S && hw < NH - RSVD) {
      asm volatile("s_waitcnt vmcnt(0)" ::: "memory");
      flag_add(lds, F_AR2, lane);
    }
    pv(bA, sl[0]);
    pv(bB, sl[1]);
    STAMP(3);
    {
      f32x4 res;
#pragma unroll
      for (int r = 0; r < 4; ++r) {
        const float own = tb ? acc[1][1][r] : acc[0][0][r], snd = tb ? acc[1][0][r] : acc[0][1][r];
        res[r] = (own + dppf<ROR8>(snd)) * fw;
      }
      *(f32x4*)(lds + L_PART + w * 1024 + lane * 16) = res;
      asm volatile("" ::: "memory");
      unsigned old = 0;
      if (lane == 0) old = __hip_atomic_fetch_add((unsigned*)(lds + L_FLAG) + F_WP + vzero(), 1u, __ATOMIC_RELAXED, __HIP_MEMORY_SCOPE_WORKGROUP);
      asm volatile("" ::: "memory");
      f32x4 pp[NWAVE];
#pragma unroll
      for (int k = 0; k < NWAVE; ++k) pp[k] = *(const f32x4*)(lds + L_PART + k * 1024 + lane * 16);
      __builtin_amdgcn_sched_barrier(0);
      if (__builtin_amdgcn_readfirstlane(old) == NWAVE - 1) {
        const f32x4 sum = ((pp[0] + pp[1]) + (pp[2] + pp[3])) + ((pp[4] + pp[5]) + (pp[6] + pp[7]));
        __builtin_amdgcn_raw_buffer_store_b128(
            u32x4{put_lo(sum[0], tagA), put_lo(sum[1], side), put_lo(sum[2], tagA >> 8), put_lo(sum[3], side >> 8)}, rws, po, 0, AUX_SC1);
        flag_add(lds, F_END, lane);
        STAMP(10);
      }
    }
    if (w < S) {
      flag_wait<1>(lds, F_WP, WOL);
      const rsrc_t ro = mkrsrc(wo + (size_t)(kvh * 256 + qh * 128 + w * 64) * DM, 64 * DM * 2);
      u32x4 wt[24];
#pragma unroll
      for (int i = 0; i < 24; ++i)
        wt[i] = __builtin_amdgcn_raw_buffer_load_b128(
            ro, 2 * (i % 3) < nwk ? ((i / 3) * 8 + (lane >> 3)) * (DM * 2) + (s & 3) * 256 + (i % 3) * 128 + (lane & 7) * 16 : OOB, 0, AUX_NT);
#pragma unroll
      for (int i = 0; i < 24; ++i)
        *(u32x4*)(lds + L_WO + w * 24576 + (i % 3) * 8192 + ((i / 3) * 8 + ((lane >> 3) & 4) + ((lane >> 2) & 2) + ((lane >> 4) & 1)) * 128 +
                  (((lane & 7) * 16) ^ (((i / 3) & 3) * 32))) = wt[i];
      flag_add(lds, F_WO, lane);
      STAMP(4);
    } else if (hw < nwk) {
      const int n = lane & 15;
      int oo = n < 2 ? WS_OX + (b * 18 + (s & 3) * 4 + hw) * 4096 + (kvh * 2 + qh) * 128 + n * 64 + la * 16 : OOB;
      asm volatile("" : "+v"(oo));
      flag_wait<1>(lds, F_WO, S);
      u32x4 wof[2][4];
      {
        typedef __attribute__((address_space(3))) s16x4* lds_s16x4;
        unsigned char* img = lds + L_WO + (hw >> 1) * 8192 + ((n >> 2) * 8 + (la >> 1) * 4 + (la & 1)) * 128 + (lane & 3) * 8;
#pragma unroll
        for (int t = 0; t < 2; ++t)
#pragma unroll
          for (int ks = 0; ks < 4; ++ks) {
            unsigned char* p = img + (ks >> 1) * 24576 + (ks & 1) * 4096 + (((hw & 1) * 2 + t) ^ (n >> 2)) * 32;
            const u32x2 lo = __builtin_bit_cast(u32x2, __builtin_amdgcn_ds_read_tr16_b64_v4i16((lds_s16x4)p));
            const u32x2 hi = __builtin_bit_cast(u32x2, __builtin_amdgcn_ds_read_tr16_b64_v4i16((lds_s16x4)(p + 256)));
            wof[t][ks] = u32x4{lo[0], lo[1], hi[0], hi[1]};
          }
      }
      u32x4 ob[4];
#pragma unroll 1
      for (unsigned spin = 0; spin < (SPIN_CAP << 4); ++spin) {
        asm volatile("" ::: "memory");
        const unsigned f = __hip_atomic_load((unsigned*)(lds + L_FLAG) + F_O, __ATOMIC_RELAXED, __HIP_MEMORY_SCOPE_WORKGROUP);
        asm volatile("" ::: "memory");
#pragma unroll
        for (int ks = 0; ks < 4; ++ks) ob[ks] = *(const u32x4*)(lds + L_OA + ks * 64 + la * 16);
        __builtin_amdgcn_sched_barrier(0);
        if (__builtin_amdgcn_readfirstlane(f) != 0u) break;
      }
      STAMP(6);
      f32x4 d0 = {0.f, 0.f, 0.f, 0.f}, d1 = {0.f, 0.f, 0.f, 0.f};
#pragma unroll
      for (int ks = 0; ks < 4; ++ks) { d0 = mfma32(wof[0][ks], ob[ks], d0); d1 = mfma32(wof[1][ks], ob[ks], d1); }
      const f32x4 d = n ? d1 : d0;
      __builtin_amdgcn_raw_buffer_store_b128(
          u32x4{put_lo(d[0], tagA), put_lo(d[1], tagA >> 8), put_lo(d[2], tagA >> 16), put_lo(d[3], tagA >> 24)}, rws, oo, 0, AUX_SC1);
      STAMP(11);
    }
  }
}

}

extern "C" void kernel_launch(void* const* d_in, const int* in_sizes, int n_in, void* d_out, int out_size, void* d_ws,
                              size_t ws_size, hipStream_t stream) {
  (void)in_sizes; (void)n_in; (void)out_size; (void)ws_size;
  fused_decode_kernel<<<dim3(NWG), dim3(NTHR), 0, stream>>>(
      (const unsigned short*)d_in[0], (const unsigned char*)d_in[1], (const unsigned char*)d_in[2],
      (const unsigned short*)d_in[3], (const unsigned short*)d_in[4], (const unsigned short*)d_in[6],
      (const unsigned short*)d_in[7], (const unsigned short*)d_in[8], (const unsigned short*)d_in[9],
      (unsigned short*)d_out, (unsigned char*)d_ws);
}
```

```cpp
#include <hip/hip_runtime.h>
#include <hip/hip_bf16.h>
#include <stdint.h>

namespace {

typedef unsigned u32x2 __attribute__((ext_vector_type(2)));
typedef unsigned u32x4 __attribute__((ext_vector_type(4)));
typedef float f32x2 __attribute__((ext_vector_type(2)));
typedef float f32x4 __attribute__((ext_vector_type(4)));
typedef short s16x4 __attribute__((ext_vector_type(4)));
typedef short s16x8 __attribute__((ext_vector_type(8)));
typedef __bf16 bf16x2 __attribute__((ext_vector_type(2)));
typedef __amdgpu_buffer_rsrc_t rsrc_t;

constexpr int DM = 576, SK = 32768, DH = 128, FF = 4608;
constexpr int NSPLIT = 8, NWG = 256, NWAVE = 8, NTHR = (NWAVE + 1) * 64;
constexpr int KEYS_WG = SK / NSPLIT;
constexpr int TILE_BYTES = 32 * DH * 2;
constexpr int NT = KEYS_WG / 32;
constexpr int SPAN = 2 * KEYS_WG * DH * 2;
constexpr int S = 4, NH = NWAVE - S;
constexpr int NPAIR = (NT / 2 - NH) / S;
static_assert(2 * (NPAIR * S + NH) == NT && NH == S, "every tile has one owner, every stream one helper");

#define SCA 160
#define SCT 427
#define GAP 25
#define CBETA 6
#define GLT 125
#define GLQ 210
#define PFD 3
#define PSL 6
#define QPS 2
#define T1D 17
#define T2D 22
constexpr int PGAP = 2 * NWAVE + 2 * S * NPAIR, PEND = 2 * NT - 2 * NH;

constexpr int AUX_NT = 2, AUX_SC1 = 16;
constexpr unsigned RSRC3 = 0x00020000u;
constexpr unsigned SPIN_CAP = 1u << 19;
constexpr int OOB = 0x7ff00000;

constexpr int WS_EPOCH = 0;
constexpr int WS_QX = 4096;
constexpr int WS_PX = WS_QX + 32 * 4096;
constexpr int WS_OX = WS_PX + NWG * 1024;
constexpr int WS_END = WS_OX + 2 * 18 * 4096;

constexpr int L_FLAG = 0;
constexpr int L_TAG = 64;
constexpr int L_XN = 80;
constexpr int L_QPART = L_XN + 1280;
constexpr int L_QS = L_QPART + 8 * 64 * 4;
constexpr int L_OA = L_QS + 512;
constexpr int L_YP = L_OA + 256;
constexpr int L_ML = L_YP + 4 * 8 * 4;
constexpr int L_PART = L_ML + NWAVE * 16;
constexpr int L_WO = L_PART + NWAVE * 1024;
constexpr int L_SC = L_WO + 128 * 384;
constexpr int L_END = L_SC + S * NPAIR * 512;
constexpr int F_QP = 0, F_Q = 1, F_WP = 2, F_END = 3, F_O = 4, F_Y = 5, F_ARR = 6, F_WO = 8, F_ML = 9, F_SH = 10, F_T = 11, F_P1 = 12;

constexpr float LOG2E = 1.4426950408889634f;
constexpr float SCALE = 0.08838834764831845f;
constexpr float LN_EPS = 1e-5f;

__device__ __forceinline__ float asf(unsigned u) { return __builtin_bit_cast(float, u); }
__device__ __forceinline__ unsigned asu(float f) { return __builtin_bit_cast(unsigned, f); }
__device__ __forceinline__ float bflo(unsigned u) { return asf(u << 16); }
__device__ __forceinline__ float bfhi(unsigned u) { return asf(u & 0xffff0000u); }
__device__ __forceinline__ unsigned pack_bf16(float lo, float hi) {
  f32x2 v = {lo, hi};
  return __builtin_bit_cast(unsigned, __builtin_convertvector(v, bf16x2));
}
__device__ __forceinline__ float rbf(float f) { return (float)(__bf16)f; }
template <int CTRL> __device__ __forceinline__ unsigned dppu(unsigned x) {
  return (unsigned)__builtin_amdgcn_mov_dpp((int)x, CTRL, 0xf, 0xf, true);
}
template <int CTRL> __device__ __forceinline__ float dppf(float x) { return asf(dppu<CTRL>(asu(x))); }
constexpr int XOR1 = 0xB1, XOR2 = 0x4E, XOR7 = 0x141, ROR4 = 0x124, ROR8 = 0x128;
constexpr int QB0 = 0x00, QB1 = 0x55, QB2 = 0xAA, QB3 = 0xFF;
__device__ __forceinline__ float rows_sum(float x) {
  auto s = __builtin_amdgcn_permlane16_swap(asu(x), asu(x), false, false);
  x = asf(s[0]) + asf(s[1]);
  auto t = __builtin_amdgcn_permlane32_swap(asu(x), asu(x), false, false);
  return asf(t[0]) + asf(t[1]);
}
__device__ __forceinline__ float rows_max(float x) {
  auto s = __builtin_amdgcn_permlane16_swap(asu(x), asu(x), false, false);
  x = fmaxf(asf(s[0]), asf(s[1]));
  auto t = __builtin_amdgcn_permlane32_swap(asu(x), asu(x), false, false);
  return fmaxf(asf(t[0]), asf(t[1]));
}
__device__ __forceinline__ float rows_sum_t(float v0, float v1, float v2, float v3) {
  const auto p0 = __builtin_amdgcn_permlane32_swap(asu(v0), asu(v2), false, false);
  const auto p1 = __builtin_amdgcn_permlane32_swap(asu(v1), asu(v3), false, false);
  const auto p2 = __builtin_amdgcn_permlane16_swap(asu(asf(p0[0]) + asf(p0[1])), asu(asf(p1[0]) + asf(p1[1])), false, false);
  return asf(p2[0]) + asf(p2[1]);
}
__device__ __forceinline__ float wave_sum(float x) {
  x += dppf<XOR1>(x); x += dppf<XOR2>(x); x += dppf<ROR4>(x); x += dppf<ROR8>(x);
  return rows_sum(x);
}
__device__ __forceinline__ float recip(float x) {
  const float r = __builtin_amdgcn_rcpf(x);
  return fmaf(fmaf(-x, r, 1.0f), r, r);
}
__device__ __forceinline__ rsrc_t mkrsrc(const void* p, int bytes) {
  return __builtin_amdgcn_make_buffer_rsrc(const_cast<void*>(p), 0, bytes, RSRC3);
}
__device__ __forceinline__ f32x4 mfma32(u32x4 a, u32x4 b, f32x4 c) {
  return __builtin_amdgcn_mfma_f32_16x16x32_bf16(__builtin_bit_cast(s16x8, a), __builtin_bit_cast(s16x8, b), c, 0, 0, 0);
}
__device__ __forceinline__ f32x4 mfma16(unsigned a0, unsigned a1, unsigned b0, unsigned b1, f32x4 c) {
  u32x2 a = {a0, a1}, b = {b0, b1};
  return __builtin_amdgcn_mfma_f32_16x16x16bf16_1k(__builtin_bit_cast(s16x4, a), __builtin_bit_cast(s16x4, b), c, 0, 0, 0);
}
__device__ __forceinline__ unsigned put_lo(float v, unsigned byte) { return (asu(v) & 0xffffff00u) | (byte & 0xffu); }
__device__ __forceinline__ float cut_lo(unsigned u) { return asf(u & 0xffffff00u); }
__device__ __forceinline__ unsigned now32() { return (unsigned)__builtin_amdgcn_s_memrealtime(); }
__device__ __forceinline__ void wait_until(unsigned t) {
  while ((int)(now32() - t) < 0) __builtin_amdgcn_s_sleep(1);
}
__device__ __forceinline__ int vzero() { int z = 0; asm("" : "+v"(z)); return z; }
__device__ __forceinline__ void flag_add(unsigned char* lds, int f, int lane) {
  asm volatile("" ::: "memory");
  if (lane == 0) __hip_atomic_fetch_add((unsigned*)(lds + L_FLAG) + f + vzero(), 1u, __ATOMIC_RELAXED, __HIP_MEMORY_SCOPE_WORKGROUP);
  asm volatile("" ::: "memory");
}
__device__ __forceinline__ unsigned flag_get(unsigned char* lds, int f) {
  return __builtin_amdgcn_readfirstlane(__hip_atomic_load((unsigned*)(lds + L_FLAG) + f, __ATOMIC_RELAXED, __HIP_MEMORY_SCOPE_WORKGROUP));
}
template <int SLEEP> __device__ __forceinline__ void flag_wait(unsigned char* lds, int f, unsigned n) {
#pragma unroll 1
  for (unsigned spin = 0; spin < (SPIN_CAP << 4); ++spin) {
    if (flag_get(lds, f) >= n) break;
    if (SLEEP) __builtin_amdgcn_s_sleep(SLEEP);
  }
  asm volatile("" ::: "memory");
}
__device__ __forceinline__ void ln_stats(const float (&v)[10], bool last_valid, float& mean_b, float& den_b) {
  float s = 0.f;
#pragma unroll
  for (int i = 0; i < 8; ++i) s += v[i];
  s += last_valid ? (v[8] + v[9]) : 0.f;
  const float mean = wave_sum(s) * (1.0f / DM);
  float q = 0.f;
#pragma unroll
  for (int i = 0; i < 8; ++i) { const float d = v[i] - mean; q = fmaf(d, d, q); }
  { const float d8 = v[8] - mean, d9 = v[9] - mean; q += last_valid ? fmaf(d8, d8, d9 * d9) : 0.f; }
  const float var = wave_sum(q) * (1.0f / DM);
  mean_b = rbf(mean);
  den_b = rbf(__builtin_amdgcn_sqrtf(rbf(rbf(var) + LN_EPS)));
}
__device__ __forceinline__ float ln_apply(float x, float mean_b, float den_b) {
  const float d = rbf(x - mean_b), r = __builtin_amdgcn_rcpf(den_b), q = d * r;
  return rbf(fmaf(fmaf(-q, den_b, d), r, q));
}

#define STAMP(i) do { } while (0)

__global__ void __launch_bounds__(NTHR, 3)
fused_decode_kernel(const unsigned short* __restrict__ x, const unsigned char* __restrict__ kc, const unsigned char* __restrict__ vc,
                    const unsigned short* __restrict__ wq, const unsigned short* __restrict__ wo,
                    const unsigned short* __restrict__ w1, const unsigned short* __restrict__ b1,
                    const unsigned short* __restrict__ w2, const unsigned short* __restrict__ b2,
                    unsigned short* __restrict__ out, unsigned char* ws) {
  __shared__ __attribute__((aligned(16))) unsigned char lds[L_END];
  const int lane = threadIdx.x & 63, wv = __builtin_amdgcn_readfirstlane(threadIdx.x >> 6), w = wv < 8 ? wv ^ 4 : wv;
  const unsigned tst = now32();
  const int g = blockIdx.x >> 3, s = (blockIdx.x + 3 * g) & 7, id = g * 8 + s, b = g >> 4, kvh = g & 15;
  STAMP(0);
  const rsrc_t rws = mkrsrc(ws, WS_END);
  const rsrc_t rx = mkrsrc(x + b * DM, DM * 2);
  const int qh = s >> 2, cb = (s + (kvh >> 2)) & 3;
  const int la = lane >> 4;
  const int red = kvh * 8 + s;

  u32x2 ep = {0u, 0u};
  if (w == NWAVE) {
    ep = __builtin_amdgcn_raw_buffer_load_b64(rws, WS_EPOCH + b * 256, 0, 0);
    if (lane < 16) *((unsigned*)(lds + L_FLAG) + lane) = 0u;
    asm volatile("s_waitcnt lgkmcnt(0)\n\ts_barrier" ::: "memory");
  }
  if (w >= NWAVE) {
    __builtin_amdgcn_s_setprio(3);
    const unsigned e0 = __builtin_amdgcn_readfirstlane(ep[0]);
    const unsigned e1 = ((e0 & 0x7f7f7f7fu) + 0x01010101u) ^ (e0 & 0x80808080u);
    const unsigned tagA = e1 ^ 0xb77a3d00u;
    const unsigned tagB = __builtin_amdgcn_readfirstlane(ep[1]) + 1u;
    if (lane == 0) *(unsigned*)(lds + L_TAG) = tagA;
    flag_wait<0>(lds, F_QP, 4);
    {
      f32x2 q2 = *(const f32x2*)(lds + L_QPART + (lane & 31) * 8);
#pragma unroll
      for (int ww = 1; ww < 8; ++ww) q2 += *(const f32x2*)(lds + L_QPART + ww * 256 + (lane & 31) * 8);
      __builtin_amdgcn_raw_buffer_store_b128(u32x4{tagA, asu(q2[0]), (tagB & 0xffffu) | (tst << 16), asu(q2[1])}, rws,
                                             lane < 32 ? WS_QX + g * 4096 + qh * 2048 + cb * 512 + lane * 16 : OOB, 0, AUX_SC1);
      STAMP(8);
    }
    __builtin_amdgcn_s_sleep(PSL);
    {
      float qa[4], qb[4];
      int ts[4];
      int of[4];
#pragma unroll
      for (int i = 0; i < 4; ++i) { qa[i] = 0.f; qb[i] = 0.f; ts[i] = 0; of[i] = WS_QX + g * 4096 + i * 1024 + lane * 16; }
#pragma unroll 1
      for (unsigned spin = 0; spin < SPIN_CAP; ++spin) {
        asm volatile("" ::: "memory");
        u32x4 gr[4];
#pragma unroll
        for (int i = 0; i < 4; ++i) gr[i] = __builtin_amdgcn_raw_buffer_load_b128(rws, of[i], 0, AUX_SC1);
        bool done = true;
#pragma unroll
        for (int i = 0; i < 4; ++i) {
          const bool fresh = (gr[i][0] == tagA) & (((gr[i][2] ^ tagB) & 0xffffu) == 0u) & (of[i] != OOB);
          ts[i] = fresh ? (int)(gr[i][2] - (tst << 16)) >> 16 : ts[i];
          qa[i] = fresh ? asf(gr[i][1]) : qa[i]; qb[i] = fresh ? asf(gr[i][3]) : qb[i];
          of[i] = fresh ? OOB : of[i];
          done &= of[i] == OOB;
        }
        if (__all(done)) break;
        __builtin_amdgcn_s_sleep(QPS);
      }
      *(unsigned*)(lds + L_QS + lane * 4) = pack_bf16(qa[0] + qa[2], qb[0] + qb[2]);
      *(unsigned*)(lds + L_QS + 256 + lane * 4) = pack_bf16(qa[1] + qa[3], qb[1] + qb[3]);
      {
        int sm = 0, mx = -32768;
#pragma unroll
        for (int i = 0; i < 4; ++i) {
          const int t0 = __builtin_amdgcn_readlane(ts[i], 0), t1 = __builtin_amdgcn_readlane(ts[i], 32);
          sm += t0 + t1; mx = max(mx, max(t0, t1));
        }
        sm >>= 3;
        if (lane == 0) *(unsigned*)(lds + L_TAG + 4) = tst + (unsigned)(sm + ((mx - sm) * CBETA >> 4));
      }
      flag_add(lds, F_Q, lane);
    }
    const int fc = (lane & 1) * 4 + la;
    const unsigned xraw = __builtin_amdgcn_raw_buffer_load_b32(rx, red * 16 + (fc >> 1) * 4, 0, 0);
    const unsigned b2v = __builtin_amdgcn_raw_buffer_load_b16(mkrsrc(b2, 4), 0, 0, 0);
    if (red == 127) {
      wait_until(tst + 2000u);
#pragma unroll 1
      for (unsigned spin = 0; spin < SPIN_CAP; ++spin) {
        asm volatile("" ::: "memory");
        const u32x4 v0 = __builtin_amdgcn_raw_buffer_load_b128(rws, WS_QX + b * 65536 + lane * 512, 0, AUX_SC1);
        const u32x4 v1 = __builtin_amdgcn_raw_buffer_load_b128(rws, WS_QX + b * 65536 + 32768 + lane * 512, 0, AUX_SC1);
        if (__all((v0[0] == tagA) & (v1[0] == tagA) & ((((v0[2] ^ tagB) | (v1[2] ^ tagB)) & 0xffffu) == 0u))) break;
        __builtin_amdgcn_s_sleep(16);
      }
      __builtin_amdgcn_raw_buffer_store_b64(u32x2{e1, tagB}, rws, lane == 0 ? WS_EPOCH + b * 256 : OOB, 0, AUX_SC1);
    }
    float yv = 0.f, xfin = (fc & 1) ? bfhi(xraw) : bflo(xraw);
    const rsrc_t rout = mkrsrc(out + b * DM, DM * 2 + 2);
    int oout = (lane & 14) == 0 ? (red * 8 + fc) * 2 : OOB;
    if (red < 72) {
      flag_wait<16>(lds, F_Y, 4);
#pragma unroll
      for (int ww = 0; ww < 4; ++ww) yv += *(const float*)(lds + L_YP + ww * 32 + fc * 4);
      yv = rbf(rbf(yv) + bflo(b2v));
      __builtin_amdgcn_raw_buffer_store_b16((unsigned short)0, rout, oout, 0, 0);
    }
    int oa = (lane & 8) ? L_QPART + lane * 8 : L_OA + la * 64 + (lane & 7) * 8;
    asm volatile("" : "+v"(yv), "+v"(xfin), "+v"(oout), "+v"(oa));
    flag_wait<16>(lds, F_WP, 1);
    u32x4 ga[4];
#pragma unroll
    for (int i = 0; i < 4; ++i) ga[i] = u32x4{0u, 0u, 0u, 0u};
    int of1 = WS_PX + (g * 8 + (s & 4)) * 1024 + lane * 16;
    asm volatile("" : "+v"(of1), "+v"(ga[0]), "+v"(ga[1]), "+v"(ga[2]), "+v"(ga[3]));
    flag_wait<0>(lds, F_END, 1);
    STAMP(5);
    __builtin_amdgcn_s_sleep(T1D);
    {
      unsigned bad = 1u;
#pragma unroll 1
      for (unsigned spin = 0; spin < SPIN_CAP; ++spin) {
        asm volatile("" ::: "memory");
        if (bad != 0u) {
#pragma unroll
          for (int i = 0; i < 4; ++i) ga[i] = __builtin_amdgcn_raw_buffer_load_b128(rws, of1 + i * 1024, 0, AUX_SC1);
        }
        bad = 0u;
#pragma unroll
        for (int i = 0; i < 4; ++i) bad |= (ga[i][0] ^ tagA) | (ga[i][2] ^ (tagA >> 8));
        bad &= 0xffu;
        if (__all(bad == 0u)) break;
        bad |= dppu<XOR1>(bad); bad |= dppu<XOR2>(bad); bad |= dppu<XOR7>(bad);
        __builtin_amdgcn_s_sleep(1);
      }
      const unsigned hm = __builtin_amdgcn_perm(ga[0][3], ga[0][1], 0x0c0c0400u) << (16 * (lane & 1));
      const unsigned hl = __builtin_amdgcn_perm(ga[1][3], ga[1][1], 0x0c0c0400u) << (16 * (lane & 1));
      const float pm = asf(hm | dppu<XOR1>(hm)), pl = asf(hl | dppu<XOR1>(hl));
      const float M = rows_max(fmaxf(pm, dppf<ROR8>(pm)));
      const float w0 = __builtin_amdgcn_exp2f((pm - M) * LOG2E) * pl;
      const float inv = recip(rows_sum(w0 + dppf<ROR8>(w0)));
      float v[4];
#pragma unroll
      for (int r = 0; r < 4; ++r) {
        const float t = rows_sum_t(w0 * cut_lo(ga[0][r]), w0 * cut_lo(ga[1][r]), w0 * cut_lo(ga[2][r]), w0 * cut_lo(ga[3][r]));
        v[r] = (t + dppf<ROR8>(t)) * inv;
      }
      *(u32x2*)(lds + oa) = u32x2{pack_bf16(v[0], v[1]), pack_bf16(v[2], v[3])};
      flag_add(lds, F_O, lane);
    }
    if (red < 72) {
      __builtin_amdgcn_s_sleep(T2D);
      u32x4 gb = {0u, 0u, 0u, 0u};
      const int of2 = WS_OX + (b * 18 + (red >> 2)) * 4096 + (lane >> 1) * 128 + (red & 3) * 32 + (lane & 1) * 16;
      unsigned bad = 1u;
#pragma unroll 1
      for (unsigned spin = 0; spin < SPIN_CAP; ++spin) {
        asm volatile("" ::: "memory");
        if (bad != 0u) gb = __builtin_amdgcn_raw_buffer_load_b128(rws, of2, 0, AUX_SC1);
        bad = (__builtin_amdgcn_perm(gb[1], gb[0], 0x0c0c0400u) | __builtin_amdgcn_perm(gb[3], gb[2], 0x04000c0cu)) ^ tagA;
        if (__all(bad == 0u)) break;
        bad |= dppu<XOR1>(bad);
        __builtin_amdgcn_s_sleep(1);
      }
      STAMP(13);
      float t = rows_sum_t(cut_lo(gb[0]), cut_lo(gb[1]), cut_lo(gb[2]), cut_lo(gb[3]));
      t += dppf<XOR2>(t); t += dppf<ROR4>(t); t += dppf<ROR8>(t);
      __builtin_amdgcn_raw_buffer_store_b16((unsigned short)pack_bf16(rbf(xfin + rbf(t)) + yv, 0.f), rout, oout, 0, 0);
    }
  } else {
    const rsrc_t rk = mkrsrc(kc + (size_t)(id >> 1) * SPAN, SPAN);
    const rsrc_t rv = mkrsrc(vc + (size_t)(id >> 1) * SPAN, SPAN);
    auto poff = [&](int d, int h) {
      return ((d & 3) | (h << 2) | ((id & 1) << 3) | ((d >> 2) << 4)) * TILE_BYTES;
    };
    const int voffK = ((lane & 15) >> 2) * 256 + la * 64 + (lane & 3) * 16;
    const int voffV = lane * 16;
    u32x4 bA[8], bB[8];
    auto request = [&](u32x4 (&x)[8], rsrc_t r, int voff, int so) {
      asm volatile("" : "+v"(voff));
#pragma unroll
      for (int u = 0; u < 8; ++u) x[u] = __builtin_amdgcn_raw_buffer_load_b128(r, voff + (u & 3) * 1024, so + (u >> 2) * 4096, AUX_NT);
      __builtin_amdgcn_sched_barrier(0);
    };
    if (w >= 4) {
      const int rg = (w - 4) * 8 + (lane >> 3), c8 = lane & 7;
      unsigned xs[5];
      u32x4 wqv[9];
#pragma unroll
      for (int i = 0; i < 5; ++i) xs[i] = __builtin_amdgcn_raw_buffer_load_b32(rx, (lane + 64 * i) * 4, 0, 0);
      __builtin_amdgcn_sched_barrier(0);
      const rsrc_t rq = mkrsrc(wq + qh * 288 * 4096 + kvh * 256 + cb * 64, (287 * 4096 + 64) * 2);
#pragma unroll
      for (int i = 0; i < 9; ++i) wqv[i] = __builtin_amdgcn_raw_buffer_load_b128(rq, (rg + 32 * i) * 8192 + c8 * 16, 0, AUX_NT);
      __builtin_amdgcn_sched_barrier(0);
      __builtin_amdgcn_s_barrier();
      __builtin_amdgcn_sched_barrier(0);
      float xv[10], mean1, den1;
#pragma unroll
      for (int i = 0; i < 5; ++i) { xv[2 * i] = bflo(xs[i]); xv[2 * i + 1] = bfhi(xs[i]); }
      ln_stats(xv, lane < 32, mean1, den1);
#pragma unroll
      for (int i = 0; i < 5; ++i)
        *(unsigned*)(lds + L_XN + (lane + 64 * i) * 4) = pack_bf16(ln_apply(xv[2 * i], mean1, den1), ln_apply(xv[2 * i + 1], mean1, den1));
      asm volatile("" ::: "memory");
      STAMP(9);
      f32x4 qa = {0.f, 0.f, 0.f, 0.f};
#pragma unroll
      for (int i = 0; i < 9; ++i) {
        const unsigned t = (unsigned)*(const unsigned short*)(lds + L_XN + (qh * 288 + rg + 32 * i) * 2) << (16 * (lane & 1));
        const int jp = c8 >> 1;
        qa = mfma32(wqv[i], u32x4{jp == 0 ? t : 0u, jp == 1 ? t : 0u, jp == 2 ? t : 0u, jp == 3 ? t : 0u}, qa);
      }
      if ((lane >> 5) == ((lane >> 3) & 1)) {
        float* qp = (float*)(lds + L_QPART) + ((w - 4) * 2 + (lane >> 5)) * 64 + (la & 1) * 32 + c8;
#pragma unroll
        for (int r = 0; r < 4; ++r) qp[r * 8] = qa[r];
      }
      flag_add(lds, F_QP, lane);
      STAMP(1);
    }
    if (w < 4) asm volatile("s_barrier" ::: "memory");
    if (w >= PFD) flag_wait<1>(lds, F_ARR, w - PFD + 1);
    __builtin_amdgcn_sched_barrier(0);
    STAMP(14);
    request(bA, rk, voffK, poff(w, 0));
    request(bB, rk, voffK, poff(w, 1));
    asm volatile("s_waitcnt vmcnt(0)" ::: "memory");
    flag_add(lds, F_ARR, lane);
    STAMP(15);
    flag_wait<1>(lds, F_Q, 1);
    if (w < S) STAMP(2);

    const int c = lane & 3, hb = (lane >> 2) & 1, tb = (lane >> 3) & 1;
    const unsigned tagA = __builtin_amdgcn_readfirstlane(*(const unsigned*)(lds + L_TAG));
    const unsigned tb0 = __builtin_amdgcn_readfirstlane(*(const unsigned*)(lds + L_TAG + 4));
    float m;
    f32x4 acc[2][2];
#pragma unroll
    for (int i = 0; i < 2; ++i)
#pragma unroll
      for (int j = 0; j < 2; ++j) acc[i][j] = f32x4{0.f, 0.f, 0.f, 0.f};
    const u32x4 qf = *(const u32x4*)(lds + L_QS + (hb * 128 + la * 32 + c * 8) * 2);
    const u32x4 zero4 = {0u, 0u, 0u, 0u};
    const u32x4 q0 = tb ? zero4 : qf, q1 = tb ? qf : zero4;
    const unsigned mask0 = c == 0 ? 0x0000ffffu : c == 1 ? 0xffff0000u : 0u;
    const unsigned mask1 = c == 2 ? 0x0000ffffu : c == 3 ? 0xffff0000u : 0u;
    auto qk = [&](const u32x4 (&x)[8], f32x4 (&sc)[4]) {
#pragma unroll
      for (int i = 0; i < 4; ++i) sc[i] = mfma32(x[i], q0, f32x4{0.f, 0.f, 0.f, 0.f});
#pragma unroll
      for (int i = 0; i < 4; ++i) sc[i] = mfma32(x[4 + i], q1, sc[i]);
    };
    auto score = [&](const f32x4 (&sc)[4]) {
      float t4[4];
#pragma unroll
      for (int i = 0; i < 4; ++i) t4[i] = (dppf<QB0>(sc[i][0]) + dppf<QB1>(sc[i][1])) + (dppf<QB2>(sc[i][2]) + dppf<QB3>(sc[i][3]));
      const float raw = (c & 2) ? ((c & 1) ? t4[3] : t4[2]) : ((c & 1) ? t4[1] : t4[0]);
      float sv = rbf(rbf(raw) * SCALE);
      asm volatile("" : "+v"(sv));
      return sv;
    };
    auto head_max = [&](float v) {
      v = fmaxf(v, dppf<XOR1>(v));
      v = fmaxf(v, dppf<XOR2>(v));
      v = fmaxf(v, dppf<ROR8>(v));
      return rows_max(v);
    };
    auto pv = [&](const u32x4 (&x)[8], float sv) {
      const float p = __builtin_amdgcn_exp2f((sv - m) * LOG2E);
      const unsigned pk = pack_bf16(p, p);
      unsigned pb0[4], pb1[4];
      pb0[0] = dppu<QB0>(pk) & mask0; pb1[0] = dppu<QB0>(pk) & mask1;
      pb0[1] = dppu<QB1>(pk) & mask0; pb1[1] = dppu<QB1>(pk) & mask1;
      pb0[2] = dppu<QB2>(pk) & mask0; pb1[2] = dppu<QB2>(pk) & mask1;
      pb0[3] = dppu<QB3>(pk) & mask0; pb1[3] = dppu<QB3>(pk) & mask1;
#pragma unroll
      for (int u = 0; u < 8; ++u) {
        acc[u >> 2][0] = mfma16(x[u][0], x[u][1], pb0[u & 3], pb1[u & 3], acc[u >> 2][0]);
        acc[u >> 2][1] = mfma16(x[u][2], x[u][3], pb0[u & 3], pb1[u & 3], acc[u >> 2][1]);
      }
    };
    auto put_ml = [&](int k, float mk, float lk) {
      lk += dppf<XOR1>(lk);
      lk += dppf<XOR2>(lk);
      lk += dppf<ROR8>(lk);
      lk = rows_sum(lk);
      if ((lane & ~4) == 0) *(f32x2*)(lds + L_ML + (k * 2 + hb) * 8) = f32x2{mk, lk};
      flag_add(lds, F_ML, lane);
    };
    const int hw = w - S, wk = w - 2, nwk = (s & 3) == 3 ? 6 : 4;
    int ontime = 0;
    auto slot = [&](int p, bool watch) {
      __builtin_amdgcn_sched_barrier(0);
      if (watch && lane == 0) {
        unsigned* fl = (unsigned*)(lds + L_FLAG) + vzero();
        const unsigned t = now32() - tb0;
        const int gap = (int)(t - __hip_atomic_fetch_max(fl + F_T, t, __ATOMIC_RELAXED, __HIP_MEMORY_SCOPE_WORKGROUP));
        if (gap > GLT && ontime >= 3) {
          __hip_atomic_fetch_max(fl + F_T, t + GLQ, __ATOMIC_RELAXED, __HIP_MEMORY_SCOPE_WORKGROUP);
          __hip_atomic_fetch_add(fl + F_SH, (unsigned)GLQ, __ATOMIC_RELAXED, __HIP_MEMORY_SCOPE_WORKGROUP);
        }
      }
      const unsigned due = tb0 + SCA + (p >= PGAP ? GAP : 0) + (unsigned)(p * SCT >> 4);
      ontime = (int)(now32() - due - flag_get(lds, F_SH)) < 0 ? ontime + 1 : 0;
      while ((int)(now32() - due - flag_get(lds, F_SH)) < 0) {}
      __builtin_amdgcn_sched_barrier(0);
    };
    f32x2 sl;
    if (w < S) {
      __builtin_amdgcn_s_setprio(2);
      unsigned char* sco = lds + L_SC + w * (NPAIR * 512) + lane * 8;
      float mx = -INFINITY;
      for (int j = 0; j < NPAIR; ++j) {
        const bool k = j + 1 < NPAIR;
        const rsrc_t r = k ? rk : rv;
        const int vo = k ? voffK : voffV, d = NWAVE + S * (k ? j : j - 1) + w;
        f32x4 sc[4];
        qk(bA, sc);
        slot(2 * NWAVE + 2 * S * j + w, true);
        request(bA, r, vo, poff(d, 0));
        const float s0 = score(sc);
        qk(bB, sc);
        slot(2 * NWAVE + 2 * S * j + S + w, true);
        request(bB, r, vo, poff(d, 1));
        const float s1 = score(sc);
        mx = fmaxf(mx, fmaxf(s0, s1));
        *(f32x2*)(sco + j * 512) = f32x2{s0, s1};
      }
      m = head_max(mx);
      if ((lane & ~4) == 0) *(float*)(lds + L_ML + (w * 2 + hb) * 8) = m;
      flag_add(lds, F_P1 + w, lane);
      for (int j = 0; j + 1 < NPAIR; ++j) {
        const f32x2 sv = *(const f32x2*)(sco + (NPAIR - 1 - j) * 512);
        const int d = j + 2 < NPAIR ? NWAVE + S * (NPAIR - 3 - j) + w : w;
        pv(bA, sv[0]);
        slot(2 * NWAVE + 2 * S * (NPAIR + j) + w, true);
        request(bA, rv, voffV, poff(d, 0));
        pv(bB, sv[1]);
        slot(2 * NWAVE + 2 * S * (NPAIR + j) + S + w, true);
        request(bB, rv, voffV, poff(d, 1));
      }
      sl = *(const f32x2*)sco;
    }
    asm volatile("" ::: "memory");
    if (w >= S) {
      {
        f32x4 sc[4];
        qk(bA, sc);
        sl[0] = score(sc);
        qk(bB, sc);
        sl[1] = score(sc);
        m = head_max(fmaxf(sl[0], sl[1]));
        put_ml(w, m, __builtin_amdgcn_exp2f((sl[0] - m) * LOG2E) + __builtin_amdgcn_exp2f((sl[1] - m) * LOG2E));
      }
      STAMP(12);
      if (red < 72) {
        const int ht = (w - 4) * 64 + lane;
        unsigned a1[9], ab[9], a2[9];
        const rsrc_t r1 = mkrsrc(w1, FF * 2), rb = mkrsrc(b1, FF * 2), r2 = mkrsrc(w2, FF * 2);
#pragma unroll
        for (int i = 0; i < 9; ++i) {
          a1[i] = __builtin_amdgcn_raw_buffer_load_b32(r1, (ht + 256 * i) * 4, 0, 0);
          ab[i] = __builtin_amdgcn_raw_buffer_load_b32(rb, (ht + 256 * i) * 4, 0, 0);
          a2[i] = __builtin_amdgcn_raw_buffer_load_b32(r2, (ht + 256 * i) * 4, 0, 0);
        }
        __builtin_amdgcn_sched_barrier(0);
        float xn[10];
#pragma unroll
        for (int i = 0; i < 5; ++i) { const unsigned v = *(const unsigned*)(lds + L_XN + (lane + 64 * i) * 4); xn[2 * i] = bflo(v); xn[2 * i + 1] = bfhi(v); }
        float mean2, den2;
        ln_stats(xn, lane < 32, mean2, den2);
        constexpr float GC0 = -2.0f * 0.7978845608028654f * LOG2E, GC1 = GC0 * 0.044715f;
#pragma unroll 1
        for (int j = 0; j < 8; ++j) {
          const float hv = ln_apply(bflo(*(const unsigned short*)(lds + L_XN + (red * 8 + j) * 2)), mean2, den2);
          float y = 0.f;
#pragma unroll
          for (int i = 0; i < 9; ++i) {
            asm volatile("" : "+v"(a1[i]), "+v"(ab[i]), "+v"(a2[i]));
            const unsigned pr = pack_bf16(hv * bflo(a1[i]), hv * bfhi(a1[i]));
            const unsigned uu = pack_bf16(bflo(pr) + bflo(ab[i]), bfhi(pr) + bfhi(ab[i]));
            const float u0 = bflo(uu), u1 = bfhi(uu);
            const float e0 = __builtin_amdgcn_exp2f(u0 * fmaf(u0 * u0, GC1, GC0));
            const float e1 = __builtin_amdgcn_exp2f(u1 * fmaf(u1 * u1, GC1, GC0));
            const unsigned gg = pack_bf16(u0 * __builtin_amdgcn_rcpf(1.0f + e0), u1 * __builtin_amdgcn_rcpf(1.0f + e1));
            y = fmaf(bflo(gg), bflo(a2[i]), y);
            y = fmaf(bfhi(gg), bfhi(a2[i]), y);
          }
          y = wave_sum(y);
          if (lane == 0) *(float*)(lds + L_YP + (w - 4) * 32 + j * 4) = y;
        }
        flag_add(lds, F_Y, lane);
      }
      {
        flag_wait<16>(lds, F_P1 + hw, 1);
        const float ms = *(const float*)(lds + L_ML + (hw * 2 + hb) * 8);
        float ls = 0.f;
#pragma unroll 1
        for (int j = 0; j < NPAIR; ++j) {
          const f32x2 sv = *(const f32x2*)(lds + L_SC + hw * (NPAIR * 512) + lane * 8 + j * 512);
          ls += __builtin_amdgcn_exp2f((sv[0] - ms) * LOG2E) + __builtin_amdgcn_exp2f((sv[1] - ms) * LOG2E);
        }
        put_ml(hw, ms, ls);
      }
      wait_until(tb0 + SCA + (unsigned)(PEND * SCT >> 4) - 60u);
      __builtin_amdgcn_s_setprio(2);
      slot(PEND + hw, false);
      request(bA, rv, voffV, poff(w, 0));
      slot(PEND + NH + hw, false);
      request(bB, rv, voffV, poff(w, 1));
    }
    int po = WS_PX + ((g * 8 + hb * 4 + la) * 8 + s) * 128 + (tb * 4 + c) * 16;
    float fw;
    unsigned side;
    {
      flag_wait<1>(lds, F_ML, NWAVE);
      f32x2 ml[NWAVE];
#pragma unroll
      for (int k = 0; k < NWAVE; ++k) ml[k] = *(const f32x2*)(lds + L_ML + (k * 2 + hb) * 8);
      float mx = ml[0][0], L = 0.f;
#pragma unroll
      for (int k = 1; k < NWAVE; ++k) mx = fmaxf(mx, ml[k][0]);
#pragma unroll
      for (int k = 0; k < NWAVE; ++k) L = fmaf(__builtin_amdgcn_exp2f((ml[k][0] - mx) * LOG2E), ml[k][1], L);
      fw = __builtin_amdgcn_exp2f((m - mx) * LOG2E) * recip(L);
      side = asu((la & 1) ? L : mx) >> (16 * (c & 1));
    }
    asm volatile("" : "+v"(po), "+v"(fw), "+v"(side));
    pv(bA, sl[0]);
    pv(bB, sl[1]);
    STAMP(3);
    {
      f32x4 res;
#pragma unroll
      for (int r = 0; r < 4; ++r) {
        const float own = tb ? acc[1][1][r] : acc[0][0][r], snd = tb ? acc[1][0][r] : acc[0][1][r];
        res[r] = (own + dppf<ROR8>(snd)) * fw;
      }
      *(f32x4*)(lds + L_PART + w * 1024 + lane * 16) = res;
      asm volatile("" ::: "memory");
      unsigned old = 0;
      if (lane == 0) old = __hip_atomic_fetch_add((unsigned*)(lds + L_FLAG) + F_WP + vzero(), 1u, __ATOMIC_RELAXED, __HIP_MEMORY_SCOPE_WORKGROUP);
      asm volatile("" ::: "memory");
      f32x4 pp[NWAVE];
#pragma unroll
      for (int k = 0; k < NWAVE; ++k) pp[k] = *(const f32x4*)(lds + L_PART + k * 1024 + lane * 16);
      __builtin_amdgcn_sched_barrier(0);
      if (__builtin_amdgcn_readfirstlane(old) == NWAVE - 1) {
        const f32x4 sum = ((pp[0] + pp[1]) + (pp[2] + pp[3])) + ((pp[4] + pp[5]) + (pp[6] + pp[7]));
        __builtin_amdgcn_raw_buffer_store_b128(
            u32x4{put_lo(sum[0], tagA), put_lo(sum[1], side), put_lo(sum[2], tagA >> 8), put_lo(sum[3], side >> 8)}, rws, po, 0, AUX_SC1);
        flag_add(lds, F_END, lane);
        STAMP(10);
      }
    }
    if (w < 2) {
      const rsrc_t ro = mkrsrc(wo + (size_t)(kvh * 256 + qh * 128 + w * 64) * DM, 64 * DM * 2);
      u32x4 wt[24];
#pragma unroll
      for (int i = 0; i < 24; ++i) {
        if (i % 12 == 0) slot(2 * NT + w + 2 * (i / 12), false);
        wt[i] = __builtin_amdgcn_raw_buffer_load_b128(
            ro, 2 * (i % 3) < nwk ? ((i / 3) * 8 + (lane >> 3)) * (DM * 2) + (s & 3) * 256 + (i % 3) * 128 + (lane & 7) * 16 : OOB, 0, AUX_NT);
      }
#pragma unroll
      for (int i = 0; i < 24; ++i)
        *(u32x4*)(lds + L_WO + w * 24576 + (i % 3) * 8192 + ((i / 3) * 8 + ((lane >> 3) & 4) + ((lane >> 2) & 2) + ((lane >> 4) & 1)) * 128 +
                  (((lane & 7) * 16) ^ (((i / 3) & 3) * 32))) = wt[i];
      flag_add(lds, F_WO, lane);
      STAMP(4);
    } else if (wk < nwk) {
      const int n = lane & 15;
      int oo = n < 2 ? WS_OX + (b * 18 + (s & 3) * 4 + wk) * 4096 + (kvh * 2 + qh) * 128 + n * 64 + la * 16 : OOB;
      asm volatile("" : "+v"(oo));
      flag_wait<1>(lds, F_WO, 2);
      u32x4 wof[2][4];
      {
        typedef __attribute__((address_space(3))) s16x4* lds_s16x4;
        unsigned char* img = lds + L_WO + (wk >> 1) * 8192 + ((n >> 2) * 8 + (la >> 1) * 4 + (la & 1)) * 128 + (lane & 3) * 8;
#pragma unroll
        for (int t = 0; t < 2; ++t)
#pragma unroll
          for (int ks = 0; ks < 4; ++ks) {
            unsigned char* p = img + (ks >> 1) * 24576 + (ks & 1) * 4096 + (((wk & 1) * 2 + t) ^ (n >> 2)) * 32;
            const u32x2 lo = __builtin_bit_cast(u32x2, __builtin_amdgcn_ds_read_tr16_b64_v4i16((lds_s16x4)p));
            const u32x2 hi = __builtin_bit_cast(u32x2, __builtin_amdgcn_ds_read_tr16_b64_v4i16((lds_s16x4)(p + 256)));
            wof[t][ks] = u32x4{lo[0], lo[1], hi[0], hi[1]};
          }
      }
      u32x4 ob[4];
#pragma unroll 1
      for (unsigned spin = 0; spin < (SPIN_CAP << 4); ++spin) {
        asm volatile("" ::: "memory");
        const unsigned f = __hip_atomic_load((unsigned*)(lds + L_FLAG) + F_O, __ATOMIC_RELAXED, __HIP_MEMORY_SCOPE_WORKGROUP);
        asm volatile("" ::: "memory");
#pragma unroll
        for (int ks = 0; ks < 4; ++ks) ob[ks] = *(const u32x4*)(lds + L_OA + ks * 64 + la * 16);
        __builtin_amdgcn_sched_barrier(0);
        if (__builtin_amdgcn_readfirstlane(f) != 0u) break;
      }
      STAMP(6);
      f32x4 d0 = {0.f, 0.f, 0.f, 0.f}, d1 = {0.f, 0.f, 0.f, 0.f};
#pragma unroll
      for (int ks = 0; ks < 4; ++ks) { d0 = mfma32(wof[0][ks], ob[ks], d0); d1 = mfma32(wof[1][ks], ob[ks], d1); }
      const f32x4 d = n ? d1 : d0;
      __builtin_amdgcn_raw_buffer_store_b128(
          u32x4{put_lo(d[0], tagA), put_lo(d[1], tagA >> 8), put_lo(d[2], tagA >> 16), put_lo(d[3], tagA >> 24)}, rws, oo, 0, AUX_SC1);
      STAMP(11);
    }
  }
}

}

extern "C" void kernel_launch(void* const* d_in, const int* in_sizes, int n_in, void* d_out, int out_size, void* d_ws,
                              size_t ws_size, hipStream_t stream) {
  (void)in_sizes; (void)n_in; (void)out_size; (void)ws_size;
  fused_decode_kernel<<<dim3(NWG), dim3(NTHR), 0, stream>>>(
      (const unsigned short*)d_in[0], (const unsigned char*)d_in[1], (const unsigned char*)d_in[2],
      (const unsigned short*)d_in[3], (const unsigned short*)d_in[4], (const unsigned short*)d_in[6],
      (const unsigned short*)d_in[7], (const unsigned short*)d_in[8], (const unsigned short*)d_in[9],
      (unsigned short*)d_out, (unsigned char*)d_ws);
}
```

```cpp
#include <hip/hip_runtime.h>
#include <hip/hip_bf16.h>
#include <stdint.h>

namespace {

typedef unsigned u32x2 __attribute__((ext_vector_type(2)));
typedef unsigned u32x4 __attribute__((ext_vector_type(4)));
typedef float f32x2 __attribute__((ext_vector_type(2)));
typedef float f32x4 __attribute__((ext_vector_type(4)));
typedef short s16x4 __attribute__((ext_vector_type(4)));
typedef short s16x8 __attribute__((ext_vector_type(8)));
typedef __bf16 bf16x2 __attribute__((ext_vector_type(2)));
typedef __amdgpu_buffer_rsrc_t rsrc_t;

constexpr int DM = 576, SK = 32768, DH = 128, FF = 4608;
constexpr int NSPLIT = 8, NWG = 256, NWAVE = 8, NTHR = (NWAVE + 1) * 64;
constexpr int KEYS_WG = SK / NSPLIT;
constexpr int TILE_BYTES = 32 * DH * 2;
constexpr int NT = KEYS_WG / 32;
constexpr int SPAN = 2 * KEYS_WG * DH * 2;
constexpr int S = 4, NH = NWAVE - S;
constexpr int NPAIR = (NT / 2 - NH) / S;
static_assert(2 * (NPAIR * S + NH) == NT && NH == S, "every tile has one owner, every stream one helper");

constexpr int SCA = 125;
constexpr int SCT = 427;
constexpr int GAP = 25;
constexpr int CBETA = 6;
constexpr int GLT = 160;
constexpr int GLQ = 210;
constexpr int PFREE = 40;
constexpr int KD = 15;
constexpr int KG = 20;
constexpr int PFD = 3;
constexpr int PSL = 6;
constexpr int QPS = 2;
constexpr int T1D = 17;
constexpr int T2D = 22;
constexpr int PGAP = 2 * NWAVE + 2 * S * NPAIR, PEND = 2 * NT - 2 * NH;

constexpr int AUX_NT = 2, AUX_SC1 = 16;
constexpr unsigned RSRC3 = 0x00020000u;
constexpr unsigned SPIN_CAP = 1u << 19;
constexpr int OOB = 0x7ff00000;

constexpr int WS_EPOCH = 0;
constexpr int WS_QX = 4096;
constexpr int WS_PX = WS_QX + 32 * 4096;
constexpr int WS_OX = WS_PX + NWG * 1024;
constexpr int WS_END = WS_OX + 2 * 18 * 4096;

constexpr int L_FLAG = 0;
constexpr int L_TAG = 64;
constexpr int L_XN = 80;
constexpr int L_QPART = L_XN + 1280;
constexpr int L_QS = L_QPART + 8 * 64 * 4;
constexpr int L_OA = L_QS + 512;
constexpr int L_YP = L_OA + 256;
constexpr int L_ML = L_YP + 4 * 8 * 4;
constexpr int L_PART = L_ML + NWAVE * 16;
constexpr int L_WO = L_PART + NWAVE * 1024;
constexpr int L_SC = L_WO + 128 * 384;
constexpr int L_END = L_SC + S * NPAIR * 512;
constexpr int F_QP = 0, F_Q = 1, F_WP = 2, F_END = 3, F_O = 4, F_Y = 5, F_ARR = 6, F_WO = 8, F_ML = 9, F_SH = 10, F_T = 11, F_P1 = 12;

constexpr float LOG2E = 1.4426950408889634f;
constexpr float SCALE = 0.08838834764831845f;
constexpr float LN_EPS = 1e-5f;

__device__ __forceinline__ float asf(unsigned u) { return __builtin_bit_cast(float, u); }
__device__ __forceinline__ unsigned asu(float f) { return __builtin_bit_cast(unsigned, f); }
__device__ __forceinline__ float bflo(unsigned u) { return asf(u << 16); }
__device__ __forceinline__ float bfhi(unsigned u) { return asf(u & 0xffff0000u); }
__device__ __forceinline__ unsigned pack_bf16(float lo, float hi) {
  f32x2 v = {lo, hi};
  return __builtin_bit_cast(unsigned, __builtin_convertvector(v, bf16x2));
}
__device__ __forceinline__ float rbf(float f) { return (float)(__bf16)f; }
template <int CTRL> __device__ __forceinline__ unsigned dppu(unsigned x) {
  return (unsigned)__builtin_amdgcn_mov_dpp((int)x, CTRL, 0xf, 0xf, true);
}
template <int CTRL> __device__ __forceinline__ float dppf(float x) { return asf(dppu<CTRL>(asu(x))); }
constexpr int XOR1 = 0xB1, XOR2 = 0x4E, XOR7 = 0x141, ROR4 = 0x124, ROR8 = 0x128;
constexpr int QB0 = 0x00, QB1 = 0x55, QB2 = 0xAA, QB3 = 0xFF;
__device__ __forceinline__ float rows_sum(float x) {
  auto s = __builtin_amdgcn_permlane16_swap(asu(x), asu(x), false, false);
  x = asf(s[0]) + asf(s[1]);
  auto t = __builtin_amdgcn_permlane32_swap(asu(x), asu(x), false, false);
  return asf(t[0]) + asf(t[1]);
}
__device__ __forceinline__ float rows_max(float x) {
  auto s = __builtin_amdgcn_permlane16_swap(asu(x), asu(x), false, false);
  x = fmaxf(asf(s[0]), asf(s[1]));
  auto t = __builtin_amdgcn_permlane32_swap(asu(x), asu(x), false, false);
  return fmaxf(asf(t[0]), asf(t[1]));
}
__device__ __forceinline__ float rows_sum_t(float v0, float v1, float v2, float v3) {
  const auto p0 = __builtin_amdgcn_permlane32_swap(asu(v0), asu(v2), false, false);
  const auto p1 = __builtin_amdgcn_permlane32_swap(asu(v1), asu(v3), false, false);
  const auto p2 = __builtin_amdgcn_permlane16_swap(asu(asf(p0[0]) + asf(p0[1])), asu(asf(p1[0]) + asf(p1[1])), false, false);
  return asf(p2[0]) + asf(p2[1]);
}
__device__ __forceinline__ float wave_sum(float x) {
  x += dppf<XOR1>(x); x += dppf<XOR2>(x); x += dppf<ROR4>(x); x += dppf<ROR8>(x);
  return rows_sum(x);
}
__device__ __forceinline__ float recip(float x) {
  const float r = __builtin_amdgcn_rcpf(x);
  return fmaf(fmaf(-x, r, 1.0f), r, r);
}
__device__ __forceinline__ rsrc_t mkrsrc(const void* p, int bytes) {
  return __builtin_amdgcn_make_buffer_rsrc(const_cast<void*>(p), 0, bytes, RSRC3);
}
__device__ __forceinline__ f32x4 mfma32(u32x4 a, u32x4 b, f32x4 c) {
  return __builtin_amdgcn_mfma_f32_16x16x32_bf16(__builtin_bit_cast(s16x8, a), __builtin_bit_cast(s16x8, b), c, 0, 0, 0);
}
__device__ __forceinline__ f32x4 mfma16(unsigned a0, unsigned a1, unsigned b0, unsigned b1, f32x4 c) {
  u32x2 a = {a0, a1}, b = {b0, b1};
  return __builtin_amdgcn_mfma_f32_16x16x16bf16_1k(__builtin_bit_cast(s16x4, a), __builtin_bit_cast(s16x4, b), c, 0, 0, 0);
}
__device__ __forceinline__ unsigned put_lo(float v, unsigned byte) { return (asu(v) & 0xffffff00u) | (byte & 0xffu); }
__device__ __forceinline__ float cut_lo(unsigned u) { return asf(u & 0xffffff00u); }
__device__ __forceinline__ unsigned now32() { return (unsigned)__builtin_amdgcn_s_memrealtime(); }
__device__ __forceinline__ void wait_until(unsigned t) {
  while ((int)(now32() - t) < 0) __builtin_amdgcn_s_sleep(1);
}
__device__ __forceinline__ int vzero() { int z = 0; asm("" : "+v"(z)); return z; }
__device__ __forceinline__ void flag_add(unsigned char* lds, int f, int lane) {
  asm volatile("" ::: "memory");
  if (lane == 0) __hip_atomic_fetch_add((unsigned*)(lds + L_FLAG) + f + vzero(), 1u, __ATOMIC_RELAXED, __HIP_MEMORY_SCOPE_WORKGROUP);
  asm volatile("" ::: "memory");
}
__device__ __forceinline__ unsigned flag_get(unsigned char* lds, int f) {
  return __builtin_amdgcn_readfirstlane(__hip_atomic_load((unsigned*)(lds + L_FLAG) + f, __ATOMIC_RELAXED, __HIP_MEMORY_SCOPE_WORKGROUP));
}
template <int SLEEP> __device__ __forceinline__ void flag_wait(unsigned char* lds, int f, unsigned n) {
#pragma unroll 1
  for (unsigned spin = 0; spin < (SPIN_CAP << 4); ++spin) {
    if (flag_get(lds, f) >= n) break;
    if (SLEEP) __builtin_amdgcn_s_sleep(SLEEP);
  }
  asm volatile("" ::: "memory");
}
__device__ __forceinline__ void ln_stats(const float (&v)[10], bool last_valid, float& mean_b, float& den_b) {
  float s = 0.f;
#pragma unroll
  for (int i = 0; i < 8; ++i) s += v[i];
  s += last_valid ? (v[8] + v[9]) : 0.f;
  const float mean = wave_sum(s) * (1.0f / DM);
  float q = 0.f;
#pragma unroll
  for (int i = 0; i < 8; ++i) { const float d = v[i] - mean; q = fmaf(d, d, q); }
  { const float d8 = v[8] - mean, d9 = v[9] - mean; q += last_valid ? fmaf(d8, d8, d9 * d9) : 0.f; }
  const float var = wave_sum(q) * (1.0f / DM);
  mean_b = rbf(mean);
  den_b = rbf(__builtin_amdgcn_sqrtf(rbf(rbf(var) + LN_EPS)));
}
__device__ __forceinline__ float ln_apply(float x, float mean_b, float den_b) {
  const float d = rbf(x - mean_b), r = __builtin_amdgcn_rcpf(den_b), q = d * r;
  return rbf(fmaf(fmaf(-q, den_b, d), r, q));
}

__global__ void __launch_bounds__(NTHR, 3)
fused_decode_kernel(const unsigned short* __restrict__ x, const unsigned char* __restrict__ kc, const unsigned char* __restrict__ vc,
                    const unsigned short* __restrict__ wq, const unsigned short* __restrict__ wo,
                    const unsigned short* __restrict__ w1, const unsigned short* __restrict__ b1,
                    const unsigned short* __restrict__ w2, const unsigned short* __restrict__ b2,
                    unsigned short* __restrict__ out, unsigned char* ws) {
  __shared__ __attribute__((aligned(16))) unsigned char lds[L_END];
  const int lane = threadIdx.x & 63, wv = __builtin_amdgcn_readfirstlane(threadIdx.x >> 6), w = wv < 8 ? wv ^ 4 : wv;
  const unsigned tst = now32();
  const int g = blockIdx.x >> 3, s = (blockIdx.x + 3 * g) & 7, id = g * 8 + s, b = g >> 4, kvh = g & 15;
  const rsrc_t rws = mkrsrc(ws, WS_END);
  const rsrc_t rx = mkrsrc(x + b * DM, DM * 2);
  const int qh = s >> 2, cb = (s + (kvh >> 2)) & 3;
  const int la = lane >> 4;
  const int red = kvh * 8 + s;

  u32x2 ep = {0u, 0u};
  if (w == NWAVE) {
    ep = __builtin_amdgcn_raw_buffer_load_b64(rws, WS_EPOCH + b * 256, 0, 0);
    if (lane < 16) *((unsigned*)(lds + L_FLAG) + lane) = 0u;
    asm volatile("s_waitcnt lgkmcnt(0)\n\ts_barrier" ::: "memory");
  }
  if (w >= NWAVE) {
    __builtin_amdgcn_s_setprio(3);
    const unsigned e0 = __builtin_amdgcn_readfirstlane(ep[0]);
    const unsigned e1 = ((e0 & 0x7f7f7f7fu) + 0x01010101u) ^ (e0 & 0x80808080u);
    const unsigned tagA = e1 ^ 0xb77a3d00u;
    const unsigned tagB = __builtin_amdgcn_readfirstlane(ep[1]) + 1u;
    if (lane == 0) *(unsigned*)(lds + L_TAG) = tagA;
    flag_wait<0>(lds, F_QP, 4);
    {
      f32x2 q2 = *(const f32x2*)(lds + L_QPART + (lane & 31) * 8);
#pragma unroll
      for (int ww = 1; ww < 8; ++ww) q2 += *(const f32x2*)(lds + L_QPART + ww * 256 + (lane & 31) * 8);
      __builtin_amdgcn_raw_buffer_store_b128(u32x4{tagA, asu(q2[0]), (tagB & 0xffffu) | (tst << 16), asu(q2[1])}, rws,
                                             lane < 32 ? WS_QX + g * 4096 + qh * 2048 + cb * 512 + lane * 16 : OOB, 0, AUX_SC1);
    }
    __builtin_amdgcn_s_sleep(PSL);
    {
      float qa[4], qb[4];
      int ts[4];
      int of[4];
#pragma unroll
      for (int i = 0; i < 4; ++i) { qa[i] = 0.f; qb[i] = 0.f; ts[i] = 0; of[i] = WS_QX + g * 4096 + i * 1024 + lane * 16; }
#pragma unroll 1
      for (unsigned spin = 0; spin < SPIN_CAP; ++spin) {
        asm volatile("" ::: "memory");
        u32x4 gr[4];
#pragma unroll
        for (int i = 0; i < 4; ++i) gr[i] = __builtin_amdgcn_raw_buffer_load_b128(rws, of[i], 0, AUX_SC1);
        bool done = true;
#pragma unroll
        for (int i = 0; i < 4; ++i) {
          const bool fresh = (gr[i][0] == tagA) & (((gr[i][2] ^ tagB) & 0xffffu) == 0u) & (of[i] != OOB);
          ts[i] = fresh ? (int)(gr[i][2] - (tst << 16)) >> 16 : ts[i];
          qa[i] = fresh ? asf(gr[i][1]) : qa[i]; qb[i] = fresh ? asf(gr[i][3]) : qb[i];
          of[i] = fresh ? OOB : of[i];
          done &= of[i] == OOB;
        }
        if (__all(done)) break;
        __builtin_amdgcn_s_sleep(QPS);
      }
      *(unsigned*)(lds + L_QS + lane * 4) = pack_bf16(qa[0] + qa[2], qb[0] + qb[2]);
      *(unsigned*)(lds + L_QS + 256 + lane * 4) = pack_bf16(qa[1] + qa[3], qb[1] + qb[3]);
      {
        int sm = 0, mx = -32768;
#pragma unroll
        for (int i = 0; i < 4; ++i) {
          const int t0 = __builtin_amdgcn_readlane(ts[i], 0), t1 = __builtin_amdgcn_readlane(ts[i], 32);
          sm += t0 + t1; mx = max(mx, max(t0, t1));
        }
        sm >>= 3;
        if (lane == 0) *(unsigned*)(lds + L_TAG + 4) = tst + (unsigned)(sm + ((mx - sm) * CBETA >> 4));
      }
      flag_add(lds, F_Q, lane);
    }
    const int fc = (lane & 1) * 4 + la;
    const unsigned xraw = __builtin_amdgcn_raw_buffer_load_b32(rx, red * 16 + (fc >> 1) * 4, 0, 0);
    const unsigned b2v = __builtin_amdgcn_raw_buffer_load_b16(mkrsrc(b2, 4), 0, 0, 0);
    if (red == 127) {
      wait_until(tst + 2000u);
#pragma unroll 1
      for (unsigned spin = 0; spin < SPIN_CAP; ++spin) {
        asm volatile("" ::: "memory");
        const u32x4 v0 = __builtin_amdgcn_raw_buffer_load_b128(rws, WS_QX + b * 65536 + lane * 512, 0, AUX_SC1);
        const u32x4 v1 = __builtin_amdgcn_raw_buffer_load_b128(rws, WS_QX + b * 65536 + 32768 + lane * 512, 0, AUX_SC1);
        if (__all((v0[0] == tagA) & (v1[0] == tagA) & ((((v0[2] ^ tagB) | (v1[2] ^ tagB)) & 0xffffu) == 0u))) break;
        __builtin_amdgcn_s_sleep(16);
      }
      __builtin_amdgcn_raw_buffer_store_b64(u32x2{e1, tagB}, rws, lane == 0 ? WS_EPOCH + b * 256 : OOB, 0, AUX_SC1);
    }
    float yv = 0.f, xfin = (fc & 1) ? bfhi(xraw) : bflo(xraw);
    const rsrc_t rout = mkrsrc(out + b * DM, DM * 2 + 2);
    int oout = (lane & 14) == 0 ? (red * 8 + fc) * 2 : OOB;
    if (red < 72) {
      flag_wait<16>(lds, F_Y, 4);
#pragma unroll
      for (int ww = 0; ww < 4; ++ww) yv += *(const float*)(lds + L_YP + ww * 32 + fc * 4);
      yv = rbf(rbf(yv) + bflo(b2v));
      __builtin_amdgcn_raw_buffer_store_b16((unsigned short)0, rout, oout, 0, 0);
    }
    int oa = (lane & 8) ? L_QPART + lane * 8 : L_OA + la * 64 + (lane & 7) * 8;
    asm volatile("" : "+v"(yv), "+v"(xfin), "+v"(oout), "+v"(oa));
    flag_wait<16>(lds, F_WP, 1);
    u32x4 ga[4];
#pragma unroll
    for (int i = 0; i < 4; ++i) ga[i] = u32x4{0u, 0u, 0u, 0u};
    int of1 = WS_PX + (g * 8 + (s & 4)) * 1024 + lane * 16;
    asm volatile("" : "+v"(of1), "+v"(ga[0]), "+v"(ga[1]), "+v"(ga[2]), "+v"(ga[3]));
    flag_wait<0>(lds, F_END, 1);
    __builtin_amdgcn_s_sleep(T1D);
    {
      unsigned bad = 1u;
#pragma unroll 1
      for (unsigned spin = 0; spin < SPIN_CAP; ++spin) {
        asm volatile("" ::: "memory");
        if (bad != 0u) {
#pragma unroll
          for (int i = 0; i < 4; ++i) ga[i] = __builtin_amdgcn_raw_buffer_load_b128(rws, of1 + i * 1024, 0, AUX_SC1);
        }
        bad = 0u;
#pragma unroll
        for (int i = 0; i < 4; ++i) bad |= (ga[i][0] ^ tagA) | (ga[i][2] ^ (tagA >> 8));
        bad &= 0xffu;
        if (__all(bad == 0u)) break;
        bad |= dppu<XOR1>(bad); bad |= dppu<XOR2>(bad); bad |= dppu<XOR7>(bad);
        __builtin_amdgcn_s_sleep(1);
      }
      const unsigned hm = __builtin_amdgcn_perm(ga[0][3], ga[0][1], 0x0c0c0400u) << (16 * (lane & 1));
      const unsigned hl = __builtin_amdgcn_perm(ga[1][3], ga[1][1], 0x0c0c0400u) << (16 * (lane & 1));
      const float pm = asf(hm | dppu<XOR1>(hm)), pl = asf(hl | dppu<XOR1>(hl));
      const float M = rows_max(fmaxf(pm, dppf<ROR8>(pm)));
      const float w0 = __builtin_amdgcn_exp2f((pm - M) * LOG2E) * pl;
      const float inv = recip(rows_sum(w0 + dppf<ROR8>(w0)));
      float v[4];
#pragma unroll
      for (int r = 0; r < 4; ++r) {
        const float t = rows_sum_t(w0 * cut_lo(ga[0][r]), w0 * cut_lo(ga[1][r]), w0 * cut_lo(ga[2][r]), w0 * cut_lo(ga[3][r]));
        v[r] = (t + dppf<ROR8>(t)) * inv;
      }
      *(u32x2*)(lds + oa) = u32x2{pack_bf16(v[0], v[1]), pack_bf16(v[2], v[3])};
      flag_add(lds, F_O, lane);
    }
    if (red < 72) {
      __builtin_amdgcn_s_sleep(T2D);
      u32x4 gb = {0u, 0u, 0u, 0u};
      const int of2 = WS_OX + (b * 18 + (red >> 2)) * 4096 + (lane >> 1) * 128 + (red & 3) * 32 + (lane & 1) * 16;
      unsigned bad = 1u;
#pragma unroll 1
      for (unsigned spin = 0; spin < SPIN_CAP; ++spin) {
        asm volatile("" ::: "memory");
        if (bad != 0u) gb = __builtin_amdgcn_raw_buffer_load_b128(rws, of2, 0, AUX_SC1);
        bad = (__builtin_amdgcn_perm(gb[1], gb[0], 0x0c0c0400u) | __builtin_amdgcn_perm(gb[3], gb[2], 0x04000c0cu)) ^ tagA;
        if (__all(bad == 0u)) break;
        bad |= dppu<XOR1>(bad);
        __builtin_amdgcn_s_sleep(1);
      }
      float t = rows_sum_t(cut_lo(gb[0]), cut_lo(gb[1]), cut_lo(gb[2]), cut_lo(gb[3]));
      t += dppf<XOR2>(t); t += dppf<ROR4>(t); t += dppf<ROR8>(t);
      __builtin_amdgcn_raw_buffer_store_b16((unsigned short)pack_bf16(rbf(xfin + rbf(t)) + yv, 0.f), rout, oout, 0, 0);
    }
  } else {
    const rsrc_t rk = mkrsrc(kc + (size_t)(id >> 1) * SPAN, SPAN);
    const rsrc_t rv = mkrsrc(vc + (size_t)(id >> 1) * SPAN, SPAN);
    auto poff = [&](int d, int h) {
      return ((d & 3) | (h << 2) | ((id & 1) << 3) | ((d >> 2) << 4)) * TILE_BYTES;
    };
    const int voffK = ((lane & 15) >> 2) * 256 + la * 64 + (lane & 3) * 16;
    const int voffV = lane * 16;
    u32x4 bA[8], bB[8];
    auto request = [&](u32x4 (&x)[8], rsrc_t r, int voff, int so) {
      asm volatile("" : "+v"(voff));
#pragma unroll
      for (int u = 0; u < 8; ++u) x[u] = __builtin_amdgcn_raw_buffer_load_b128(r, voff + (u & 3) * 1024, so + (u >> 2) * 4096, AUX_NT);
      __builtin_amdgcn_sched_barrier(0);
    };
    if (w >= 4) {
      const int rg = (w - 4) * 8 + (lane >> 3), c8 = lane & 7;
      unsigned xs[5];
      u32x4 wqv[9];
#pragma unroll
      for (int i = 0; i < 5; ++i) xs[i] = __builtin_amdgcn_raw_buffer_load_b32(rx, (lane + 64 * i) * 4, 0, 0);
      __builtin_amdgcn_sched_barrier(0);
      const rsrc_t rq = mkrsrc(wq + qh * 288 * 4096 + kvh * 256 + cb * 64, (287 * 4096 + 64) * 2);
#pragma unroll
      for (int i = 0; i < 9; ++i) wqv[i] = __builtin_amdgcn_raw_buffer_load_b128(rq, (rg + 32 * i) * 8192 + c8 * 16, 0, AUX_NT);
      __builtin_amdgcn_sched_barrier(0);
      __builtin_amdgcn_s_barrier();
      __builtin_amdgcn_sched_barrier(0);
      float xv[10], mean1, den1;
#pragma unroll
      for (int i = 0; i < 5; ++i) { xv[2 * i] = bflo(xs[i]); xv[2 * i + 1] = bfhi(xs[i]); }
      ln_stats(xv, lane < 32, mean1, den1);
#pragma unroll
      for (int i = 0; i < 5; ++i)
        *(unsigned*)(lds + L_XN + (lane + 64 * i) * 4) = pack_bf16(ln_apply(xv[2 * i], mean1, den1), ln_apply(xv[2 * i + 1], mean1, den1));
      asm volatile("" ::: "memory");
      f32x4 qa = {0.f, 0.f, 0.f, 0.f};
#pragma unroll
      for (int i = 0; i < 9; ++i) {
        const unsigned t = (unsigned)*(const unsigned short*)(lds + L_XN + (qh * 288 + rg + 32 * i) * 2) << (16 * (lane & 1));
        const int jp = c8 >> 1;
        qa = mfma32(wqv[i], u32x4{jp == 0 ? t : 0u, jp == 1 ? t : 0u, jp == 2 ? t : 0u, jp == 3 ? t : 0u}, qa);
      }
      if ((lane >> 5) == ((lane >> 3) & 1)) {
        float* qp = (float*)(lds + L_QPART) + ((w - 4) * 2 + (lane >> 5)) * 64 + (la & 1) * 32 + c8;
#pragma unroll
        for (int r = 0; r < 4; ++r) qp[r * 8] = qa[r];
      }
      flag_add(lds, F_QP, lane);
    }
    if (w < 4) asm volatile("s_barrier" ::: "memory");
    if (w >= PFD) flag_wait<1>(lds, F_ARR, w - PFD + 1);
    __builtin_amdgcn_sched_barrier(0);
    request(bA, rk, voffK, poff(w, 0));
    request(bB, rk, voffK, poff(w, 1));
    asm volatile("s_waitcnt vmcnt(0)" ::: "memory");
    flag_add(lds, F_ARR, lane);
    flag_wait<1>(lds, F_Q, 1);

    const int c = lane & 3, hb = (lane >> 2) & 1, tb = (lane >> 3) & 1;
    const unsigned tagA = __builtin_amdgcn_readfirstlane(*(const unsigned*)(lds + L_TAG));
    const unsigned tb0 = __builtin_amdgcn_readfirstlane(*(const unsigned*)(lds + L_TAG + 4));
    float m;
    f32x4 acc[2][2];
#pragma unroll
    for (int i = 0; i < 2; ++i)
#pragma unroll
      for (int j = 0; j < 2; ++j) acc[i][j] = f32x4{0.f, 0.f, 0.f, 0.f};
    const u32x4 qf = *(const u32x4*)(lds + L_QS + (hb * 128 + la * 32 + c * 8) * 2);
    const u32x4 zero4 = {0u, 0u, 0u, 0u};
    const u32x4 q0 = tb ? zero4 : qf, q1 = tb ? qf : zero4;
    const unsigned mask0 = c == 0 ? 0x0000ffffu : c == 1 ? 0xffff0000u : 0u;
    const unsigned mask1 = c == 2 ? 0x0000ffffu : c == 3 ? 0xffff0000u : 0u;
    auto qk = [&](const u32x4 (&x)[8], f32x4 (&sc)[4]) {
#pragma unroll
      for (int i = 0; i < 4; ++i) sc[i] = mfma32(x[i], q0, f32x4{0.f, 0.f, 0.f, 0.f});
#pragma unroll
      for (int i = 0; i < 4; ++i) sc[i] = mfma32(x[4 + i], q1, sc[i]);
    };
    auto score = [&](const f32x4 (&sc)[4]) {
      float t4[4];
#pragma unroll
      for (int i = 0; i < 4; ++i) t4[i] = (dppf<QB0>(sc[i][0]) + dppf<QB1>(sc[i][1])) + (dppf<QB2>(sc[i][2]) + dppf<QB3>(sc[i][3]));
      const float raw = (c & 2) ? ((c & 1) ? t4[3] : t4[2]) : ((c & 1) ? t4[1] : t4[0]);
      float sv = rbf(rbf(raw) * SCALE);
      asm volatile("" : "+v"(sv));
      return sv;
    };
    auto head_max = [&](float v) {
      v = fmaxf(v, dppf<XOR1>(v));
      v = fmaxf(v, dppf<XOR2>(v));
      v = fmaxf(v, dppf<ROR8>(v));
      return rows_max(v);
    };
    auto pv = [&](const u32x4 (&x)[8], float sv) {
      const float p = __builtin_amdgcn_exp2f((sv - m) * LOG2E);
      const unsigned pk = pack_bf16(p, p);
      unsigned pb0[4], pb1[4];
      pb0[0] = dppu<QB0>(pk) & mask0; pb1[0] = dppu<QB0>(pk) & mask1;
      pb0[1] = dppu<QB1>(pk) & mask0; pb1[1] = dppu<QB1>(pk) & mask1;
      pb0[2] = dppu<QB2>(pk) & mask0; pb1[2] = dppu<QB2>(pk) & mask1;
      pb0[3] = dppu<QB3>(pk) & mask0; pb1[3] = dppu<QB3>(pk) & mask1;
#pragma unroll
      for (int u = 0; u < 8; ++u) {
        acc[u >> 2][0] = mfma16(x[u][0], x[u][1], pb0[u & 3], pb1[u & 3], acc[u >> 2][0]);
        acc[u >> 2][1] = mfma16(x[u][2], x[u][3], pb0[u & 3], pb1[u & 3], acc[u >> 2][1]);
      }
    };
    auto put_ml = [&](int k, float mk, float lk) {
      lk += dppf<XOR1>(lk);
      lk += dppf<XOR2>(lk);
      lk += dppf<ROR8>(lk);
      lk = rows_sum(lk);
      if ((lane & ~4) == 0) *(f32x2*)(lds + L_ML + (k * 2 + hb) * 8) = f32x2{mk, lk};
      flag_add(lds, F_ML, lane);
    };
    const int hw = w - S, wk = w - 2, nwk = (s & 3) == 3 ? 6 : 4;
    int ontime = 0;
    auto slot = [&](int p, bool watch) {
      __builtin_amdgcn_sched_barrier(0);
      if (watch && lane == 0) {
        unsigned* fl = (unsigned*)(lds + L_FLAG) + vzero();
        const unsigned t = now32() - tb0;
        const int gap = (int)(t - __hip_atomic_fetch_max(fl + F_T, t, __ATOMIC_RELAXED, __HIP_MEMORY_SCOPE_WORKGROUP));
        if (gap > GLT && ontime >= 3 && (p < PGAP - 2 * S || p >= PGAP + 4 * S)) {
          __hip_atomic_fetch_max(fl + F_T, t + GLQ, __ATOMIC_RELAXED, __HIP_MEMORY_SCOPE_WORKGROUP);
          __hip_atomic_fetch_add(fl + F_SH, (unsigned)GLQ, __ATOMIC_RELAXED, __HIP_MEMORY_SCOPE_WORKGROUP);
        }
      }
      const unsigned due = tb0 + SCA + (p >= PGAP ? GAP : 0) + (unsigned)(p * SCT >> 4);
      ontime = (int)(now32() - due - flag_get(lds, F_SH)) < 0 ? ontime + 1 : 0;
      if (watch && p >= PFREE) {
        asm volatile("s_waitcnt vmcnt(0)" ::: "memory");
        const unsigned t = now32();
        const int late = (int)(t - due - flag_get(lds, F_SH)) + KD;
        const unsigned lim = t + (unsigned)max(KD - (max(late, 0) * KG >> 8), 0);
        while ((int)(now32() - lim) < 0) {}
      }
      while ((int)(now32() - due - flag_get(lds, F_SH)) < 0) {}
      __builtin_amdgcn_sched_barrier(0);
    };
    f32x2 sl;
    if (w < S) {
      __builtin_amdgcn_s_setprio(2);
      unsigned char* sco = lds + L_SC + w * (NPAIR * 512) + lane * 8;
      float mx = -INFINITY;
      for (int j = 0; j < NPAIR; ++j) {
        const bool k = j + 1 < NPAIR;
        const rsrc_t r = k ? rk : rv;
        const int vo = k ? voffK : voffV, d = NWAVE + S * (k ? j : j - 1) + w;
        f32x4 sc[4];
        qk(bA, sc);
        slot(2 * NWAVE + 2 * S * j + w, true);
        request(bA, r, vo, poff(d, 0));
        const float s0 = score(sc);
        qk(bB, sc);
        slot(2 * NWAVE + 2 * S * j + S + w, true);
        request(bB, r, vo, poff(d, 1));
        const float s1 = score(sc);
        mx = fmaxf(mx, fmaxf(s0, s1));
        *(f32x2*)(sco + j * 512) = f32x2{s0, s1};
      }
      m = head_max(mx);
      if ((lane & ~4) == 0) *(float*)(lds + L_ML + (w * 2 + hb) * 8) = m;
      flag_add(lds, F_P1 + w, lane);
      for (int j = 0; j + 1 < NPAIR; ++j) {
        const f32x2 sv = *(const f32x2*)(sco + (NPAIR - 1 - j) * 512);
        const int d = j + 2 < NPAIR ? NWAVE + S * (NPAIR - 3 - j) + w : w;
        pv(bA, sv[0]);
        slot(2 * NWAVE + 2 * S * (NPAIR + j) + w, true);
        request(bA, rv, voffV, poff(d, 0));
        pv(bB, sv[1]);
        slot(2 * NWAVE + 2 * S * (NPAIR + j) + S + w, true);
        request(bB, rv, voffV, poff(d, 1));
      }
      sl = *(const f32x2*)sco;
    }
    asm volatile("" ::: "memory");
    if (w >= S) {
      {
        f32x4 sc[4];
        qk(bA, sc);
        sl[0] = score(sc);
        qk(bB, sc);
        sl[1] = score(sc);
        m = head_max(fmaxf(sl[0], sl[1]));
        put_ml(w, m, __builtin_amdgcn_exp2f((sl[0] - m) * LOG2E) + __builtin_amdgcn_exp2f((sl[1] - m) * LOG2E));
      }
      if (red < 72) {
        const int ht = (w - 4) * 64 + lane;
        unsigned a1[9], ab[9], a2[9];
        const rsrc_t r1 = mkrsrc(w1, FF * 2), rb = mkrsrc(b1, FF * 2), r2 = mkrsrc(w2, FF * 2);
#pragma unroll
        for (int i = 0; i < 9; ++i) {
          a1[i] = __builtin_amdgcn_raw_buffer_load_b32(r1, (ht + 256 * i) * 4, 0, 0);
          ab[i] = __builtin_amdgcn_raw_buffer_load_b32(rb, (ht + 256 * i) * 4, 0, 0);
          a2[i] = __builtin_amdgcn_raw_buffer_load_b32(r2, (ht + 256 * i) * 4, 0, 0);
        }
        __builtin_amdgcn_sched_barrier(0);
        float xn[10];
#pragma unroll
        for (int i = 0; i < 5; ++i) { const unsigned v = *(const unsigned*)(lds + L_XN + (lane + 64 * i) * 4); xn[2 * i] = bflo(v); xn[2 * i + 1] = bfhi(v); }
        float mean2, den2;
        ln_stats(xn, lane < 32, mean2, den2);
        constexpr float GC0 = -2.0f * 0.7978845608028654f * LOG2E, GC1 = GC0 * 0.044715f;
#pragma unroll 1
        for (int j = 0; j < 8; ++j) {
          const float hv = ln_apply(bflo(*(const unsigned short*)(lds + L_XN + (red * 8 + j) * 2)), mean2, den2);
          float y = 0.f;
#pragma unroll
          for (int i = 0; i < 9; ++i) {
            asm volatile("" : "+v"(a1[i]), "+v"(ab[i]), "+v"(a2[i]));
            const unsigned pr = pack_bf16(hv * bflo(a1[i]), hv * bfhi(a1[i]));
            const unsigned uu = pack_bf16(bflo(pr) + bflo(ab[i]), bfhi(pr) + bfhi(ab[i]));
            const float u0 = bflo(uu), u1 = bfhi(uu);
            const float e0 = __builtin_amdgcn_exp2f(u0 * fmaf(u0 * u0, GC1, GC0));
            const float e1 = __builtin_amdgcn_exp2f(u1 * fmaf(u1 * u1, GC1, GC0));
            const unsigned gg = pack_bf16(u0 * __builtin_amdgcn_rcpf(1.0f + e0), u1 * __builtin_amdgcn_rcpf(1.0f + e1));
            y = fmaf(bflo(gg), bflo(a2[i]), y);
            y = fmaf(bfhi(gg), bfhi(a2[i]), y);
          }
          y = wave_sum(y);
          if (lane == 0) *(float*)(lds + L_YP + (w - 4) * 32 + j * 4) = y;
        }
        flag_add(lds, F_Y, lane);
      }
      {
        flag_wait<16>(lds, F_P1 + hw, 1);
        const float ms = *(const float*)(lds + L_ML + (hw * 2 + hb) * 8);
        float ls = 0.f;
#pragma unroll 1
        for (int j = 0; j < NPAIR; ++j) {
          const f32x2 sv = *(const f32x2*)(lds + L_SC + hw * (NPAIR * 512) + lane * 8 + j * 512);
          ls += __builtin_amdgcn_exp2f((sv[0] - ms) * LOG2E) + __builtin_amdgcn_exp2f((sv[1] - ms) * LOG2E);
        }
        put_ml(hw, ms, ls);
      }
      wait_until(tb0 + SCA + (unsigned)(PEND * SCT >> 4) - 60u);
      __builtin_amdgcn_s_setprio(2);
      slot(PEND + hw, false);
      request(bA, rv, voffV, poff(w, 0));
      slot(PEND + NH + hw, false);
      request(bB, rv, voffV, poff(w, 1));
    }
    int po = WS_PX + ((g * 8 + hb * 4 + la) * 8 + s) * 128 + (tb * 4 + c) * 16;
    float fw;
    unsigned side;
    {
      flag_wait<1>(lds, F_ML, NWAVE);
      f32x2 ml[NWAVE];
#pragma unroll
      for (int k = 0; k < NWAVE; ++k) ml[k] = *(const f32x2*)(lds + L_ML + (k * 2 + hb) * 8);
      float mx = ml[0][0], L = 0.f;
#pragma unroll
      for (int k = 1; k < NWAVE; ++k) mx = fmaxf(mx, ml[k][0]);
#pragma unroll
      for (int k = 0; k < NWAVE; ++k) L = fmaf(__builtin_amdgcn_exp2f((ml[k][0] - mx) * LOG2E), ml[k][1], L);
      fw = __builtin_amdgcn_exp2f((m - mx) * LOG2E) * recip(L);
      side = asu((la & 1) ? L : mx) >> (16 * (c & 1));
    }
    asm volatile("" : "+v"(po), "+v"(fw), "+v"(side));
    pv(bA, sl[0]);
    pv(bB, sl[1]);
    {
      f32x4 res;
#pragma unroll
      for (int r = 0; r < 4; ++r) {
        const float own = tb ? acc[1][1][r] : acc[0][0][r], snd = tb ? acc[1][0][r] : acc[0][1][r];
        res[r] = (own + dppf<ROR8>(snd)) * fw;
      }
      *(f32x4*)(lds + L_PART + w * 1024 + lane * 16) = res;
      asm volatile("" ::: "memory");
      unsigned old = 0;
      if (lane == 0) old = __hip_atomic_fetch_add((unsigned*)(lds + L_FLAG) + F_WP + vzero(), 1u, __ATOMIC_RELAXED, __HIP_MEMORY_SCOPE_WORKGROUP);
      asm volatile("" ::: "memory");
      f32x4 pp[NWAVE];
#pragma unroll
      for (int k = 0; k < NWAVE; ++k) pp[k] = *(const f32x4*)(lds + L_PART + k * 1024 + lane * 16);
      __builtin_amdgcn_sched_barrier(0);
      if (__builtin_amdgcn_readfirstlane(old) == NWAVE - 1) {
        const f32x4 sum = ((pp[0] + pp[1]) + (pp[2] + pp[3])) + ((pp[4] + pp[5]) + (pp[6] + pp[7]));
        __builtin_amdgcn_raw_buffer_store_b128(
            u32x4{put_lo(sum[0], tagA), put_lo(sum[1], side), put_lo(sum[2], tagA >> 8), put_lo(sum[3], side >> 8)}, rws, po, 0, AUX_SC1);
        flag_add(lds, F_END, lane);
      }
    }
    if (w < 2) {
      const rsrc_t ro = mkrsrc(wo + (size_t)(kvh * 256 + qh * 128 + w * 64) * DM, 64 * DM * 2);
      u32x4 wt[24];
#pragma unroll
      for (int i = 0; i < 24; ++i) {
        if (i % 12 == 0) slot(2 * NT + w + 2 * (i / 12), false);
        wt[i] = __builtin_amdgcn_raw_buffer_load_b128(
            ro, 2 * (i % 3) < nwk ? ((i / 3) * 8 + (lane >> 3)) * (DM * 2) + (s & 3) * 256 + (i % 3) * 128 + (lane & 7) * 16 : OOB, 0, AUX_NT);
      }
#pragma unroll
      for (int i = 0; i < 24; ++i)
        *(u32x4*)(lds + L_WO + w * 24576 + (i % 3) * 8192 + ((i / 3) * 8 + ((lane >> 3) & 4) + ((lane >> 2) & 2) + ((lane >> 4) & 1)) * 128 +
                  (((lane & 7) * 16) ^ (((i / 3) & 3) * 32))) = wt[i];
      flag_add(lds, F_WO, lane);
    } else if (wk < nwk) {
      const int n = lane & 15;
      int oo = n < 2 ? WS_OX + (b * 18 + (s & 3) * 4 + wk) * 4096 + (kvh * 2 + qh) * 128 + n * 64 + la * 16 : OOB;
      asm volatile("" : "+v"(oo));
      flag_wait<1>(lds, F_WO, 2);
      u32x4 wof[2][4];
      {
        typedef __attribute__((address_space(3))) s16x4* lds_s16x4;
        unsigned char* img = lds + L_WO + (wk >> 1) * 8192 + ((n >> 2) * 8 + (la >> 1) * 4 + (la & 1)) * 128 + (lane & 3) * 8;
#pragma unroll
        for (int t = 0; t < 2; ++t)
#pragma unroll
          for (int ks = 0; ks < 4; ++ks) {
            unsigned char* p = img + (ks >> 1) * 24576 + (ks & 1) * 4096 + (((wk & 1) * 2 + t) ^ (n >> 2)) * 32;
            const u32x2 lo = __builtin_bit_cast(u32x2, __builtin_amdgcn_ds_read_tr16_b64_v4i16((lds_s16x4)p));
            const u32x2 hi = __builtin_bit_cast(u32x2, __builtin_amdgcn_ds_read_tr16_b64_v4i16((lds_s16x4)(p + 256)));
            wof[t][ks] = u32x4{lo[0], lo[1], hi[0], hi[1]};
          }
      }
      u32x4 ob[4];
#pragma unroll 1
      for (unsigned spin = 0; spin < (SPIN_CAP << 4); ++spin) {
        asm volatile("" ::: "memory");
        const unsigned f = __hip_atomic_load((unsigned*)(lds + L_FLAG) + F_O, __ATOMIC_RELAXED, __HIP_MEMORY_SCOPE_WORKGROUP);
        asm volatile("" ::: "memory");
#pragma unroll
        for (int ks = 0; ks < 4; ++ks) ob[ks] = *(const u32x4*)(lds + L_OA + ks * 64 + la * 16);
        __builtin_amdgcn_sched_barrier(0);
        if (__builtin_amdgcn_readfirstlane(f) != 0u) break;
      }
      f32x4 d0 = {0.f, 0.f, 0.f, 0.f}, d1 = {0.f, 0.f, 0.f, 0.f};
#pragma unroll
      for (int ks = 0; ks < 4; ++ks) { d0 = mfma32(wof[0][ks], ob[ks], d0); d1 = mfma32(wof[1][ks], ob[ks], d1); }
      const f32x4 d = n ? d1 : d0;
      __builtin_amdgcn_raw_buffer_store_b128(
          u32x4{put_lo(d[0], tagA), put_lo(d[1], tagA >> 8), put_lo(d[2], tagA >> 16), put_lo(d[3], tagA >> 24)}, rws, oo, 0, AUX_SC1);
    }
  }
}

}

extern "C" void kernel_launch(void* const* d_in, const int* in_sizes, int n_in, void* d_out, int out_size, void* d_ws,
                              size_t ws_size, hipStream_t stream) {
  (void)in_sizes; (void)n_in; (void)out_size; (void)ws_size;
  fused_decode_kernel<<<dim3(NWG), dim3(NTHR), 0, stream>>>(
      (const unsigned short*)d_in[0], (const unsigned char*)d_in[1], (const unsigned char*)d_in[2],
      (const unsigned short*)d_in[3], (const unsigned short*)d_in[4], (const unsigned short*)d_in[6],
      (const unsigned short*)d_in[7], (const unsigned short*)d_in[8], (const unsigned short*)d_in[9],
      (unsigned short*)d_out, (unsigned char*)d_ws);
}
```

```cpp
#include <hip/hip_runtime.h>
#include <hip/hip_bf16.h>
#include <stdint.h>

namespace {

typedef unsigned u32x2 __attribute__((ext_vector_type(2)));
typedef unsigned u32x4 __attribute__((ext_vector_type(4)));
typedef float f32x2 __attribute__((ext_vector_type(2)));
typedef float f32x4 __attribute__((ext_vector_type(4)));
typedef short s16x4 __attribute__((ext_vector_type(4)));
typedef short s16x8 __attribute__((ext_vector_type(8)));
typedef __bf16 bf16x2 __attribute__((ext_vector_type(2)));
typedef __amdgpu_buffer_rsrc_t rsrc_t;

constexpr int DM = 576, SK = 32768, DH = 128, FF = 4608;
constexpr int NSPLIT = 8, NWG = 256, NWAVE = 8, NTHR = (NWAVE + 1) * 64;
constexpr int KEYS_WG = SK / NSPLIT;
constexpr int TILE_BYTES = 32 * DH * 2;
constexpr int NT = KEYS_WG / 32;
constexpr int SPAN = 2 * KEYS_WG * DH * 2;
constexpr int S = 4, NH = NWAVE - S;
constexpr int NPAIR = (NT / 2 - NH) / S;
static_assert(2 * (NPAIR * S + NH) == NT && NH == S, "every tile has one owner, every stream one helper");

#define SCA 125
#define SCT 427
#define GAP 25
#define CBETA 6
#define GLT 160
#define GLQ 210
#define PFREE 40
#define KD 15
#define KG 20
#define STL 1
#define PFD 3
#define PSL 6
#define QPS 2
#define T1D 17
#define T2D 22
constexpr int PGAP = 2 * NWAVE + 2 * S * NPAIR, PEND = 2 * NT - 2 * NH;

constexpr int AUX_NT = 2, AUX_SC1 = 16;
constexpr unsigned RSRC3 = 0x00020000u;
constexpr unsigned SPIN_CAP = 1u << 19;
constexpr int OOB = 0x7ff00000;

constexpr int WS_EPOCH = 0;
constexpr int WS_QX = 4096;
constexpr int WS_PX = WS_QX + 32 * 4096;
constexpr int WS_OX = WS_PX + NWG * 1024;
constexpr int WS_END = WS_OX + 2 * 18 * 4096;

constexpr int L_FLAG = 0;
constexpr int L_TAG = 64;
constexpr int L_XN = 80;
constexpr int L_QPART = L_XN + 1280;
constexpr int L_QS = L_QPART + 8 * 64 * 4;
constexpr int L_OA = L_QS + 512;
constexpr int L_YP = L_OA + 256;
constexpr int L_ML = L_YP + 4 * 8 * 4;
constexpr int L_PART = L_ML + NWAVE * 16;
constexpr int L_WO = L_PART + NWAVE * 1024;
constexpr int L_SC = L_WO + 128 * 384;
constexpr int L_END = L_SC + S * NPAIR * 512;
constexpr int F_QP = 0, F_Q = 1, F_WP = 2, F_END = 3, F_O = 4, F_Y = 5, F_ARR = 6, F_WO = 8, F_ML = 9, F_SH = 10, F_T = 11, F_P1 = 12;

constexpr float LOG2E = 1.4426950408889634f;
constexpr float SCALE = 0.08838834764831845f;
constexpr float LN_EPS = 1e-5f;

__device__ __forceinline__ float asf(unsigned u) { return __builtin_bit_cast(float, u); }
__device__ __forceinline__ unsigned asu(float f) { return __builtin_bit_cast(unsigned, f); }
__device__ __forceinline__ float bflo(unsigned u) { return asf(u << 16); }
__device__ __forceinline__ float bfhi(unsigned u) { return asf(u & 0xffff0000u); }
__device__ __forceinline__ unsigned pack_bf16(float lo, float hi) {
  f32x2 v = {lo, hi};
  return __builtin_bit_cast(unsigned, __builtin_convertvector(v, bf16x2));
}
__device__ __forceinline__ float rbf(float f) { return (float)(__bf16)f; }
template <int CTRL> __device__ __forceinline__ unsigned dppu(unsigned x) {
  return (unsigned)__builtin_amdgcn_mov_dpp((int)x, CTRL, 0xf, 0xf, true);
}
template <int CTRL> __device__ __forceinline__ float dppf(float x) { return asf(dppu<CTRL>(asu(x))); }
constexpr int XOR1 = 0xB1, XOR2 = 0x4E, XOR7 = 0x141, ROR4 = 0x124, ROR8 = 0x128;
constexpr int QB0 = 0x00, QB1 = 0x55, QB2 = 0xAA, QB3 = 0xFF;
__device__ __forceinline__ float rows_sum(float x) {
  auto s = __builtin_amdgcn_permlane16_swap(asu(x), asu(x), false, false);
  x = asf(s[0]) + asf(s[1]);
  auto t = __builtin_amdgcn_permlane32_swap(asu(x), asu(x), false, false);
  return asf(t[0]) + asf(t[1]);
}
__device__ __forceinline__ float rows_max(float x) {
  auto s = __builtin_amdgcn_permlane16_swap(asu(x), asu(x), false, false);
  x = fmaxf(asf(s[0]), asf(s[1]));
  auto t = __builtin_amdgcn_permlane32_swap(asu(x), asu(x), false, false);
  return fmaxf(asf(t[0]), asf(t[1]));
}
__device__ __forceinline__ float rows_sum_t(float v0, float v1, float v2, float v3) {
  const auto p0 = __builtin_amdgcn_permlane32_swap(asu(v0), asu(v2), false, false);
  const auto p1 = __builtin_amdgcn_permlane32_swap(asu(v1), asu(v3), false, false);
  const auto p2 = __builtin_amdgcn_permlane16_swap(asu(asf(p0[0]) + asf(p0[1])), asu(asf(p1[0]) + asf(p1[1])), false, false);
  return asf(p2[0]) + asf(p2[1]);
}
__device__ __forceinline__ float wave_sum(float x) {
  x += dppf<XOR1>(x); x += dppf<XOR2>(x); x += dppf<ROR4>(x); x += dppf<ROR8>(x);
  return rows_sum(x);
}
__device__ __forceinline__ float recip(float x) {
  const float r = __builtin_amdgcn_rcpf(x);
  return fmaf(fmaf(-x, r, 1.0f), r, r);
}
__device__ __forceinline__ rsrc_t mkrsrc(const void* p, int bytes) {
  return __builtin_amdgcn_make_buffer_rsrc(const_cast<void*>(p), 0, bytes, RSRC3);
}
__device__ __forceinline__ f32x4 mfma32(u32x4 a, u32x4 b, f32x4 c) {
  return __builtin_amdgcn_mfma_f32_16x16x32_bf16(__builtin_bit_cast(s16x8, a), __builtin_bit_cast(s16x8, b), c, 0, 0, 0);
}
__device__ __forceinline__ f32x4 mfma16(unsigned a0, unsigned a1, unsigned b0, unsigned b1, f32x4 c) {
  u32x2 a = {a0, a1}, b = {b0, b1};
  return __builtin_amdgcn_mfma_f32_16x16x16bf16_1k(__builtin_bit_cast(s16x4, a), __builtin_bit_cast(s16x4, b), c, 0, 0, 0);
}
__device__ __forceinline__ unsigned put_lo(float v, unsigned byte) { return (asu(v) & 0xffffff00u) | (byte & 0xffu); }
__device__ __forceinline__ float cut_lo(unsigned u) { return asf(u & 0xffffff00u); }
__device__ __forceinline__ unsigned now32() { return (unsigned)__builtin_amdgcn_s_memrealtime(); }
__device__ __forceinline__ void wait_until(unsigned t) {
  while ((int)(now32() - t) < 0) __builtin_amdgcn_s_sleep(1);
}
__device__ __forceinline__ int vzero() { int z = 0; asm("" : "+v"(z)); return z; }
__device__ __forceinline__ void stouch(const void* p) {
  const char* q = (const char*)((unsigned long long)p & ~3ull);
  asm volatile("" : "+s"(q));
  const unsigned v = *(const __attribute__((address_space(4))) unsigned*)q;
  asm volatile("" :: "s"(v));
}
__device__ __forceinline__ void flag_add(unsigned char* lds, int f, int lane) {
  asm volatile("" ::: "memory");
  if (lane == 0) __hip_atomic_fetch_add((unsigned*)(lds + L_FLAG) + f + vzero(), 1u, __ATOMIC_RELAXED, __HIP_MEMORY_SCOPE_WORKGROUP);
  asm volatile("" ::: "memory");
}
__device__ __forceinline__ unsigned flag_get(unsigned char* lds, int f) {
  return __builtin_amdgcn_readfirstlane(__hip_atomic_load((unsigned*)(lds + L_FLAG) + f, __ATOMIC_RELAXED, __HIP_MEMORY_SCOPE_WORKGROUP));
}
template <int SLEEP> __device__ __forceinline__ void flag_wait(unsigned char* lds, int f, unsigned n) {
#pragma unroll 1
  for (unsigned spin = 0; spin < (SPIN_CAP << 4); ++spin) {
    if (flag_get(lds, f) >= n) break;
    if (SLEEP) __builtin_amdgcn_s_sleep(SLEEP);
  }
  asm volatile("" ::: "memory");
}
__device__ __forceinline__ void ln_stats(const float (&v)[10], bool last_valid, float& mean_b, float& den_b) {
  float s = 0.f;
#pragma unroll
  for (int i = 0; i < 8; ++i) s += v[i];
  s += last_valid ? (v[8] + v[9]) : 0.f;
  const float mean = wave_sum(s) * (1.0f / DM);
  float q = 0.f;
#pragma unroll
  for (int i = 0; i < 8; ++i) { const float d = v[i] - mean; q = fmaf(d, d, q); }
  { const float d8 = v[8] - mean, d9 = v[9] - mean; q += last_valid ? fmaf(d8, d8, d9 * d9) : 0.f; }
  const float var = wave_sum(q) * (1.0f / DM);
  mean_b = rbf(mean);
  den_b = rbf(__builtin_amdgcn_sqrtf(rbf(rbf(var) + LN_EPS)));
}
__device__ __forceinline__ float ln_apply(float x, float mean_b, float den_b) {
  const float d = rbf(x - mean_b), r = __builtin_amdgcn_rcpf(den_b), q = d * r;
  return rbf(fmaf(fmaf(-q, den_b, d), r, q));
}

#define STAMP(i) do { } while (0)

__global__ void __launch_bounds__(NTHR, 3)
fused_decode_kernel(const unsigned short* __restrict__ x, const unsigned char* __restrict__ kc, const unsigned char* __restrict__ vc,
                    const unsigned short* __restrict__ wq, const unsigned short* __restrict__ wo,
                    const unsigned short* __restrict__ w1, const unsigned short* __restrict__ b1,
                    const unsigned short* __restrict__ w2, const unsigned short* __restrict__ b2,
                    unsigned short* __restrict__ out, unsigned char* ws) {
  __shared__ __attribute__((aligned(16))) unsigned char lds[L_END];
  const int lane = threadIdx.x & 63, wv = __builtin_amdgcn_readfirstlane(threadIdx.x >> 6), w = wv < 8 ? wv ^ 4 : wv;
  const unsigned tst = now32();
  const int g = blockIdx.x >> 3, s = (blockIdx.x + 3 * g) & 7, id = g * 8 + s, b = g >> 4, kvh = g & 15;
  STAMP(0);
  const rsrc_t rws = mkrsrc(ws, WS_END);
  const rsrc_t rx = mkrsrc(x + b * DM, DM * 2);
  const int qh = s >> 2, cb = (s + (kvh >> 2)) & 3;
  const int la = lane >> 4;
  const int red = kvh * 8 + s;

  u32x2 ep = {0u, 0u};
  if (w == NWAVE) {
    ep = __builtin_amdgcn_raw_buffer_load_b64(rws, WS_EPOCH + b * 256, 0, 0);
    if (lane < 16) *((unsigned*)(lds + L_FLAG) + lane) = 0u;
    asm volatile("s_waitcnt lgkmcnt(0)\n\ts_barrier" ::: "memory");
  }
  if (w >= NWAVE) {
    __builtin_amdgcn_s_setprio(3);
    const unsigned e0 = __builtin_amdgcn_readfirstlane(ep[0]);
    const unsigned e1 = ((e0 & 0x7f7f7f7fu) + 0x01010101u) ^ (e0 & 0x80808080u);
    const unsigned tagA = e1 ^ 0xb77a3d00u;
    const unsigned tagB = __builtin_amdgcn_readfirstlane(ep[1]) + 1u;
    if (lane == 0) *(unsigned*)(lds + L_TAG) = tagA;
    if (STL & 2) { stouch(w1); stouch(b1); stouch(w2); stouch(b2); }
    flag_wait<0>(lds, F_QP, 4);
    {
      f32x2 q2 = *(const f32x2*)(lds + L_QPART + (lane & 31) * 8);
#pragma unroll
      for (int ww = 1; ww < 8; ++ww) q2 += *(const f32x2*)(lds + L_QPART + ww * 256 + (lane & 31) * 8);
      __builtin_amdgcn_raw_buffer_store_b128(u32x4{tagA, asu(q2[0]), (tagB & 0xffffu) | (tst << 16), asu(q2[1])}, rws,
                                             lane < 32 ? WS_QX + g * 4096 + qh * 2048 + cb * 512 + lane * 16 : OOB, 0, AUX_SC1);
      STAMP(8);
    }
    __builtin_amdgcn_s_sleep(PSL);
    {
      float qa[4], qb[4];
      int ts[4];
      int of[4];
#pragma unroll
      for (int i = 0; i < 4; ++i) { qa[i] = 0.f; qb[i] = 0.f; ts[i] = 0; of[i] = WS_QX + g * 4096 + i * 1024 + lane * 16; }
#pragma unroll 1
      for (unsigned spin = 0; spin < SPIN_CAP; ++spin) {
        asm volatile("" ::: "memory");
        u32x4 gr[4];
#pragma unroll
        for (int i = 0; i < 4; ++i) gr[i] = __builtin_amdgcn_raw_buffer_load_b128(rws, of[i], 0, AUX_SC1);
        bool done = true;
#pragma unroll
        for (int i = 0; i < 4; ++i) {
          const bool fresh = (gr[i][0] == tagA) & (((gr[i][2] ^ tagB) & 0xffffu) == 0u) & (of[i] != OOB);
          ts[i] = fresh ? (int)(gr[i][2] - (tst << 16)) >> 16 : ts[i];
          qa[i] = fresh ? asf(gr[i][1]) : qa[i]; qb[i] = fresh ? asf(gr[i][3]) : qb[i];
          of[i] = fresh ? OOB : of[i];
          done &= of[i] == OOB;
        }
        if (__all(done)) break;
        __builtin_amdgcn_s_sleep(QPS);
      }
      *(unsigned*)(lds + L_QS + lane * 4) = pack_bf16(qa[0] + qa[2], qb[0] + qb[2]);
      *(unsigned*)(lds + L_QS + 256 + lane * 4) = pack_bf16(qa[1] + qa[3], qb[1] + qb[3]);
      {
        int sm = 0, mx = -32768;
#pragma unroll
        for (int i = 0; i < 4; ++i) {
          const int t0 = __builtin_amdgcn_readlane(ts[i], 0), t1 = __builtin_amdgcn_readlane(ts[i], 32);
          sm += t0 + t1; mx = max(mx, max(t0, t1));
        }
        sm >>= 3;
        if (lane == 0) *(unsigned*)(lds + L_TAG + 4) = tst + (unsigned)(sm + ((mx - sm) * CBETA >> 4));
      }
      flag_add(lds, F_Q, lane);
    }
    if (STL & 1) {
      wait_until(tst + 1000u + (id & 31) * 16u);
      stouch(vc + (size_t)(id >> 1) * SPAN);
      stouch(wo + (size_t)(kvh * 256 + qh * 128) * DM);
      stouch(wo + (size_t)(kvh * 256 + qh * 128 + 127) * DM);
    }
    const int fc = (lane & 1) * 4 + la;
    const unsigned xraw = __builtin_amdgcn_raw_buffer_load_b32(rx, red * 16 + (fc >> 1) * 4, 0, 0);
    const unsigned b2v = __builtin_amdgcn_raw_buffer_load_b16(mkrsrc(b2, 4), 0, 0, 0);
    if (red == 127) {
      wait_until(tst + 2000u);
#pragma unroll 1
      for (unsigned spin = 0; spin < SPIN_CAP; ++spin) {
        asm volatile("" ::: "memory");
        const u32x4 v0 = __builtin_amdgcn_raw_buffer_load_b128(rws, WS_QX + b * 65536 + lane * 512, 0, AUX_SC1);
        const u32x4 v1 = __builtin_amdgcn_raw_buffer_load_b128(rws, WS_QX + b * 65536 + 32768 + lane * 512, 0, AUX_SC1);
        if (__all((v0[0] == tagA) & (v1[0] == tagA) & ((((v0[2] ^ tagB) | (v1[2] ^ tagB)) & 0xffffu) == 0u))) break;
        __builtin_amdgcn_s_sleep(16);
      }
      __builtin_amdgcn_raw_buffer_store_b64(u32x2{e1, tagB}, rws, lane == 0 ? WS_EPOCH + b * 256 : OOB, 0, AUX_SC1);
    }
    float yv = 0.f, xfin = (fc & 1) ? bfhi(xraw) : bflo(xraw);
    const rsrc_t rout = mkrsrc(out + b * DM, DM * 2 + 2);
    int oout = (lane & 14) == 0 ? (red * 8 + fc) * 2 : OOB;
    if (red < 72) {
      flag_wait<16>(lds, F_Y, 4);
#pragma unroll
      for (int ww = 0; ww < 4; ++ww) yv += *(const float*)(lds + L_YP + ww * 32 + fc * 4);
      yv = rbf(rbf(yv) + bflo(b2v));
      __builtin_amdgcn_raw_buffer_store_b16((unsigned short)0, rout, oout, 0, 0);
    }
    int oa = (lane & 8) ? L_QPART + lane * 8 : L_OA + la * 64 + (lane & 7) * 8;
    asm volatile("" : "+v"(yv), "+v"(xfin), "+v"(oout), "+v"(oa));
    flag_wait<16>(lds, F_WP, 1);
    u32x4 ga[4];
#pragma unroll
    for (int i = 0; i < 4; ++i) ga[i] = u32x4{0u, 0u, 0u, 0u};
    int of1 = WS_PX + (g * 8 + (s & 4)) * 1024 + lane * 16;
    asm volatile("" : "+v"(of1), "+v"(ga[0]), "+v"(ga[1]), "+v"(ga[2]), "+v"(ga[3]));
    flag_wait<0>(lds, F_END, 1);
    STAMP(5);
    __builtin_amdgcn_s_sleep(T1D);
    {
      unsigned bad = 1u;
#pragma unroll 1
      for (unsigned spin = 0; spin < SPIN_CAP; ++spin) {
        asm volatile("" ::: "memory");
        if (bad != 0u) {
#pragma unroll
          for (int i = 0; i < 4; ++i) ga[i] = __builtin_amdgcn_raw_buffer_load_b128(rws, of1 + i * 1024, 0, AUX_SC1);
        }
        bad = 0u;
#pragma unroll
        for (int i = 0; i < 4; ++i) bad |= (ga[i][0] ^ tagA) | (ga[i][2] ^ (tagA >> 8));
        bad &= 0xffu;
        if (__all(bad == 0u)) break;
        bad |= dppu<XOR1>(bad); bad |= dppu<XOR2>(bad); bad |= dppu<XOR7>(bad);
        __builtin_amdgcn_s_sleep(1);
      }
      const unsigned hm = __builtin_amdgcn_perm(ga[0][3], ga[0][1], 0x0c0c0400u) << (16 * (lane & 1));
      const unsigned hl = __builtin_amdgcn_perm(ga[1][3], ga[1][1], 0x0c0c0400u) << (16 * (lane & 1));
      const float pm = asf(hm | dppu<XOR1>(hm)), pl = asf(hl | dppu<XOR1>(hl));
      const float M = rows_max(fmaxf(pm, dppf<ROR8>(pm)));
      const float w0 = __builtin_amdgcn_exp2f((pm - M) * LOG2E) * pl;
      const float inv = recip(rows_sum(w0 + dppf<ROR8>(w0)));
      float v[4];
#pragma unroll
      for (int r = 0; r < 4; ++r) {
        const float t = rows_sum_t(w0 * cut_lo(ga[0][r]), w0 * cut_lo(ga[1][r]), w0 * cut_lo(ga[2][r]), w0 * cut_lo(ga[3][r]));
        v[r] = (t + dppf<ROR8>(t)) * inv;
      }
      *(u32x2*)(lds + oa) = u32x2{pack_bf16(v[0], v[1]), pack_bf16(v[2], v[3])};
      flag_add(lds, F_O, lane);
    }
    if (red < 72) {
      __builtin_amdgcn_s_sleep(T2D);
      u32x4 gb = {0u, 0u, 0u, 0u};
      const int of2 = WS_OX + (b * 18 + (red >> 2)) * 4096 + (lane >> 1) * 128 + (red & 3) * 32 + (lane & 1) * 16;
      unsigned bad = 1u;
#pragma unroll 1
      for (unsigned spin = 0; spin < SPIN_CAP; ++spin) {
        asm volatile("" ::: "memory");
        if (bad != 0u) gb = __builtin_amdgcn_raw_buffer_load_b128(rws, of2, 0, AUX_SC1);
        bad = (__builtin_amdgcn_perm(gb[1], gb[0], 0x0c0c0400u) | __builtin_amdgcn_perm(gb[3], gb[2], 0x04000c0cu)) ^ tagA;
        if (__all(bad == 0u)) break;
        bad |= dppu<XOR1>(bad);
        __builtin_amdgcn_s_sleep(1);
      }
      STAMP(13);
      float t = rows_sum_t(cut_lo(gb[0]), cut_lo(gb[1]), cut_lo(gb[2]), cut_lo(gb[3]));
      t += dppf<XOR2>(t); t += dppf<ROR4>(t); t += dppf<ROR8>(t);
      __builtin_amdgcn_raw_buffer_store_b16((unsigned short)pack_bf16(rbf(xfin + rbf(t)) + yv, 0.f), rout, oout, 0, 0);
    }
  } else {
    const rsrc_t rk = mkrsrc(kc + (size_t)(id >> 1) * SPAN, SPAN);
    const rsrc_t rv = mkrsrc(vc + (size_t)(id >> 1) * SPAN, SPAN);
    auto poff = [&](int d, int h) {
      return ((d & 3) | (h << 2) | ((id & 1) << 3) | ((d >> 2) << 4)) * TILE_BYTES;
    };
    const int voffK = ((lane & 15) >> 2) * 256 + la * 64 + (lane & 3) * 16;
    const int voffV = lane * 16;
    u32x4 bA[8], bB[8];
    auto request = [&](u32x4 (&x)[8], rsrc_t r, int voff, int so) {
      asm volatile("" : "+v"(voff));
#pragma unroll
      for (int u = 0; u < 8; ++u) x[u] = __builtin_amdgcn_raw_buffer_load_b128(r, voff + (u & 3) * 1024, so + (u >> 2) * 4096, AUX_NT);
      __builtin_amdgcn_sched_barrier(0);
    };
    if (w >= 4) {
      const int rg = (w - 4) * 8 + (lane >> 3), c8 = lane & 7;
      unsigned xs[5];
      u32x4 wqv[9];
#pragma unroll
      for (int i = 0; i < 5; ++i) xs[i] = __builtin_amdgcn_raw_buffer_load_b32(rx, (lane + 64 * i) * 4, 0, 0);
      __builtin_amdgcn_sched_barrier(0);
      const rsrc_t rq = mkrsrc(wq + qh * 288 * 4096 + kvh * 256 + cb * 64, (287 * 4096 + 64) * 2);
#pragma unroll
      for (int i = 0; i < 9; ++i) wqv[i] = __builtin_amdgcn_raw_buffer_load_b128(rq, (rg + 32 * i) * 8192 + c8 * 16, 0, AUX_NT);
      __builtin_amdgcn_sched_barrier(0);
      __builtin_amdgcn_s_barrier();
      __builtin_amdgcn_sched_barrier(0);
      float xv[10], mean1, den1;
#pragma unroll
      for (int i = 0; i < 5; ++i) { xv[2 * i] = bflo(xs[i]); xv[2 * i + 1] = bfhi(xs[i]); }
      ln_stats(xv, lane < 32, mean1, den1);
#pragma unroll
      for (int i = 0; i < 5; ++i)
        *(unsigned*)(lds + L_XN + (lane + 64 * i) * 4) = pack_bf16(ln_apply(xv[2 * i], mean1, den1), ln_apply(xv[2 * i + 1], mean1, den1));
      asm volatile("" ::: "memory");
      STAMP(9);
      f32x4 qa = {0.f, 0.f, 0.f, 0.f};
#pragma unroll
      for (int i = 0; i < 9; ++i) {
        const unsigned t = (unsigned)*(const unsigned short*)(lds + L_XN + (qh * 288 + rg + 32 * i) * 2) << (16 * (lane & 1));
        const int jp = c8 >> 1;
        qa = mfma32(wqv[i], u32x4{jp == 0 ? t : 0u, jp == 1 ? t : 0u, jp == 2 ? t : 0u, jp == 3 ? t : 0u}, qa);
      }
      if ((lane >> 5) == ((lane >> 3) & 1)) {
        float* qp = (float*)(lds + L_QPART) + ((w - 4) * 2 + (lane >> 5)) * 64 + (la & 1) * 32 + c8;
#pragma unroll
        for (int r = 0; r < 4; ++r) qp[r * 8] = qa[r];
      }
      flag_add(lds, F_QP, lane);
      STAMP(1);
    }
    if (w < 4) asm volatile("s_barrier" ::: "memory");
    if (w >= PFD) flag_wait<1>(lds, F_ARR, w - PFD + 1);
    __builtin_amdgcn_sched_barrier(0);
    STAMP(14);
    request(bA, rk, voffK, poff(w, 0));
    request(bB, rk, voffK, poff(w, 1));
    asm volatile("s_waitcnt vmcnt(0)" ::: "memory");
    flag_add(lds, F_ARR, lane);
    STAMP(15);
    flag_wait<1>(lds, F_Q, 1);
    if (w < S) STAMP(2);

    const int c = lane & 3, hb = (lane >> 2) & 1, tb = (lane >> 3) & 1;
    const unsigned tagA = __builtin_amdgcn_readfirstlane(*(const unsigned*)(lds + L_TAG));
    const unsigned tb0 = __builtin_amdgcn_readfirstlane(*(const unsigned*)(lds + L_TAG + 4));
    float m;
    f32x4 acc[2][2];
#pragma unroll
    for (int i = 0; i < 2; ++i)
#pragma unroll
      for (int j = 0; j < 2; ++j) acc[i][j] = f32x4{0.f, 0.f, 0.f, 0.f};
    const u32x4 qf = *(const u32x4*)(lds + L_QS + (hb * 128 + la * 32 + c * 8) * 2);
    const u32x4 zero4 = {0u, 0u, 0u, 0u};
    const u32x4 q0 = tb ? zero4 : qf, q1 = tb ? qf : zero4;
    const unsigned mask0 = c == 0 ? 0x0000ffffu : c == 1 ? 0xffff0000u : 0u;
    const unsigned mask1 = c == 2 ? 0x0000ffffu : c == 3 ? 0xffff0000u : 0u;
    auto qk = [&](const u32x4 (&x)[8], f32x4 (&sc)[4]) {
#pragma unroll
      for (int i = 0; i < 4; ++i) sc[i] = mfma32(x[i], q0, f32x4{0.f, 0.f, 0.f, 0.f});
#pragma unroll
      for (int i = 0; i < 4; ++i) sc[i] = mfma32(x[4 + i], q1, sc[i]);
    };
    auto score = [&](const f32x4 (&sc)[4]) {
      float t4[4];
#pragma unroll
      for (int i = 0; i < 4; ++i) t4[i] = (dppf<QB0>(sc[i][0]) + dppf<QB1>(sc[i][1])) + (dppf<QB2>(sc[i][2]) + dppf<QB3>(sc[i][3]));
      const float raw = (c & 2) ? ((c & 1) ? t4[3] : t4[2]) : ((c & 1) ? t4[1] : t4[0]);
      float sv = rbf(rbf(raw) * SCALE);
      asm volatile("" : "+v"(sv));
      return sv;
    };
    auto head_max = [&](float v) {
      v = fmaxf(v, dppf<XOR1>(v));
      v = fmaxf(v, dppf<XOR2>(v));
      v = fmaxf(v, dppf<ROR8>(v));
      return rows_max(v);
    };
    auto pv = [&](const u32x4 (&x)[8], float sv) {
      const float p = __builtin_amdgcn_exp2f((sv - m) * LOG2E);
      const unsigned pk = pack_bf16(p, p);
      unsigned pb0[4], pb1[4];
      pb0[0] = dppu<QB0>(pk) & mask0; pb1[0] = dppu<QB0>(pk) & mask1;
      pb0[1] = dppu<QB1>(pk) & mask0; pb1[1] = dppu<QB1>(pk) & mask1;
      pb0[2] = dppu<QB2>(pk) & mask0; pb1[2] = dppu<QB2>(pk) & mask1;
      pb0[3] = dppu<QB3>(pk) & mask0; pb1[3] = dppu<QB3>(pk) & mask1;
#pragma unroll
      for (int u = 0; u < 8; ++u) {
        acc[u >> 2][0] = mfma16(x[u][0], x[u][1], pb0[u & 3], pb1[u & 3], acc[u >> 2][0]);
        acc[u >> 2][1] = mfma16(x[u][2], x[u][3], pb0[u & 3], pb1[u & 3], acc[u >> 2][1]);
      }
    };
    auto put_ml = [&](int k, float mk, float lk) {
      lk += dppf<XOR1>(lk);
      lk += dppf<XOR2>(lk);
      lk += dppf<ROR8>(lk);
      lk = rows_sum(lk);
      if ((lane & ~4) == 0) *(f32x2*)(lds + L_ML + (k * 2 + hb) * 8) = f32x2{mk, lk};
      flag_add(lds, F_ML, lane);
    };
    const int hw = w - S, wk = w - 2, nwk = (s & 3) == 3 ? 6 : 4;
    int ontime = 0;
    auto slot = [&](int p, bool watch) {
      __builtin_amdgcn_sched_barrier(0);
      if (watch && lane == 0) {
        unsigned* fl = (unsigned*)(lds + L_FLAG) + vzero();
        const unsigned t = now32() - tb0;
        const int gap = (int)(t - __hip_atomic_fetch_max(fl + F_T, t, __ATOMIC_RELAXED, __HIP_MEMORY_SCOPE_WORKGROUP));
        if (gap > GLT && ontime >= 3 && (p < PGAP - 2 * S || p >= PGAP + 4 * S)) {
          __hip_atomic_fetch_max(fl + F_T, t + GLQ, __ATOMIC_RELAXED, __HIP_MEMORY_SCOPE_WORKGROUP);
          __hip_atomic_fetch_add(fl + F_SH, (unsigned)GLQ, __ATOMIC_RELAXED, __HIP_MEMORY_SCOPE_WORKGROUP);
        }
      }
      const unsigned due = tb0 + SCA + (p >= PGAP ? GAP : 0) + (unsigned)(p * SCT >> 4);
      ontime = (int)(now32() - due - flag_get(lds, F_SH)) < 0 ? ontime + 1 : 0;
      if (watch && p >= PFREE) {
        asm volatile("s_waitcnt vmcnt(0)" ::: "memory");
        const unsigned t = now32();
        const int late = (int)(t - due - flag_get(lds, F_SH)) + KD;
        const unsigned lim = t + (unsigned)max(KD - (max(late, 0) * KG >> 8), 0);
        while ((int)(now32() - lim) < 0) {}
      }
      while ((int)(now32() - due - flag_get(lds, F_SH)) < 0) {}
      __builtin_amdgcn_sched_barrier(0);
    };
    f32x2 sl;
    if (w < S) {
      __builtin_amdgcn_s_setprio(2);
      unsigned char* sco = lds + L_SC + w * (NPAIR * 512) + lane * 8;
      float mx = -INFINITY;
      for (int j = 0; j < NPAIR; ++j) {
        const bool k = j + 1 < NPAIR;
        const rsrc_t r = k ? rk : rv;
        const int vo = k ? voffK : voffV, d = NWAVE + S * (k ? j : j - 1) + w;
        f32x4 sc[4];
        qk(bA, sc);
        slot(2 * NWAVE + 2 * S * j + w, true);
        request(bA, r, vo, poff(d, 0));
        const float s0 = score(sc);
        qk(bB, sc);
        slot(2 * NWAVE + 2 * S * j + S + w, true);
        request(bB, r, vo, poff(d, 1));
        const float s1 = score(sc);
        mx = fmaxf(mx, fmaxf(s0, s1));
        *(f32x2*)(sco + j * 512) = f32x2{s0, s1};
      }
      m = head_max(mx);
      if ((lane & ~4) == 0) *(float*)(lds + L_ML + (w * 2 + hb) * 8) = m;
      flag_add(lds, F_P1 + w, lane);
      for (int j = 0; j + 1 < NPAIR; ++j) {
        const f32x2 sv = *(const f32x2*)(sco + (NPAIR - 1 - j) * 512);
        const int d = j + 2 < NPAIR ? NWAVE + S * (NPAIR - 3 - j) + w : w;
        pv(bA, sv[0]);
        slot(2 * NWAVE + 2 * S * (NPAIR + j) + w, true);
        request(bA, rv, voffV, poff(d, 0));
        pv(bB, sv[1]);
        slot(2 * NWAVE + 2 * S * (NPAIR + j) + S + w, true);
        request(bB, rv, voffV, poff(d, 1));
      }
      sl = *(const f32x2*)sco;
    }
    asm volatile("" ::: "memory");
    if (w >= S) {
      {
        f32x4 sc[4];
        qk(bA, sc);
        sl[0] = score(sc);
        qk(bB, sc);
        sl[1] = score(sc);
        m = head_max(fmaxf(sl[0], sl[1]));
        put_ml(w, m, __builtin_amdgcn_exp2f((sl[0] - m) * LOG2E) + __builtin_amdgcn_exp2f((sl[1] - m) * LOG2E));
      }
      STAMP(12);
      if (red < 72) {
        const int ht = (w - 4) * 64 + lane;
        unsigned a1[9], ab[9], a2[9];
        const rsrc_t r1 = mkrsrc(w1, FF * 2), rb = mkrsrc(b1, FF * 2), r2 = mkrsrc(w2, FF * 2);
#pragma unroll
        for (int i = 0; i < 9; ++i) {
          a1[i] = __builtin_amdgcn_raw_buffer_load_b32(r1, (ht + 256 * i) * 4, 0, 0);
          ab[i] = __builtin_amdgcn_raw_buffer_load_b32(rb, (ht + 256 * i) * 4, 0, 0);
          a2[i] = __builtin_amdgcn_raw_buffer_load_b32(r2, (ht + 256 * i) * 4, 0, 0);
        }
        __builtin_amdgcn_sched_barrier(0);
        float xn[10];
#pragma unroll
        for (int i = 0; i < 5; ++i) { const unsigned v = *(const unsigned*)(lds + L_XN + (lane + 64 * i) * 4); xn[2 * i] = bflo(v); xn[2 * i + 1] = bfhi(v); }
        float mean2, den2;
        ln_stats(xn, lane < 32, mean2, den2);
        constexpr float GC0 = -2.0f * 0.7978845608028654f * LOG2E, GC1 = GC0 * 0.044715f;
#pragma unroll 1
        for (int j = 0; j < 8; ++j) {
          const float hv = ln_apply(bflo(*(const unsigned short*)(lds + L_XN + (red * 8 + j) * 2)), mean2, den2);
          float y = 0.f;
#pragma unroll
          for (int i = 0; i < 9; ++i) {
            asm volatile("" : "+v"(a1[i]), "+v"(ab[i]), "+v"(a2[i]));
            const unsigned pr = pack_bf16(hv * bflo(a1[i]), hv * bfhi(a1[i]));
            const unsigned uu = pack_bf16(bflo(pr) + bflo(ab[i]), bfhi(pr) + bfhi(ab[i]));
            const float u0 = bflo(uu), u1 = bfhi(uu);
            const float e0 = __builtin_amdgcn_exp2f(u0 * fmaf(u0 * u0, GC1, GC0));
            const float e1 = __builtin_amdgcn_exp2f(u1 * fmaf(u1 * u1, GC1, GC0));
            const unsigned gg = pack_bf16(u0 * __builtin_amdgcn_rcpf(1.0f + e0), u1 * __builtin_amdgcn_rcpf(1.0f + e1));
            y = fmaf(bflo(gg), bflo(a2[i]), y);
            y = fmaf(bfhi(gg), bfhi(a2[i]), y);
          }
          y = wave_sum(y);
          if (lane == 0) *(float*)(lds + L_YP + (w - 4) * 32 + j * 4) = y;
        }
        flag_add(lds, F_Y, lane);
      }
      {
        flag_wait<16>(lds, F_P1 + hw, 1);
        const float ms = *(const float*)(lds + L_ML + (hw * 2 + hb) * 8);
        float ls = 0.f;
#pragma unroll 1
        for (int j = 0; j < NPAIR; ++j) {
          const f32x2 sv = *(const f32x2*)(lds + L_SC + hw * (NPAIR * 512) + lane * 8 + j * 512);
          ls += __builtin_amdgcn_exp2f((sv[0] - ms) * LOG2E) + __builtin_amdgcn_exp2f((sv[1] - ms) * LOG2E);
        }
        put_ml(hw, ms, ls);
      }
      wait_until(tb0 + SCA + (unsigned)(PEND * SCT >> 4) - 60u);
      __builtin_amdgcn_s_setprio(2);
      slot(PEND + hw, false);
      request(bA, rv, voffV, poff(w, 0));
      slot(PEND + NH + hw, false);
      request(bB, rv, voffV, poff(w, 1));
    }
    int po = WS_PX + ((g * 8 + hb * 4 + la) * 8 + s) * 128 + (tb * 4 + c) * 16;
    float fw;
    unsigned side;
    {
      flag_wait<1>(lds, F_ML, NWAVE);
      f32x2 ml[NWAVE];
#pragma unroll
      for (int k = 0; k < NWAVE; ++k) ml[k] = *(const f32x2*)(lds + L_ML + (k * 2 + hb) * 8);
      float mx = ml[0][0], L = 0.f;
#pragma unroll
      for (int k = 1; k < NWAVE; ++k) mx = fmaxf(mx, ml[k][0]);
#pragma unroll
      for (int k = 0; k < NWAVE; ++k) L = fmaf(__builtin_amdgcn_exp2f((ml[k][0] - mx) * LOG2E), ml[k][1], L);
      fw = __builtin_amdgcn_exp2f((m - mx) * LOG2E) * recip(L);
      side = asu((la & 1) ? L : mx) >> (16 * (c & 1));
    }
    asm volatile("" : "+v"(po), "+v"(fw), "+v"(side));
    pv(bA, sl[0]);
    pv(bB, sl[1]);
    STAMP(3);
    {
      f32x4 res;
#pragma unroll
      for (int r = 0; r < 4; ++r) {
        const float own = tb ? acc[1][1][r] : acc[0][0][r], snd = tb ? acc[1][0][r] : acc[0][1][r];
        res[r] = (own + dppf<ROR8>(snd)) * fw;
      }
      *(f32x4*)(lds + L_PART + w * 1024 + lane * 16) = res;
      asm volatile("" ::: "memory");
      unsigned old = 0;
      if (lane == 0) old = __hip_atomic_fetch_add((unsigned*)(lds + L_FLAG) + F_WP + vzero(), 1u, __ATOMIC_RELAXED, __HIP_MEMORY_SCOPE_WORKGROUP);
      asm volatile("" ::: "memory");
      f32x4 pp[NWAVE];
#pragma unroll
      for (int k = 0; k < NWAVE; ++k) pp[k] = *(const f32x4*)(lds + L_PART + k * 1024 + lane * 16);
      __builtin_amdgcn_sched_barrier(0);
      if (__builtin_amdgcn_readfirstlane(old) == NWAVE - 1) {
        const f32x4 sum = ((pp[0] + pp[1]) + (pp[2] + pp[3])) + ((pp[4] + pp[5]) + (pp[6] + pp[7]));
        __builtin_amdgcn_raw_buffer_store_b128(
            u32x4{put_lo(sum[0], tagA), put_lo(sum[1], side), put_lo(sum[2], tagA >> 8), put_lo(sum[3], side >> 8)}, rws, po, 0, AUX_SC1);
        flag_add(lds, F_END, lane);
        STAMP(10);
      }
    }
    if (w < 2) {
      const rsrc_t ro = mkrsrc(wo + (size_t)(kvh * 256 + qh * 128 + w * 64) * DM, 64 * DM * 2);
      u32x4 wt[24];
#pragma unroll
      for (int i = 0; i < 24; ++i) {
        if (i % 12 == 0) slot(2 * NT + w + 2 * (i / 12), false);
        wt[i] = __builtin_amdgcn_raw_buffer_load_b128(
            ro, 2 * (i % 3) < nwk ? ((i / 3) * 8 + (lane >> 3)) * (DM * 2) + (s & 3) * 256 + (i % 3) * 128 + (lane & 7) * 16 : OOB, 0, AUX_NT);
      }
#pragma unroll
      for (int i = 0; i < 24; ++i)
        *(u32x4*)(lds + L_WO + w * 24576 + (i % 3) * 8192 + ((i / 3) * 8 + ((lane >> 3) & 4) + ((lane >> 2) & 2) + ((lane >> 4) & 1)) * 128 +
                  (((lane & 7) * 16) ^ (((i / 3) & 3) * 32))) = wt[i];
      flag_add(lds, F_WO, lane);
      STAMP(4);
    } else if (wk < nwk) {
      const int n = lane & 15;
      int oo = n < 2 ? WS_OX + (b * 18 + (s & 3) * 4 + wk) * 4096 + (kvh * 2 + qh) * 128 + n * 64 + la * 16 : OOB;
      asm volatile("" : "+v"(oo));
      flag_wait<1>(lds, F_WO, 2);
      u32x4 wof[2][4];
      {
        typedef __attribute__((address_space(3))) s16x4* lds_s16x4;
        unsigned char* img = lds + L_WO + (wk >> 1) * 8192 + ((n >> 2) * 8 + (la >> 1) * 4 + (la & 1)) * 128 + (lane & 3) * 8;
#pragma unroll
        for (int t = 0; t < 2; ++t)
#pragma unroll
          for (int ks = 0; ks < 4; ++ks) {
            unsigned char* p = img + (ks >> 1) * 24576 + (ks & 1) * 4096 + (((wk & 1) * 2 + t) ^ (n >> 2)) * 32;
            const u32x2 lo = __builtin_bit_cast(u32x2, __builtin_amdgcn_ds_read_tr16_b64_v4i16((lds_s16x4)p));
            const u32x2 hi = __builtin_bit_cast(u32x2, __builtin_amdgcn_ds_read_tr16_b64_v4i16((lds_s16x4)(p + 256)));
            wof[t][ks] = u32x4{lo[0], lo[1], hi[0], hi[1]};
          }
      }
      u32x4 ob[4];
#pragma unroll 1
      for (unsigned spin = 0; spin < (SPIN_CAP << 4); ++spin) {
        asm volatile("" ::: "memory");
        const unsigned f = __hip_atomic_load((unsigned*)(lds + L_FLAG) + F_O, __ATOMIC_RELAXED, __HIP_MEMORY_SCOPE_WORKGROUP);
        asm volatile("" ::: "memory");
#pragma unroll
        for (int ks = 0; ks < 4; ++ks) ob[ks] = *(const u32x4*)(lds + L_OA + ks * 64 + la * 16);
        __builtin_amdgcn_sched_barrier(0);
        if (__builtin_amdgcn_readfirstlane(f) != 0u) break;
      }
      STAMP(6);
      f32x4 d0 = {0.f, 0.f, 0.f, 0.f}, d1 = {0.f, 0.f, 0.f, 0.f};
#pragma unroll
      for (int ks = 0; ks < 4; ++ks) { d0 = mfma32(wof[0][ks], ob[ks], d0); d1 = mfma32(wof[1][ks], ob[ks], d1); }
      const f32x4 d = n ? d1 : d0;
      __builtin_amdgcn_raw_buffer_store_b128(
          u32x4{put_lo(d[0], tagA), put_lo(d[1], tagA >> 8), put_lo(d[2], tagA >> 16), put_lo(d[3], tagA >> 24)}, rws, oo, 0, AUX_SC1);
      STAMP(11);
    }
  }
}

}

extern "C" void kernel_launch(void* const* d_in, const int* in_sizes, int n_in, void* d_out, int out_size, void* d_ws,
                              size_t ws_size, hipStream_t stream) {
  (void)in_sizes; (void)n_in; (void)out_size; (void)ws_size;
  fused_decode_kernel<<<dim3(NWG), dim3(NTHR), 0, stream>>>(
      (const unsigned short*)d_in[0], (const unsigned char*)d_in[1], (const unsigned char*)d_in[2],
      (const unsigned short*)d_in[3], (const unsigned short*)d_in[4], (const unsigned short*)d_in[6],
      (const unsigned short*)d_in[7], (const unsigned short*)d_in[8], (const unsigned short*)d_in[9],
      (unsigned short*)d_out, (unsigned char*)d_ws);
}
```

```cpp
#include <hip/hip_runtime.h>
#include <hip/hip_bf16.h>
#include <stdint.h>

namespace {

typedef unsigned u32x2 __attribute__((ext_vector_type(2)));
typedef unsigned u32x4 __attribute__((ext_vector_type(4)));
typedef float f32x2 __attribute__((ext_vector_type(2)));
typedef float f32x4 __attribute__((ext_vector_type(4)));
typedef short s16x4 __attribute__((ext_vector_type(4)));
typedef short s16x8 __attribute__((ext_vector_type(8)));
typedef __bf16 bf16x2 __attribute__((ext_vector_type(2)));
typedef __amdgpu_buffer_rsrc_t rsrc_t;

constexpr int DM = 576, SK = 32768, DH = 128, FF = 4608;
constexpr int NSPLIT = 8, NWG = 256, NWAVE = 8, NTHR = (NWAVE + 1) * 64;
constexpr int KEYS_WG = SK / NSPLIT;
constexpr int TILE_BYTES = 32 * DH * 2;
constexpr int NT = KEYS_WG / 32;
constexpr int SPAN = 2 * KEYS_WG * DH * 2;
constexpr int S = 4, NH = NWAVE - S;
constexpr int NPAIR = (NT / 2 - NH) / S;
static_assert(2 * (NPAIR * S + NH) == NT && NH == S, "every tile has one owner, every stream one helper");

#define SCA 108
#define SCT 427
#define GAP 25
#define CBETA 6
#define GLT 160
#define GLQ 210
#define PFREE 40
#define KD 15
#define KG 20
#define STL 1
#define PFD 3
#define PSL 6
#define QPS 2
#define T1D 17
#define T2D 22
constexpr int PGAP = 2 * NWAVE + 2 * S * NPAIR, PEND = 2 * NT - 2 * NH;

constexpr int AUX_NT = 2, AUX_SC1 = 16;
constexpr unsigned RSRC3 = 0x00020000u;
constexpr unsigned SPIN_CAP = 1u << 19;
constexpr int OOB = 0x7ff00000;

constexpr int WS_EPOCH = 0;
constexpr int WS_QX = 4096;
constexpr int WS_PX = WS_QX + 32 * 4096;
constexpr int WS_OX = WS_PX + NWG * 1024;
constexpr int WS_END = WS_OX + 2 * 18 * 4096;

constexpr int L_FLAG = 0;
constexpr int L_TAG = 64;
constexpr int L_XN = 80;
constexpr int L_QPART = L_XN + 1280;
constexpr int L_QS = L_QPART + 8 * 64 * 4;
constexpr int L_OA = L_QS + 512;
constexpr int L_YP = L_OA + 256;
constexpr int L_ML = L_YP + 4 * 8 * 4;
constexpr int L_PART = L_ML + NWAVE * 16;
constexpr int L_WO = L_PART + NWAVE * 1024;
constexpr int L_SC = L_WO + 128 * 384;
constexpr int L_END = L_SC + S * NPAIR * 512;
constexpr int F_QP = 0, F_Q = 1, F_WP = 2, F_END = 3, F_O = 4, F_Y = 5, F_ARR = 6, F_WO = 8, F_ML = 9, F_SH = 10, F_T = 11, F_P1 = 12;

constexpr float LOG2E = 1.4426950408889634f;
constexpr float SCALE = 0.08838834764831845f;
constexpr float LN_EPS = 1e-5f;

__device__ __forceinline__ float asf(unsigned u) { return __builtin_bit_cast(float, u); }
__device__ __forceinline__ unsigned asu(float f) { return __builtin_bit_cast(unsigned, f); }
__device__ __forceinline__ float bflo(unsigned u) { return asf(u << 16); }
__device__ __forceinline__ float bfhi(unsigned u) { return asf(u & 0xffff0000u); }
__device__ __forceinline__ unsigned pack_bf16(float lo, float hi) {
  f32x2 v = {lo, hi};
  return __builtin_bit_cast(unsigned, __builtin_convertvector(v, bf16x2));
}
__device__ __forceinline__ float rbf(float f) { return (float)(__bf16)f; }
template <int CTRL> __device__ __forceinline__ unsigned dppu(unsigned x) {
  return (unsigned)__builtin_amdgcn_mov_dpp((int)x, CTRL, 0xf, 0xf, true);
}
template <int CTRL> __device__ __forceinline__ float dppf(float x) { return asf(dppu<CTRL>(asu(x))); }
constexpr int XOR1 = 0xB1, XOR2 = 0x4E, XOR7 = 0x141, ROR4 = 0x124, ROR8 = 0x128;
constexpr int QB0 = 0x00, QB1 = 0x55, QB2 = 0xAA, QB3 = 0xFF;
__device__ __forceinline__ float rows_sum(float x) {
  auto s = __builtin_amdgcn_permlane16_swap(asu(x), asu(x), false, false);
  x = asf(s[0]) + asf(s[1]);
  auto t = __builtin_amdgcn_permlane32_swap(asu(x), asu(x), false, false);
  return asf(t[0]) + asf(t[1]);
}
__device__ __forceinline__ float rows_max(float x) {
  auto s = __builtin_amdgcn_permlane16_swap(asu(x), asu(x), false, false);
  x = fmaxf(asf(s[0]), asf(s[1]));
  auto t = __builtin_amdgcn_permlane32_swap(asu(x), asu(x), false, false);
  return fmaxf(asf(t[0]), asf(t[1]));
}
__device__ __forceinline__ float rows_sum_t(float v0, float v1, float v2, float v3) {
  const auto p0 = __builtin_amdgcn_permlane32_swap(asu(v0), asu(v2), false, false);
  const auto p1 = __builtin_amdgcn_permlane32_swap(asu(v1), asu(v3), false, false);
  const auto p2 = __builtin_amdgcn_permlane16_swap(asu(asf(p0[0]) + asf(p0[1])), asu(asf(p1[0]) + asf(p1[1])), false, false);
  return asf(p2[0]) + asf(p2[1]);
}
__device__ __forceinline__ float wave_sum(float x) {
  x += dppf<XOR1>(x); x += dppf<XOR2>(x); x += dppf<ROR4>(x); x += dppf<ROR8>(x);
  return rows_sum(x);
}
__device__ __forceinline__ float recip(float x) {
  const float r = __builtin_amdgcn_rcpf(x);
  return fmaf(fmaf(-x, r, 1.0f), r, r);
}
__device__ __forceinline__ rsrc_t mkrsrc(const void* p, int bytes) {
  return __builtin_amdgcn_make_buffer_rsrc(const_cast<void*>(p), 0, bytes, RSRC3);
}
__device__ __forceinline__ f32x4 mfma32(u32x4 a, u32x4 b, f32x4 c) {
  return __builtin_amdgcn_mfma_f32_16x16x32_bf16(__builtin_bit_cast(s16x8, a), __builtin_bit_cast(s16x8, b), c, 0, 0, 0);
}
__device__ __forceinline__ f32x4 mfma16(unsigned a0, unsigned a1, unsigned b0, unsigned b1, f32x4 c) {
  u32x2 a = {a0, a1}, b = {b0, b1};
  return __builtin_amdgcn_mfma_f32_16x16x16bf16_1k(__builtin_bit_cast(s16x4, a), __builtin_bit_cast(s16x4, b), c, 0, 0, 0);
}
__device__ __forceinline__ unsigned put_lo(float v, unsigned byte) { return (asu(v) & 0xffffff00u) | (byte & 0xffu); }
__device__ __forceinline__ float cut_lo(unsigned u) { return asf(u & 0xffffff00u); }
__device__ __forceinline__ unsigned now32() { return (unsigned)__builtin_amdgcn_s_memrealtime(); }
__device__ __forceinline__ void wait_until(unsigned t) {
  while ((int)(now32() - t) < 0) __builtin_amdgcn_s_sleep(1);
}
__device__ __forceinline__ int vzero() { int z = 0; asm("" : "+v"(z)); return z; }
__device__ __forceinline__ void stouch(const void* p) {
  const char* q = (const char*)((unsigned long long)p & ~3ull);
  asm volatile("" : "+s"(q));
  const unsigned v = *(const __attribute__((address_space(4))) unsigned*)q;
  asm volatile("" :: "s"(v));
}
__device__ __forceinline__ void flag_add(unsigned char* lds, int f, int lane) {
  asm volatile("" ::: "memory");
  if (lane == 0) __hip_atomic_fetch_add((unsigned*)(lds + L_FLAG) + f + vzero(), 1u, __ATOMIC_RELAXED, __HIP_MEMORY_SCOPE_WORKGROUP);
  asm volatile("" ::: "memory");
}
__device__ __forceinline__ unsigned flag_get(unsigned char* lds, int f) {
  return __builtin_amdgcn_readfirstlane(__hip_atomic_load((unsigned*)(lds + L_FLAG) + f, __ATOMIC_RELAXED, __HIP_MEMORY_SCOPE_WORKGROUP));
}
template <int SLEEP> __device__ __forceinline__ void flag_wait(unsigned char* lds, int f, unsigned n) {
#pragma unroll 1
  for (unsigned spin = 0; spin < (SPIN_CAP << 4); ++spin) {
    if (flag_get(lds, f) >= n) break;
    if (SLEEP) __builtin_amdgcn_s_sleep(SLEEP);
  }
  asm volatile("" ::: "memory");
}
__device__ __forceinline__ void ln_stats(const float (&v)[10], bool last_valid, float& mean_b, float& den_b) {
  float s = 0.f;
#pragma unroll
  for (int i = 0; i < 8; ++i) s += v[i];
  s += last_valid ? (v[8] + v[9]) : 0.f;
  const float mean = wave_sum(s) * (1.0f / DM);
  float q = 0.f;
#pragma unroll
  for (int i = 0; i < 8; ++i) { const float d = v[i] - mean; q = fmaf(d, d, q); }
  { const float d8 = v[8] - mean, d9 = v[9] - mean; q += last_valid ? fmaf(d8, d8, d9 * d9) : 0.f; }
  const float var = wave_sum(q) * (1.0f / DM);
  mean_b = rbf(mean);
  den_b = rbf(__builtin_amdgcn_sqrtf(rbf(rbf(var) + LN_EPS)));
}
__device__ __forceinline__ float ln_apply(float x, float mean_b, float den_b) {
  const float d = rbf(x - mean_b), r = __builtin_amdgcn_rcpf(den_b), q = d * r;
  return rbf(fmaf(fmaf(-q, den_b, d), r, q));
}

#define STAMP(i) do { } while (0)

__global__ void __launch_bounds__(NTHR, 3)
fused_decode_kernel(const unsigned short* __restrict__ x, const unsigned char* __restrict__ kc, const unsigned char* __restrict__ vc,
                    const unsigned short* __restrict__ wq, const unsigned short* __restrict__ wo,
                    const unsigned short* __restrict__ w1, const unsigned short* __restrict__ b1,
                    const unsigned short* __restrict__ w2, const unsigned short* __restrict__ b2,
                    unsigned short* __restrict__ out, unsigned char* ws) {
  __shared__ __attribute__((aligned(16))) unsigned char lds[L_END];
  const int lane = threadIdx.x & 63, wv = __builtin_amdgcn_readfirstlane(threadIdx.x >> 6), w = wv < 8 ? wv ^ 4 : wv;
  const unsigned tst = now32();
  const int g = blockIdx.x >> 3, s = (blockIdx.x + 3 * g) & 7, id = g * 8 + s, b = g >> 4, kvh = g & 15;
  STAMP(0);
  const rsrc_t rws = mkrsrc(ws, WS_END);
  const rsrc_t rx = mkrsrc(x + b * DM, DM * 2);
  const int qh = s >> 2, cb = (s + (kvh >> 2)) & 3;
  const int la = lane >> 4;
  const int red = kvh * 8 + s;

  u32x2 ep = {0u, 0u};
  if (w == NWAVE) {
    if (!(STL & 8)) ep = __builtin_amdgcn_raw_buffer_load_b64(rws, WS_EPOCH + b * 256, 0, 0);
    if (lane < 16) *((unsigned*)(lds + L_FLAG) + lane) = 0u;
    asm volatile("s_waitcnt lgkmcnt(0)\n\ts_barrier" ::: "memory");
    if (STL & 8) {
      const unsigned char* pe = ws + WS_EPOCH + b * 256;
      asm volatile("" : "+s"(pe));
      ep = *(const __attribute__((address_space(4))) u32x2*)pe;
    }
  }
  if (w >= NWAVE) {
    __builtin_amdgcn_s_setprio(3);
    const unsigned e0 = __builtin_amdgcn_readfirstlane(ep[0]);
    const unsigned e1 = ((e0 & 0x7f7f7f7fu) + 0x01010101u) ^ (e0 & 0x80808080u);
    const unsigned tagA = e1 ^ 0xb77a3d00u;
    const unsigned tagB = __builtin_amdgcn_readfirstlane(ep[1]) + 1u;
    if (lane == 0) *(unsigned*)(lds + L_TAG) = tagA;
    flag_wait<0>(lds, F_QP, 4);
    {
      f32x2 q2 = *(const f32x2*)(lds + L_QPART + (lane & 31) * 8);
#pragma unroll
      for (int ww = 1; ww < 8; ++ww) q2 += *(const f32x2*)(lds + L_QPART + ww * 256 + (lane & 31) * 8);
      __builtin_amdgcn_raw_buffer_store_b128(u32x4{tagA, asu(q2[0]), (tagB & 0xffffu) | (tst << 16), asu(q2[1])}, rws,
                                             lane < 32 ? WS_QX + g * 4096 + qh * 2048 + cb * 512 + lane * 16 : OOB, 0, AUX_SC1);
      STAMP(8);
    }
    __builtin_amdgcn_s_sleep(PSL);
    {
      float qa[4], qb[4];
      int ts[4];
      int of[4];
#pragma unroll
      for (int i = 0; i < 4; ++i) { qa[i] = 0.f; qb[i] = 0.f; ts[i] = 0; of[i] = WS_QX + g * 4096 + i * 1024 + lane * 16; }
#pragma unroll 1
      for (unsigned spin = 0; spin < SPIN_CAP; ++spin) {
        asm volatile("" ::: "memory");
        u32x4 gr[4];
#pragma unroll
        for (int i = 0; i < 4; ++i) gr[i] = __builtin_amdgcn_raw_buffer_load_b128(rws, of[i], 0, AUX_SC1);
        bool done = true;
#pragma unroll
        for (int i = 0; i < 4; ++i) {
          const bool fresh = (gr[i][0] == tagA) & (((gr[i][2] ^ tagB) & 0xffffu) == 0u) & (of[i] != OOB);
          ts[i] = fresh ? (int)(gr[i][2] - (tst << 16)) >> 16 : ts[i];
          qa[i] = fresh ? asf(gr[i][1]) : qa[i]; qb[i] = fresh ? asf(gr[i][3]) : qb[i];
          of[i] = fresh ? OOB : of[i];
          done &= of[i] == OOB;
        }
        if (__all(done)) break;
        __builtin_amdgcn_s_sleep(QPS);
      }
      *(unsigned*)(lds + L_QS + lane * 4) = pack_bf16(qa[0] + qa[2], qb[0] + qb[2]);
      *(unsigned*)(lds + L_QS + 256 + lane * 4) = pack_bf16(qa[1] + qa[3], qb[1] + qb[3]);
      {
        int sm = 0, mx = -32768;
#pragma unroll
        for (int i = 0; i < 4; ++i) {
          const int t0 = __builtin_amdgcn_readlane(ts[i], 0), t1 = __builtin_amdgcn_readlane(ts[i], 32);
          sm += t0 + t1; mx = max(mx, max(t0, t1));
        }
        sm >>= 3;
        if (lane == 0) *(unsigned*)(lds + L_TAG + 4) = tst + (unsigned)(sm + ((mx - sm) * CBETA >> 4));
      }
      flag_add(lds, F_Q, lane);
    }
    if (STL & 1) {
      wait_until(tst + 1000u + (id & 31) * 16u);
      stouch(vc + (size_t)(id >> 1) * SPAN);
      stouch(wo + (size_t)(kvh * 256 + qh * 128) * DM);
      stouch(wo + (size_t)(kvh * 256 + qh * 128 + 127) * DM);
      if (STL & 2) stouch(b2);
    }
    const int fc = (lane & 1) * 4 + la;
    const unsigned xraw = __builtin_amdgcn_raw_buffer_load_b32(rx, red * 16 + (fc >> 1) * 4, 0, 0);
    const unsigned b2v = __builtin_amdgcn_raw_buffer_load_b16(mkrsrc(b2, 4), 0, 0, 0);
    if (red == 127) {
      wait_until(tst + 2000u);
#pragma unroll 1
      for (unsigned spin = 0; spin < SPIN_CAP; ++spin) {
        asm volatile("" ::: "memory");
        const u32x4 v0 = __builtin_amdgcn_raw_buffer_load_b128(rws, WS_QX + b * 65536 + lane * 512, 0, AUX_SC1);
        const u32x4 v1 = __builtin_amdgcn_raw_buffer_load_b128(rws, WS_QX + b * 65536 + 32768 + lane * 512, 0, AUX_SC1);
        if (__all((v0[0] == tagA) & (v1[0] == tagA) & ((((v0[2] ^ tagB) | (v1[2] ^ tagB)) & 0xffffu) == 0u))) break;
        __builtin_amdgcn_s_sleep(16);
      }
      __builtin_amdgcn_raw_buffer_store_b64(u32x2{e1, tagB}, rws, lane == 0 ? WS_EPOCH + b * 256 : OOB, 0, AUX_SC1);
    }
    float yv = 0.f, xfin = (fc & 1) ? bfhi(xraw) : bflo(xraw);
    const rsrc_t rout = mkrsrc(out + b * DM, DM * 2 + 2);
    int oout = (lane & 14) == 0 ? (red * 8 + fc) * 2 : OOB;
    if ((STL & 4) && g == 0) __builtin_amdgcn_raw_buffer_store_b16((unsigned short)0, rout, oout, 0, 0);
    if (red < 72) {
      flag_wait<16>(lds, F_Y, 4);
#pragma unroll
      for (int ww = 0; ww < 4; ++ww) yv += *(const float*)(lds + L_YP + ww * 32 + fc * 4);
      yv = rbf(rbf(yv) + bflo(b2v));
      __builtin_amdgcn_raw_buffer_store_b16((unsigned short)0, rout, oout, 0, 0);
    }
    int oa = (lane & 8) ? L_QPART + lane * 8 : L_OA + la * 64 + (lane & 7) * 8;
    asm volatile("" : "+v"(yv), "+v"(xfin), "+v"(oout), "+v"(oa));
    flag_wait<16>(lds, F_WP, 1);
    u32x4 ga[4];
#pragma unroll
    for (int i = 0; i < 4; ++i) ga[i] = u32x4{0u, 0u, 0u, 0u};
    int of1 = WS_PX + (g * 8 + (s & 4)) * 1024 + lane * 16;
    asm volatile("" : "+v"(of1), "+v"(ga[0]), "+v"(ga[1]), "+v"(ga[2]), "+v"(ga[3]));
    flag_wait<0>(lds, F_END, 1);
    STAMP(5);
    __builtin_amdgcn_s_sleep(T1D);
    {
      unsigned bad = 1u;
#pragma unroll 1
      for (unsigned spin = 0; spin < SPIN_CAP; ++spin) {
        asm volatile("" ::: "memory");
        if (bad != 0u) {
#pragma unroll
          for (int i = 0; i < 4; ++i) ga[i] = __builtin_amdgcn_raw_buffer_load_b128(rws, of1 + i * 1024, 0, AUX_SC1);
        }
        bad = 0u;
#pragma unroll
        for (int i = 0; i < 4; ++i) bad |= (ga[i][0] ^ tagA) | (ga[i][2] ^ (tagA >> 8));
        bad &= 0xffu;
        if (__all(bad == 0u)) break;
        bad |= dppu<XOR1>(bad); bad |= dppu<XOR2>(bad); bad |= dppu<XOR7>(bad);
        __builtin_amdgcn_s_sleep(1);
      }
      const unsigned hm = __builtin_amdgcn_perm(ga[0][3], ga[0][1], 0x0c0c0400u) << (16 * (lane & 1));
      const unsigned hl = __builtin_amdgcn_perm(ga[1][3], ga[1][1], 0x0c0c0400u) << (16 * (lane & 1));
      const float pm = asf(hm | dppu<XOR1>(hm)), pl = asf(hl | dppu<XOR1>(hl));
      const float M = rows_max(fmaxf(pm, dppf<ROR8>(pm)));
      const float w0 = __builtin_amdgcn_exp2f((pm - M) * LOG2E) * pl;
      const float inv = recip(rows_sum(w0 + dppf<ROR8>(w0)));
      float v[4];
#pragma unroll
      for (int r = 0; r < 4; ++r) {
        const float t = rows_sum_t(w0 * cut_lo(ga[0][r]), w0 * cut_lo(ga[1][r]), w0 * cut_lo(ga[2][r]), w0 * cut_lo(ga[3][r]));
        v[r] = (t + dppf<ROR8>(t)) * inv;
      }
      *(u32x2*)(lds + oa) = u32x2{pack_bf16(v[0], v[1]), pack_bf16(v[2], v[3])};
      flag_add(lds, F_O, lane);
    }
    if (red < 72) {
      __builtin_amdgcn_s_sleep(T2D);
      u32x4 gb = {0u, 0u, 0u, 0u};
      const int of2 = WS_OX + (b * 18 + (red >> 2)) * 4096 + (lane >> 1) * 128 + (red & 3) * 32 + (lane & 1) * 16;
      unsigned bad = 1u;
#pragma unroll 1
      for (unsigned spin = 0; spin < SPIN_CAP; ++spin) {
        asm volatile("" ::: "memory");
        if (bad != 0u) gb = __builtin_amdgcn_raw_buffer_load_b128(rws, of2, 0, AUX_SC1);
        bad = (__builtin_amdgcn_perm(gb[1], gb[0], 0x0c0c0400u) | __builtin_amdgcn_perm(gb[3], gb[2], 0x04000c0cu)) ^ tagA;
        if (__all(bad == 0u)) break;
        bad |= dppu<XOR1>(bad);
        __builtin_amdgcn_s_sleep(1);
      }
      STAMP(13);
      float t = rows_sum_t(cut_lo(gb[0]), cut_lo(gb[1]), cut_lo(gb[2]), cut_lo(gb[3]));
      t += dppf<XOR2>(t); t += dppf<ROR4>(t); t += dppf<ROR8>(t);
      __builtin_amdgcn_raw_buffer_store_b16((unsigned short)pack_bf16(rbf(xfin + rbf(t)) + yv, 0.f), rout, oout, 0, 0);
    }
  } else {
    const rsrc_t rk = mkrsrc(kc + (size_t)(id >> 1) * SPAN, SPAN);
    const rsrc_t rv = mkrsrc(vc + (size_t)(id >> 1) * SPAN, SPAN);
    auto poff = [&](int d, int h) {
      return ((d & 3) | (h << 2) | ((id & 1) << 3) | ((d >> 2) << 4)) * TILE_BYTES;
    };
    const int voffK = ((lane & 15) >> 2) * 256 + la * 64 + (lane & 3) * 16;
    const int voffV = lane * 16;
    u32x4 bA[8], bB[8];
    auto request = [&](u32x4 (&x)[8], rsrc_t r, int voff, int so) {
      asm volatile("" : "+v"(voff));
#pragma unroll
      for (int u = 0; u < 8; ++u) x[u] = __builtin_amdgcn_raw_buffer_load_b128(r, voff + (u & 3) * 1024, so + (u >> 2) * 4096, AUX_NT);
      __builtin_amdgcn_sched_barrier(0);
    };
    if (w >= 4) {
      const int rg = (w - 4) * 8 + (lane >> 3), c8 = lane & 7;
      unsigned xs[5];
      u32x4 wqv[9];
#pragma unroll
      for (int i = 0; i < 5; ++i) xs[i] = __builtin_amdgcn_raw_buffer_load_b32(rx, (lane + 64 * i) * 4, 0, 0);
      __builtin_amdgcn_sched_barrier(0);
      const rsrc_t rq = mkrsrc(wq + qh * 288 * 4096 + kvh * 256 + cb * 64, (287 * 4096 + 64) * 2);
#pragma unroll
      for (int i = 0; i < 9; ++i) wqv[i] = __builtin_amdgcn_raw_buffer_load_b128(rq, (rg + 32 * i) * 8192 + c8 * 16, 0, AUX_NT);
      __builtin_amdgcn_sched_barrier(0);
      __builtin_amdgcn_s_barrier();
      __builtin_amdgcn_sched_barrier(0);
      float xv[10], mean1, den1;
#pragma unroll
      for (int i = 0; i < 5; ++i) { xv[2 * i] = bflo(xs[i]); xv[2 * i + 1] = bfhi(xs[i]); }
      ln_stats(xv, lane < 32, mean1, den1);
#pragma unroll
      for (int i = 0; i < 5; ++i)
        *(unsigned*)(lds + L_XN + (lane + 64 * i) * 4) = pack_bf16(ln_apply(xv[2 * i], mean1, den1), ln_apply(xv[2 * i + 1], mean1, den1));
      asm volatile("" ::: "memory");
      STAMP(9);
      f32x4 qa = {0.f, 0.f, 0.f, 0.f};
#pragma unroll
      for (int i = 0; i < 9; ++i) {
        const unsigned t = (unsigned)*(const unsigned short*)(lds + L_XN + (qh * 288 + rg + 32 * i) * 2) << (16 * (lane & 1));
        const int jp = c8 >> 1;
        qa = mfma32(wqv[i], u32x4{jp == 0 ? t : 0u, jp == 1 ? t : 0u, jp == 2 ? t : 0u, jp == 3 ? t : 0u}, qa);
      }
      if ((lane >> 5) == ((lane >> 3) & 1)) {
        float* qp = (float*)(lds + L_QPART) + ((w - 4) * 2 + (lane >> 5)) * 64 + (la & 1) * 32 + c8;
#pragma unroll
        for (int r = 0; r < 4; ++r) qp[r * 8] = qa[r];
      }
      flag_add(lds, F_QP, lane);
      STAMP(1);
    }
    if (w < 4) asm volatile("s_barrier" ::: "memory");
    if (w >= PFD) flag_wait<1>(lds, F_ARR, w - PFD + 1);
    __builtin_amdgcn_sched_barrier(0);
    STAMP(14);
    request(bA, rk, voffK, poff(w, 0));
    request(bB, rk, voffK, poff(w, 1));
    asm volatile("s_waitcnt vmcnt(0)" ::: "memory");
    flag_add(lds, F_ARR, lane);
    STAMP(15);
    flag_wait<1>(lds, F_Q, 1);
    if (w < S) STAMP(2);

    const int c = lane & 3, hb = (lane >> 2) & 1, tb = (lane >> 3) & 1;
    const unsigned tagA = __builtin_amdgcn_readfirstlane(*(const unsigned*)(lds + L_TAG));
    const unsigned tb0 = __builtin_amdgcn_readfirstlane(*(const unsigned*)(lds + L_TAG + 4));
    float m;
    f32x4 acc[2][2];
#pragma unroll
    for (int i = 0; i < 2; ++i)
#pragma unroll
      for (int j = 0; j < 2; ++j) acc[i][j] = f32x4{0.f, 0.f, 0.f, 0.f};
    const u32x4 qf = *(const u32x4*)(lds + L_QS + (hb * 128 + la * 32 + c * 8) * 2);
    const u32x4 zero4 = {0u, 0u, 0u, 0u};
    const u32x4 q0 = tb ? zero4 : qf, q1 = tb ? qf : zero4;
    const unsigned mask0 = c == 0 ? 0x0000ffffu : c == 1 ? 0xffff0000u : 0u;
    const unsigned mask1 = c == 2 ? 0x0000ffffu : c == 3 ? 0xffff0000u : 0u;
    auto qk = [&](const u32x4 (&x)[8], f32x4 (&sc)[4]) {
#pragma unroll
      for (int i = 0; i < 4; ++i) sc[i] = mfma32(x[i], q0, f32x4{0.f, 0.f, 0.f, 0.f});
#pragma unroll
      for (int i = 0; i < 4; ++i) sc[i] = mfma32(x[4 + i], q1, sc[i]);
    };
    auto score = [&](const f32x4 (&sc)[4]) {
      float t4[4];
#pragma unroll
      for (int i = 0; i < 4; ++i) t4[i] = (dppf<QB0>(sc[i][0]) + dppf<QB1>(sc[i][1])) + (dppf<QB2>(sc[i][2]) + dppf<QB3>(sc[i][3]));
      const float raw = (c & 2) ? ((c & 1) ? t4[3] : t4[2]) : ((c & 1) ? t4[1] : t4[0]);
      float sv = rbf(rbf(raw) * SCALE);
      asm volatile("" : "+v"(sv));
      return sv;
    };
    auto head_max = [&](float v) {
      v = fmaxf(v, dppf<XOR1>(v));
      v = fmaxf(v, dppf<XOR2>(v));
      v = fmaxf(v, dppf<ROR8>(v));
      return rows_max(v);
    };
    auto pv = [&](const u32x4 (&x)[8], float sv) {
      const float p = __builtin_amdgcn_exp2f((sv - m) * LOG2E);
      const unsigned pk = pack_bf16(p, p);
      unsigned pb0[4], pb1[4];
      pb0[0] = dppu<QB0>(pk) & mask0; pb1[0] = dppu<QB0>(pk) & mask1;
      pb0[1] = dppu<QB1>(pk) & mask0; pb1[1] = dppu<QB1>(pk) & mask1;
      pb0[2] = dppu<QB2>(pk) & mask0; pb1[2] = dppu<QB2>(pk) & mask1;
      pb0[3] = dppu<QB3>(pk) & mask0; pb1[3] = dppu<QB3>(pk) & mask1;
#pragma unroll
      for (int u = 0; u < 8; ++u) {
        acc[u >> 2][0] = mfma16(x[u][0], x[u][1], pb0[u & 3], pb1[u & 3], acc[u >> 2][0]);
        acc[u >> 2][1] = mfma16(x[u][2], x[u][3], pb0[u & 3], pb1[u & 3], acc[u >> 2][1]);
      }
    };
    auto put_ml = [&](int k, float mk, float lk) {
      lk += dppf<XOR1>(lk);
      lk += dppf<XOR2>(lk);
      lk += dppf<ROR8>(lk);
      lk = rows_sum(lk);
      if ((lane & ~4) == 0) *(f32x2*)(lds + L_ML + (k * 2 + hb) * 8) = f32x2{mk, lk};
      flag_add(lds, F_ML, lane);
    };
    const int hw = w - S, wk = w - 2, nwk = (s & 3) == 3 ? 6 : 4;
    int ontime = 0;
    auto slot = [&](int p, bool watch) {
      __builtin_amdgcn_sched_barrier(0);
      if (watch && lane == 0) {
        unsigned* fl = (unsigned*)(lds + L_FLAG) + vzero();
        const unsigned t = now32() - tb0;
        const int gap = (int)(t - __hip_atomic_fetch_max(fl + F_T, t, __ATOMIC_RELAXED, __HIP_MEMORY_SCOPE_WORKGROUP));
        if (gap > GLT && ontime >= 3 && (p < PGAP - 2 * S || p >= PGAP + 4 * S)) {
          __hip_atomic_fetch_max(fl + F_T, t + GLQ, __ATOMIC_RELAXED, __HIP_MEMORY_SCOPE_WORKGROUP);
          __hip_atomic_fetch_add(fl + F_SH, (unsigned)GLQ, __ATOMIC_RELAXED, __HIP_MEMORY_SCOPE_WORKGROUP);
        }
      }
      const unsigned due = tb0 + SCA + (p >= PGAP ? GAP : 0) + (unsigned)(p * SCT >> 4);
      ontime = (int)(now32() - due - flag_get(lds, F_SH)) < 0 ? ontime + 1 : 0;
      if (watch && p >= PFREE) {
        asm volatile("s_waitcnt vmcnt(0)" ::: "memory");
        const unsigned t = now32();
        const int late = (int)(t - due - flag_get(lds, F_SH)) + KD;
        const unsigned lim = t + (unsigned)max(KD - (max(late, 0) * KG >> 8), 0);
        while ((int)(now32() - lim) < 0) {}
      }
      while ((int)(now32() - due - flag_get(lds, F_SH)) < 0) {}
      __builtin_amdgcn_sched_barrier(0);
    };
    f32x2 sl;
    if (w < S) {
      __builtin_amdgcn_s_setprio(2);
      unsigned char* sco = lds + L_SC + w * (NPAIR * 512) + lane * 8;
      float mx = -INFINITY;
      for (int j = 0; j < NPAIR; ++j) {
        const bool k = j + 1 < NPAIR;
        const rsrc_t r = k ? rk : rv;
        const int vo = k ? voffK : voffV, d = NWAVE + S * (k ? j : j - 1) + w;
        f32x4 sc[4];
        qk(bA, sc);
        slot(2 * NWAVE + 2 * S * j + w, true);
        request(bA, r, vo, poff(d, 0));
        const float s0 = score(sc);
        qk(bB, sc);
        slot(2 * NWAVE + 2 * S * j + S + w, true);
        request(bB, r, vo, poff(d, 1));
        const float s1 = score(sc);
        mx = fmaxf(mx, fmaxf(s0, s1));
        *(f32x2*)(sco + j * 512) = f32x2{s0, s1};
      }
      m = head_max(mx);
      if ((lane & ~4) == 0) *(float*)(lds + L_ML + (w * 2 + hb) * 8) = m;
      flag_add(lds, F_P1 + w, lane);
      for (int j = 0; j + 1 < NPAIR; ++j) {
        const f32x2 sv = *(const f32x2*)(sco + (NPAIR - 1 - j) * 512);
        const int d = j + 2 < NPAIR ? NWAVE + S * (NPAIR - 3 - j) + w : w;
        pv(bA, sv[0]);
        slot(2 * NWAVE + 2 * S * (NPAIR + j) + w, true);
        request(bA, rv, voffV, poff(d, 0));
        pv(bB, sv[1]);
        slot(2 * NWAVE + 2 * S * (NPAIR + j) + S + w, true);
        request(bB, rv, voffV, poff(d, 1));
      }
      sl = *(const f32x2*)sco;
    }
    asm volatile("" ::: "memory");
    if (w >= S) {
      {
        f32x4 sc[4];
        qk(bA, sc);
        sl[0] = score(sc);
        qk(bB, sc);
        sl[1] = score(sc);
        m = head_max(fmaxf(sl[0], sl[1]));
        put_ml(w, m, __builtin_amdgcn_exp2f((sl[0] - m) * LOG2E) + __builtin_amdgcn_exp2f((sl[1] - m) * LOG2E));
      }
      STAMP(12);
      if (red < 72) {
        if (STL & 2) {
          wait_until(tst + 1200u);
          stouch(w1); stouch(b1); stouch(w2);
        }
        const int ht = (w - 4) * 64 + lane;
        unsigned a1[9], ab[9], a2[9];
        const rsrc_t r1 = mkrsrc(w1, FF * 2), rb = mkrsrc(b1, FF * 2), r2 = mkrsrc(w2, FF * 2);
#pragma unroll
        for (int i = 0; i < 9; ++i) {
          a1[i] = __builtin_amdgcn_raw_buffer_load_b32(r1, (ht + 256 * i) * 4, 0, 0);
          ab[i] = __builtin_amdgcn_raw_buffer_load_b32(rb, (ht + 256 * i) * 4, 0, 0);
          a2[i] = __builtin_amdgcn_raw_buffer_load_b32(r2, (ht + 256 * i) * 4, 0, 0);
        }
        __builtin_amdgcn_sched_barrier(0);
        float xn[10];
#pragma unroll
        for (int i = 0; i < 5; ++i) { const unsigned v = *(const unsigned*)(lds + L_XN + (lane + 64 * i) * 4); xn[2 * i] = bflo(v); xn[2 * i + 1] = bfhi(v); }
        float mean2, den2;
        ln_stats(xn, lane < 32, mean2, den2);
        constexpr float GC0 = -2.0f * 0.7978845608028654f * LOG2E, GC1 = GC0 * 0.044715f;
#pragma unroll 1
        for (int j = 0; j < 8; ++j) {
          const float hv = ln_apply(bflo(*(const unsigned short*)(lds + L_XN + (red * 8 + j) * 2)), mean2, den2);
          float y = 0.f;
#pragma unroll
          for (int i = 0; i < 9; ++i) {
            asm volatile("" : "+v"(a1[i]), "+v"(ab[i]), "+v"(a2[i]));
            const unsigned pr = pack_bf16(hv * bflo(a1[i]), hv * bfhi(a1[i]));
            const unsigned uu = pack_bf16(bflo(pr) + bflo(ab[i]), bfhi(pr) + bfhi(ab[i]));
            const float u0 = bflo(uu), u1 = bfhi(uu);
            const float e0 = __builtin_amdgcn_exp2f(u0 * fmaf(u0 * u0, GC1, GC0));
            const float e1 = __builtin_amdgcn_exp2f(u1 * fmaf(u1 * u1, GC1, GC0));
            const unsigned gg = pack_bf16(u0 * __builtin_amdgcn_rcpf(1.0f + e0), u1 * __builtin_amdgcn_rcpf(1.0f + e1));
            y = fmaf(bflo(gg), bflo(a2[i]), y);
            y = fmaf(bfhi(gg), bfhi(a2[i]), y);
          }
          y = wave_sum(y);
          if (lane == 0) *(float*)(lds + L_YP + (w - 4) * 32 + j * 4) = y;
        }
        flag_add(lds, F_Y, lane);
      }
      {
        flag_wait<16>(lds, F_P1 + hw, 1);
        const float ms = *(const float*)(lds + L_ML + (hw * 2 + hb) * 8);
        float ls = 0.f;
#pragma unroll 1
        for (int j = 0; j < NPAIR; ++j) {
          const f32x2 sv = *(const f32x2*)(lds + L_SC + hw * (NPAIR * 512) + lane * 8 + j * 512);
          ls += __builtin_amdgcn_exp2f((sv[0] - ms) * LOG2E) + __builtin_amdgcn_exp2f((sv[1] - ms) * LOG2E);
        }
        put_ml(hw, ms, ls);
      }
      wait_until(tb0 + SCA + (unsigned)(PEND * SCT >> 4) - 60u);
      __builtin_amdgcn_s_setprio(2);
      slot(PEND + hw, false);
      request(bA, rv, voffV, poff(w, 0));
      slot(PEND + NH + hw, false);
      request(bB, rv, voffV, poff(w, 1));
    }
    int po = WS_PX + ((g * 8 + hb * 4 + la) * 8 + s) * 128 + (tb * 4 + c) * 16;
    float fw;
    unsigned side;
    {
      flag_wait<1>(lds, F_ML, NWAVE);
      f32x2 ml[NWAVE];
#pragma unroll
      for (int k = 0; k < NWAVE; ++k) ml[k] = *(const f32x2*)(lds + L_ML + (k * 2 + hb) * 8);
      float mx = ml[0][0], L = 0.f;
#pragma unroll
      for (int k = 1; k < NWAVE; ++k) mx = fmaxf(mx, ml[k][0]);
#pragma unroll
      for (int k = 0; k < NWAVE; ++k) L = fmaf(__builtin_amdgcn_exp2f((ml[k][0] - mx) * LOG2E), ml[k][1], L);
      fw = __builtin_amdgcn_exp2f((m - mx) * LOG2E) * recip(L);
      side = asu((la & 1) ? L : mx) >> (16 * (c & 1));
    }
    asm volatile("" : "+v"(po), "+v"(fw), "+v"(side));
    pv(bA, sl[0]);
    pv(bB, sl[1]);
    STAMP(3);
    {
      f32x4 res;
#pragma unroll
      for (int r = 0; r < 4; ++r) {
        const float own = tb ? acc[1][1][r] : acc[0][0][r], snd = tb ? acc[1][0][r] : acc[0][1][r];
        res[r] = (own + dppf<ROR8>(snd)) * fw;
      }
      *(f32x4*)(lds + L_PART + w * 1024 + lane * 16) = res;
      asm volatile("" ::: "memory");
      unsigned old = 0;
      if (lane == 0) old = __hip_atomic_fetch_add((unsigned*)(lds + L_FLAG) + F_WP + vzero(), 1u, __ATOMIC_RELAXED, __HIP_MEMORY_SCOPE_WORKGROUP);
      asm volatile("" ::: "memory");
      f32x4 pp[NWAVE];
#pragma unroll
      for (int k = 0; k < NWAVE; ++k) pp[k] = *(const f32x4*)(lds + L_PART + k * 1024 + lane * 16);
      __builtin_amdgcn_sched_barrier(0);
      if (__builtin_amdgcn_readfirstlane(old) == NWAVE - 1) {
        const f32x4 sum = ((pp[0] + pp[1]) + (pp[2] + pp[3])) + ((pp[4] + pp[5]) + (pp[6] + pp[7]));
        __builtin_amdgcn_raw_buffer_store_b128(
            u32x4{put_lo(sum[0], tagA), put_lo(sum[1], side), put_lo(sum[2], tagA >> 8), put_lo(sum[3], side >> 8)}, rws, po, 0, AUX_SC1);
        flag_add(lds, F_END, lane);
        STAMP(10);
      }
    }
    if (w < 2) {
      const rsrc_t ro = mkrsrc(wo + (size_t)(kvh * 256 + qh * 128 + w * 64) * DM, 64 * DM * 2);
      u32x4 wt[24];
#pragma unroll
      for (int i = 0; i < 24; ++i) {
        if (i % 12 == 0) slot(2 * NT + w + 2 * (i / 12), false);
        wt[i] = __builtin_amdgcn_raw_buffer_load_b128(
            ro, 2 * (i % 3) < nwk ? ((i / 3) * 8 + (lane >> 3)) * (DM * 2) + (s & 3) * 256 + (i % 3) * 128 + (lane & 7) * 16 : OOB, 0, AUX_NT);
      }
#pragma unroll
      for (int i = 0; i < 24; ++i)
        *(u32x4*)(lds + L_WO + w * 24576 + (i % 3) * 8192 + ((i / 3) * 8 + ((lane >> 3) & 4) + ((lane >> 2) & 2) + ((lane >> 4) & 1)) * 128 +
                  (((lane & 7) * 16) ^ (((i / 3) & 3) * 32))) = wt[i];
      flag_add(lds, F_WO, lane);
      STAMP(4);
    } else if (wk < nwk) {
      const int n = lane & 15;
      int oo = n < 2 ? WS_OX + (b * 18 + (s & 3) * 4 + wk) * 4096 + (kvh * 2 + qh) * 128 + n * 64 + la * 16 : OOB;
      asm volatile("" : "+v"(oo));
      flag_wait<1>(lds, F_WO, 2);
      u32x4 wof[2][4];
      {
        typedef __attribute__((address_space(3))) s16x4* lds_s16x4;
        unsigned char* img = lds + L_WO + (wk >> 1) * 8192 + ((n >> 2) * 8 + (la >> 1) * 4 + (la & 1)) * 128 + (lane & 3) * 8;
#pragma unroll
        for (int t = 0; t < 2; ++t)
#pragma unroll
          for (int ks = 0; ks < 4; ++ks) {
            unsigned char* p = img + (ks >> 1) * 24576 + (ks & 1) * 4096 + (((wk & 1) * 2 + t) ^ (n >> 2)) * 32;
            const u32x2 lo = __builtin_bit_cast(u32x2, __builtin_amdgcn_ds_read_tr16_b64_v4i16((lds_s16x4)p));
            const u32x2 hi = __builtin_bit_cast(u32x2, __builtin_amdgcn_ds_read_tr16_b64_v4i16((lds_s16x4)(p + 256)));
            wof[t][ks] = u32x4{lo[0], lo[1], hi[0], hi[1]};
          }
      }
      u32x4 ob[4];
#pragma unroll 1
      for (unsigned spin = 0; spin < (SPIN_CAP << 4); ++spin) {
        asm volatile("" ::: "memory");
        const unsigned f = __hip_atomic_load((unsigned*)(lds + L_FLAG) + F_O, __ATOMIC_RELAXED, __HIP_MEMORY_SCOPE_WORKGROUP);
        asm volatile("" ::: "memory");
#pragma unroll
        for (int ks = 0; ks < 4; ++ks) ob[ks] = *(const u32x4*)(lds + L_OA + ks * 64 + la * 16);
        __builtin_amdgcn_sched_barrier(0);
        if (__builtin_amdgcn_readfirstlane(f) != 0u) break;
      }
      STAMP(6);
      f32x4 d0 = {0.f, 0.f, 0.f, 0.f}, d1 = {0.f, 0.f, 0.f, 0.f};
#pragma unroll
      for (int ks = 0; ks < 4; ++ks) { d0 = mfma32(wof[0][ks], ob[ks], d0); d1 = mfma32(wof[1][ks], ob[ks], d1); }
      const f32x4 d = n ? d1 : d0;
      __builtin_amdgcn_raw_buffer_store_b128(
          u32x4{put_lo(d[0], tagA), put_lo(d[1], tagA >> 8), put_lo(d[2], tagA >> 16), put_lo(d[3], tagA >> 24)}, rws, oo, 0, AUX_SC1);
      STAMP(11);
    }
  }
}

}

extern "C" void kernel_launch(void* const* d_in, const int* in_sizes, int n_in, void* d_out, int out_size, void* d_ws,
                              size_t ws_size, hipStream_t stream) {
  (void)in_sizes; (void)n_in; (void)out_size; (void)ws_size;
  fused_decode_kernel<<<dim3(NWG), dim3(NTHR), 0, stream>>>(
      (const unsigned short*)d_in[0], (const unsigned char*)d_in[1], (const unsigned char*)d_in[2],
      (const unsigned short*)d_in[3], (const unsigned short*)d_in[4], (const unsigned short*)d_in[6],
      (const unsigned short*)d_in[7], (const unsigned short*)d_in[8], (const unsigned short*)d_in[9],
      (unsigned short*)d_out, (unsigned char*)d_ws);
}
```

```cpp
#include <hip/hip_runtime.h>
#include <hip/hip_bf16.h>
#include <stdint.h>

namespace {

typedef unsigned u32x2 __attribute__((ext_vector_type(2)));
typedef unsigned u32x4 __attribute__((ext_vector_type(4)));
typedef float f32x2 __attribute__((ext_vector_type(2)));
typedef float f32x4 __attribute__((ext_vector_type(4)));
typedef short s16x4 __attribute__((ext_vector_type(4)));
typedef short s16x8 __attribute__((ext_vector_type(8)));
typedef __bf16 bf16x2 __attribute__((ext_vector_type(2)));
typedef __amdgpu_buffer_rsrc_t rsrc_t;

constexpr int DM = 576, SK = 32768, DH = 128, FF = 4608;
constexpr int NSPLIT = 8, NWG = 256, NWAVE = 8, NTHR = (NWAVE + 1) * 64;
constexpr int KEYS_WG = SK / NSPLIT;
constexpr int TILE_BYTES = 32 * DH * 2;
constexpr int NT = KEYS_WG / 32;
constexpr int SPAN = 2 * KEYS_WG * DH * 2;
constexpr int S = 4, NH = NWAVE - S;
constexpr int NPAIR = (NT / 2 - NH) / S;
static_assert(2 * (NPAIR * S + NH) == NT && NH == S, "every tile has one owner, every stream one helper");

#define SCA 115
#define SCT 427
#define GAP 25
#define CBETA 6
#define GLT 160
#define GLQ 210
#define PFREE 40
#define KD 15
#define KG 20
#define OTL 0
#define PFD 3
#define PSL 6
#define QPS 2
#define T1D 17
#define T2D 22
constexpr int PGAP = 2 * NWAVE + 2 * S * NPAIR, PEND = 2 * NT - 2 * NH;

constexpr int AUX_NT = 2, AUX_SC1 = 16;
constexpr unsigned RSRC3 = 0x00020000u;
constexpr unsigned SPIN_CAP = 1u << 19;
constexpr int OOB = 0x7ff00000;

constexpr int WS_EPOCH = 0;
constexpr int WS_QX = 4096;
constexpr int WS_PX = WS_QX + 32 * 4096;
constexpr int WS_OX = WS_PX + NWG * 1024;
constexpr int WS_END = WS_OX + 2 * 18 * 4096;

constexpr int L_FLAG = 0;
constexpr int L_TAG = 64;
constexpr int L_XN = 80;
constexpr int L_QPART = L_XN + 1280;
constexpr int L_QS = L_QPART + 8 * 64 * 4;
constexpr int L_OA = L_QS + 512;
constexpr int L_YP = L_OA + 256;
constexpr int L_ML = L_YP + 4 * 8 * 4;
constexpr int L_PART = L_ML + NWAVE * 16;
constexpr int L_WO = L_PART + NWAVE * 1024;
constexpr int L_SC = L_WO + 128 * 384;
constexpr int L_END = L_SC + S * NPAIR * 512;
constexpr int F_QP = 0, F_Q = 1, F_WP = 2, F_END = 3, F_O = 4, F_Y = 5, F_ARR = 6, F_WO = 8, F_ML = 9, F_SH = 10, F_T = 11, F_P1 = 12;

constexpr float LOG2E = 1.4426950408889634f;
constexpr float SCALE = 0.08838834764831845f;
constexpr float LN_EPS = 1e-5f;

__device__ __forceinline__ float asf(unsigned u) { return __builtin_bit_cast(float, u); }
__device__ __forceinline__ unsigned asu(float f) { return __builtin_bit_cast(unsigned, f); }
__device__ __forceinline__ float bflo(unsigned u) { return asf(u << 16); }
__device__ __forceinline__ float bfhi(unsigned u) { return asf(u & 0xffff0000u); }
__device__ __forceinline__ unsigned pack_bf16(float lo, float hi) {
  f32x2 v = {lo, hi};
  return __builtin_bit_cast(unsigned, __builtin_convertvector(v, bf16x2));
}
__device__ __forceinline__ float rbf(float f) { return (float)(__bf16)f; }
template <int CTRL> __device__ __forceinline__ unsigned dppu(unsigned x) {
  return (unsigned)__builtin_amdgcn_mov_dpp((int)x, CTRL, 0xf, 0xf, true);
}
template <int CTRL> __device__ __forceinline__ float dppf(float x) { return asf(dppu<CTRL>(asu(x))); }
constexpr int XOR1 = 0xB1, XOR2 = 0x4E, XOR7 = 0x141, ROR4 = 0x124, ROR8 = 0x128;
constexpr int QB0 = 0x00, QB1 = 0x55, QB2 = 0xAA, QB3 = 0xFF;
__device__ __forceinline__ float rows_sum(float x) {
  auto s = __builtin_amdgcn_permlane16_swap(asu(x), asu(x), false, false);
  x = asf(s[0]) + asf(s[1]);
  auto t = __builtin_amdgcn_permlane32_swap(asu(x), asu(x), false, false);
  return asf(t[0]) + asf(t[1]);
}
__device__ __forceinline__ float rows_max(float x) {
  auto s = __builtin_amdgcn_permlane16_swap(asu(x), asu(x), false, false);
  x = fmaxf(asf(s[0]), asf(s[1]));
  auto t = __builtin_amdgcn_permlane32_swap(asu(x), asu(x), false, false);
  return fmaxf(asf(t[0]), asf(t[1]));
}
__device__ __forceinline__ float rows_sum_t(float v0, float v1, float v2, float v3) {
  const auto p0 = __builtin_amdgcn_permlane32_swap(asu(v0), asu(v2), false, false);
  const auto p1 = __builtin_amdgcn_permlane32_swap(asu(v1), asu(v3), false, false);
  const auto p2 = __builtin_amdgcn_permlane16_swap(asu(asf(p0[0]) + asf(p0[1])), asu(asf(p1[0]) + asf(p1[1])), false, false);
  return asf(p2[0]) + asf(p2[1]);
}
__device__ __forceinline__ float wave_sum(float x) {
  x += dppf<XOR1>(x); x += dppf<XOR2>(x); x += dppf<ROR4>(x); x += dppf<ROR8>(x);
  return rows_sum(x);
}
__device__ __forceinline__ float recip(float x) {
  const float r = __builtin_amdgcn_rcpf(x);
  return fmaf(fmaf(-x, r, 1.0f), r, r);
}
__device__ __forceinline__ rsrc_t mkrsrc(const void* p, int bytes) {
  return __builtin_amdgcn_make_buffer_rsrc(const_cast<void*>(p), 0, bytes, RSRC3);
}
__device__ __forceinline__ f32x4 mfma32(u32x4 a, u32x4 b, f32x4 c) {
  return __builtin_amdgcn_mfma_f32_16x16x32_bf16(__builtin_bit_cast(s16x8, a), __builtin_bit_cast(s16x8, b), c, 0, 0, 0);
}
__device__ __forceinline__ f32x4 mfma16(unsigned a0, unsigned a1, unsigned b0, unsigned b1, f32x4 c) {
  u32x2 a = {a0, a1}, b = {b0, b1};
  return __builtin_amdgcn_mfma_f32_16x16x16bf16_1k(__builtin_bit_cast(s16x4, a), __builtin_bit_cast(s16x4, b), c, 0, 0, 0);
}
__device__ __forceinline__ unsigned put_lo(float v, unsigned byte) { return (asu(v) & 0xffffff00u) | (byte & 0xffu); }
__device__ __forceinline__ float cut_lo(unsigned u) { return asf(u & 0xffffff00u); }
__device__ __forceinline__ unsigned now32() { return (unsigned)__builtin_amdgcn_s_memrealtime(); }
__device__ __forceinline__ void wait_until(unsigned t) {
  while ((int)(now32() - t) < 0) __builtin_amdgcn_s_sleep(1);
}
__device__ __forceinline__ int vzero() { int z = 0; asm("" : "+v"(z)); return z; }
__device__ __forceinline__ unsigned sload(const void* p) {
  const char* q = (const char*)((unsigned long long)p & ~3ull);
  asm volatile("" : "+s"(q));
  unsigned v = *(const __attribute__((address_space(4))) unsigned*)q;
  asm volatile("" : "+s"(v));
  return v;
}
__device__ __forceinline__ void flag_add(unsigned char* lds, int f, int lane) {
  asm volatile("" ::: "memory");
  if (lane == 0) __hip_atomic_fetch_add((unsigned*)(lds + L_FLAG) + f + vzero(), 1u, __ATOMIC_RELAXED, __HIP_MEMORY_SCOPE_WORKGROUP);
  asm volatile("" ::: "memory");
}
__device__ __forceinline__ unsigned flag_get(unsigned char* lds, int f) {
  return __builtin_amdgcn_readfirstlane(__hip_atomic_load((unsigned*)(lds + L_FLAG) + f, __ATOMIC_RELAXED, __HIP_MEMORY_SCOPE_WORKGROUP));
}
template <int SLEEP> __device__ __forceinline__ void flag_wait(unsigned char* lds, int f, unsigned n) {
#pragma unroll 1
  for (unsigned spin = 0; spin < (SPIN_CAP << 4); ++spin) {
    if (flag_get(lds, f) >= n) break;
    if (SLEEP) __builtin_amdgcn_s_sleep(SLEEP);
  }
  asm volatile("" ::: "memory");
}
__device__ __forceinline__ void ln_stats(const float (&v)[10], bool last_valid, float& mean_b, float& den_b) {
  float s = 0.f;
#pragma unroll
  for (int i = 0; i < 8; ++i) s += v[i];
  s += last_valid ? (v[8] + v[9]) : 0.f;
  const float mean = wave_sum(s) * (1.0f / DM);
  float q = 0.f;
#pragma unroll
  for (int i = 0; i < 8; ++i) { const float d = v[i] - mean; q = fmaf(d, d, q); }
  { const float d8 = v[8] - mean, d9 = v[9] - mean; q += last_valid ? fmaf(d8, d8, d9 * d9) : 0.f; }
  const float var = wave_sum(q) * (1.0f / DM);
  mean_b = rbf(mean);
  den_b = rbf(__builtin_amdgcn_sqrtf(rbf(rbf(var) + LN_EPS)));
}
__device__ __forceinline__ float ln_apply(float x, float mean_b, float den_b) {
  const float d = rbf(x - mean_b), r = __builtin_amdgcn_rcpf(den_b), q = d * r;
  return rbf(fmaf(fmaf(-q, den_b, d), r, q));
}

#define STAMP(i) do { } while (0)

__global__ void __launch_bounds__(NTHR, 3)
fused_decode_kernel(const unsigned short* __restrict__ x, const unsigned char* __restrict__ kc, const unsigned char* __restrict__ vc,
                    const unsigned short* __restrict__ wq, const unsigned short* __restrict__ wo,
                    const unsigned short* __restrict__ w1, const unsigned short* __restrict__ b1,
                    const unsigned short* __restrict__ w2, const unsigned short* __restrict__ b2,
                    unsigned short* __restrict__ out, unsigned char* ws) {
  __shared__ __attribute__((aligned(16))) unsigned char lds[L_END];
  const int lane = threadIdx.x & 63, wv = __builtin_amdgcn_readfirstlane(threadIdx.x >> 6), w = wv < 8 ? wv ^ 4 : wv;
  const unsigned tst = now32();
  const int g = blockIdx.x >> 3, s = (blockIdx.x + 3 * g) & 7, id = g * 8 + s, b = g >> 4, kvh = g & 15;
  STAMP(0);
  const rsrc_t rws = mkrsrc(ws, WS_END);
  const rsrc_t rx = mkrsrc(x + b * DM, DM * 2);
  const int qh = s >> 2, cb = (s + (kvh >> 2)) & 3;
  const int la = lane >> 4;
  const int red = kvh * 8 + s;

  u32x2 ep = {0u, 0u};
  if (w == NWAVE) {
    ep = __builtin_amdgcn_raw_buffer_load_b64(rws, WS_EPOCH + b * 256, 0, 0);
    if (lane < 16) *((unsigned*)(lds + L_FLAG) + lane) = 0u;
    asm volatile("s_waitcnt lgkmcnt(0)\n\ts_barrier" ::: "memory");
  }
  if (w >= NWAVE) {
    __builtin_amdgcn_s_setprio(3);
    const unsigned e0 = __builtin_amdgcn_readfirstlane(ep[0]);
    const unsigned e1 = ((e0 & 0x7f7f7f7fu) + 0x01010101u) ^ (e0 & 0x80808080u);
    const unsigned tagA = e1 ^ 0xb77a3d00u;
    const unsigned tagB = __builtin_amdgcn_readfirstlane(ep[1]) + 1u;
    if (lane == 0) *(unsigned*)(lds + L_TAG) = tagA;
    flag_wait<0>(lds, F_QP, 4);
    {
      f32x2 q2 = *(const f32x2*)(lds + L_QPART + (lane & 31) * 8);
#pragma unroll
      for (int ww = 1; ww < 8; ++ww) q2 += *(const f32x2*)(lds + L_QPART + ww * 256 + (lane & 31) * 8);
      __builtin_amdgcn_raw_buffer_store_b128(u32x4{tagA, asu(q2[0]), (tagB & 0xffffu) | (tst << 16), asu(q2[1])}, rws,
                                             lane < 32 ? WS_QX + g * 4096 + qh * 2048 + cb * 512 + lane * 16 : OOB, 0, AUX_SC1);
      STAMP(8);
    }
    __builtin_amdgcn_s_sleep(PSL);
    {
      float qa[4], qb[4];
      int ts[4];
      int of[4];
#pragma unroll
      for (int i = 0; i < 4; ++i) { qa[i] = 0.f; qb[i] = 0.f; ts[i] = 0; of[i] = WS_QX + g * 4096 + i * 1024 + lane * 16; }
#pragma unroll 1
      for (unsigned spin = 0; spin < SPIN_CAP; ++spin) {
        asm volatile("" ::: "memory");
        u32x4 gr[4];
#pragma unroll
        for (int i = 0; i < 4; ++i) gr[i] = __builtin_amdgcn_raw_buffer_load_b128(rws, of[i], 0, AUX_SC1);
        bool done = true;
#pragma unroll
        for (int i = 0; i < 4; ++i) {
          const bool fresh = (gr[i][0] == tagA) & (((gr[i][2] ^ tagB) & 0xffffu) == 0u) & (of[i] != OOB);
          ts[i] = fresh ? (int)(gr[i][2] - (tst << 16)) >> 16 : ts[i];
          qa[i] = fresh ? asf(gr[i][1]) : qa[i]; qb[i] = fresh ? asf(gr[i][3]) : qb[i];
          of[i] = fresh ? OOB : of[i];
          done &= of[i] == OOB;
        }
        if (__all(done)) break;
        __builtin_amdgcn_s_sleep(QPS);
      }
      *(unsigned*)(lds + L_QS + lane * 4) = pack_bf16(qa[0] + qa[2], qb[0] + qb[2]);
      *(unsigned*)(lds + L_QS + 256 + lane * 4) = pack_bf16(qa[1] + qa[3], qb[1] + qb[3]);
      {
        int sm = 0, mx = -32768;
#pragma unroll
        for (int i = 0; i < 4; ++i) {
          const int t0 = __builtin_amdgcn_readlane(ts[i], 0), t1 = __builtin_amdgcn_readlane(ts[i], 32);
          sm += t0 + t1; mx = max(mx, max(t0, t1));
        }
        sm >>= 3;
        if (lane == 0) *(unsigned*)(lds + L_TAG + 4) = tst + (unsigned)(sm + ((mx - sm) * CBETA >> 4));
      }
      flag_add(lds, F_Q, lane);
    }
    wait_until(tst + 1000u + (id & 31) * 16u);
    sload(vc + (size_t)(id >> 1) * SPAN);
    sload(wo + (size_t)(kvh * 256 + qh * 128) * DM);
    sload(wo + (size_t)(kvh * 256 + qh * 128 + 127) * DM);
    if (OTL && g == 0) sload(out);
    const int fc = (lane & 1) * 4 + la;
    const unsigned xraw = __builtin_amdgcn_raw_buffer_load_b32(rx, red * 16 + (fc >> 1) * 4, 0, 0);
    const unsigned b2v = sload(b2) >> (((unsigned long long)b2 & 2) * 8);
    if (red == 127) {
      wait_until(tst + 2000u);
#pragma unroll 1
      for (unsigned spin = 0; spin < SPIN_CAP; ++spin) {
        asm volatile("" ::: "memory");
        const u32x4 v0 = __builtin_amdgcn_raw_buffer_load_b128(rws, WS_QX + b * 65536 + lane * 512, 0, AUX_SC1);
        const u32x4 v1 = __builtin_amdgcn_raw_buffer_load_b128(rws, WS_QX + b * 65536 + 32768 + lane * 512, 0, AUX_SC1);
        if (__all((v0[0] == tagA) & (v1[0] == tagA) & ((((v0[2] ^ tagB) | (v1[2] ^ tagB)) & 0xffffu) == 0u))) break;
        __builtin_amdgcn_s_sleep(16);
      }
      __builtin_amdgcn_raw_buffer_store_b64(u32x2{e1, tagB}, rws, lane == 0 ? WS_EPOCH + b * 256 : OOB, 0, AUX_SC1);
    }
    float yv = 0.f, xfin = (fc & 1) ? bfhi(xraw) : bflo(xraw);
    const rsrc_t rout = mkrsrc(out + b * DM, DM * 2 + 2);
    int oout = (lane & 14) == 0 ? (red * 8 + fc) * 2 : OOB;
    if (red < 72) {
      flag_wait<16>(lds, F_Y, 4);
#pragma unroll
      for (int ww = 0; ww < 4; ++ww) yv += *(const float*)(lds + L_YP + ww * 32 + fc * 4);
      yv = rbf(rbf(yv) + bflo(b2v));
      __builtin_amdgcn_raw_buffer_store_b16((unsigned short)0, rout, oout, 0, 0);
    }
    int oa = (lane & 8) ? L_QPART + lane * 8 : L_OA + la * 64 + (lane & 7) * 8;
    asm volatile("" : "+v"(yv), "+v"(xfin), "+v"(oout), "+v"(oa));
    flag_wait<16>(lds, F_WP, 1);
    u32x4 ga[4];
#pragma unroll
    for (int i = 0; i < 4; ++i) ga[i] = u32x4{0u, 0u, 0u, 0u};
    int of1 = WS_PX + (g * 8 + (s & 4)) * 1024 + lane * 16;
    asm volatile("" : "+v"(of1), "+v"(ga[0]), "+v"(ga[1]), "+v"(ga[2]), "+v"(ga[3]));
    flag_wait<0>(lds, F_END, 1);
    STAMP(5);
    __builtin_amdgcn_s_sleep(T1D);
    {
      unsigned bad = 1u;
#pragma unroll 1
      for (unsigned spin = 0; spin < SPIN_CAP; ++spin) {
        asm volatile("" ::: "memory");
        if (bad != 0u) {
#pragma unroll
          for (int i = 0; i < 4; ++i) ga[i] = __builtin_amdgcn_raw_buffer_load_b128(rws, of1 + i * 1024, 0, AUX_SC1);
        }
        bad = 0u;
#pragma unroll
        for (int i = 0; i < 4; ++i) bad |= (ga[i][0] ^ tagA) | (ga[i][2] ^ (tagA >> 8));
        bad &= 0xffu;
        if (__all(bad == 0u)) break;
        bad |= dppu<XOR1>(bad); bad |= dppu<XOR2>(bad); bad |= dppu<XOR7>(bad);
        __builtin_amdgcn_s_sleep(1);
      }
      const unsigned hm = __builtin_amdgcn_perm(ga[0][3], ga[0][1], 0x0c0c0400u) << (16 * (lane & 1));
      const unsigned hl = __builtin_amdgcn_perm(ga[1][3], ga[1][1], 0x0c0c0400u) << (16 * (lane & 1));
      const float pm = asf(hm | dppu<XOR1>(hm)), pl = asf(hl | dppu<XOR1>(hl));
      const float M = rows_max(fmaxf(pm, dppf<ROR8>(pm)));
      const float w0 = __builtin_amdgcn_exp2f((pm - M) * LOG2E) * pl;
      const float inv = recip(rows_sum(w0 + dppf<ROR8>(w0)));
      float v[4];
#pragma unroll
      for (int r = 0; r < 4; ++r) {
        const float t = rows_sum_t(w0 * cut_lo(ga[0][r]), w0 * cut_lo(ga[1][r]), w0 * cut_lo(ga[2][r]), w0 * cut_lo(ga[3][r]));
        v[r] = (t + dppf<ROR8>(t)) * inv;
      }
      *(u32x2*)(lds + oa) = u32x2{pack_bf16(v[0], v[1]), pack_bf16(v[2], v[3])};
      flag_add(lds, F_O, lane);
    }
    if (red < 72) {
      __builtin_amdgcn_s_sleep(T2D);
      u32x4 gb = {0u, 0u, 0u, 0u};
      const int of2 = WS_OX + (b * 18 + (red >> 2)) * 4096 + (lane >> 1) * 128 + (red & 3) * 32 + (lane & 1) * 16;
      unsigned bad = 1u;
#pragma unroll 1
      for (unsigned spin = 0; spin < SPIN_CAP; ++spin) {
        asm volatile("" ::: "memory");
        if (bad != 0u) gb = __builtin_amdgcn_raw_buffer_load_b128(rws, of2, 0, AUX_SC1);
        bad = (__builtin_amdgcn_perm(gb[1], gb[0], 0x0c0c0400u) | __builtin_amdgcn_perm(gb[3], gb[2], 0x04000c0cu)) ^ tagA;
        if (__all(bad == 0u)) break;
        bad |= dppu<XOR1>(bad);
        __builtin_amdgcn_s_sleep(1);
      }
      STAMP(13);
      float t = rows_sum_t(cut_lo(gb[0]), cut_lo(gb[1]), cut_lo(gb[2]), cut_lo(gb[3]));
      t += dppf<XOR2>(t); t += dppf<ROR4>(t); t += dppf<ROR8>(t);
      __builtin_amdgcn_raw_buffer_store_b16((unsigned short)pack_bf16(rbf(xfin + rbf(t)) + yv, 0.f), rout, oout, 0, 0);
    }
  } else {
    const rsrc_t rk = mkrsrc(kc + (size_t)(id >> 1) * SPAN, SPAN);
    const rsrc_t rv = mkrsrc(vc + (size_t)(id >> 1) * SPAN, SPAN);
    auto poff = [&](int d, int h) {
      return ((d & 3) | (h << 2) | ((id & 1) << 3) | ((d >> 2) << 4)) * TILE_BYTES;
    };
    const int voffK = ((lane & 15) >> 2) * 256 + la * 64 + (lane & 3) * 16;
    const int voffV = lane * 16;
    u32x4 bA[8], bB[8];
    auto request = [&](u32x4 (&x)[8], rsrc_t r, int voff, int so) {
      asm volatile("" : "+v"(voff));
#pragma unroll
      for (int u = 0; u < 8; ++u) x[u] = __builtin_amdgcn_raw_buffer_load_b128(r, voff + (u & 3) * 1024, so + (u >> 2) * 4096, AUX_NT);
      __builtin_amdgcn_sched_barrier(0);
    };
    if (w >= 4) {
      const int rg = (w - 4) * 8 + (lane >> 3), c8 = lane & 7;
      unsigned xs[5];
      u32x4 wqv[9];
#pragma unroll
      for (int i = 0; i < 5; ++i) xs[i] = __builtin_amdgcn_raw_buffer_load_b32(rx, (lane + 64 * i) * 4, 0, 0);
      __builtin_amdgcn_sched_barrier(0);
      const rsrc_t rq = mkrsrc(wq + qh * 288 * 4096 + kvh * 256 + cb * 64, (287 * 4096 + 64) * 2);
#pragma unroll
      for (int i = 0; i < 9; ++i) wqv[i] = __builtin_amdgcn_raw_buffer_load_b128(rq, (rg + 32 * i) * 8192 + c8 * 16, 0, AUX_NT);
      __builtin_amdgcn_sched_barrier(0);
      __builtin_amdgcn_s_barrier();
      __builtin_amdgcn_sched_barrier(0);
      float xv[10], mean1, den1;
#pragma unroll
      for (int i = 0; i < 5; ++i) { xv[2 * i] = bflo(xs[i]); xv[2 * i + 1] = bfhi(xs[i]); }
      ln_stats(xv, lane < 32, mean1, den1);
#pragma unroll
      for (int i = 0; i < 5; ++i)
        *(unsigned*)(lds + L_XN + (lane + 64 * i) * 4) = pack_bf16(ln_apply(xv[2 * i], mean1, den1), ln_apply(xv[2 * i + 1], mean1, den1));
      asm volatile("" ::: "memory");
      STAMP(9);
      f32x4 qa = {0.f, 0.f, 0.f, 0.f};
#pragma unroll
      for (int i = 0; i < 9; ++i) {
        const unsigned t = (unsigned)*(const unsigned short*)(lds + L_XN + (qh * 288 + rg + 32 * i) * 2) << (16 * (lane & 1));
        const int jp = c8 >> 1;
        qa = mfma32(wqv[i], u32x4{jp == 0 ? t : 0u, jp == 1 ? t : 0u, jp == 2 ? t : 0u, jp == 3 ? t : 0u}, qa);
      }
      if ((lane >> 5) == ((lane >> 3) & 1)) {
        float* qp = (float*)(lds + L_QPART) + ((w - 4) * 2 + (lane >> 5)) * 64 + (la & 1) * 32 + c8;
#pragma unroll
        for (int r = 0; r < 4; ++r) qp[r * 8] = qa[r];
      }
      flag_add(lds, F_QP, lane);
      STAMP(1);
    }
    if (w < 4) asm volatile("s_barrier" ::: "memory");
    if (w >= PFD) flag_wait<1>(lds, F_ARR, w - PFD + 1);
    __builtin_amdgcn_sched_barrier(0);
    STAMP(14);
    request(bA, rk, voffK, poff(w, 0));
    request(bB, rk, voffK, poff(w, 1));
    asm volatile("s_waitcnt vmcnt(0)" ::: "memory");
    flag_add(lds, F_ARR, lane);
    STAMP(15);
    flag_wait<1>(lds, F_Q, 1);
    if (w < S) STAMP(2);

    const int c = lane & 3, hb = (lane >> 2) & 1, tb = (lane >> 3) & 1;
    const unsigned tagA = __builtin_amdgcn_readfirstlane(*(const unsigned*)(lds + L_TAG));
    const unsigned tb0 = __builtin_amdgcn_readfirstlane(*(const unsigned*)(lds + L_TAG + 4));
    float m;
    f32x4 acc[2][2];
#pragma unroll
    for (int i = 0; i < 2; ++i)
#pragma unroll
      for (int j = 0; j < 2; ++j) acc[i][j] = f32x4{0.f, 0.f, 0.f, 0.f};
    const u32x4 qf = *(const u32x4*)(lds + L_QS + (hb * 128 + la * 32 + c * 8) * 2);
    const u32x4 zero4 = {0u, 0u, 0u, 0u};
    const u32x4 q0 = tb ? zero4 : qf, q1 = tb ? qf : zero4;
    const unsigned mask0 = c == 0 ? 0x0000ffffu : c == 1 ? 0xffff0000u : 0u;
    const unsigned mask1 = c == 2 ? 0x0000ffffu : c == 3 ? 0xffff0000u : 0u;
    auto qk = [&](const u32x4 (&x)[8], f32x4 (&sc)[4]) {
#pragma unroll
      for (int i = 0; i < 4; ++i) sc[i] = mfma32(x[i], q0, f32x4{0.f, 0.f, 0.f, 0.f});
#pragma unroll
      for (int i = 0; i < 4; ++i) sc[i] = mfma32(x[4 + i], q1, sc[i]);
    };
    auto score = [&](const f32x4 (&sc)[4]) {
      float t4[4];
#pragma unroll
      for (int i = 0; i < 4; ++i) t4[i] = (dppf<QB0>(sc[i][0]) + dppf<QB1>(sc[i][1])) + (dppf<QB2>(sc[i][2]) + dppf<QB3>(sc[i][3]));
      const float raw = (c & 2) ? ((c & 1) ? t4[3] : t4[2]) : ((c & 1) ? t4[1] : t4[0]);
      float sv = rbf(rbf(raw) * SCALE);
      asm volatile("" : "+v"(sv));
      return sv;
    };
    auto head_max = [&](float v) {
      v = fmaxf(v, dppf<XOR1>(v));
      v = fmaxf(v, dppf<XOR2>(v));
      v = fmaxf(v, dppf<ROR8>(v));
      return rows_max(v);
    };
    auto pv = [&](const u32x4 (&x)[8], float sv) {
      const float p = __builtin_amdgcn_exp2f((sv - m) * LOG2E);
      const unsigned pk = pack_bf16(p, p);
      unsigned pb0[4], pb1[4];
      pb0[0] = dppu<QB0>(pk) & mask0; pb1[0] = dppu<QB0>(pk) & mask1;
      pb0[1] = dppu<QB1>(pk) & mask0; pb1[1] = dppu<QB1>(pk) & mask1;
      pb0[2] = dppu<QB2>(pk) & mask0; pb1[2] = dppu<QB2>(pk) & mask1;
      pb0[3] = dppu<QB3>(pk) & mask0; pb1[3] = dppu<QB3>(pk) & mask1;
#pragma unroll
      for (int u = 0; u < 8; ++u) {
        acc[u >> 2][0] = mfma16(x[u][0], x[u][1], pb0[u & 3], pb1[u & 3], acc[u >> 2][0]);
        acc[u >> 2][1] = mfma16(x[u][2], x[u][3], pb0[u & 3], pb1[u & 3], acc[u >> 2][1]);
      }
    };
    auto put_ml = [&](int k, float mk, float lk) {
      lk += dppf<XOR1>(lk);
      lk += dppf<XOR2>(lk);
      lk += dppf<ROR8>(lk);
      lk = rows_sum(lk);
      if ((lane & ~4) == 0) *(f32x2*)(lds + L_ML + (k * 2 + hb) * 8) = f32x2{mk, lk};
      flag_add(lds, F_ML, lane);
    };
    const int hw = w - S, wk = w - 2, nwk = (s & 3) == 3 ? 6 : 4;
    int ontime = 0;
    auto slot = [&](int p, bool watch) {
      __builtin_amdgcn_sched_barrier(0);
      if (watch && lane == 0) {
        unsigned* fl = (unsigned*)(lds + L_FLAG) + vzero();
        const unsigned t = now32() - tb0;
        const int gap = (int)(t - __hip_atomic_fetch_max(fl + F_T, t, __ATOMIC_RELAXED, __HIP_MEMORY_SCOPE_WORKGROUP));
        if (gap > GLT && ontime >= 3 && (p < PGAP - 2 * S || p >= PGAP + 4 * S)) {
          __hip_atomic_fetch_max(fl + F_T, t + GLQ, __ATOMIC_RELAXED, __HIP_MEMORY_SCOPE_WORKGROUP);
          __hip_atomic_fetch_add(fl + F_SH, (unsigned)GLQ, __ATOMIC_RELAXED, __HIP_MEMORY_SCOPE_WORKGROUP);
        }
      }
      const unsigned due = tb0 + SCA + (p >= PGAP ? GAP : 0) + (unsigned)(p * SCT >> 4);
      ontime = (int)(now32() - due - flag_get(lds, F_SH)) < 0 ? ontime + 1 : 0;
      if (watch && p >= PFREE) {
        asm volatile("s_waitcnt vmcnt(0)" ::: "memory");
        const unsigned t = now32();
        const int late = (int)(t - due - flag_get(lds, F_SH)) + KD;
        const unsigned lim = t + (unsigned)max(KD - (max(late, 0) * KG >> 8), 0);
        while ((int)(now32() - lim) < 0) {}
      }
      while ((int)(now32() - due - flag_get(lds, F_SH)) < 0) {}
      __builtin_amdgcn_sched_barrier(0);
    };
    f32x2 sl;
    if (w < S) {
      __builtin_amdgcn_s_setprio(2);
      unsigned char* sco = lds + L_SC + w * (NPAIR * 512) + lane * 8;
      float mx = -INFINITY;
      for (int j = 0; j < NPAIR; ++j) {
        const bool k = j + 1 < NPAIR;
        const rsrc_t r = k ? rk : rv;
        const int vo = k ? voffK : voffV, d = NWAVE + S * (k ? j : j - 1) + w;
        f32x4 sc[4];
        qk(bA, sc);
        slot(2 * NWAVE + 2 * S * j + w, true);
        request(bA, r, vo, poff(d, 0));
        const float s0 = score(sc);
        qk(bB, sc);
        slot(2 * NWAVE + 2 * S * j + S + w, true);
        request(bB, r, vo, poff(d, 1));
        const float s1 = score(sc);
        mx = fmaxf(mx, fmaxf(s0, s1));
        *(f32x2*)(sco + j * 512) = f32x2{s0, s1};
      }
      m = head_max(mx);
      if ((lane & ~4) == 0) *(float*)(lds + L_ML + (w * 2 + hb) * 8) = m;
      flag_add(lds, F_P1 + w, lane);
      for (int j = 0; j + 1 < NPAIR; ++j) {
        const f32x2 sv = *(const f32x2*)(sco + (NPAIR - 1 - j) * 512);
        const int d = j + 2 < NPAIR ? NWAVE + S * (NPAIR - 3 - j) + w : w;
        pv(bA, sv[0]);
        slot(2 * NWAVE + 2 * S * (NPAIR + j) + w, true);
        request(bA, rv, voffV, poff(d, 0));
        pv(bB, sv[1]);
        slot(2 * NWAVE + 2 * S * (NPAIR + j) + S + w, true);
        request(bB, rv, voffV, poff(d, 1));
      }
      sl = *(const f32x2*)sco;
    }
    asm volatile("" ::: "memory");
    if (w >= S) {
      {
        f32x4 sc[4];
        qk(bA, sc);
        sl[0] = score(sc);
        qk(bB, sc);
        sl[1] = score(sc);
        m = head_max(fmaxf(sl[0], sl[1]));
        put_ml(w, m, __builtin_amdgcn_exp2f((sl[0] - m) * LOG2E) + __builtin_amdgcn_exp2f((sl[1] - m) * LOG2E));
      }
      STAMP(12);
      if (red < 72) {
        const int ht = (w - 4) * 64 + lane;
        unsigned a1[9], ab[9], a2[9];
        const rsrc_t r1 = mkrsrc(w1, FF * 2), rb = mkrsrc(b1, FF * 2), r2 = mkrsrc(w2, FF * 2);
#pragma unroll
        for (int i = 0; i < 9; ++i) {
          a1[i] = __builtin_amdgcn_raw_buffer_load_b32(r1, (ht + 256 * i) * 4, 0, 0);
          ab[i] = __builtin_amdgcn_raw_buffer_load_b32(rb, (ht + 256 * i) * 4, 0, 0);
          a2[i] = __builtin_amdgcn_raw_buffer_load_b32(r2, (ht + 256 * i) * 4, 0, 0);
        }
        __builtin_amdgcn_sched_barrier(0);
        float xn[10];
#pragma unroll
        for (int i = 0; i < 5; ++i) { const unsigned v = *(const unsigned*)(lds + L_XN + (lane + 64 * i) * 4); xn[2 * i] = bflo(v); xn[2 * i + 1] = bfhi(v); }
        float mean2, den2;
        ln_stats(xn, lane < 32, mean2, den2);
        constexpr float GC0 = -2.0f * 0.7978845608028654f * LOG2E, GC1 = GC0 * 0.044715f;
#pragma unroll 1
        for (int j = 0; j < 8; ++j) {
          const float hv = ln_apply(bflo(*(const unsigned short*)(lds + L_XN + (red * 8 + j) * 2)), mean2, den2);
          float y = 0.f;
#pragma unroll
          for (int i = 0; i < 9; ++i) {
            asm volatile("" : "+v"(a1[i]), "+v"(ab[i]), "+v"(a2[i]));
            const unsigned pr = pack_bf16(hv * bflo(a1[i]), hv * bfhi(a1[i]));
            const unsigned uu = pack_bf16(bflo(pr) + bflo(ab[i]), bfhi(pr) + bfhi(ab[i]));
            const float u0 = bflo(uu), u1 = bfhi(uu);
            const float e0 = __builtin_amdgcn_exp2f(u0 * fmaf(u0 * u0, GC1, GC0));
            const float e1 = __builtin_amdgcn_exp2f(u1 * fmaf(u1 * u1, GC1, GC0));
            const unsigned gg = pack_bf16(u0 * __builtin_amdgcn_rcpf(1.0f + e0), u1 * __builtin_amdgcn_rcpf(1.0f + e1));
            y = fmaf(bflo(gg), bflo(a2[i]), y);
            y = fmaf(bfhi(gg), bfhi(a2[i]), y);
          }
          y = wave_sum(y);
          if (lane == 0) *(float*)(lds + L_YP + (w - 4) * 32 + j * 4) = y;
        }
        flag_add(lds, F_Y, lane);
      }
      {
        flag_wait<16>(lds, F_P1 + hw, 1);
        const float ms = *(const float*)(lds + L_ML + (hw * 2 + hb) * 8);
        float ls = 0.f;
#pragma unroll 1
        for (int j = 0; j < NPAIR; ++j) {
          const f32x2 sv = *(const f32x2*)(lds + L_SC + hw * (NPAIR * 512) + lane * 8 + j * 512);
          ls += __builtin_amdgcn_exp2f((sv[0] - ms) * LOG2E) + __builtin_amdgcn_exp2f((sv[1] - ms) * LOG2E);
        }
        put_ml(hw, ms, ls);
      }
      wait_until(tb0 + SCA + (unsigned)(PEND * SCT >> 4) - 60u);
      __builtin_amdgcn_s_setprio(2);
      slot(PEND + hw, false);
      request(bA, rv, voffV, poff(w, 0));
      slot(PEND + NH + hw, false);
      request(bB, rv, voffV, poff(w, 1));
    }
    int po = WS_PX + ((g * 8 + hb * 4 + la) * 8 + s) * 128 + (tb * 4 + c) * 16;
    float fw;
    unsigned side;
    {
      flag_wait<1>(lds, F_ML, NWAVE);
      f32x2 ml[NWAVE];
#pragma unroll
      for (int k = 0; k < NWAVE; ++k) ml[k] = *(const f32x2*)(lds + L_ML + (k * 2 + hb) * 8);
      float mx = ml[0][0], L = 0.f;
#pragma unroll
      for (int k = 1; k < NWAVE; ++k) mx = fmaxf(mx, ml[k][0]);
#pragma unroll
      for (int k = 0; k < NWAVE; ++k) L = fmaf(__builtin_amdgcn_exp2f((ml[k][0] - mx) * LOG2E), ml[k][1], L);
      fw = __builtin_amdgcn_exp2f((m - mx) * LOG2E) * recip(L);
      side = asu((la & 1) ? L : mx) >> (16 * (c & 1));
    }
    asm volatile("" : "+v"(po), "+v"(fw), "+v"(side));
    pv(bA, sl[0]);
    pv(bB, sl[1]);
    STAMP(3);
    {
      f32x4 res;
#pragma unroll
      for (int r = 0; r < 4; ++r) {
        const float own = tb ? acc[1][1][r] : acc[0][0][r], snd = tb ? acc[1][0][r] : acc[0][1][r];
        res[r] = (own + dppf<ROR8>(snd)) * fw;
      }
      *(f32x4*)(lds + L_PART + w * 1024 + lane * 16) = res;
      asm volatile("" ::: "memory");
      unsigned old = 0;
      if (lane == 0) old = __hip_atomic_fetch_add((unsigned*)(lds + L_FLAG) + F_WP + vzero(), 1u, __ATOMIC_RELAXED, __HIP_MEMORY_SCOPE_WORKGROUP);
      asm volatile("" ::: "memory");
      f32x4 pp[NWAVE];
#pragma unroll
      for (int k = 0; k < NWAVE; ++k) pp[k] = *(const f32x4*)(lds + L_PART + k * 1024 + lane * 16);
      __builtin_amdgcn_sched_barrier(0);
      if (__builtin_amdgcn_readfirstlane(old) == NWAVE - 1) {
        const f32x4 sum = ((pp[0] + pp[1]) + (pp[2] + pp[3])) + ((pp[4] + pp[5]) + (pp[6] + pp[7]));
        __builtin_amdgcn_raw_buffer_store_b128(
            u32x4{put_lo(sum[0], tagA), put_lo(sum[1], side), put_lo(sum[2], tagA >> 8), put_lo(sum[3], side >> 8)}, rws, po, 0, AUX_SC1);
        flag_add(lds, F_END, lane);
        STAMP(10);
      }
    }
    if (w < 2) {
      const rsrc_t ro = mkrsrc(wo + (size_t)(kvh * 256 + qh * 128 + w * 64) * DM, 64 * DM * 2);
      u32x4 wt[24];
#pragma unroll
      for (int i = 0; i < 24; ++i) {
        if (i % 12 == 0) slot(2 * NT + w + 2 * (i / 12), false);
        wt[i] = __builtin_amdgcn_raw_buffer_load_b128(
            ro, 2 * (i % 3) < nwk ? ((i / 3) * 8 + (lane >> 3)) * (DM * 2) + (s & 3) * 256 + (i % 3) * 128 + (lane & 7) * 16 : OOB, 0, AUX_NT);
      }
#pragma unroll
      for (int i = 0; i < 24; ++i)
        *(u32x4*)(lds + L_WO + w * 24576 + (i % 3) * 8192 + ((i / 3) * 8 + ((lane >> 3) & 4) + ((lane >> 2) & 2) + ((lane >> 4) & 1)) * 128 +
                  (((lane & 7) * 16) ^ (((i / 3) & 3) * 32))) = wt[i];
      flag_add(lds, F_WO, lane);
      STAMP(4);
    } else if (wk < nwk) {
      const int n = lane & 15;
      int oo = n < 2 ? WS_OX + (b * 18 + (s & 3) * 4 + wk) * 4096 + (kvh * 2 + qh) * 128 + n * 64 + la * 16 : OOB;
      asm volatile("" : "+v"(oo));
      flag_wait<1>(lds, F_WO, 2);
      u32x4 wof[2][4];
      {
        typedef __attribute__((address_space(3))) s16x4* lds_s16x4;
        unsigned char* img = lds + L_WO + (wk >> 1) * 8192 + ((n >> 2) * 8 + (la >> 1) * 4 + (la & 1)) * 128 + (lane & 3) * 8;
#pragma unroll
        for (int t = 0; t < 2; ++t)
#pragma unroll
          for (int ks = 0; ks < 4; ++ks) {
            unsigned char* p = img + (ks >> 1) * 24576 + (ks & 1) * 4096 + (((wk & 1) * 2 + t) ^ (n >> 2)) * 32;
            const u32x2 lo = __builtin_bit_cast(u32x2, __builtin_amdgcn_ds_read_tr16_b64_v4i16((lds_s16x4)p));
            const u32x2 hi = __builtin_bit_cast(u32x2, __builtin_amdgcn_ds_read_tr16_b64_v4i16((lds_s16x4)(p + 256)));
            wof[t][ks] = u32x4{lo[0], lo[1], hi[0], hi[1]};
          }
      }
      u32x4 ob[4];
#pragma unroll 1
      for (unsigned spin = 0; spin < (SPIN_CAP << 4); ++spin) {
        asm volatile("" ::: "memory");
        const unsigned f = __hip_atomic_load((unsigned*)(lds + L_FLAG) + F_O, __ATOMIC_RELAXED, __HIP_MEMORY_SCOPE_WORKGROUP);
        asm volatile("" ::: "memory");
#pragma unroll
        for (int ks = 0; ks < 4; ++ks) ob[ks] = *(const u32x4*)(lds + L_OA + ks * 64 + la * 16);
        __builtin_amdgcn_sched_barrier(0);
        if (__builtin_amdgcn_readfirstlane(f) != 0u) break;
      }
      STAMP(6);
      f32x4 d0 = {0.f, 0.f, 0.f, 0.f}, d1 = {0.f, 0.f, 0.f, 0.f};
#pragma unroll
      for (int ks = 0; ks < 4; ++ks) { d0 = mfma32(wof[0][ks], ob[ks], d0); d1 = mfma32(wof[1][ks], ob[ks], d1); }
      const f32x4 d = n ? d1 : d0;
      __builtin_amdgcn_raw_buffer_store_b128(
          u32x4{put_lo(d[0], tagA), put_lo(d[1], tagA >> 8), put_lo(d[2], tagA >> 16), put_lo(d[3], tagA >> 24)}, rws, oo, 0, AUX_SC1);
      STAMP(11);
    }
  }
}

}

extern "C" void kernel_launch(void* const* d_in, const int* in_sizes, int n_in, void* d_out, int out_size, void* d_ws,
                              size_t ws_size, hipStream_t stream) {
  (void)in_sizes; (void)n_in; (void)out_size; (void)ws_size;
  fused_decode_kernel<<<dim3(NWG), dim3(NTHR), 0, stream>>>(
      (const unsigned short*)d_in[0], (const unsigned char*)d_in[1], (const unsigned char*)d_in[2],
      (const unsigned short*)d_in[3], (const unsigned short*)d_in[4], (const unsigned short*)d_in[6],
      (const unsigned short*)d_in[7], (const unsigned short*)d_in[8], (const unsigned short*)d_in[9],
      (unsigned short*)d_out, (unsigned char*)d_ws);
}
```

```cpp
#include <hip/hip_runtime.h>
#include <hip/hip_bf16.h>
#include <stdint.h>

namespace {

typedef unsigned u32x2 __attribute__((ext_vector_type(2)));
typedef unsigned u32x4 __attribute__((ext_vector_type(4)));
typedef float f32x2 __attribute__((ext_vector_type(2)));
typedef float f32x4 __attribute__((ext_vector_type(4)));
typedef short s16x4 __attribute__((ext_vector_type(4)));
typedef short s16x8 __attribute__((ext_vector_type(8)));
typedef __bf16 bf16x2 __attribute__((ext_vector_type(2)));
typedef __amdgpu_buffer_rsrc_t rsrc_t;

constexpr int DM = 576, SK = 32768, DH = 128, FF = 4608;
constexpr int NSPLIT = 8, NWG = 256, NWAVE = 8, NTHR = (NWAVE + 1) * 64;
constexpr int KEYS_WG = SK / NSPLIT;
constexpr int TILE_BYTES = 32 * DH * 2;
constexpr int NT = KEYS_WG / 32;
constexpr int SPAN = 2 * KEYS_WG * DH * 2;
constexpr int S = 4, NH = NWAVE - S;
constexpr int NPAIR = (NT / 2 - NH) / S;
static_assert(2 * (NPAIR * S + NH) == NT && NH == S, "every tile has one owner, every stream one helper");

constexpr int SCA = 125;
constexpr int SCT = 427;
constexpr int GAP = 25;
constexpr int CBETA = 6;
constexpr int GLT = 160;
constexpr int GLQ = 210;
constexpr int PFREE = 40;
constexpr int KD = 15;
constexpr int KG = 20;
constexpr int PFD = 3;
constexpr int PSL = 6;
constexpr int QPS = 2;
constexpr int T1D = 17;
constexpr int T2D = 22;
constexpr int PGAP = 2 * NWAVE + 2 * S * NPAIR, PEND = 2 * NT - 2 * NH;

constexpr int AUX_NT = 2, AUX_SC1 = 16;
constexpr unsigned RSRC3 = 0x00020000u;
constexpr unsigned SPIN_CAP = 1u << 19;
constexpr int OOB = 0x7ff00000;

constexpr int WS_EPOCH = 0;
constexpr int WS_QX = 4096;
constexpr int WS_PX = WS_QX + 32 * 4096;
constexpr int WS_OX = WS_PX + NWG * 1024;
constexpr int WS_END = WS_OX + 2 * 18 * 4096;

constexpr int L_FLAG = 0;
constexpr int L_TAG = 64;
constexpr int L_XN = 80;
constexpr int L_QPART = L_XN + 1280;
constexpr int L_QS = L_QPART + 8 * 64 * 4;
constexpr int L_OA = L_QS + 512;
constexpr int L_YP = L_OA + 256;
constexpr int L_ML = L_YP + 4 * 8 * 4;
constexpr int L_PART = L_ML + NWAVE * 16;
constexpr int L_WO = L_PART + NWAVE * 1024;
constexpr int L_SC = L_WO + 128 * 384;
constexpr int L_END = L_SC + S * NPAIR * 512;
constexpr int F_QP = 0, F_Q = 1, F_WP = 2, F_END = 3, F_O = 4, F_Y = 5, F_ARR = 6, F_WO = 8, F_ML = 9, F_SH = 10, F_T = 11, F_P1 = 12;

constexpr float LOG2E = 1.4426950408889634f;
constexpr float SCALE = 0.08838834764831845f;
constexpr float LN_EPS = 1e-5f;

__device__ __forceinline__ float asf(unsigned u) { return __builtin_bit_cast(float, u); }
__device__ __forceinline__ unsigned asu(float f) { return __builtin_bit_cast(unsigned, f); }
__device__ __forceinline__ float bflo(unsigned u) { return asf(u << 16); }
__device__ __forceinline__ float bfhi(unsigned u) { return asf(u & 0xffff0000u); }
__device__ __forceinline__ unsigned pack_bf16(float lo, float hi) {
  f32x2 v = {lo, hi};
  return __builtin_bit_cast(unsigned, __builtin_convertvector(v, bf16x2));
}
__device__ __forceinline__ float rbf(float f) { return (float)(__bf16)f; }
template <int CTRL> __device__ __forceinline__ unsigned dppu(unsigned x) {
  return (unsigned)__builtin_amdgcn_mov_dpp((int)x, CTRL, 0xf, 0xf, true);
}
template <int CTRL> __device__ __forceinline__ float dppf(float x) { return asf(dppu<CTRL>(asu(x))); }
constexpr int XOR1 = 0xB1, XOR2 = 0x4E, XOR7 = 0x141, ROR4 = 0x124, ROR8 = 0x128;
constexpr int QB0 = 0x00, QB1 = 0x55, QB2 = 0xAA, QB3 = 0xFF;
__device__ __forceinline__ float rows_sum(float x) {
  auto s = __builtin_amdgcn_permlane16_swap(asu(x), asu(x), false, false);
  x = asf(s[0]) + asf(s[1]);
  auto t = __builtin_amdgcn_permlane32_swap(asu(x), asu(x), false, false);
  return asf(t[0]) + asf(t[1]);
}
__device__ __forceinline__ float rows_max(float x) {
  auto s = __builtin_amdgcn_permlane16_swap(asu(x), asu(x), false, false);
  x = fmaxf(asf(s[0]), asf(s[1]));
  auto t = __builtin_amdgcn_permlane32_swap(asu(x), asu(x), false, false);
  return fmaxf(asf(t[0]), asf(t[1]));
}
__device__ __forceinline__ float rows_sum_t(float v0, float v1, float v2, float v3) {
  const auto p0 = __builtin_amdgcn_permlane32_swap(asu(v0), asu(v2), false, false);
  const auto p1 = __builtin_amdgcn_permlane32_swap(asu(v1), asu(v3), false, false);
  const auto p2 = __builtin_amdgcn_permlane16_swap(asu(asf(p0[0]) + asf(p0[1])), asu(asf(p1[0]) + asf(p1[1])), false, false);
  return asf(p2[0]) + asf(p2[1]);
}
__device__ __forceinline__ float wave_sum(float x) {
  x += dppf<XOR1>(x); x += dppf<XOR2>(x); x += dppf<ROR4>(x); x += dppf<ROR8>(x);
  return rows_sum(x);
}
__device__ __forceinline__ float recip(float x) {
  const float r = __builtin_amdgcn_rcpf(x);
  return fmaf(fmaf(-x, r, 1.0f), r, r);
}
__device__ __forceinline__ rsrc_t mkrsrc(const void* p, int bytes) {
  return __builtin_amdgcn_make_buffer_rsrc(const_cast<void*>(p), 0, bytes, RSRC3);
}
__device__ __forceinline__ f32x4 mfma32(u32x4 a, u32x4 b, f32x4 c) {
  return __builtin_amdgcn_mfma_f32_16x16x32_bf16(__builtin_bit_cast(s16x8, a), __builtin_bit_cast(s16x8, b), c, 0, 0, 0);
}
__device__ __forceinline__ f32x4 mfma16(unsigned a0, unsigned a1, unsigned b0, unsigned b1, f32x4 c) {
  u32x2 a = {a0, a1}, b = {b0, b1};
  return __builtin_amdgcn_mfma_f32_16x16x16bf16_1k(__builtin_bit_cast(s16x4, a), __builtin_bit_cast(s16x4, b), c, 0, 0, 0);
}
__device__ __forceinline__ unsigned put_lo(float v, unsigned byte) { return (asu(v) & 0xffffff00u) | (byte & 0xffu); }
__device__ __forceinline__ float cut_lo(unsigned u) { return asf(u & 0xffffff00u); }
__device__ __forceinline__ unsigned now32() { return (unsigned)__builtin_amdgcn_s_memrealtime(); }
__device__ __forceinline__ void wait_until(unsigned t) {
  while ((int)(now32() - t) < 0) __builtin_amdgcn_s_sleep(1);
}
__device__ __forceinline__ int vzero() { int z = 0; asm("" : "+v"(z)); return z; }
__device__ __forceinline__ void stouch(const void* p) {
  asm volatile("" : "+s"(p));
  const unsigned v = *(const __attribute__((address_space(4))) unsigned*)p;
  asm volatile("" :: "s"(v));
}
__device__ __forceinline__ void flag_add(unsigned char* lds, int f, int lane) {
  asm volatile("" ::: "memory");
  if (lane == 0) __hip_atomic_fetch_add((unsigned*)(lds + L_FLAG) + f + vzero(), 1u, __ATOMIC_RELAXED, __HIP_MEMORY_SCOPE_WORKGROUP);
  asm volatile("" ::: "memory");
}
__device__ __forceinline__ unsigned flag_get(unsigned char* lds, int f) {
  return __builtin_amdgcn_readfirstlane(__hip_atomic_load((unsigned*)(lds + L_FLAG) + f, __ATOMIC_RELAXED, __HIP_MEMORY_SCOPE_WORKGROUP));
}
template <int SLEEP> __device__ __forceinline__ void flag_wait(unsigned char* lds, int f, unsigned n) {
#pragma unroll 1
  for (unsigned spin = 0; spin < (SPIN_CAP << 4); ++spin) {
    if (flag_get(lds, f) >= n) break;
    if (SLEEP) __builtin_amdgcn_s_sleep(SLEEP);
  }
  asm volatile("" ::: "memory");
}
__device__ __forceinline__ void ln_stats(const float (&v)[10], bool last_valid, float& mean_b, float& den_b) {
  float s = 0.f;
#pragma unroll
  for (int i = 0; i < 8; ++i) s += v[i];
  s += last_valid ? (v[8] + v[9]) : 0.f;
  const float mean = wave_sum(s) * (1.0f / DM);
  float q = 0.f;
#pragma unroll
  for (int i = 0; i < 8; ++i) { const float d = v[i] - mean; q = fmaf(d, d, q); }
  { const float d8 = v[8] - mean, d9 = v[9] - mean; q += last_valid ? fmaf(d8, d8, d9 * d9) : 0.f; }
  const float var = wave_sum(q) * (1.0f / DM);
  mean_b = rbf(mean);
  den_b = rbf(__builtin_amdgcn_sqrtf(rbf(rbf(var) + LN_EPS)));
}
__device__ __forceinline__ float ln_apply(float x, float mean_b, float den_b) {
  const float d = rbf(x - mean_b), r = __builtin_amdgcn_rcpf(den_b), q = d * r;
  return rbf(fmaf(fmaf(-q, den_b, d), r, q));
}

__global__ void __launch_bounds__(NTHR, 3)
fused_decode_kernel(const unsigned short* __restrict__ x, const unsigned char* __restrict__ kc, const unsigned char* __restrict__ vc,
                    const unsigned short* __restrict__ wq, const unsigned short* __restrict__ wo,
                    const unsigned short* __restrict__ w1, const unsigned short* __restrict__ b1,
                    const unsigned short* __restrict__ w2, const unsigned short* __restrict__ b2,
                    unsigned short* __restrict__ out, unsigned char* ws) {
  __shared__ __attribute__((aligned(16))) unsigned char lds[L_END];
  const int lane = threadIdx.x & 63, wv = __builtin_amdgcn_readfirstlane(threadIdx.x >> 6), w = wv < 8 ? wv ^ 4 : wv;
  const unsigned tst = now32();
  const int g = blockIdx.x >> 3, s = (blockIdx.x + 3 * g) & 7, id = g * 8 + s, b = g >> 4, kvh = g & 15;
  const rsrc_t rws = mkrsrc(ws, WS_END);
  const rsrc_t rx = mkrsrc(x + b * DM, DM * 2);
  const int qh = s >> 2, cb = (s + (kvh >> 2)) & 3;
  const int la = lane >> 4;
  const int red = kvh * 8 + s;

  u32x2 ep = {0u, 0u};
  if (w == NWAVE) {
    ep = __builtin_amdgcn_raw_buffer_load_b64(rws, WS_EPOCH + b * 256, 0, 0);
    if (lane < 16) *((unsigned*)(lds + L_FLAG) + lane) = 0u;
    asm volatile("s_waitcnt lgkmcnt(0)\n\ts_barrier" ::: "memory");
  }
  if (w >= NWAVE) {
    __builtin_amdgcn_s_setprio(3);
    const unsigned e0 = __builtin_amdgcn_readfirstlane(ep[0]);
    const unsigned e1 = ((e0 & 0x7f7f7f7fu) + 0x01010101u) ^ (e0 & 0x80808080u);
    const unsigned tagA = e1 ^ 0xb77a3d00u;
    const unsigned tagB = __builtin_amdgcn_readfirstlane(ep[1]) + 1u;
    if (lane == 0) *(unsigned*)(lds + L_TAG) = tagA;
    flag_wait<0>(lds, F_QP, 4);
    {
      f32x2 q2 = *(const f32x2*)(lds + L_QPART + (lane & 31) * 8);
#pragma unroll
      for (int ww = 1; ww < 8; ++ww) q2 += *(const f32x2*)(lds + L_QPART + ww * 256 + (lane & 31) * 8);
      __builtin_amdgcn_raw_buffer_store_b128(u32x4{tagA, asu(q2[0]), (tagB & 0xffffu) | (tst << 16), asu(q2[1])}, rws,
                                             lane < 32 ? WS_QX + g * 4096 + qh * 2048 + cb * 512 + lane * 16 : OOB, 0, AUX_SC1);
    }
    __builtin_amdgcn_s_sleep(PSL);
    {
      float qa[4], qb[4];
      int ts[4];
      int of[4];
#pragma unroll
      for (int i = 0; i < 4; ++i) { qa[i] = 0.f; qb[i] = 0.f; ts[i] = 0; of[i] = WS_QX + g * 4096 + i * 1024 + lane * 16; }
#pragma unroll 1
      for (unsigned spin = 0; spin < SPIN_CAP; ++spin) {
        asm volatile("" ::: "memory");
        u32x4 gr[4];
#pragma unroll
        for (int i = 0; i < 4; ++i) gr[i] = __builtin_amdgcn_raw_buffer_load_b128(rws, of[i], 0, AUX_SC1);
        bool done = true;
#pragma unroll
        for (int i = 0; i < 4; ++i) {
          const bool fresh = (gr[i][0] == tagA) & (((gr[i][2] ^ tagB) & 0xffffu) == 0u) & (of[i] != OOB);
          ts[i] = fresh ? (int)(gr[i][2] - (tst << 16)) >> 16 : ts[i];
          qa[i] = fresh ? asf(gr[i][1]) : qa[i]; qb[i] = fresh ? asf(gr[i][3]) : qb[i];
          of[i] = fresh ? OOB : of[i];
          done &= of[i] == OOB;
        }
        if (__all(done)) break;
        __builtin_amdgcn_s_sleep(QPS);
      }
      *(unsigned*)(lds + L_QS + lane * 4) = pack_bf16(qa[0] + qa[2], qb[0] + qb[2]);
      *(unsigned*)(lds + L_QS + 256 + lane * 4) = pack_bf16(qa[1] + qa[3], qb[1] + qb[3]);
      {
        int sm = 0, mx = -32768;
#pragma unroll
        for (int i = 0; i < 4; ++i) {
          const int t0 = __builtin_amdgcn_readlane(ts[i], 0), t1 = __builtin_amdgcn_readlane(ts[i], 32);
          sm += t0 + t1; mx = max(mx, max(t0, t1));
        }
        sm >>= 3;
        if (lane == 0) *(unsigned*)(lds + L_TAG + 4) = tst + (unsigned)(sm + ((mx - sm) * CBETA >> 4));
      }
      flag_add(lds, F_Q, lane);
    }
    const int fc = (lane & 1) * 4 + la;
    const unsigned xraw = __builtin_amdgcn_raw_buffer_load_b32(rx, red * 16 + (fc >> 1) * 4, 0, 0);
    const unsigned b2v = __builtin_amdgcn_raw_buffer_load_b16(mkrsrc(b2, 4), 0, 0, 0);
    wait_until(tst + 1000u + (id & 31) * 16u);
    stouch(vc + (size_t)(id >> 1) * SPAN);
    stouch(wo + (size_t)(kvh * 256 + qh * 128) * DM);
    stouch(wo + (size_t)(kvh * 256 + qh * 128 + 127) * DM);
    if (red == 127) {
      wait_until(tst + 2000u);
#pragma unroll 1
      for (unsigned spin = 0; spin < SPIN_CAP; ++spin) {
        asm volatile("" ::: "memory");
        const u32x4 v0 = __builtin_amdgcn_raw_buffer_load_b128(rws, WS_QX + b * 65536 + lane * 512, 0, AUX_SC1);
        const u32x4 v1 = __builtin_amdgcn_raw_buffer_load_b128(rws, WS_QX + b * 65536 + 32768 + lane * 512, 0, AUX_SC1);
        if (__all((v0[0] == tagA) & (v1[0] == tagA) & ((((v0[2] ^ tagB) | (v1[2] ^ tagB)) & 0xffffu) == 0u))) break;
        __builtin_amdgcn_s_sleep(16);
      }
      __builtin_amdgcn_raw_buffer_store_b64(u32x2{e1, tagB}, rws, lane == 0 ? WS_EPOCH + b * 256 : OOB, 0, AUX_SC1);
    }
    float yv = 0.f, xfin = (fc & 1) ? bfhi(xraw) : bflo(xraw);
    const rsrc_t rout = mkrsrc(out + b * DM, DM * 2 + 2);
    int oout = (lane & 14) == 0 ? (red * 8 + fc) * 2 : OOB;
    if (red < 72) {
      flag_wait<16>(lds, F_Y, 4);
#pragma unroll
      for (int ww = 0; ww < 4; ++ww) yv += *(const float*)(lds + L_YP + ww * 32 + fc * 4);
      yv = rbf(rbf(yv) + bflo(b2v));
      __builtin_amdgcn_raw_buffer_store_b16((unsigned short)0, rout, oout, 0, 0);
    }
    int oa = (lane & 8) ? L_QPART + lane * 8 : L_OA + la * 64 + (lane & 7) * 8;
    asm volatile("" : "+v"(yv), "+v"(xfin), "+v"(oout), "+v"(oa));
    flag_wait<16>(lds, F_WP, 1);
    u32x4 ga[4];
#pragma unroll
    for (int i = 0; i < 4; ++i) ga[i] = u32x4{0u, 0u, 0u, 0u};
    int of1 = WS_PX + (g * 8 + (s & 4)) * 1024 + lane * 16;
    asm volatile("" : "+v"(of1), "+v"(ga[0]), "+v"(ga[1]), "+v"(ga[2]), "+v"(ga[3]));
    flag_wait<0>(lds, F_END, 1);
    __builtin_amdgcn_s_sleep(T1D);
    {
      unsigned bad = 1u;
#pragma unroll 1
      for (unsigned spin = 0; spin < SPIN_CAP; ++spin) {
        asm volatile("" ::: "memory");
        if (bad != 0u) {
#pragma unroll
          for (int i = 0; i < 4; ++i) ga[i] = __builtin_amdgcn_raw_buffer_load_b128(rws, of1 + i * 1024, 0, AUX_SC1);
        }
        bad = 0u;
#pragma unroll
        for (int i = 0; i < 4; ++i) bad |= (ga[i][0] ^ tagA) | (ga[i][2] ^ (tagA >> 8));
        bad &= 0xffu;
        if (__all(bad == 0u)) break;
        bad |= dppu<XOR1>(bad); bad |= dppu<XOR2>(bad); bad |= dppu<XOR7>(bad);
        __builtin_amdgcn_s_sleep(1);
      }
      const unsigned hm = __builtin_amdgcn_perm(ga[0][3], ga[0][1], 0x0c0c0400u) << (16 * (lane & 1));
      const unsigned hl = __builtin_amdgcn_perm(ga[1][3], ga[1][1], 0x0c0c0400u) << (16 * (lane & 1));
      const float pm = asf(hm | dppu<XOR1>(hm)), pl = asf(hl | dppu<XOR1>(hl));
      const float M = rows_max(fmaxf(pm, dppf<ROR8>(pm)));
      const float w0 = __builtin_amdgcn_exp2f((pm - M) * LOG2E) * pl;
      const float inv = recip(rows_sum(w0 + dppf<ROR8>(w0)));
      float v[4];
#pragma unroll
      for (int r = 0; r < 4; ++r) {
        const float t = rows_sum_t(w0 * cut_lo(ga[0][r]), w0 * cut_lo(ga[1][r]), w0 * cut_lo(ga[2][r]), w0 * cut_lo(ga[3][r]));
        v[r] = (t + dppf<ROR8>(t)) * inv;
      }
      *(u32x2*)(lds + oa) = u32x2{pack_bf16(v[0], v[1]), pack_bf16(v[2], v[3])};
      flag_add(lds, F_O, lane);
    }
    if (red < 72) {
      __builtin_amdgcn_s_sleep(T2D);
      u32x4 gb = {0u, 0u, 0u, 0u};
      const int of2 = WS_OX + (b * 18 + (red >> 2)) * 4096 + (lane >> 1) * 128 + (red & 3) * 32 + (lane & 1) * 16;
      unsigned bad = 1u;
#pragma unroll 1
      for (unsigned spin = 0; spin < SPIN_CAP; ++spin) {
        asm volatile("" ::: "memory");
        if (bad != 0u) gb = __builtin_amdgcn_raw_buffer_load_b128(rws, of2, 0, AUX_SC1);
        bad = (__builtin_amdgcn_perm(gb[1], gb[0], 0x0c0c0400u) | __builtin_amdgcn_perm(gb[3], gb[2], 0x04000c0cu)) ^ tagA;
        if (__all(bad == 0u)) break;
        bad |= dppu<XOR1>(bad);
        __builtin_amdgcn_s_sleep(1);
      }
      float t = rows_sum_t(cut_lo(gb[0]), cut_lo(gb[1]), cut_lo(gb[2]), cut_lo(gb[3]));
      t += dppf<XOR2>(t); t += dppf<ROR4>(t); t += dppf<ROR8>(t);
      __builtin_amdgcn_raw_buffer_store_b16((unsigned short)pack_bf16(rbf(xfin + rbf(t)) + yv, 0.f), rout, oout, 0, 0);
    }
  } else {
    const rsrc_t rk = mkrsrc(kc + (size_t)(id >> 1) * SPAN, SPAN);
    const rsrc_t rv = mkrsrc(vc + (size_t)(id >> 1) * SPAN, SPAN);
    auto poff = [&](int d, int h) {
      return ((d & 3) | (h << 2) | ((id & 1) << 3) | ((d >> 2) << 4)) * TILE_BYTES;
    };
    const int voffK = ((lane & 15) >> 2) * 256 + la * 64 + (lane & 3) * 16;
    const int voffV = lane * 16;
    u32x4 bA[8], bB[8];
    auto request = [&](u32x4 (&x)[8], rsrc_t r, int voff, int so) {
      asm volatile("" : "+v"(voff));
#pragma unroll
      for (int u = 0; u < 8; ++u) x[u] = __builtin_amdgcn_raw_buffer_load_b128(r, voff + (u & 3) * 1024, so + (u >> 2) * 4096, AUX_NT);
      __builtin_amdgcn_sched_barrier(0);
    };
    if (w >= 4) {
      const int rg = (w - 4) * 8 + (lane >> 3), c8 = lane & 7;
      unsigned xs[5];
      u32x4 wqv[9];
#pragma unroll
      for (int i = 0; i < 5; ++i) xs[i] = __builtin_amdgcn_raw_buffer_load_b32(rx, (lane + 64 * i) * 4, 0, 0);
      __builtin_amdgcn_sched_barrier(0);
      const rsrc_t rq = mkrsrc(wq + qh * 288 * 4096 + kvh * 256 + cb * 64, (287 * 4096 + 64) * 2);
#pragma unroll
      for (int i = 0; i < 9; ++i) wqv[i] = __builtin_amdgcn_raw_buffer_load_b128(rq, (rg + 32 * i) * 8192 + c8 * 16, 0, AUX_NT);
      __builtin_amdgcn_sched_barrier(0);
      __builtin_amdgcn_s_barrier();
      __builtin_amdgcn_sched_barrier(0);
      float xv[10], mean1, den1;
#pragma unroll
      for (int i = 0; i < 5; ++i) { xv[2 * i] = bflo(xs[i]); xv[2 * i + 1] = bfhi(xs[i]); }
      ln_stats(xv, lane < 32, mean1, den1);
#pragma unroll
      for (int i = 0; i < 5; ++i)
        *(unsigned*)(lds + L_XN + (lane + 64 * i) * 4) = pack_bf16(ln_apply(xv[2 * i], mean1, den1), ln_apply(xv[2 * i + 1], mean1, den1));
      asm volatile("" ::: "memory");
      f32x4 qa = {0.f, 0.f, 0.f, 0.f};
#pragma unroll
      for (int i = 0; i < 9; ++i) {
        const unsigned t = (unsigned)*(const unsigned short*)(lds + L_XN + (qh * 288 + rg + 32 * i) * 2) << (16 * (lane & 1));
        const int jp = c8 >> 1;
        qa = mfma32(wqv[i], u32x4{jp == 0 ? t : 0u, jp == 1 ? t : 0u, jp == 2 ? t : 0u, jp == 3 ? t : 0u}, qa);
      }
      if ((lane >> 5) == ((lane >> 3) & 1)) {
        float* qp = (float*)(lds + L_QPART) + ((w - 4) * 2 + (lane >> 5)) * 64 + (la & 1) * 32 + c8;
#pragma unroll
        for (int r = 0; r < 4; ++r) qp[r * 8] = qa[r];
      }
      flag_add(lds, F_QP, lane);
    }
    if (w < 4) asm volatile("s_barrier" ::: "memory");
    if (w >= PFD) flag_wait<1>(lds, F_ARR, w - PFD + 1);
    __builtin_amdgcn_sched_barrier(0);
    request(bA, rk, voffK, poff(w, 0));
    request(bB, rk, voffK, poff(w, 1));
    asm volatile("s_waitcnt vmcnt(0)" ::: "memory");
    flag_add(lds, F_ARR, lane);
    flag_wait<1>(lds, F_Q, 1);

    const int c = lane & 3, hb = (lane >> 2) & 1, tb = (lane >> 3) & 1;
    const unsigned tagA = __builtin_amdgcn_readfirstlane(*(const unsigned*)(lds + L_TAG));
    const unsigned tb0 = __builtin_amdgcn_readfirstlane(*(const unsigned*)(lds + L_TAG + 4));
    float m;
    f32x4 acc[2][2];
#pragma unroll
    for (int i = 0; i < 2; ++i)
#pragma unroll
      for (int j = 0; j < 2; ++j) acc[i][j] = f32x4{0.f, 0.f, 0.f, 0.f};
    const u32x4 qf = *(const u32x4*)(lds + L_QS + (hb * 128 + la * 32 + c * 8) * 2);
    const u32x4 zero4 = {0u, 0u, 0u, 0u};
    const u32x4 q0 = tb ? zero4 : qf, q1 = tb ? qf : zero4;
    const unsigned mask0 = c == 0 ? 0x0000ffffu : c == 1 ? 0xffff0000u : 0u;
    const unsigned mask1 = c == 2 ? 0x0000ffffu : c == 3 ? 0xffff0000u : 0u;
    auto qk = [&](const u32x4 (&x)[8], f32x4 (&sc)[4]) {
#pragma unroll
      for (int i = 0; i < 4; ++i) sc[i] = mfma32(x[i], q0, f32x4{0.f, 0.f, 0.f, 0.f});
#pragma unroll
      for (int i = 0; i < 4; ++i) sc[i] = mfma32(x[4 + i], q1, sc[i]);
    };
    auto score = [&](const f32x4 (&sc)[4]) {
      float t4[4];
#pragma unroll
      for (int i = 0; i < 4; ++i) t4[i] = (dppf<QB0>(sc[i][0]) + dppf<QB1>(sc[i][1])) + (dppf<QB2>(sc[i][2]) + dppf<QB3>(sc[i][3]));
      const float raw = (c & 2) ? ((c & 1) ? t4[3] : t4[2]) : ((c & 1) ? t4[1] : t4[0]);
      float sv = rbf(rbf(raw) * SCALE);
      asm volatile("" : "+v"(sv));
      return sv;
    };
    auto head_max = [&](float v) {
      v = fmaxf(v, dppf<XOR1>(v));
      v = fmaxf(v, dppf<XOR2>(v));
      v = fmaxf(v, dppf<ROR8>(v));
      return rows_max(v);
    };
    auto pv = [&](const u32x4 (&x)[8], float sv) {
      const float p = __builtin_amdgcn_exp2f((sv - m) * LOG2E);
      const unsigned pk = pack_bf16(p, p);
      unsigned pb0[4], pb1[4];
      pb0[0] = dppu<QB0>(pk) & mask0; pb1[0] = dppu<QB0>(pk) & mask1;
      pb0[1] = dppu<QB1>(pk) & mask0; pb1[1] = dppu<QB1>(pk) & mask1;
      pb0[2] = dppu<QB2>(pk) & mask0; pb1[2] = dppu<QB2>(pk) & mask1;
      pb0[3] = dppu<QB3>(pk) & mask0; pb1[3] = dppu<QB3>(pk) & mask1;
#pragma unroll
      for (int u = 0; u < 8; ++u) {
        acc[u >> 2][0] = mfma16(x[u][0], x[u][1], pb0[u & 3], pb1[u & 3], acc[u >> 2][0]);
        acc[u >> 2][1] = mfma16(x[u][2], x[u][3], pb0[u & 3], pb1[u & 3], acc[u >> 2][1]);
      }
    };
    auto put_ml = [&](int k, float mk, float lk) {
      lk += dppf<XOR1>(lk);
      lk += dppf<XOR2>(lk);
      lk += dppf<ROR8>(lk);
      lk = rows_sum(lk);
      if ((lane & ~4) == 0) *(f32x2*)(lds + L_ML + (k * 2 + hb) * 8) = f32x2{mk, lk};
      flag_add(lds, F_ML, lane);
    };
    const int hw = w - S, wk = w - 2, nwk = (s & 3) == 3 ? 6 : 4;
    int ontime = 0;
    auto slot = [&](int p, bool watch) {
      __builtin_amdgcn_sched_barrier(0);
      if (watch && lane == 0) {
        unsigned* fl = (unsigned*)(lds + L_FLAG) + vzero();
        const unsigned t = now32() - tb0;
        const int gap = (int)(t - __hip_atomic_fetch_max(fl + F_T, t, __ATOMIC_RELAXED, __HIP_MEMORY_SCOPE_WORKGROUP));
        if (gap > GLT && ontime >= 3 && (p < PGAP - 2 * S || p >= PGAP + 4 * S)) {
          __hip_atomic_fetch_max(fl + F_T, t + GLQ, __ATOMIC_RELAXED, __HIP_MEMORY_SCOPE_WORKGROUP);
          __hip_atomic_fetch_add(fl + F_SH, (unsigned)GLQ, __ATOMIC_RELAXED, __HIP_MEMORY_SCOPE_WORKGROUP);
        }
      }
      const unsigned due = tb0 + SCA + (p >= PGAP ? GAP : 0) + (unsigned)(p * SCT >> 4);
      ontime = (int)(now32() - due - flag_get(lds, F_SH)) < 0 ? ontime + 1 : 0;
      if (watch && p >= PFREE) {
        asm volatile("s_waitcnt vmcnt(0)" ::: "memory");
        const unsigned t = now32();
        const int late = (int)(t - due - flag_get(lds, F_SH)) + KD;
        const unsigned lim = t + (unsigned)max(KD - (max(late, 0) * KG >> 8), 0);
        while ((int)(now32() - lim) < 0) {}
      }
      while ((int)(now32() - due - flag_get(lds, F_SH)) < 0) {}
      __builtin_amdgcn_sched_barrier(0);
    };
    f32x2 sl;
    if (w < S) {
      __builtin_amdgcn_s_setprio(2);
      unsigned char* sco = lds + L_SC + w * (NPAIR * 512) + lane * 8;
      float mx = -INFINITY;
      for (int j = 0; j < NPAIR; ++j) {
        const bool k = j + 1 < NPAIR;
        const rsrc_t r = k ? rk : rv;
        const int vo = k ? voffK : voffV, d = NWAVE + S * (k ? j : j - 1) + w;
        f32x4 sc[4];
        qk(bA, sc);
        slot(2 * NWAVE + 2 * S * j + w, true);
        request(bA, r, vo, poff(d, 0));
        const float s0 = score(sc);
        qk(bB, sc);
        slot(2 * NWAVE + 2 * S * j + S + w, true);
        request(bB, r, vo, poff(d, 1));
        const float s1 = score(sc);
        mx = fmaxf(mx, fmaxf(s0, s1));
        *(f32x2*)(sco + j * 512) = f32x2{s0, s1};
      }
      m = head_max(mx);
      if ((lane & ~4) == 0) *(float*)(lds + L_ML + (w * 2 + hb) * 8) = m;
      flag_add(lds, F_P1 + w, lane);
      for (int j = 0; j + 1 < NPAIR; ++j) {
        const f32x2 sv = *(const f32x2*)(sco + (NPAIR - 1 - j) * 512);
        const int d = j + 2 < NPAIR ? NWAVE + S * (NPAIR - 3 - j) + w : w;
        pv(bA, sv[0]);
        slot(2 * NWAVE + 2 * S * (NPAIR + j) + w, true);
        request(bA, rv, voffV, poff(d, 0));
        pv(bB, sv[1]);
        slot(2 * NWAVE + 2 * S * (NPAIR + j) + S + w, true);
        request(bB, rv, voffV, poff(d, 1));
      }
      sl = *(const f32x2*)sco;
    }
    asm volatile("" ::: "memory");
    if (w >= S) {
      {
        f32x4 sc[4];
        qk(bA, sc);
        sl[0] = score(sc);
        qk(bB, sc);
        sl[1] = score(sc);
        m = head_max(fmaxf(sl[0], sl[1]));
        put_ml(w, m, __builtin_amdgcn_exp2f((sl[0] - m) * LOG2E) + __builtin_amdgcn_exp2f((sl[1] - m) * LOG2E));
      }
      if (red < 72) {
        const int ht = (w - 4) * 64 + lane;
        unsigned a1[9], ab[9], a2[9];
        const rsrc_t r1 = mkrsrc(w1, FF * 2), rb = mkrsrc(b1, FF * 2), r2 = mkrsrc(w2, FF * 2);
#pragma unroll
        for (int i = 0; i < 9; ++i) {
          a1[i] = __builtin_amdgcn_raw_buffer_load_b32(r1, (ht + 256 * i) * 4, 0, 0);
          ab[i] = __builtin_amdgcn_raw_buffer_load_b32(rb, (ht + 256 * i) * 4, 0, 0);
          a2[i] = __builtin_amdgcn_raw_buffer_load_b32(r2, (ht + 256 * i) * 4, 0, 0);
        }
        __builtin_amdgcn_sched_barrier(0);
        float xn[10];
#pragma unroll
        for (int i = 0; i < 5; ++i) { const unsigned v = *(const unsigned*)(lds + L_XN + (lane + 64 * i) * 4); xn[2 * i] = bflo(v); xn[2 * i + 1] = bfhi(v); }
        float mean2, den2;
        ln_stats(xn, lane < 32, mean2, den2);
        constexpr float GC0 = -2.0f * 0.7978845608028654f * LOG2E, GC1 = GC0 * 0.044715f;
#pragma unroll 1
        for (int j = 0; j < 8; ++j) {
          const float hv = ln_apply(bflo(*(const unsigned short*)(lds + L_XN + (red * 8 + j) * 2)), mean2, den2);
          float y = 0.f;
#pragma unroll
          for (int i = 0; i < 9; ++i) {
            asm volatile("" : "+v"(a1[i]), "+v"(ab[i]), "+v"(a2[i]));
            const unsigned pr = pack_bf16(hv * bflo(a1[i]), hv * bfhi(a1[i]));
            const unsigned uu = pack_bf16(bflo(pr) + bflo(ab[i]), bfhi(pr) + bfhi(ab[i]));
            const float u0 = bflo(uu), u1 = bfhi(uu);
            const float e0 = __builtin_amdgcn_exp2f(u0 * fmaf(u0 * u0, GC1, GC0));
            const float e1 = __builtin_amdgcn_exp2f(u1 * fmaf(u1 * u1, GC1, GC0));
            const unsigned gg = pack_bf16(u0 * __builtin_amdgcn_rcpf(1.0f + e0), u1 * __builtin_amdgcn_rcpf(1.0f + e1));
            y = fmaf(bflo(gg), bflo(a2[i]), y);
            y = fmaf(bfhi(gg), bfhi(a2[i]), y);
          }
          y = wave_sum(y);
          if (lane == 0) *(float*)(lds + L_YP + (w - 4) * 32 + j * 4) = y;
        }
        flag_add(lds, F_Y, lane);
      }
      {
        flag_wait<16>(lds, F_P1 + hw, 1);
        const float ms = *(const float*)(lds + L_ML + (hw * 2 + hb) * 8);
        float ls = 0.f;
#pragma unroll 1
        for (int j = 0; j < NPAIR; ++j) {
          const f32x2 sv = *(const f32x2*)(lds + L_SC + hw * (NPAIR * 512) + lane * 8 + j * 512);
          ls += __builtin_amdgcn_exp2f((sv[0] - ms) * LOG2E) + __builtin_amdgcn_exp2f((sv[1] - ms) * LOG2E);
        }
        put_ml(hw, ms, ls);
      }
      wait_until(tb0 + SCA + (unsigned)(PEND * SCT >> 4) - 60u);
      __builtin_amdgcn_s_setprio(2);
      slot(PEND + hw, false);
      request(bA, rv, voffV, poff(w, 0));
      slot(PEND + NH + hw, false);
      request(bB, rv, voffV, poff(w, 1));
    }
    int po = WS_PX + ((g * 8 + hb * 4 + la) * 8 + s) * 128 + (tb * 4 + c) * 16;
    float fw;
    unsigned side;
    {
      flag_wait<1>(lds, F_ML, NWAVE);
      f32x2 ml[NWAVE];
#pragma unroll
      for (int k = 0; k < NWAVE; ++k) ml[k] = *(const f32x2*)(lds + L_ML + (k * 2 + hb) * 8);
      float mx = ml[0][0], L = 0.f;
#pragma unroll
      for (int k = 1; k < NWAVE; ++k) mx = fmaxf(mx, ml[k][0]);
#pragma unroll
      for (int k = 0; k < NWAVE; ++k) L = fmaf(__builtin_amdgcn_exp2f((ml[k][0] - mx) * LOG2E), ml[k][1], L);
      fw = __builtin_amdgcn_exp2f((m - mx) * LOG2E) * recip(L);
      side = asu((la & 1) ? L : mx) >> (16 * (c & 1));
    }
    asm volatile("" : "+v"(po), "+v"(fw), "+v"(side));
    pv(bA, sl[0]);
    pv(bB, sl[1]);
    {
      f32x4 res;
#pragma unroll
      for (int r = 0; r < 4; ++r) {
        const float own = tb ? acc[1][1][r] : acc[0][0][r], snd = tb ? acc[1][0][r] : acc[0][1][r];
        res[r] = (own + dppf<ROR8>(snd)) * fw;
      }
      *(f32x4*)(lds + L_PART + w * 1024 + lane * 16) = res;
      asm volatile("" ::: "memory");
      unsigned old = 0;
      if (lane == 0) old = __hip_atomic_fetch_add((unsigned*)(lds + L_FLAG) + F_WP + vzero(), 1u, __ATOMIC_RELAXED, __HIP_MEMORY_SCOPE_WORKGROUP);
      asm volatile("" ::: "memory");
      f32x4 pp[NWAVE];
#pragma unroll
      for (int k = 0; k < NWAVE; ++k) pp[k] = *(const f32x4*)(lds + L_PART + k * 1024 + lane * 16);
      __builtin_amdgcn_sched_barrier(0);
      if (__builtin_amdgcn_readfirstlane(old) == NWAVE - 1) {
        const f32x4 sum = ((pp[0] + pp[1]) + (pp[2] + pp[3])) + ((pp[4] + pp[5]) + (pp[6] + pp[7]));
        __builtin_amdgcn_raw_buffer_store_b128(
            u32x4{put_lo(sum[0], tagA), put_lo(sum[1], side), put_lo(sum[2], tagA >> 8), put_lo(sum[3], side >> 8)}, rws, po, 0, AUX_SC1);
        flag_add(lds, F_END, lane);
      }
    }
    if (w < 2) {
      const rsrc_t ro = mkrsrc(wo + (size_t)(kvh * 256 + qh * 128 + w * 64) * DM, 64 * DM * 2);
      u32x4 wt[24];
#pragma unroll
      for (int i = 0; i < 24; ++i) {
        if (i % 12 == 0) slot(2 * NT + w + 2 * (i / 12), false);
        wt[i] = __builtin_amdgcn_raw_buffer_load_b128(
            ro, 2 * (i % 3) < nwk ? ((i / 3) * 8 + (lane >> 3)) * (DM * 2) + (s & 3) * 256 + (i % 3) * 128 + (lane & 7) * 16 : OOB, 0, AUX_NT);
      }
#pragma unroll
      for (int i = 0; i < 24; ++i)
        *(u32x4*)(lds + L_WO + w * 24576 + (i % 3) * 8192 + ((i / 3) * 8 + ((lane >> 3) & 4) + ((lane >> 2) & 2) + ((lane >> 4) & 1)) * 128 +
                  (((lane & 7) * 16) ^ (((i / 3) & 3) * 32))) = wt[i];
      flag_add(lds, F_WO, lane);
    } else if (wk < nwk) {
      const int n = lane & 15;
      int oo = n < 2 ? WS_OX + (b * 18 + (s & 3) * 4 + wk) * 4096 + (kvh * 2 + qh) * 128 + n * 64 + la * 16 : OOB;
      asm volatile("" : "+v"(oo));
      flag_wait<1>(lds, F_WO, 2);
      u32x4 wof[2][4];
      {
        typedef __attribute__((address_space(3))) s16x4* lds_s16x4;
        unsigned char* img = lds + L_WO + (wk >> 1) * 8192 + ((n >> 2) * 8 + (la >> 1) * 4 + (la & 1)) * 128 + (lane & 3) * 8;
#pragma unroll
        for (int t = 0; t < 2; ++t)
#pragma unroll
          for (int ks = 0; ks < 4; ++ks) {
            unsigned char* p = img + (ks >> 1) * 24576 + (ks & 1) * 4096 + (((wk & 1) * 2 + t) ^ (n >> 2)) * 32;
            const u32x2 lo = __builtin_bit_cast(u32x2, __builtin_amdgcn_ds_read_tr16_b64_v4i16((lds_s16x4)p));
            const u32x2 hi = __builtin_bit_cast(u32x2, __builtin_amdgcn_ds_read_tr16_b64_v4i16((lds_s16x4)(p + 256)));
            wof[t][ks] = u32x4{lo[0], lo[1], hi[0], hi[1]};
          }
      }
      u32x4 ob[4];
#pragma unroll 1
      for (unsigned spin = 0; spin < (SPIN_CAP << 4); ++spin) {
        asm volatile("" ::: "memory");
        const unsigned f = __hip_atomic_load((unsigned*)(lds + L_FLAG) + F_O, __ATOMIC_RELAXED, __HIP_MEMORY_SCOPE_WORKGROUP);
        asm volatile("" ::: "memory");
#pragma unroll
        for (int ks = 0; ks < 4; ++ks) ob[ks] = *(const u32x4*)(lds + L_OA + ks * 64 + la * 16);
        __builtin_amdgcn_sched_barrier(0);
        if (__builtin_amdgcn_readfirstlane(f) != 0u) break;
      }
      f32x4 d0 = {0.f, 0.f, 0.f, 0.f}, d1 = {0.f, 0.f, 0.f, 0.f};
#pragma unroll
      for (int ks = 0; ks < 4; ++ks) { d0 = mfma32(wof[0][ks], ob[ks], d0); d1 = mfma32(wof[1][ks], ob[ks], d1); }
      const f32x4 d = n ? d1 : d0;
      __builtin_amdgcn_raw_buffer_store_b128(
          u32x4{put_lo(d[0], tagA), put_lo(d[1], tagA >> 8), put_lo(d[2], tagA >> 16), put_lo(d[3], tagA >> 24)}, rws, oo, 0, AUX_SC1);
    }
  }
}

}

extern "C" void kernel_launch(void* const* d_in, const int* in_sizes, int n_in, void* d_out, int out_size, void* d_ws,
                              size_t ws_size, hipStream_t stream) {
  (void)in_sizes; (void)n_in; (void)out_size; (void)ws_size;
  fused_decode_kernel<<<dim3(NWG), dim3(NTHR), 0, stream>>>(
      (const unsigned short*)d_in[0], (const unsigned char*)d_in[1], (const unsigned char*)d_in[2],
      (const unsigned short*)d_in[3], (const unsigned short*)d_in[4], (const unsigned short*)d_in[6],
      (const unsigned short*)d_in[7], (const unsigned short*)d_in[8], (const unsigned short*)d_in[9],
      (unsigned short*)d_out, (unsigned char*)d_ws);
}
```
